```python
import torch

def _softmax(x, axis=-1):
    return torch.softmax(x, dim=axis)
def _gelu(x):
    return torch.nn.functional.gelu(x, approximate="tanh")
def _silu(x):
    return torch.nn.functional.silu(x)
def _sigmoid(x):
    return torch.sigmoid(x)
def _layernorm(x, axis=-1, eps=1e-5):
    m = x.mean(dim=axis, keepdim=True)
    v = x.var(dim=axis, keepdim=True, unbiased=False)
    return (x - m) / torch.sqrt(v + eps)
def _scatter_add(tgt, idx, src, axis=0):
    return tgt.clone().index_add_(axis, idx, src)
def _segment_sum(src, seg, n):
    out = torch.zeros((n,) + src.shape[1:], dtype=src.dtype)
    return out.index_add_(0, seg, src)
def _pad(x, pw):
    flat = tuple(v for pair in reversed(pw) for v in pair)
    return torch.nn.functional.pad(x, flat)

kernel_name = 'concat_heads__compressed_kv_cla__mha_block_cached__decode'

def setup_inputs():

    _w = (lambda a: (lambda t: t.to(torch.bfloat16) if t.is_floating_point() else t)(torch.as_tensor(a)))
    def _A_setup():
        B, H, Sq, Sk, D, M = 4, 32, 1, 4096, 128, 512
        Dc = 4 * D
        return {
            "x": _w((lambda *s: torch.randn(*s, dtype=torch.bfloat16) * 3.2)(B, Sq, M)),
            "c_kv": _w((lambda *s: torch.randn(*s, dtype=torch.bfloat16) * 0.1)(B, Sk, Dc)),
            "w_q": _w((lambda *s: torch.randn(*s, dtype=torch.bfloat16) * 6.4)(M, H, D) * (M ** -0.5)),
            "w_uk": _w((lambda *s: torch.randn(*s, dtype=torch.bfloat16) * 6.4)(Dc, H, D) * (Dc ** -0.5)),
            "w_uv": _w((lambda *s: torch.randn(*s, dtype=torch.bfloat16) * 0.1)(Dc, H, D) * (Dc ** -0.5)),
            "w_o": _w((lambda *s: torch.randn(*s, dtype=torch.bfloat16) * 0.1)(H, D, M) * ((H * D) ** -0.5)),
        }
    _A_in = _A_setup()

    def _B_setup():
        B, Sq, D = 4, 1, 512
        H, Sk, Dh = 32, 4096, 128
        return {
            "x": _w((lambda *s: torch.randn(*s, dtype=torch.bfloat16) * 1.6)(B, Sq, D)),
            "k_cache": _w((lambda *s: torch.randn(*s, dtype=torch.bfloat16) * 1.6)(B, H, Sk, Dh)),
            "v_cache": _w((lambda *s: torch.randn(*s, dtype=torch.bfloat16) * 0.05)(B, H, Sk, Dh)),
            "wq": _w((lambda *s: torch.randn(*s, dtype=torch.bfloat16) * 1.6)(D, H * Dh) * (D ** -0.5)),
            "wo": _w((lambda *s: torch.randn(*s, dtype=torch.bfloat16) * 0.05)(H * Dh, D) * ((H * Dh) ** -0.5)),
        }
    _B_in = _B_setup()

    return {
        "A_x": _A_in["x"],
        "A_c_kv": _A_in["c_kv"],
        "A_w_q": _A_in["w_q"],
        "A_w_uk": _A_in["w_uk"],
        "A_w_uv": _A_in["w_uv"],
        "A_w_o": _A_in["w_o"],
        "B_x": _B_in["x"],
        "B_k_cache": _B_in["k_cache"],
        "B_v_cache": _B_in["v_cache"],
        "B_wq": _B_in["wq"],
        "B_wo": _B_in["wo"],
    }

def reference(A_x, A_c_kv, A_w_q, A_w_uk, A_w_uv, A_w_o, B_x, B_k_cache, B_v_cache, B_wq, B_wo):
    def _A_ref(x, c_kv, w_q, w_uk, w_uv, w_o):
        B, Sq, M = x.shape[0], x.shape[1], x.shape[2]
        Sk = c_kv.shape[1]
        H, D = w_q.shape[1], w_q.shape[2]
        q = torch.einsum("bqm,mhd->bhqd", x, w_q)
        k = torch.einsum("bkc,chd->bhkd", c_kv, w_uk)
        v = torch.einsum("bkc,chd->bhkd", c_kv, w_uv)
        scores = torch.einsum("bhqd,bhkd->bhqk", q, k) * (D ** -0.5)
        off = Sk - Sq
        qi = torch.arange(Sq)[:, None]
        kj = torch.arange(Sk)[None, :]
        cmask = kj <= (qi + off)
        scores = torch.where(cmask[None, None], scores, -1e9)
        p = _softmax(scores, axis=-1)
        ctx = torch.einsum("bhqk,bhkd->bhqd", p, v)
        return torch.einsum("bhqd,hdm->bqm", ctx, w_o)

    def _B_ref(x, k_cache, v_cache, wq, wo):
        B, Sq, D = x.shape
        H, Dh = 32, 128
        q = (lambda x, *d: x.permute(*d))((x @ wq).reshape(B, Sq, H, Dh), 0, 2, 1, 3)
        scores = torch.einsum("bhqd,bhkd->bhqk", q, k_cache) * (Dh ** -0.5)
        p = _softmax(scores, axis=-1)
        o = torch.einsum("bhqk,bhkd->bhqd", p, v_cache)
        return (lambda x, *d: x.permute(*d))(o, 0, 2, 1, 3).reshape(B, Sq, H * Dh) @ wo

    a = _A_ref(A_x, A_c_kv, A_w_q, A_w_uk, A_w_uv, A_w_o)
    b = _B_ref(B_x, B_k_cache, B_v_cache, B_wq, B_wo)
    return torch.concatenate([a, b], axis=-1)
```

```cpp
#include <hip/hip_runtime.h>
#include <hip/hip_bf16.h>
#include <stdint.h>

typedef __attribute__((ext_vector_type(4))) unsigned u32x4;
typedef __attribute__((ext_vector_type(2))) unsigned u32x2;
typedef __attribute__((ext_vector_type(4))) float f32x4;
typedef __attribute__((ext_vector_type(2))) float f32x2;
typedef __attribute__((ext_vector_type(16))) float f32x16;
typedef __attribute__((ext_vector_type(8))) __bf16 bf16x8;
typedef __attribute__((ext_vector_type(2))) __bf16 bf16x2;
typedef __attribute__((address_space(1))) unsigned gu32;
typedef __attribute__((address_space(1))) unsigned long long gu64;
typedef unsigned long long u64;

constexpr int NB = 4;
constexpr int NH = 32;
constexpr int SK = 4096;
constexpr int DH = 128;
constexpr int DM = 512;
constexpr int DC = 512;
constexpr int HD = NH * DH;

constexpr int SPLIT_A = 8;
constexpr int KA = SK / SPLIT_A;
constexpr int SS = 171;
constexpr int RD = 18;
constexpr int NP = SS - 8 * RD + 8;
constexpr int NP4 = (NP + 3) & ~3;
constexpr int RST = 144;
constexpr int GI = 6;
constexpr int RE = 15;
constexpr int PRIOF = 4;

constexpr float SCALE = 0.08838834764831845f;
constexpr float LOG2E = 1.4426950408889634f;
constexpr int NT = 2;
constexpr int SC1 = 16;

constexpr size_t WS_EPA  = 0;
constexpr size_t WS_EPB  = 256;
constexpr size_t WS_F1   = 4096;
constexpr size_t WS_F2   = WS_F1 + 1024;
constexpr size_t WS_FP   = 8192;
constexpr size_t WS_QABS = 16384;
constexpr size_t WS_PAML = WS_QABS + (size_t)NB * NH * DC * 2;
constexpr size_t WS_PAO  = WS_PAML + (size_t)NB * SPLIT_A * NH * 2 * 4;
constexpr size_t WS_PA   = WS_PAO + (size_t)NB * SPLIT_A * NH * DC * 2;
constexpr size_t WS_PBML = WS_PA + (size_t)NB * NH * DM * 8;
constexpr size_t WS_PBG  = WS_PBML + (size_t)NB * 64 * 16;
constexpr size_t WS_END  = WS_PBG + (size_t)NB * 64 * DM * 8;

__device__ __forceinline__ float bf_lo(unsigned u) { return __uint_as_float(u << 16); }
__device__ __forceinline__ float bf_hi(unsigned u) { return __uint_as_float(u & 0xffff0000u); }
__device__ __forceinline__ unsigned cvtpk(float lo, float hi) {
  f32x2 v = {lo, hi};
  return __builtin_bit_cast(unsigned, __builtin_convertvector(v, bf16x2));
}
__device__ __forceinline__ float rbf(float x) { return __uint_as_float(cvtpk(x, x) << 16); }
__device__ __forceinline__ float dot2(unsigned k, unsigned q, float acc) {
  return __builtin_amdgcn_fdot2_f32_bf16(__builtin_bit_cast(bf16x2, k), __builtin_bit_cast(bf16x2, q), acc, false);
}
template <int CTRL> __device__ __forceinline__ float dpp(float x) {
  return __builtin_bit_cast(float, __builtin_amdgcn_mov_dpp(__builtin_bit_cast(int, x), CTRL, 0xf, 0xf, true));
}
__device__ __forceinline__ float sum16(float x) {
  x += dpp<0x128>(x);
  x += dpp<0x124>(x);
  x += dpp<0x4E>(x);
  x += dpp<0xB1>(x);
  return x;
}
__device__ __forceinline__ float xrow_max(float x) {
  auto s = __builtin_amdgcn_permlane16_swap(__float_as_uint(x), __float_as_uint(x), false, false);
  x = fmaxf(__uint_as_float(s[0]), __uint_as_float(s[1]));
  auto t = __builtin_amdgcn_permlane32_swap(__float_as_uint(x), __float_as_uint(x), false, false);
  return fmaxf(__uint_as_float(t[0]), __uint_as_float(t[1]));
}
__device__ __forceinline__ float xrow_sum(float x) {
  auto s = __builtin_amdgcn_permlane16_swap(__float_as_uint(x), __float_as_uint(x), false, false);
  x = __uint_as_float(s[0]) + __uint_as_float(s[1]);
  auto t = __builtin_amdgcn_permlane32_swap(__float_as_uint(x), __float_as_uint(x), false, false);
  return __uint_as_float(t[0]) + __uint_as_float(t[1]);
}
__device__ __forceinline__ float fexp2(float x) { return __builtin_amdgcn_exp2f(x); }
__device__ __forceinline__ __amdgpu_buffer_rsrc_t mkrs(const void* p, unsigned bytes) {
  return __builtin_amdgcn_make_buffer_rsrc((void*)p, 0, (int)bytes, 0x00020000);
}


constexpr int PTHR = 60;
constexpr unsigned SPIN_CAP = 1u << 21;
__device__ __forceinline__ u64 ld_u64(const void* p) {
  return __hip_atomic_load((gu64*)p, __ATOMIC_RELAXED, __HIP_MEMORY_SCOPE_AGENT);
}
__device__ __forceinline__ void st_u64(void* p, u64 v) {
  __hip_atomic_store((gu64*)p, v, __ATOMIC_RELAXED, __HIP_MEMORY_SCOPE_AGENT);
}
__device__ __forceinline__ u64 next_tag(u64 e) { return e + 0x0000000100000001ull; }
__device__ __forceinline__ u64 uni64(u64 v) {
  const unsigned lo = __builtin_amdgcn_readfirstlane((unsigned)v), hi = __builtin_amdgcn_readfirstlane((unsigned)(v >> 32));
  return ((u64)hi << 32) | lo;
}
__device__ __forceinline__ void signal_flag(char* flag, u64 tag) {
  asm volatile("s_waitcnt vmcnt(0)" ::: "memory");
  __syncthreads();
  if (threadIdx.x == 0) st_u64(flag, tag);
}
__device__ __forceinline__ void wait_gate(const char* flag, u64 tag) {
  if (threadIdx.x < 64) {
    for (unsigned spins = 0; spins < SPIN_CAP; ++spins) {
      if (ld_u64(flag) == tag) break;
      __builtin_amdgcn_s_sleep(4);
    }
  }
}
__device__ __forceinline__ void st16_sc1(__amdgpu_buffer_rsrc_t rs, unsigned off, f32x4 v) {
  __builtin_amdgcn_raw_buffer_store_b128(__builtin_bit_cast(u32x4, v), rs, off, 0, SC1);
}
__device__ __forceinline__ void st16u_sc1(__amdgpu_buffer_rsrc_t rs, unsigned off, u32x4 v) {
  __builtin_amdgcn_raw_buffer_store_b128(v, rs, off, 0, SC1);
}
__device__ __forceinline__ f32x4 ld16_sc1(__amdgpu_buffer_rsrc_t rs, unsigned off) {
  return __builtin_bit_cast(f32x4, __builtin_amdgcn_raw_buffer_load_b128(rs, off, 0, SC1));
}
__device__ __forceinline__ u32x4 ld16u_sc1(__amdgpu_buffer_rsrc_t rs, unsigned off) {
  return __builtin_amdgcn_raw_buffer_load_b128(rs, off, 0, SC1);
}
__device__ __forceinline__ void st8_sc1(void* p, f32x2 v) { st_u64(p, __builtin_bit_cast(u64, v)); }
__device__ __forceinline__ f32x2 ld8_sc1(const void* p) { return __builtin_bit_cast(f32x2, ld_u64(p)); }


__device__ __forceinline__ void item_dots(const u32x4 (&ks)[4], const u32x4 q, float (&s_)[4]) {
#pragma unroll
  for (int u = 0; u < 4; u++) {
    float a = dot2(ks[u][0], q[0], 0.f);
    a = dot2(ks[u][1], q[1], a);
    a = dot2(ks[u][2], q[2], a);
    a = dot2(ks[u][3], q[3], a);
    s_[u] = a;
    asm volatile("" : "+v"(s_[u]));
  }
}
__device__ __forceinline__ void item_unpack(const u32x4 (&vs)[4], f32x2 (&vf)[4][4]) {
#pragma unroll
  for (int u = 0; u < 4; u++)
#pragma unroll
    for (int j = 0; j < 4; j++) { vf[u][j] = f32x2{bf_lo(vs[u][j]), bf_hi(vs[u][j])}; asm volatile("" : "+v"(vf[u][j])); }
}
__device__ __forceinline__ f32x2 rows_sum(const float (&o)[8]) {
  float r4[4], r2[2];
#pragma unroll
  for (int j = 0; j < 4; j++) {
    auto sx = __builtin_amdgcn_permlane32_swap(__float_as_uint(o[j]), __float_as_uint(o[j + 4]), false, false);
    r4[j] = __uint_as_float(sx[0]) + __uint_as_float(sx[1]);
  }
#pragma unroll
  for (int j = 0; j < 2; j++) {
    auto sx = __builtin_amdgcn_permlane16_swap(__float_as_uint(r4[j]), __float_as_uint(r4[j + 2]), false, false);
    r2[j] = __uint_as_float(sx[0]) + __uint_as_float(sx[1]);
  }
  return f32x2{r2[0], r2[1]};
}
__device__ __forceinline__ f32x2 item_partial(const float (&s_)[4], const f32x2 (&vf)[4][4], float& mx, float& ll) {
  float s[4];
#pragma unroll
  for (int u = 0; u < 4; u++) s[u] = rbf(rbf(sum16(s_[u])) * SCALE);
  mx = xrow_max(fmaxf(fmaxf(s[0], s[1]), fmaxf(s[2], s[3])));
  float p[4];
#pragma unroll
  for (int u = 0; u < 4; u++) p[u] = fexp2((s[u] - mx) * LOG2E);
  ll = xrow_sum((p[0] + p[1]) + (p[2] + p[3]));
  float o[8];
#pragma unroll
  for (int j = 0; j < 4; j++) {
    f32x2 v = vf[0][j] * p[0];
#pragma unroll
    for (int u = 1; u < 4; u++) v = __builtin_elementwise_fma(vf[u][j], f32x2{p[u], p[u]}, v);
    o[2 * j] = v[0]; o[2 * j + 1] = v[1];
  }
  return rows_sum(o);
}
__device__ __forceinline__ void role_B(int h, int j6, const __hip_bfloat16* __restrict__ xB,
                                       const __hip_bfloat16* __restrict__ kc, const __hip_bfloat16* __restrict__ vc,
                                       const __hip_bfloat16* __restrict__ wqB, const __hip_bfloat16* __restrict__ woB,
                                       char* __restrict__ ws, __amdgpu_buffer_rsrc_t rs,
                                       __hip_bfloat16* __restrict__ out, char* lds) {
  const int t = threadIdx.x, l = t & 63;
  const int w = __builtin_amdgcn_readfirstlane(t >> 6);
  const int g = l >> 4, c = l & 15;
  const int pr = j6 >= 3 ? 1 : 0, k = j6 - 3 * pr;
  const int b0 = 2 * pr;
  const int NI = k == 1 ? 2 * (256 - SS) : SS;
  const int e0 = k == 1 ? (h & 1) : (k >> 1);
  u32x4 xr[2][2], wr[16];
  {
    const __amdgpu_buffer_rsrc_t rx = mkrs(xB, NB * DM * 2);
    const unsigned xo = (unsigned)((b0 * DM + w * 64 + g * 16) * 2);
#pragma unroll
    for (int bb = 0; bb < 2; bb++)
#pragma unroll
      for (int i = 0; i < 2; i++) xr[bb][i] = __builtin_amdgcn_raw_buffer_load_b128(rx, xo + bb * DM * 2 + i * 16, 0, 0);
    const __amdgpu_buffer_rsrc_t rq = mkrs(wqB, DM * HD * 2);
    const unsigned vo = (unsigned)(((w * 64 + g * 16) * HD + h * DH + c * 8) * 2);
#pragma unroll
    for (int i = 0; i < 16; i++) wr[i] = __builtin_amdgcn_raw_buffer_load_b128(rq, vo, i * HD * 2, NT);
  }
  constexpr unsigned KVB = (unsigned)((size_t)NB * NH * SK * DH * 2);
  const __amdgpu_buffer_rsrc_t rk = mkrs(kc, KVB), rv = mkrs(vc, KVB);
  const unsigned lo = (unsigned)(g * 256 + c * 16);
  const unsigned hb0 = ((unsigned)((b0 + e0) * NH + h) << 20) + (k == 1 ? (unsigned)SS << 12 : 0u);
  const unsigned hb1 = ((unsigned)((b0 + 1 - e0) * NH + h) << 20) + ((unsigned)(2 * SS - 256) << 12);
  auto soff = [&](int x) { return ((k == 1 && x >= 256 - SS) ? hb1 : hb0) + ((unsigned)x << 12); };
  auto ld4 = [&](u32x4 (&d)[4], __amdgpu_buffer_rsrc_t r, unsigned so) {
#pragma unroll
    for (int u = 0; u < 4; u++) d[u] = __builtin_amdgcn_raw_buffer_load_b128(r, lo + u * 1024, so, NT);
  };
  u32x4 kr[4], vr[4], ke[4], ve[4];
  ld4(kr, rk, soff(w));
  ld4(vr, rv, soff(w));
  u64* eps = (u64*)(lds + 33808);
  u64 epv = 0;
  if (t < 3) epv = ld_u64(ws + (t == 0 ? WS_EPA : WS_EPB + 256 * (b0 + (t == 1 ? e0 : 1 - e0))));
  float* red = (float*)lds;
  unsigned* qs = (unsigned*)(lds + 32768);
  unsigned* cnt0 = (unsigned*)(lds + 33792);
  unsigned* cntd = (unsigned*)(lds + 33796);
  float* rec0 = (float*)(lds + 36864);
  float* mlr0 = (float*)(lds + 40960);
  char* prv = lds + 45056 + w * 512;
  float* recd = (float*)(lds + 65536);
  float* mld = (float*)(lds + 98304);
  const unsigned cnta = (unsigned)(uintptr_t)(__attribute__((address_space(3))) char*)cntd;
  float* mxd = (float*)(lds + 33800);
  if (t == 0) { *cnt0 = 0u; *cntd = 8u * (RD + 1); mxd[0] = -INFINITY; mxd[1] = -INFINITY; }
  if (t < 64 * (NP4 - NP + 1)) *(f32x2*)(recd + (NP - 1 + (t >> 6)) * RST + 2 * l) = f32x2{0.f, 0.f};
  {
    float a0[8], a1[8];
#pragma unroll
    for (int j = 0; j < 8; j++) { a0[j] = 0.f; a1[j] = 0.f; }
#pragma unroll
    for (int i = 0; i < 16; i += 2) {
      const unsigned x0 = xr[0][i >> 3][(i >> 1) & 3], x1 = xr[1][i >> 3][(i >> 1) & 3];
#pragma unroll
      for (int j = 0; j < 4; j++) {
        const unsigned pl = __builtin_amdgcn_perm(wr[i + 1][j], wr[i][j], 0x05040100u);
        const unsigned ph = __builtin_amdgcn_perm(wr[i + 1][j], wr[i][j], 0x07060302u);
        a0[2 * j] = dot2(pl, x0, a0[2 * j]); a0[2 * j + 1] = dot2(ph, x0, a0[2 * j + 1]);
        a1[2 * j] = dot2(pl, x1, a1[2 * j]); a1[2 * j + 1] = dot2(ph, x1, a1[2 * j + 1]);
      }
    }
#pragma unroll
    for (int j = 0; j < 8; j++) asm volatile("" : "+v"(a0[j]), "+v"(a1[j]));
    __builtin_amdgcn_sched_barrier(0);
    ld4(ke, rk, soff(w + 8));
    ld4(ve, rv, soff(w + 8));
    __builtin_amdgcn_sched_barrier(0);
    float* rp = red + (w * 4 + g) * 256 + c * 8;
    *(f32x4*)(rp) = f32x4{a0[0], a0[1], a0[2], a0[3]};
    *(f32x4*)(rp + 4) = f32x4{a0[4], a0[5], a0[6], a0[7]};
    *(f32x4*)(rp + 128) = f32x4{a1[0], a1[1], a1[2], a1[3]};
    *(f32x4*)(rp + 132) = f32x4{a1[4], a1[5], a1[6], a1[7]};
  }
  __syncthreads();
  {
    const int o = t >> 1, hf = t & 1;
    float s = 0.f;
#pragma unroll
    for (int i = 0; i < 16; i++) s += red[(hf * 16 + i) * 256 + o];
    s += dpp<0xB1>(s);
    const float sn = dpp<0x4E>(s);
    if ((t & 3) == 0) qs[o >> 1] = cvtpk(s, sn);
    if (t < 3) eps[t] = epv;
  }
  __syncthreads();
  const u64 epa = uni64(eps[0]), tg0 = next_tag(uni64(eps[1])), tg1 = next_tag(uni64(eps[2]));
  u32x4 q = *(const u32x4*)(qs + e0 * 64 + c * 4);
  const u32x4 q1 = *(const u32x4*)(qs + (1 - e0) * 64 + c * 4);
  float m = -INFINITY, lsum = 0.f, acc[8];
#pragma unroll
  for (int j = 0; j < 8; j++) acc[j] = 0.f;
  float s_[4]; f32x2 vf[4][4];
  auto part0 = [&](u32x4 (&ks)[4]) { item_dots(ks, q, s_); };
  auto part1 = [&](u32x4 (&vs)[4]) { item_unpack(vs, vf); };
  auto part2 = [&]() {
    float s[4];
#pragma unroll
    for (int u = 0; u < 4; u++) s[u] = rbf(rbf(sum16(s_[u])) * SCALE);
    const float mx = fmaxf(fmaxf(m, fmaxf(s[0], s[1])), fmaxf(s[2], s[3]));
    const float alpha = fexp2((m - mx) * LOG2E);
    m = mx;
    float p[4];
#pragma unroll
    for (int u = 0; u < 4; u++) p[u] = fexp2((s[u] - mx) * LOG2E);
    lsum = lsum * alpha + ((p[0] + p[1]) + (p[2] + p[3]));
#pragma unroll
    for (int j = 0; j < 8; j++) acc[j] *= alpha;
#pragma unroll
    for (int u = 0; u < 4; u++)
#pragma unroll
      for (int j = 0; j < 4; j++) {
        acc[2 * j]     = fmaf(p[u], vf[u][j][0], acc[2 * j]);
        acc[2 * j + 1] = fmaf(p[u], vf[u][j][1], acc[2 * j + 1]);
      }
  };
  auto front = [&](__amdgpu_buffer_rsrc_t rk2, __amdgpu_buffer_rsrc_t rv2, unsigned so) {
    __builtin_amdgcn_sched_barrier(0);
    ld4(kr, rk2, so);
    __builtin_amdgcn_sched_barrier(0);
    part1(vr);
    __builtin_amdgcn_sched_barrier(0);
    ld4(vr, rv2, so);
    __builtin_amdgcn_sched_barrier(0);
  };
  auto put_partial = [&](f32x2 r2, float mw, float ll, float* rec_, float* ml_, float* mx_) {
    *(f32x2*)(rec_ + c * 8 + g * 2) = r2;
    if (l == 0) {
      *(f32x2*)ml_ = f32x2{mw, ll};
      asm volatile("ds_max_f32 %0, %1" :: "v"((unsigned)(uintptr_t)(__attribute__((address_space(3))) float*)mx_), "v"(mw) : "memory");
    }
  };
  auto wave_partial = [&](float* rec_, float* ml_, float* mx_) {
    const float mw = xrow_max(m);
    const float fq = fexp2((m - mw) * LOG2E);
    const float ll = xrow_sum(lsum * fq);
    float o[8];
#pragma unroll
    for (int j = 0; j < 8; j++) o[j] = acc[j] * fq;
    put_partial(rows_sum(o), mw, ll, rec_, ml_, mx_);
  };
  auto part2c = [&](float* rec_, float* ml_) {
    float mx, ll;
    const f32x2 r2 = item_partial(s_, vf, mx, ll);
    put_partial(r2, mx, ll, rec_, ml_, mxd);
  };
  bool sw = k != 1;
  auto do_switch = [&]() {
    wave_partial(rec0 + w * 128, mlr0 + w * 2, mxd + 1);
    asm volatile("s_waitcnt lgkmcnt(0)" ::: "memory");
    if (l == 0) asm volatile("ds_add_u32 %0, %1" :: "v"(cnta - 4u), "v"(1u) : "memory");
    m = -INFINITY; lsum = 0.f;
#pragma unroll
    for (int j = 0; j < 8; j++) acc[j] = 0.f;
    q = q1; sw = true;
  };
  if (w >= 4) __builtin_amdgcn_s_setprio(1);
  part0(kr);
  front(rk, rv, soff(w + 16));
  part2();
  part0(ke); part1(ve); part2();
  int x = w + 16;
  const int rg = l >> 3, cc = l & 7;
  const int rw = 3 + 4 * (h >> 3);
  const int re = k != 1 ? -1 : rw + 2 > RE ? rw + 2 : RE;
  u32x4 wo[16];
  __builtin_amdgcn_sched_barrier(0);
#pragma unroll
  for (int i = 0; i < 16; i++) asm volatile("" : "=v"(wo[i]));
  const int nseg = k == 1 ? 2 : 1, sp = k == 1 ? 1 : 0;
  auto store_off = [&](int sg) {
    const int b = b0 + (sg ? 1 - e0 : e0);
    const unsigned og_ = (unsigned)(WS_PBG + ((size_t)(b * 64 + h * 2 + sp) * DM + w * 64 + cc * 8 + g * 2) * 8);
    const unsigned om_ = (unsigned)(WS_PBML + (size_t)(b * 64 + h * 2 + sp) * 16);
    return (l & 8) == 0 ? og_ : t == 8 ? om_ : 0x7ffffff0u;
  };
  const unsigned apo = (unsigned)((c >> 3) * 256 + (g * 32 + ((c >> 2) & 1) * 16) * 2);
  auto project = [&](const char* ap, float M, float L, bool lred) {
    const u32x4 a0 = *(const u32x4*)(ap), a1 = *(const u32x4*)(ap + 16);
    f32x4 de = {0.f, 0.f, 0.f, 0.f}, dq = {0.f, 0.f, 0.f, 0.f};
#pragma unroll
    for (int p = 0; p < 8; p++) {
      const unsigned d = p < 4 ? a0[p & 3] : a1[p & 3];
      const u32x4 af = {(c & 3) == 0 ? d : 0u, (c & 3) == 1 ? d : 0u, (c & 3) == 2 ? d : 0u, (c & 3) == 3 ? d : 0u};
      de = __builtin_amdgcn_mfma_f32_16x16x32_bf16(__builtin_bit_cast(bf16x8, af), __builtin_bit_cast(bf16x8, wo[2 * p]), de, 0, 0, 0);
      dq = __builtin_amdgcn_mfma_f32_16x16x32_bf16(__builtin_bit_cast(bf16x8, af), __builtin_bit_cast(bf16x8, wo[2 * p + 1]), dq, 0, 0, 0);
    }
    if (lred && w == 0) L = xrow_sum(sum16(L));
    const bool val = (g & 1) == ((l >> 3) & 1);
    float r4[4], r2[2];
#pragma unroll
    for (int j = 0; j < 4; j++) {
      const float u0 = (j & 1) ? dq[j >> 1] : de[j >> 1], u1 = (j & 1) ? dq[2 + (j >> 1)] : de[2 + (j >> 1)];
      auto sx = __builtin_amdgcn_permlane32_swap(__float_as_uint(u0), __float_as_uint(u1), false, false);
      const float v = __uint_as_float(sx[0]) + __uint_as_float(sx[1]);
      r4[j] = val ? v : 0.f;
    }
#pragma unroll
    for (int j = 0; j < 2; j++) {
      auto sx = __builtin_amdgcn_permlane16_swap(__float_as_uint(r4[j]), __float_as_uint(r4[j + 2]), false, false);
      const float v = __uint_as_float(sx[0]) + __uint_as_float(sx[1]);
      r2[j] = v + dpp<0x128>(v);
    }
    const bool lo8 = (l & 8) == 0;
    return f32x2{lo8 ? r2[0] : M, lo8 ? r2[1] : L};
  };
  auto publish = [&](f32x2 v, u64 tg, unsigned sto) {
    st16u_sc1(rs, sto, u32x4{__float_as_uint(v[0]), (unsigned)tg, __float_as_uint(v[1]), (unsigned)(tg >> 32)});
  };
  f32x2 seg0 = {0.f, 0.f};
#pragma unroll 1
  for (; x < 8 * RD; x += 8) {
    const int rd = x >> 3;
    if (rd == PRIOF) __builtin_amdgcn_s_setprio(0);
    if (!sw && x >= 256 - SS) do_switch();
    if (rd == rw + 2) {
#pragma unroll
      for (int p = 0; p < 8; p++) {
#pragma unroll
        for (int i = 0; i < 4; i++) {
          const unsigned u0 = wo[2 * p][i], u1 = wo[2 * p + 1][i];
          wo[2 * p][i] = __builtin_amdgcn_perm(u1, u0, 0x05040100u);
          wo[2 * p + 1][i] = __builtin_amdgcn_perm(u1, u0, 0x07060302u);
        }
        asm volatile("" : "+v"(wo[2 * p]), "+v"(wo[2 * p + 1]));
      }
    }
    if (rd == re) {
    for (unsigned spins = 0; __hip_atomic_load(cnt0, __ATOMIC_RELAXED, __HIP_MEMORY_SCOPE_WORKGROUP) != 8u && spins < SPIN_CAP; ++spins)
      __builtin_amdgcn_s_sleep(1);
    asm volatile("" ::: "memory");
    f32x4 q4[4];
#pragma unroll
    for (int i = 0; i < 4; i++) q4[i] = *(const f32x4*)(mlr0 + i * 4);
    const float M = mxd[1];
    float L = 0.f;
    f32x2 a2 = {0.f, 0.f};
#pragma unroll
    for (int i = 0; i < 8; i++) {
      const float fw = fexp2((q4[i >> 1][(i & 1) * 2] - M) * LOG2E);
      L = fmaf(q4[i >> 1][(i & 1) * 2 + 1], fw, L);
      a2 += *(const f32x2*)(rec0 + i * 128 + 2 * l) * fw;
    }
    const unsigned hi = cvtpk(a2[0], a2[1]);
    *(unsigned*)(prv + l * 4) = hi;
    *(unsigned*)(prv + 256 + l * 4) = cvtpk(a2[0] - bf_lo(hi), a2[1] - bf_hi(hi));
    seg0 = project(prv + apo, M, L, false);
    }
    if (rd == rw) {
      const __amdgpu_buffer_rsrc_t ro = mkrs(woB, HD * DM * 2);
      const unsigned vo = (unsigned)(((h * DH + rg * 16) * DM + w * 64 + cc * 8) * 2);
#pragma unroll
      for (int i = 0; i < 16; i++) wo[i] = __builtin_amdgcn_raw_buffer_load_b128(ro, vo, i * DM * 2, NT);
    }
    part0(kr);
    front(rk, rv, soff(x + 8));
    part2();
  }
  wave_partial(recd + w * RST, mld + w * 2, mxd);
  unsigned o_ml = 98304u + (l < NP ? l : 0) * 8, o_rv = 65536u + (g * RST + w * 16 + c) * 4, o_fw = 45056u + w * 512 + ((l & 3) * 16 + (l >> 2)) * 4;
  unsigned o_fr = 45056u + w * 512 + g * 64, o_oi = (g < 2 && (c & 1) == 0) ? 41216u + g * 256 + (w * 16 + c) * 2 : 45056u + w * 512 + 256 + l * 4;
  unsigned o_ap = 41216u + apo, o_st = store_off(nseg - 1);
  unsigned o_s0 = k == 1 ? store_off(0) : 0x7ffffff0u;
  asm volatile("" : "+v"(o_s0));
  asm volatile("" : "+v"(o_ml), "+v"(o_rv), "+v"(o_fw), "+v"(o_fr), "+v"(o_oi), "+v"(o_ap), "+v"(o_st));
  {
    {
#pragma unroll 1
      while (x < NI) {
        unsigned dr; u64 sv;
        asm volatile("s_mov_b64 %1, exec\n\ts_mov_b64 exec, 1\n\tds_add_rtn_u32 %0, %2, %3\n\ts_mov_b64 exec, %1"
                     : "=&v"(dr), "=&s"(sv) : "v"(cnta), "v"(1u), "v"(kr[0][0]) : "memory");
        __builtin_amdgcn_sched_barrier(0);
        part0(kr);
        asm volatile("s_waitcnt lgkmcnt(0)" : "+v"(dr) :: "memory");
        const int xn = __builtin_amdgcn_readfirstlane(dr);
        if (h >= 24 && j6 == 2 && xn == NI - GI && l == 0) st_u64(ws + WS_FP, next_tag(epa));
        const bool ok = xn < NI;
        front(mkrs(kc, ok ? KVB : 0u), mkrs(vc, ok ? KVB : 0u), ok ? soff(xn) : 0u);
        part2c(recd + (x - 8 * RD + 8) * RST, mld + (x - 8 * RD + 8) * 2);
        x = xn;
      }
      asm volatile("s_waitcnt lgkmcnt(0)" ::: "memory");
      __syncthreads();
    }
    {
      f32x2 mli = *(const f32x2*)(lds + o_ml);
      float rv_[NP4 / 4];
#pragma unroll
      for (int r = 0; r < NP4 / 4; r++) rv_[r] = *(const float*)(lds + o_rv + r * 4 * RST * 4);
      if (l >= NI - 8 * RD + 8) mli = f32x2{-INFINITY, 0.f};
      const float M = *mxd;
      const float fi = fexp2((mli[0] - M) * LOG2E);
      *(float*)(lds + o_fw) = fi;
      f32x4 f4[(NP4 / 4 + 3) / 4];
#pragma unroll
      for (int i = 0; i < (NP4 / 4 + 3) / 4; i++) f4[i] = *(const f32x4*)(lds + o_fr + i * 16);
      float a = 0.f;
#pragma unroll
      for (int r = 0; r < NP4 / 4; r++) a = fmaf(f4[r >> 2][r & 3], rv_[r], a);
      a = xrow_sum(a);
      const float an = dpp<0xB1>(a);
      const unsigned hi = cvtpk(a, an);
      const unsigned lw = cvtpk(a - bf_lo(hi), an - bf_hi(hi));
      *(unsigned*)(lds + o_oi) = g ? lw : hi;
      __syncthreads();
      publish(project(lds + o_ap, M, fi * mli[1], true), k == 1 ? tg1 : tg0, o_st);
      publish(seg0, tg0, o_s0);
    }
    if (w < nseg) {
      const int sg = w;
      const int b = b0 + (sg ? 1 - e0 : e0);
      const int j64 = h * 2 + sp;
      const u64 tg = sg ? tg1 : tg0;
      const unsigned ta = (unsigned)tg, tb = (unsigned)(tg >> 32);
      const unsigned oml = (unsigned)(WS_PBML + (size_t)(b * 64 + l) * 16);
      const unsigned om4 = (unsigned)(WS_PBML + (size_t)(b * 64 + (l >> 2)) * 16);
      const unsigned og = (unsigned)(WS_PBG + ((size_t)(b * 64 + (l >> 2)) * DM + j64 * 8) * 8 + (l & 3) * 16);
      u32x4 ml[4], p[4];
      __builtin_amdgcn_s_sleep(6);
#pragma unroll 1
      for (unsigned spins = 0; spins < SPIN_CAP; ++spins) {
        asm volatile("" ::: "memory");
#pragma unroll
        for (int i = 0; i < 4; i++) ml[i] = ld16u_sc1(rs, om4 + i * 256);
#pragma unroll
        for (int i = 0; i < 4; i++) p[i] = ld16u_sc1(rs, og + i * 16 * DM * 8);
        int n = 0;
#pragma unroll
        for (int i = 0; i < 4; i++) n += __popcll(__ballot(ml[i][1] == ta && ml[i][3] == tb && p[i][1] == ta && p[i][3] == tb));
        if (n == 256) break;
#pragma unroll 1
        for (; n < 4 * PTHR && spins < SPIN_CAP; ++spins) {
          __builtin_amdgcn_s_sleep(4);
          asm volatile("" ::: "memory");
          const u32x4 m1 = ld16u_sc1(rs, oml);
          n = 4 * __popcll(__ballot(m1[1] == ta && m1[3] == tb));
        }
      }
      float v0 = 0.f, v1 = 0.f;
#pragma unroll
      for (int i = 0; i < 4; i++) {
        const float mm = __uint_as_float(ml[i][0]);
        const float Mh = fmaxf(mm, dpp<0x141>(mm));
        const float fq = fexp2((mm - Mh) * LOG2E);
        float L = fq * __uint_as_float(ml[i][2]);
        L += dpp<0x141>(L);
        const float wgt = fq * __builtin_amdgcn_rcpf(L);
        v0 = fmaf(__uint_as_float(p[i][0]), wgt, v0);
        v1 = fmaf(__uint_as_float(p[i][2]), wgt, v1);
      }
      v0 += dpp<0x128>(v0); v0 += dpp<0x124>(v0); v0 = xrow_sum(v0);
      v1 += dpp<0x128>(v1); v1 += dpp<0x124>(v1); v1 = xrow_sum(v1);
      if (l < 4) {
        *(unsigned*)(out + b * 1024 + 512 + j64 * 8 + l * 2) = cvtpk(v0, v1);
        if (j64 == 0 && l == 0) st_u64(ws + WS_EPB + 256 * b, tg);
      }
    }
  }
}

__device__ __forceinline__ unsigned lds_off(int row, int ch) {
  return 256 * row + 16 * (ch ^ (((row & 3) << 2) | ((row >> 2) & 3)));
}

__device__ __forceinline__ void role_A1(int h, const __hip_bfloat16* __restrict__ xA, const __hip_bfloat16* __restrict__ wqA,
                                        const __hip_bfloat16* __restrict__ wuk, const __hip_bfloat16* __restrict__ wuv,
                                        const __hip_bfloat16* __restrict__ woA, char* __restrict__ ws,
                                        __amdgpu_buffer_rsrc_t rs, __hip_bfloat16* __restrict__ out, char* lds) {
  const int t = threadIdx.x, l = t & 63;
  const int w = __builtin_amdgcn_readfirstlane(t >> 6);
  const int g = l >> 4, c = l & 15;
  float* red = (float*)(lds + 16384);
  const unsigned vo = (unsigned)(((w * 64 + g * 16) * HD + h * DH + c * 8) * 2);
  u64 tag;
  {
    u32x4 xr[NB][2], wr[16], wk[16];
    const __amdgpu_buffer_rsrc_t rx = mkrs(xA, NB * DM * 2);
    const unsigned xo = (unsigned)((w * 64 + g * 16) * 2);
#pragma unroll
    for (int bb = 0; bb < NB; bb++)
#pragma unroll
      for (int i = 0; i < 2; i++) xr[bb][i] = __builtin_amdgcn_raw_buffer_load_b128(rx, xo + bb * DM * 2 + i * 16, 0, 0);
    const __amdgpu_buffer_rsrc_t rq = mkrs(wqA, DM * HD * 2);
#pragma unroll
    for (int i = 0; i < 16; i++) wr[i] = __builtin_amdgcn_raw_buffer_load_b128(rq, vo, i * HD * 2, NT);
    u64 epv = 0;
    if (t == 0) epv = ld_u64(ws + WS_EPA);
    unsigned* qs = (unsigned*)lds;
    unsigned short* qo = (unsigned short*)(lds + 4096);
    float acc[NB][8];
#pragma unroll
    for (int bb = 0; bb < NB; bb++)
#pragma unroll
      for (int j = 0; j < 8; j++) acc[bb][j] = 0.f;
#pragma unroll
    for (int i = 0; i < 16; i += 2) {
#pragma unroll
      for (int j = 0; j < 4; j++) {
        const unsigned pl = __builtin_amdgcn_perm(wr[i + 1][j], wr[i][j], 0x05040100u);
        const unsigned ph = __builtin_amdgcn_perm(wr[i + 1][j], wr[i][j], 0x07060302u);
#pragma unroll
        for (int bb = 0; bb < NB; bb++) {
          const unsigned x = xr[bb][i >> 3][(i >> 1) & 3];
          acc[bb][2 * j] = dot2(pl, x, acc[bb][2 * j]);
          acc[bb][2 * j + 1] = dot2(ph, x, acc[bb][2 * j + 1]);
        }
      }
    }
    float* rp = red + (w * 4 + g) * 512 + c * 8;
#pragma unroll
    for (int bb = 0; bb < NB; bb++) {
      *(f32x4*)(rp + bb * 128) = f32x4{acc[bb][0], acc[bb][1], acc[bb][2], acc[bb][3]};
      *(f32x4*)(rp + bb * 128 + 4) = f32x4{acc[bb][4], acc[bb][5], acc[bb][6], acc[bb][7]};
    }
    __syncthreads();
    {
      const __amdgpu_buffer_rsrc_t rku = mkrs(wuk, DC * HD * 2);
#pragma unroll
      for (int i = 0; i < 16; i++) wk[i] = __builtin_amdgcn_raw_buffer_load_b128(rku, vo, i * HD * 2, NT);
    }
    {
      float s = 0.f;
#pragma unroll
      for (int i = 0; i < 32; i++) s += red[i * 512 + t];
      const float sn = dpp<0xB1>(s);
      if ((t & 1) == 0) qs[t >> 1] = cvtpk(s, sn);
      if (t == 0) *(u64*)(lds + 2048) = epv;
    }
    __syncthreads();
    tag = next_tag(uni64(*(const u64*)(lds + 2048)));
    {
      u32x4 qq[NB];
#pragma unroll
      for (int bb = 0; bb < NB; bb++) qq[bb] = *(const u32x4*)(qs + bb * 64 + c * 4);
#pragma unroll
      for (int i = 0; i < 16; i++) {
        float s[NB];
#pragma unroll
        for (int bb = 0; bb < NB; bb++) {
          float v = dot2(wk[i][0], qq[bb][0], 0.f);
          v = dot2(wk[i][1], qq[bb][1], v);
          v = dot2(wk[i][2], qq[bb][2], v);
          v = dot2(wk[i][3], qq[bb][3], v);
          s[bb] = sum16(v);
        }
        const float v = (c & 2) ? ((c & 1) ? s[3] : s[2]) : ((c & 1) ? s[1] : s[0]);
        if (c < NB) qo[c * 512 + w * 64 + g * 16 + i] = (unsigned short)(cvtpk(v, v) & 0xffff);
      }
    }
    __syncthreads();
    if (t < 256) {
      const int bb = t >> 6, ch = t & 63;
      st16u_sc1(rs, (unsigned)(WS_QABS + ((size_t)(bb * NH + h) * DC + ch * 8) * 2), *(const u32x4*)(qo + bb * 512 + ch * 8));
    }
    signal_flag(ws + WS_F1 + h * 8, tag);
  }
  const unsigned ta = (unsigned)tag, tb = (unsigned)(tag >> 32);
  {
    u32x4 bv[8][2], bo[8][2];
    u32x4 pre[2][8], wo[16];
    {
      const __amdgpu_buffer_rsrc_t ruv = mkrs(wuv, DC * HD * 2);
      const __amdgpu_buffer_rsrc_t rwo = mkrs(woA, HD * DM * 2);
      const unsigned wvo = (unsigned)(((h * DH + (l >> 3) * 16) * DM + w * 64 + (l & 7) * 8) * 2);
#pragma unroll
      for (int i = 0; i < 16; i++) pre[i >> 3][i & 7] = __builtin_amdgcn_raw_buffer_load_b128(ruv, vo, i * HD * 2, NT);
#pragma unroll
      for (int i = 0; i < 16; i++) wo[i] = __builtin_amdgcn_raw_buffer_load_b128(rwo, wvo, i * DM * 2, NT);
    }
#pragma unroll
    for (int j = 0; j < 8; j++)
#pragma unroll
      for (int s = 0; s < 2; s++) {
#pragma unroll
        for (int e = 0; e < 4; e++)
          bv[j][s][e] = __builtin_amdgcn_perm(pre[s][2 * e + 1][j >> 1], pre[s][2 * e][j >> 1], (j & 1) ? 0x07060302u : 0x05040100u);
        asm volatile("" : "+v"(bv[j][s]));
      }
#pragma unroll
    for (int j = 0; j < 8; j++)
#pragma unroll
      for (int s = 0; s < 2; s++) {
#pragma unroll
        for (int e = 0; e < 4; e++)
          bo[j][s][e] = __builtin_amdgcn_perm(wo[8 * s + 2 * e + 1][j >> 1], wo[8 * s + 2 * e][j >> 1], (j & 1) ? 0x07060302u : 0x05040100u);
        asm volatile("" : "+v"(bo[j][s]));
      }
    constexpr int LAS = 1040;
    char* la = lds;
    unsigned short* cs = (unsigned short*)(lds + 16384);
    float* outs = (float*)(lds + 20480);
    float* cred = (float*)(lds + 32768);
    {
    {
    wait_gate(ws + WS_FP, tag);
    __syncthreads();
    }
    if (t < 256) {
      const int bb = t >> 6, ch = t & 63;
      u32x4 ov[SPLIT_A];
      float mm[SPLIT_A], ll[SPLIT_A];
      float M = -INFINITY;
      u32x2 mv[SPLIT_A];
      {
#pragma unroll
        for (int s = 0; s < SPLIT_A; s++) { asm volatile("" : "=v"(ov[s])); asm volatile("" : "=v"(mv[s])); }
        unsigned got = 0;
#pragma unroll 1
        for (unsigned spins = 0; got != 0xffu && spins < SPIN_CAP; ++spins) {
          const u64 f = ld_u64(ws + WS_F2 + (w * SPLIT_A + (l & 7)) * 8);
          const unsigned now = (unsigned)__ballot(f == tag) & 0xffu;
          const unsigned fresh = now & ~got;
#pragma unroll
          for (int s = 0; s < SPLIT_A; s++)
            if ((fresh >> s) & 1u) {
              ov[s] = ld16u_sc1(rs, (unsigned)(WS_PAO + (((size_t)(bb * SPLIT_A + s) * NH + h) * DC + ch * 8) * 2));
              mv[s] = __builtin_amdgcn_raw_buffer_load_b64(rs, (unsigned)(WS_PAML + ((size_t)(bb * SPLIT_A + s) * NH + h) * 8), 0, SC1);
            }
          got = now;
          if (got != 0xffu) __builtin_amdgcn_s_sleep(6);
        }
      }
#pragma unroll
      for (int s = 0; s < SPLIT_A; s++) {
        mm[s] = __uint_as_float(mv[s][0]); ll[s] = __uint_as_float(mv[s][1]);
        M = fmaxf(M, mm[s]);
      }
      float L = 0.f;
#pragma unroll
      for (int s = 0; s < SPLIT_A; s++) { mm[s] = fexp2((mm[s] - M) * LOG2E); L += mm[s] * ll[s]; }
      const float inv = 1.f / L;
      float av[8];
#pragma unroll
      for (int j = 0; j < 8; j++) av[j] = 0.f;
#pragma unroll
      for (int s = 0; s < SPLIT_A; s++)
#pragma unroll
        for (int j = 0; j < 4; j++) {
          av[2 * j] = fmaf(bf_lo(ov[s][j]), mm[s], av[2 * j]);
          av[2 * j + 1] = fmaf(bf_hi(ov[s][j]), mm[s], av[2 * j + 1]);
        }
      u32x4 hi, lo;
#pragma unroll
      for (int j = 0; j < 4; j++) {
        const float v0 = av[2 * j] * inv, v1 = av[2 * j + 1] * inv;
        hi[j] = cvtpk(v0, v1);
        lo[j] = cvtpk(v0 - bf_lo(hi[j]), v1 - bf_hi(hi[j]));
      }
      *(u32x4*)(la + bb * LAS + ch * 16) = hi;
      *(u32x4*)(la + (4 + bb) * LAS + ch * 16) = lo;
    }
    __syncthreads();
    {
      const char* ap = la + (c & 7) * LAS + (w * 64 + g * 16) * 2;
      const u32x4 a0 = *(const u32x4*)(ap), a1 = *(const u32x4*)(ap + 16);
      f32x4 dd[8];
#pragma unroll
      for (int j = 0; j < 8; j++)
        dd[j] = __builtin_amdgcn_mfma_f32_16x16x32_bf16(__builtin_bit_cast(bf16x8, a0), __builtin_bit_cast(bf16x8, bv[j][0]), f32x4{0.f, 0.f, 0.f, 0.f}, 0, 0, 0);
#pragma unroll
      for (int j = 0; j < 8; j++)
        dd[j] = __builtin_amdgcn_mfma_f32_16x16x32_bf16(__builtin_bit_cast(bf16x8, a1), __builtin_bit_cast(bf16x8, bv[j][1]), dd[j], 0, 0, 0);
#pragma unroll
      for (int r = 0; r < 4; r++) {
        f32x4 v;
#pragma unroll
        for (int j = 0; j < 4; j++) {
          auto sx = __builtin_amdgcn_permlane16_swap(__float_as_uint(dd[j][r]), __float_as_uint(dd[4 + j][r]), false, false);
          v[j] = __uint_as_float(sx[0]) + __uint_as_float(sx[1]);
        }
        if (g < 2) *(f32x4*)(cred + (w * 4 + r) * 128 + c * 8 + g * 4) = v;
      }
    }
    __syncthreads();
    {
      float s = 0.f;
#pragma unroll
      for (int i = 0; i < 8; i++) s += cred[i * 512 + t];
      const float sn = dpp<0xB1>(s);
      if ((t & 1) == 0) *(unsigned*)(cs + t) = cvtpk(s, sn);
    }
    __syncthreads();
    {
      const char* ap = (const char*)cs + ((c >> 1) & 3) * 256 + (g * 32 + (c & 1) * 16) * 2;
      const u32x4 a0 = *(const u32x4*)(ap), a1 = *(const u32x4*)(ap + 16);
      f32x4 dd[8];
#pragma unroll
      for (int j = 0; j < 8; j++)
        dd[j] = __builtin_amdgcn_mfma_f32_16x16x32_bf16(__builtin_bit_cast(bf16x8, a0), __builtin_bit_cast(bf16x8, bo[j][0]), f32x4{0.f, 0.f, 0.f, 0.f}, 0, 0, 0);
#pragma unroll
      for (int j = 0; j < 8; j++)
        dd[j] = __builtin_amdgcn_mfma_f32_16x16x32_bf16(__builtin_bit_cast(bf16x8, a1), __builtin_bit_cast(bf16x8, bo[j][1]), dd[j], 0, 0, 0);
      f32x4 v4[2];
#pragma unroll
      for (int j = 0; j < 8; j++) {
        const float u0 = (l & 8) ? dd[j][1] : dd[j][0], u1 = (l & 8) ? dd[j][3] : dd[j][2];
        const float keep = (l & 8) ? u1 : u0, give = (l & 8) ? u0 : u1;
        v4[j >> 2][j & 3] = keep + dpp<0x128>(give);
      }
      if (g < 2) {
        float* op = outs + (g * 2 + ((l >> 3) & 1)) * 512 + w * 64 + (l & 7) * 8;
        *(f32x4*)op = v4[0];
        *(f32x4*)(op + 4) = v4[1];
      }
    }
    __syncthreads();
    {
      const f32x2 va = *(const f32x2*)(outs + t * 2), vb = *(const f32x2*)(outs + 1024 + t * 2);
      const unsigned oo = (unsigned)(WS_PA + ((size_t)((t >> 8) * NH + h) * DM + (t & 255) * 2) * 8);
      st16u_sc1(rs, oo, u32x4{__float_as_uint(va[0]), ta, __float_as_uint(va[1]), tb});
      st16u_sc1(rs, oo + 2 * NH * DM * 8, u32x4{__float_as_uint(vb[0]), ta, __float_as_uint(vb[1]), tb});
    }
  if (w < 4) {
    const int bb = h >> 3, n0 = (h & 7) * 64 + w * 16;
    const unsigned og = (unsigned)(WS_PA + ((size_t)(bb * NH + (l >> 3)) * DM + n0) * 8 + (l & 7) * 16);
    u32x4 p[4];
    __builtin_amdgcn_s_sleep(10);
#pragma unroll 1
    for (unsigned spins = 0; spins < SPIN_CAP; ++spins) {
      asm volatile("" ::: "memory");
#pragma unroll
      for (int i = 0; i < 4; i++) p[i] = ld16u_sc1(rs, og + i * 8 * DM * 8);
      bool ok = true;
#pragma unroll
      for (int i = 0; i < 4; i++) ok = ok && p[i][1] == ta && p[i][3] == tb;
      if (__all(ok)) break;
      __builtin_amdgcn_s_sleep(4);
    }
    float v0 = (__uint_as_float(p[0][0]) + __uint_as_float(p[1][0])) + (__uint_as_float(p[2][0]) + __uint_as_float(p[3][0]));
    float v1 = (__uint_as_float(p[0][2]) + __uint_as_float(p[1][2])) + (__uint_as_float(p[2][2]) + __uint_as_float(p[3][2]));
    v0 += dpp<0x128>(v0); v0 = xrow_sum(v0);
    v1 += dpp<0x128>(v1); v1 = xrow_sum(v1);
    if (l < 8) {
      *(unsigned*)(out + bb * 1024 + n0 + l * 2) = cvtpk(v0, v1);
      if (h == 0 && t == 0) st_u64(ws + WS_EPA, tag);
    }
  }
    }
  }
}

__device__ __forceinline__ void role_A2(int a, const __hip_bfloat16* __restrict__ ckv, char* __restrict__ ws,
                                        __amdgpu_buffer_rsrc_t rs, char* lds) {
  const int t = threadIdx.x, l = t & 63;
  const int w = __builtin_amdgcn_readfirstlane(t >> 6);
  const int g = l >> 4, c = l & 15;
  u64 ep = 0;
  if (t == 0) ep = ld_u64(ws + WS_EPA);
  const int grp = w >> 2, wq = w & 3;
  const int b = a / SPLIT_A, sp = a % SPLIT_A;
  const int r = l & 31, hh = l >> 5;
  constexpr unsigned CB = (unsigned)((size_t)NB * SK * DC * 2);
  constexpr int NTL = KA / 64;
  const unsigned cvo = (unsigned)((g * DC + 128 * wq + c * 8) * 2);
  const unsigned cso = (unsigned)(((size_t)b * SK + sp * KA + grp * 32) * DC * 2);
  auto load_tile = [&](u32x4 (&pr)[8], int tile) {
    const bool ok = tile < NTL;
    const __amdgpu_buffer_rsrc_t rc = mkrs(ckv, ok ? CB : 0u);
    const unsigned so = ok ? cso + (unsigned)((tile + sp) & (NTL - 1)) * 64 * DC * 2 : 0u;
#pragma unroll
    for (int u = 0; u < 8; u++) pr[u] = __builtin_amdgcn_raw_buffer_load_b128(rc, cvo, so + u * 4 * DC * 2, NT);
  };
  char* img = lds + w * 8192;
  float* sred = (float*)(lds + 65536 + grp * 16384);
  float* mxs = (float*)(lds + 98304 + grp * 512);
  unsigned* pbuf = (unsigned*)(lds + 102400 + grp * 2048);
  float* lall = (float*)(lds + 106496);
  const unsigned imga = (unsigned)(uintptr_t)(__attribute__((address_space(3))) char*)img;
  auto put_tile = [&](u32x4 (&pr)[8]) {
#pragma unroll
    for (int u = 0; u < 8; u++) *(u32x4*)(img + lds_off(4 * u + g, c)) = pr[u];
  };
  u32x4 pre[2][8];
  load_tile(pre[0], 0);
  load_tile(pre[1], 1);
  const u64 tag = next_tag(uni64(ep));
  put_tile(pre[0]);
  __builtin_amdgcn_sched_barrier(0);
  load_tile(pre[0], 2);
  if (t < 64) {
#pragma unroll 1
    for (unsigned spins = 0; spins < SPIN_CAP; ++spins) {
      __builtin_amdgcn_s_sleep(32);
      if (__all(ld_u64(ws + WS_F1 + (l & 31) * 8) == tag)) break;
    }
  }
  __syncthreads();
  bf16x8 qf[8];
  {
    char* qi = lds + 65536;
    const unsigned qo = (unsigned)(WS_QABS + (size_t)b * NH * DC * 2 + t * 16);
    u32x4 qv[4];
#pragma unroll
    for (int i = 0; i < 4; i++) qv[i] = ld16u_sc1(rs, qo + i * 8192);
#pragma unroll
    for (int i = 0; i < 4; i++) *(u32x4*)(qi + (i * 8 + w) * 1040 + l * 16) = qv[i];
    __syncthreads();
#pragma unroll
    for (int s = 0; s < 8; s++) qf[s] = __builtin_bit_cast(bf16x8, *(const u32x4*)(qi + r * 1040 + (128 * wq + 16 * s + 8 * hh) * 2));
#pragma unroll
    for (int s = 0; s < 8; s++) asm volatile("" : "+v"(qf[s]));
    __syncthreads();
  }
  f32x16 o[4];
#pragma unroll
  for (int i = 0; i < 4; i++)
#pragma unroll
    for (int j = 0; j < 16; j++) o[i][j] = 0.f;
  float m = -INFINITY, lsum = 0.f;
  const int q4 = (l & 15) >> 2, p4 = l & 3, colh = g & 1;
  auto tile_body = [&](int tile, u32x4 (&nx)[8]) {
    f32x16 s;
#pragma unroll
    for (int j = 0; j < 16; j++) s[j] = 0.f;
    {
      u32x4 af[8];
#pragma unroll
      for (int ks = 0; ks < 8; ks++) af[ks] = *(const u32x4*)(img + lds_off(r, 2 * ks + hh));
      __builtin_amdgcn_sched_barrier(0);
#pragma unroll
      for (int ks = 0; ks < 8; ks++) s = __builtin_amdgcn_mfma_f32_32x32x16_bf16(__builtin_bit_cast(bf16x8, af[ks]), qf[ks], s, 0, 0, 0);
    }
    f32x4* sr = (f32x4*)sred;
#pragma unroll
    for (int k4 = 0; k4 < 4; k4++) sr[(k4 * 4 + wq) * 64 + l] = f32x4{s[4 * k4], s[4 * k4 + 1], s[4 * k4 + 2], s[4 * k4 + 3]};
    __syncthreads();
    float x[4];
    {
      const f32x4 v = (sr[(wq * 4) * 64 + l] + sr[(wq * 4 + 1) * 64 + l]) + (sr[(wq * 4 + 2) * 64 + l] + sr[(wq * 4 + 3) * 64 + l]);
#pragma unroll
      for (int e = 0; e < 4; e += 2) {
        const unsigned r1 = cvtpk(v[e], v[e + 1]);
        const unsigned r2 = cvtpk(bf_lo(r1) * SCALE, bf_hi(r1) * SCALE);
        x[e] = bf_lo(r2); x[e + 1] = bf_hi(r2);
      }
      float mq = fmaxf(fmaxf(x[0], x[1]), fmaxf(x[2], x[3]));
      auto sx = __builtin_amdgcn_permlane32_swap(__float_as_uint(mq), __float_as_uint(mq), false, false);
      mq = fmaxf(__uint_as_float(sx[0]), __uint_as_float(sx[1]));
      if (hh == 0) mxs[r * 4 + wq] = mq;
    }
    __syncthreads();
    const f32x4 m4 = *(const f32x4*)(mxs + r * 4);
    const float mn = fmaxf(fmaxf(m, fmaxf(m4[0], m4[1])), fmaxf(m4[2], m4[3]));
    const float alpha = fexp2((m - mn) * LOG2E);
    {
      const unsigned p0 = cvtpk(fexp2((x[0] - mn) * LOG2E), fexp2((x[1] - mn) * LOG2E));
      const unsigned p1 = cvtpk(fexp2((x[2] - mn) * LOG2E), fexp2((x[3] - mn) * LOG2E));
      *(u32x2*)(pbuf + l * 8 + wq * 2) = u32x2{p0, p1};
      lsum = lsum * alpha + ((bf_lo(p0) + bf_hi(p0)) + (bf_lo(p1) + bf_hi(p1)));
    }
    m = mn;
    __syncthreads();
    unsigned pk[8];
    {
      const u32x4 a0 = *(const u32x4*)(pbuf + l * 8), a1 = *(const u32x4*)(pbuf + l * 8 + 4);
      pk[0] = a0[0]; pk[1] = a0[1]; pk[2] = a0[2]; pk[3] = a0[3]; pk[4] = a1[0]; pk[5] = a1[1]; pk[6] = a1[2]; pk[7] = a1[3];
    }
    if (__any(alpha != 1.f)) {
#pragma unroll
      for (int i = 0; i < 4; i++)
#pragma unroll
        for (int j = 0; j < 16; j++) o[i][j] *= alpha;
    }
#pragma unroll
    for (int s2 = 0; s2 < 2; s2++) {
      u32x2 tv[4][2];
#pragma unroll
      for (int ct = 0; ct < 4; ct++) {
        const int ch = 4 * ct + 2 * colh + (p4 >> 1);
        const unsigned a0 = lds_off(4 * hh + q4, ch) + 8 * (p4 & 1);
        const unsigned a1 = lds_off(8 + 4 * hh + q4, ch) + 8 * (p4 & 1);
        if (s2 == 0) {
          asm volatile("ds_read_b64_tr_b16 %0, %1" : "=v"(tv[ct][0]) : "v"(imga + a0) : "memory");
          asm volatile("ds_read_b64_tr_b16 %0, %1" : "=v"(tv[ct][1]) : "v"(imga + a1) : "memory");
        } else {
          asm volatile("ds_read_b64_tr_b16 %0, %1 offset:4096" : "=v"(tv[ct][0]) : "v"(imga + a0) : "memory");
          asm volatile("ds_read_b64_tr_b16 %0, %1 offset:4096" : "=v"(tv[ct][1]) : "v"(imga + a1) : "memory");
        }
      }
      asm volatile("s_waitcnt lgkmcnt(0)" ::: "memory");
      __builtin_amdgcn_sched_barrier(0);
      u32x4 pb = {pk[4 * s2], pk[4 * s2 + 1], pk[4 * s2 + 2], pk[4 * s2 + 3]};
#pragma unroll
      for (int ct = 0; ct < 4; ct++) {
        u32x4 av = {tv[ct][0][0], tv[ct][0][1], tv[ct][1][0], tv[ct][1][1]};
        o[ct] = __builtin_amdgcn_mfma_f32_32x32x16_bf16(__builtin_bit_cast(bf16x8, av), __builtin_bit_cast(bf16x8, pb), o[ct], 0, 0, 0);
      }
    }
    put_tile(nx);
    __builtin_amdgcn_sched_barrier(0);
    load_tile(nx, tile + 3);
  };
#pragma unroll 1
  for (int tile = 0; tile < NTL; tile += 2) {
    tile_body(tile, pre[1]);
    tile_body(tile + 1, pre[0]);
  }
  constexpr int OST = 516;
  float* ob = (float*)lds;
  float* mlb = (float*)(lds + 32 * OST * 4);
  lall[w * 64 + l] = lsum;
  __syncthreads();
  {
    const float* lp = lall + grp * 256 + r;
    lsum = ((lp[0] + lp[32]) + (lp[64] + lp[96])) + ((lp[128] + lp[160]) + (lp[192] + lp[224]));
  }
  if (grp == 1) {
#pragma unroll
    for (int ct = 0; ct < 4; ct++)
#pragma unroll
      for (int j4 = 0; j4 < 4; j4++)
        *(f32x4*)(ob + r * OST + 128 * wq + 32 * ct + 8 * j4 + 4 * hh) =
            f32x4{o[ct][4 * j4], o[ct][4 * j4 + 1], o[ct][4 * j4 + 2], o[ct][4 * j4 + 3]};
    if (wq == 0 && l < 32) { mlb[2 * r] = m; mlb[2 * r + 1] = lsum; }
  }
  __syncthreads();
  if (grp == 0) {
    const float m1 = mlb[2 * r], l1 = mlb[2 * r + 1];
    const float M = fmaxf(m, m1);
    const float f0 = fexp2((m - M) * LOG2E), f1 = fexp2((m1 - M) * LOG2E);
#pragma unroll
    for (int ct = 0; ct < 4; ct++)
#pragma unroll
      for (int j4 = 0; j4 < 4; j4++) {
        float* p = ob + r * OST + 128 * wq + 32 * ct + 8 * j4 + 4 * hh;
        const f32x4 o1 = *(const f32x4*)p;
        *(f32x4*)p = f32x4{o[ct][4 * j4] * f0 + o1[0] * f1, o[ct][4 * j4 + 1] * f0 + o1[1] * f1,
                           o[ct][4 * j4 + 2] * f0 + o1[2] * f1, o[ct][4 * j4 + 3] * f0 + o1[3] * f1};
      }
    if (wq == 0 && l < 32) {
      f32x2 v = {M, lsum * f0 + l1 * f1};
      st8_sc1(ws + WS_PAML + ((size_t)(b * SPLIT_A + sp) * NH + r) * 8, v);
    }
  }
  __syncthreads();
  {
    const unsigned pao = (unsigned)(WS_PAO + (size_t)(b * SPLIT_A + sp) * NH * DC * 2);
    u32x4 pv[4];
#pragma unroll
    for (int i = 0; i < 4; i++) {
      const int idx = i * 512 + t;
      const int hd = idx >> 6, c8 = idx & 63;
      const f32x4 v0 = *(const f32x4*)(ob + hd * OST + c8 * 8), v1 = *(const f32x4*)(ob + hd * OST + c8 * 8 + 4);
      pv[i] = u32x4{cvtpk(v0[0], v0[1]), cvtpk(v0[2], v0[3]), cvtpk(v1[0], v1[1]), cvtpk(v1[2], v1[3])};
      asm volatile("" : "+v"(pv[i]));
    }
    wait_gate(ws + WS_FP, tag);
    __syncthreads();
#pragma unroll
    for (int i = 0; i < 4; i++) st16u_sc1(rs, pao + (unsigned)(i * 512 + t) * 16, pv[i]);
  }
  signal_flag(ws + WS_F2 + a * 8, tag);
}

__global__ __launch_bounds__(512, 2) void k_fused(const __hip_bfloat16* __restrict__ xA, const __hip_bfloat16* __restrict__ ckv,
                                                  const __hip_bfloat16* __restrict__ wqA, const __hip_bfloat16* __restrict__ wuk,
                                                  const __hip_bfloat16* __restrict__ wuv, const __hip_bfloat16* __restrict__ woA,
                                                  const __hip_bfloat16* __restrict__ xB, const __hip_bfloat16* __restrict__ kc,
                                                  const __hip_bfloat16* __restrict__ vc, const __hip_bfloat16* __restrict__ wqB,
                                                  const __hip_bfloat16* __restrict__ woB, char* __restrict__ ws,
                                                  __hip_bfloat16* __restrict__ out) {
  __shared__ __attribute__((aligned(16))) char lds[131072];
  asm volatile("" :: "s"(xA), "s"(ckv), "s"(wqA), "s"(wuk), "s"(wuv), "s"(woA), "s"(xB), "s"(kc), "s"(vc), "s"(wqB), "s"(woB), "s"(ws), "s"(out));
  const int blk = blockIdx.x, xcd = blk & 7, r = blk >> 3;
  unsigned pfv = 0;
  {
    u64 pc; unsigned len;
    asm volatile("s_getpc_b64 %0\n.Lcode_begin:\n\ts_mov_b32 %1, .Lcode_end-.Lcode_begin" : "=s"(pc), "=s"(len));
    const unsigned idx = (unsigned)(r * 8 + (threadIdx.x >> 6));
    const __amdgpu_buffer_rsrc_t rc = mkrs((const void*)pc, len);
    pfv = __builtin_amdgcn_raw_buffer_load_b32(rc, idx * 1024 + (threadIdx.x & 63) * 16, 0, 0);
  }
  const __amdgpu_buffer_rsrc_t rs = mkrs(ws, (unsigned)WS_END);
  if (r < 8) {
    if (r < 4) role_A1(xcd + 8 * r, xA, wqA, wuk, wuv, woA, ws, rs, out, lds);
    else role_A2(xcd * 4 + r - 4, ckv, ws, rs, lds);
  } else {
    const int rr = r - 8, hq = rr / 6;
    role_B(((0x75643120u >> (4 * xcd)) & 7) + 8 * hq, rr - 6 * hq, xB, kc, vc, wqB, woB, ws, rs, out, lds);
  }
  asm volatile(".Lcode_end:" :: "v"(pfv));
}

extern "C" void kernel_launch(void* const* d_in, const int* in_sizes, int n_in,
                              void* d_out, int out_size, void* d_ws, size_t ws_size,
                              hipStream_t stream) {
  k_fused<<<256, 512, 0, stream>>>((const __hip_bfloat16*)d_in[0], (const __hip_bfloat16*)d_in[1], (const __hip_bfloat16*)d_in[2],
                                   (const __hip_bfloat16*)d_in[3], (const __hip_bfloat16*)d_in[4], (const __hip_bfloat16*)d_in[5],
                                   (const __hip_bfloat16*)d_in[6], (const __hip_bfloat16*)d_in[7], (const __hip_bfloat16*)d_in[8],
                                   (const __hip_bfloat16*)d_in[9], (const __hip_bfloat16*)d_in[10], (char*)d_ws,
                                   (__hip_bfloat16*)d_out);
}
```

```cpp
#include <hip/hip_runtime.h>
#include <hip/hip_bf16.h>
#include <stdint.h>

typedef __attribute__((ext_vector_type(4))) unsigned u32x4;
typedef __attribute__((ext_vector_type(2))) unsigned u32x2;
typedef __attribute__((ext_vector_type(4))) float f32x4;
typedef __attribute__((ext_vector_type(2))) float f32x2;
typedef __attribute__((ext_vector_type(16))) float f32x16;
typedef __attribute__((ext_vector_type(8))) __bf16 bf16x8;
typedef __attribute__((ext_vector_type(2))) __bf16 bf16x2;
typedef __attribute__((address_space(1))) unsigned gu32;
typedef __attribute__((address_space(1))) unsigned long long gu64;
typedef unsigned long long u64;

constexpr int NB = 4;
constexpr int NH = 32;
constexpr int SK = 4096;
constexpr int DH = 128;
constexpr int DM = 512;
constexpr int DC = 512;
constexpr int HD = NH * DH;

constexpr int SPLIT_A = 8;
constexpr int KA = SK / SPLIT_A;
constexpr int SS = 171;
constexpr int RD = 18;
constexpr int NP = SS - 8 * RD + 8;
constexpr int NP4 = (NP + 3) & ~3;
constexpr int RST = 144;
constexpr int GI = 6;
constexpr int RE = 15;
constexpr int PRIOF = 4;

constexpr float SCALE = 0.08838834764831845f;
constexpr float LOG2E = 1.4426950408889634f;
constexpr int NT = 2;
constexpr int SC1 = 16;

constexpr size_t WS_EPA  = 0;
constexpr size_t WS_EPB  = 256;
constexpr size_t WS_F1   = 4096;
constexpr size_t WS_F2   = WS_F1 + 1024;
constexpr size_t WS_FP   = 8192;
constexpr size_t WS_QABS = 16384;
constexpr size_t WS_PAML = WS_QABS + (size_t)NB * NH * DC * 2;
constexpr size_t WS_PAO  = WS_PAML + (size_t)NB * SPLIT_A * NH * 2 * 4;
constexpr size_t WS_PA   = WS_PAO + (size_t)NB * SPLIT_A * NH * DC * 2;
constexpr size_t WS_PBML = WS_PA + (size_t)NB * NH * DM * 8;
constexpr size_t WS_PBG  = WS_PBML + (size_t)NB * 64 * 16;
constexpr size_t WS_END  = WS_PBG + (size_t)NB * 64 * DM * 8;

__device__ __forceinline__ float bf_lo(unsigned u) { return __uint_as_float(u << 16); }
__device__ __forceinline__ float bf_hi(unsigned u) { return __uint_as_float(u & 0xffff0000u); }
__device__ __forceinline__ unsigned cvtpk(float lo, float hi) {
  f32x2 v = {lo, hi};
  return __builtin_bit_cast(unsigned, __builtin_convertvector(v, bf16x2));
}
__device__ __forceinline__ float rbf(float x) { return __uint_as_float(cvtpk(x, x) << 16); }
__device__ __forceinline__ float dot2(unsigned k, unsigned q, float acc) {
  return __builtin_amdgcn_fdot2_f32_bf16(__builtin_bit_cast(bf16x2, k), __builtin_bit_cast(bf16x2, q), acc, false);
}
template <int CTRL> __device__ __forceinline__ float dpp(float x) {
  return __builtin_bit_cast(float, __builtin_amdgcn_mov_dpp(__builtin_bit_cast(int, x), CTRL, 0xf, 0xf, true));
}
__device__ __forceinline__ float sum16(float x) {
  x += dpp<0x128>(x);
  x += dpp<0x124>(x);
  x += dpp<0x4E>(x);
  x += dpp<0xB1>(x);
  return x;
}
__device__ __forceinline__ float xrow_max(float x) {
  auto s = __builtin_amdgcn_permlane16_swap(__float_as_uint(x), __float_as_uint(x), false, false);
  x = fmaxf(__uint_as_float(s[0]), __uint_as_float(s[1]));
  auto t = __builtin_amdgcn_permlane32_swap(__float_as_uint(x), __float_as_uint(x), false, false);
  return fmaxf(__uint_as_float(t[0]), __uint_as_float(t[1]));
}
__device__ __forceinline__ float xrow_sum(float x) {
  auto s = __builtin_amdgcn_permlane16_swap(__float_as_uint(x), __float_as_uint(x), false, false);
  x = __uint_as_float(s[0]) + __uint_as_float(s[1]);
  auto t = __builtin_amdgcn_permlane32_swap(__float_as_uint(x), __float_as_uint(x), false, false);
  return __uint_as_float(t[0]) + __uint_as_float(t[1]);
}
__device__ __forceinline__ float fexp2(float x) { return __builtin_amdgcn_exp2f(x); }
__device__ __forceinline__ __amdgpu_buffer_rsrc_t mkrs(const void* p, unsigned bytes) {
  return __builtin_amdgcn_make_buffer_rsrc((void*)p, 0, (int)bytes, 0x00020000);
}


constexpr int PTHR = 60;
constexpr unsigned SPIN_CAP = 1u << 21;
__device__ __forceinline__ u64 ld_u64(const void* p) {
  return __hip_atomic_load((gu64*)p, __ATOMIC_RELAXED, __HIP_MEMORY_SCOPE_AGENT);
}
__device__ __forceinline__ void st_u64(void* p, u64 v) {
  __hip_atomic_store((gu64*)p, v, __ATOMIC_RELAXED, __HIP_MEMORY_SCOPE_AGENT);
}
__device__ __forceinline__ u64 next_tag(u64 e) { return e + 0x0000000100000001ull; }
__device__ __forceinline__ u64 uni64(u64 v) {
  const unsigned lo = __builtin_amdgcn_readfirstlane((unsigned)v), hi = __builtin_amdgcn_readfirstlane((unsigned)(v >> 32));
  return ((u64)hi << 32) | lo;
}
__device__ __forceinline__ void signal_flag(char* flag, u64 tag) {
  asm volatile("s_waitcnt vmcnt(0)" ::: "memory");
  __syncthreads();
  if (threadIdx.x == 0) st_u64(flag, tag);
}
__device__ __forceinline__ void wait_gate(const char* flag, u64 tag) {
  if (threadIdx.x < 64) {
    for (unsigned spins = 0; spins < SPIN_CAP; ++spins) {
      if (ld_u64(flag) == tag) break;
      __builtin_amdgcn_s_sleep(4);
    }
  }
}
__device__ __forceinline__ void st16_sc1(__amdgpu_buffer_rsrc_t rs, unsigned off, f32x4 v) {
  __builtin_amdgcn_raw_buffer_store_b128(__builtin_bit_cast(u32x4, v), rs, off, 0, SC1);
}
__device__ __forceinline__ void st16u_sc1(__amdgpu_buffer_rsrc_t rs, unsigned off, u32x4 v) {
  __builtin_amdgcn_raw_buffer_store_b128(v, rs, off, 0, SC1);
}
__device__ __forceinline__ f32x4 ld16_sc1(__amdgpu_buffer_rsrc_t rs, unsigned off) {
  return __builtin_bit_cast(f32x4, __builtin_amdgcn_raw_buffer_load_b128(rs, off, 0, SC1));
}
__device__ __forceinline__ u32x4 ld16u_sc1(__amdgpu_buffer_rsrc_t rs, unsigned off) {
  return __builtin_amdgcn_raw_buffer_load_b128(rs, off, 0, SC1);
}
__device__ __forceinline__ void st8_sc1(void* p, f32x2 v) { st_u64(p, __builtin_bit_cast(u64, v)); }
__device__ __forceinline__ f32x2 ld8_sc1(const void* p) { return __builtin_bit_cast(f32x2, ld_u64(p)); }


__device__ __forceinline__ void item_dots(const u32x4 (&ks)[4], const u32x4 q, float (&s_)[4]) {
#pragma unroll
  for (int u = 0; u < 4; u++) {
    float a = dot2(ks[u][0], q[0], 0.f);
    a = dot2(ks[u][1], q[1], a);
    a = dot2(ks[u][2], q[2], a);
    a = dot2(ks[u][3], q[3], a);
    s_[u] = a;
    asm volatile("" : "+v"(s_[u]));
  }
}
__device__ __forceinline__ void item_unpack(const u32x4 (&vs)[4], f32x2 (&vf)[4][4]) {
#pragma unroll
  for (int u = 0; u < 4; u++)
#pragma unroll
    for (int j = 0; j < 4; j++) { vf[u][j] = f32x2{bf_lo(vs[u][j]), bf_hi(vs[u][j])}; asm volatile("" : "+v"(vf[u][j])); }
}
__device__ __forceinline__ f32x2 rows_sum(const float (&o)[8]) {
  float r4[4], r2[2];
#pragma unroll
  for (int j = 0; j < 4; j++) {
    auto sx = __builtin_amdgcn_permlane32_swap(__float_as_uint(o[j]), __float_as_uint(o[j + 4]), false, false);
    r4[j] = __uint_as_float(sx[0]) + __uint_as_float(sx[1]);
  }
#pragma unroll
  for (int j = 0; j < 2; j++) {
    auto sx = __builtin_amdgcn_permlane16_swap(__float_as_uint(r4[j]), __float_as_uint(r4[j + 2]), false, false);
    r2[j] = __uint_as_float(sx[0]) + __uint_as_float(sx[1]);
  }
  return f32x2{r2[0], r2[1]};
}
__device__ __forceinline__ f32x2 item_partial(const float (&s_)[4], const f32x2 (&vf)[4][4], float& mx, float& ll) {
  float s[4];
#pragma unroll
  for (int u = 0; u < 4; u++) s[u] = rbf(rbf(sum16(s_[u])) * SCALE);
  mx = xrow_max(fmaxf(fmaxf(s[0], s[1]), fmaxf(s[2], s[3])));
  float p[4];
#pragma unroll
  for (int u = 0; u < 4; u++) p[u] = fexp2((s[u] - mx) * LOG2E);
  ll = xrow_sum((p[0] + p[1]) + (p[2] + p[3]));
  float o[8];
#pragma unroll
  for (int j = 0; j < 4; j++) {
    f32x2 v = vf[0][j] * p[0];
#pragma unroll
    for (int u = 1; u < 4; u++) v = __builtin_elementwise_fma(vf[u][j], f32x2{p[u], p[u]}, v);
    o[2 * j] = v[0]; o[2 * j + 1] = v[1];
  }
  return rows_sum(o);
}
__device__ __forceinline__ void role_B(int h, int j6, const __hip_bfloat16* __restrict__ xB,
                                       const __hip_bfloat16* __restrict__ kc, const __hip_bfloat16* __restrict__ vc,
                                       const __hip_bfloat16* __restrict__ wqB, const __hip_bfloat16* __restrict__ woB,
                                       char* __restrict__ ws, __amdgpu_buffer_rsrc_t rs,
                                       __hip_bfloat16* __restrict__ out, char* lds) {
  const int t = threadIdx.x, l = t & 63;
  const int w = __builtin_amdgcn_readfirstlane(t >> 6);
  const int g = l >> 4, c = l & 15;
  const int pr = j6 >= 3 ? 1 : 0, k = j6 - 3 * pr;
  const int b0 = 2 * pr;
  const int NI = k == 1 ? 2 * (256 - SS) : SS;
  const int e0 = k == 1 ? (h & 1) : (k >> 1);
  u32x4 xr[2][2], wr[16];
  {
    const __amdgpu_buffer_rsrc_t rx = mkrs(xB, NB * DM * 2);
    const unsigned xo = (unsigned)((b0 * DM + w * 64 + g * 16) * 2);
#pragma unroll
    for (int bb = 0; bb < 2; bb++)
#pragma unroll
      for (int i = 0; i < 2; i++) xr[bb][i] = __builtin_amdgcn_raw_buffer_load_b128(rx, xo + bb * DM * 2 + i * 16, 0, 0);
    const __amdgpu_buffer_rsrc_t rq = mkrs(wqB, DM * HD * 2);
    const unsigned vo = (unsigned)(((w * 64 + g * 16) * HD + h * DH + c * 8) * 2);
#pragma unroll
    for (int i = 0; i < 16; i++) wr[i] = __builtin_amdgcn_raw_buffer_load_b128(rq, vo, i * HD * 2, NT);
  }
  constexpr unsigned KVB = (unsigned)((size_t)NB * NH * SK * DH * 2);
  const __amdgpu_buffer_rsrc_t rk = mkrs(kc, KVB), rv = mkrs(vc, KVB);
  const unsigned lo = (unsigned)(g * 256 + c * 16);
  const unsigned hb0 = ((unsigned)((b0 + e0) * NH + h) << 20) + (k == 1 ? (unsigned)SS << 12 : 0u);
  const unsigned hb1 = ((unsigned)((b0 + 1 - e0) * NH + h) << 20) + ((unsigned)(2 * SS - 256) << 12);
  auto soff = [&](int x) { return ((k == 1 && x >= 256 - SS) ? hb1 : hb0) + ((unsigned)x << 12); };
  auto ld4 = [&](u32x4 (&d)[4], __amdgpu_buffer_rsrc_t r, unsigned so) {
#pragma unroll
    for (int u = 0; u < 4; u++) d[u] = __builtin_amdgcn_raw_buffer_load_b128(r, lo + u * 1024, so, NT);
  };
  u32x4 kr[4], vr[4], ke[4], ve[4];
  ld4(kr, rk, soff(w));
  ld4(vr, rv, soff(w));
  u64* eps = (u64*)(lds + 33808);
  u64 epv = 0;
  if (t < 3) epv = ld_u64(ws + (t == 0 ? WS_EPA : WS_EPB + 256 * (b0 + (t == 1 ? e0 : 1 - e0))));
  float* red = (float*)lds;
  unsigned* qs = (unsigned*)(lds + 32768);
  unsigned* cnt0 = (unsigned*)(lds + 33792);
  unsigned* cntd = (unsigned*)(lds + 33796);
  float* rec0 = (float*)(lds + 36864);
  float* mlr0 = (float*)(lds + 40960);
  char* prv = lds + 45056 + w * 512;
  float* recd = (float*)(lds + 65536);
  float* mld = (float*)(lds + 98304);
  const unsigned cnta = (unsigned)(uintptr_t)(__attribute__((address_space(3))) char*)cntd;
  float* mxd = (float*)(lds + 33800);
  if (t == 0) { *cnt0 = 0u; *cntd = 8u * (RD + 1); mxd[0] = -INFINITY; mxd[1] = -INFINITY; }
  if (t < 64 * (NP4 - NP + 1)) *(f32x2*)(recd + (NP - 1 + (t >> 6)) * RST + 2 * l) = f32x2{0.f, 0.f};
  {
    float a0[8], a1[8];
#pragma unroll
    for (int j = 0; j < 8; j++) { a0[j] = 0.f; a1[j] = 0.f; }
#pragma unroll
    for (int i = 0; i < 16; i += 2) {
      const unsigned x0 = xr[0][i >> 3][(i >> 1) & 3], x1 = xr[1][i >> 3][(i >> 1) & 3];
#pragma unroll
      for (int j = 0; j < 4; j++) {
        const unsigned pl = __builtin_amdgcn_perm(wr[i + 1][j], wr[i][j], 0x05040100u);
        const unsigned ph = __builtin_amdgcn_perm(wr[i + 1][j], wr[i][j], 0x07060302u);
        a0[2 * j] = dot2(pl, x0, a0[2 * j]); a0[2 * j + 1] = dot2(ph, x0, a0[2 * j + 1]);
        a1[2 * j] = dot2(pl, x1, a1[2 * j]); a1[2 * j + 1] = dot2(ph, x1, a1[2 * j + 1]);
      }
    }
#pragma unroll
    for (int j = 0; j < 8; j++) asm volatile("" : "+v"(a0[j]), "+v"(a1[j]));
    __builtin_amdgcn_sched_barrier(0);
    ld4(ke, rk, soff(w + 8));
    ld4(ve, rv, soff(w + 8));
    __builtin_amdgcn_sched_barrier(0);
    float* rp = red + (w * 4 + g) * 256 + c * 8;
    *(f32x4*)(rp) = f32x4{a0[0], a0[1], a0[2], a0[3]};
    *(f32x4*)(rp + 4) = f32x4{a0[4], a0[5], a0[6], a0[7]};
    *(f32x4*)(rp + 128) = f32x4{a1[0], a1[1], a1[2], a1[3]};
    *(f32x4*)(rp + 132) = f32x4{a1[4], a1[5], a1[6], a1[7]};
  }
  __syncthreads();
  {
    const int o = t >> 1, hf = t & 1;
    float s = 0.f;
#pragma unroll
    for (int i = 0; i < 16; i++) s += red[(hf * 16 + i) * 256 + o];
    s += dpp<0xB1>(s);
    const float sn = dpp<0x4E>(s);
    if ((t & 3) == 0) qs[o >> 1] = cvtpk(s, sn);
    if (t < 3) eps[t] = epv;
  }
  __syncthreads();
  const u64 epa = uni64(eps[0]), tg0 = next_tag(uni64(eps[1])), tg1 = next_tag(uni64(eps[2]));
  u32x4 q = *(const u32x4*)(qs + e0 * 64 + c * 4);
  const u32x4 q1 = *(const u32x4*)(qs + (1 - e0) * 64 + c * 4);
  float m = -INFINITY, lsum = 0.f, acc[8];
#pragma unroll
  for (int j = 0; j < 8; j++) acc[j] = 0.f;
  float s_[4]; f32x2 vf[4][4];
  auto part0 = [&](u32x4 (&ks)[4]) { item_dots(ks, q, s_); };
  auto part1 = [&](u32x4 (&vs)[4]) { item_unpack(vs, vf); };
  auto part2 = [&]() {
    float s[4];
#pragma unroll
    for (int u = 0; u < 4; u++) s[u] = rbf(rbf(sum16(s_[u])) * SCALE);
    const float mx = fmaxf(fmaxf(m, fmaxf(s[0], s[1])), fmaxf(s[2], s[3]));
    const float alpha = fexp2((m - mx) * LOG2E);
    m = mx;
    float p[4];
#pragma unroll
    for (int u = 0; u < 4; u++) p[u] = fexp2((s[u] - mx) * LOG2E);
    lsum = lsum * alpha + ((p[0] + p[1]) + (p[2] + p[3]));
#pragma unroll
    for (int j = 0; j < 8; j++) acc[j] *= alpha;
#pragma unroll
    for (int u = 0; u < 4; u++)
#pragma unroll
      for (int j = 0; j < 4; j++) {
        acc[2 * j]     = fmaf(p[u], vf[u][j][0], acc[2 * j]);
        acc[2 * j + 1] = fmaf(p[u], vf[u][j][1], acc[2 * j + 1]);
      }
  };
  auto front = [&](__amdgpu_buffer_rsrc_t rk2, __amdgpu_buffer_rsrc_t rv2, unsigned so) {
    __builtin_amdgcn_sched_barrier(0);
    ld4(kr, rk2, so);
    __builtin_amdgcn_sched_barrier(0);
    part1(vr);
    __builtin_amdgcn_sched_barrier(0);
    ld4(vr, rv2, so);
    __builtin_amdgcn_sched_barrier(0);
  };
  auto put_partial = [&](f32x2 r2, float mw, float ll, float* rec_, float* ml_, float* mx_) {
    *(f32x2*)(rec_ + c * 8 + g * 2) = r2;
    if (l == 0) {
      *(f32x2*)ml_ = f32x2{mw, ll};
      asm volatile("ds_max_f32 %0, %1" :: "v"((unsigned)(uintptr_t)(__attribute__((address_space(3))) float*)mx_), "v"(mw) : "memory");
    }
  };
  auto wave_partial = [&](float* rec_, float* ml_, float* mx_) {
    const float mw = xrow_max(m);
    const float fq = fexp2((m - mw) * LOG2E);
    const float ll = xrow_sum(lsum * fq);
    float o[8];
#pragma unroll
    for (int j = 0; j < 8; j++) o[j] = acc[j] * fq;
    put_partial(rows_sum(o), mw, ll, rec_, ml_, mx_);
  };
  auto part2c = [&](float* rec_, float* ml_) {
    float mx, ll;
    const f32x2 r2 = item_partial(s_, vf, mx, ll);
    put_partial(r2, mx, ll, rec_, ml_, mxd);
  };
  bool sw = k != 1;
  auto do_switch = [&]() {
    wave_partial(rec0 + w * 128, mlr0 + w * 2, mxd + 1);
    asm volatile("s_waitcnt lgkmcnt(0)" ::: "memory");
    if (l == 0) asm volatile("ds_add_u32 %0, %1" :: "v"(cnta - 4u), "v"(1u) : "memory");
    m = -INFINITY; lsum = 0.f;
#pragma unroll
    for (int j = 0; j < 8; j++) acc[j] = 0.f;
    q = q1; sw = true;
  };
  if (w >= 4) __builtin_amdgcn_s_setprio(1);
  part0(kr);
  front(rk, rv, soff(w + 16));
  part2();
  part0(ke); part1(ve); part2();
  int x = w + 16;
  const int rg = l >> 3, cc = l & 7;
  const int rw = 3 + 4 * (h >> 3);
  const int re = k != 1 ? -1 : rw + 2 > RE ? rw + 2 : RE;
  u32x4 wo[16];
  __builtin_amdgcn_sched_barrier(0);
#pragma unroll
  for (int i = 0; i < 16; i++) asm volatile("" : "=v"(wo[i]));
  const int nseg = k == 1 ? 2 : 1, sp = k == 1 ? 1 : 0;
  auto store_off = [&](int sg) {
    const int b = b0 + (sg ? 1 - e0 : e0);
    const unsigned og_ = (unsigned)(WS_PBG + ((size_t)(b * 64 + h * 2 + sp) * DM + w * 64 + cc * 8 + g * 2) * 8);
    const unsigned om_ = (unsigned)(WS_PBML + (size_t)(b * 64 + h * 2 + sp) * 16);
    return (l & 8) == 0 ? og_ : t == 8 ? om_ : 0x7ffffff0u;
  };
  const unsigned apo = (unsigned)((c >> 3) * 256 + (g * 32 + ((c >> 2) & 1) * 16) * 2);
  auto project = [&](const char* ap, float M, float L, bool lred) {
    const u32x4 a0 = *(const u32x4*)(ap), a1 = *(const u32x4*)(ap + 16);
    f32x4 de = {0.f, 0.f, 0.f, 0.f}, dq = {0.f, 0.f, 0.f, 0.f};
#pragma unroll
    for (int p = 0; p < 8; p++) {
      const unsigned d = p < 4 ? a0[p & 3] : a1[p & 3];
      const u32x4 af = {(c & 3) == 0 ? d : 0u, (c & 3) == 1 ? d : 0u, (c & 3) == 2 ? d : 0u, (c & 3) == 3 ? d : 0u};
      de = __builtin_amdgcn_mfma_f32_16x16x32_bf16(__builtin_bit_cast(bf16x8, af), __builtin_bit_cast(bf16x8, wo[2 * p]), de, 0, 0, 0);
      dq = __builtin_amdgcn_mfma_f32_16x16x32_bf16(__builtin_bit_cast(bf16x8, af), __builtin_bit_cast(bf16x8, wo[2 * p + 1]), dq, 0, 0, 0);
    }
    if (lred && w == 0) L = xrow_sum(sum16(L));
    const bool val = (g & 1) == ((l >> 3) & 1);
    float r4[4], r2[2];
#pragma unroll
    for (int j = 0; j < 4; j++) {
      const float u0 = (j & 1) ? dq[j >> 1] : de[j >> 1], u1 = (j & 1) ? dq[2 + (j >> 1)] : de[2 + (j >> 1)];
      auto sx = __builtin_amdgcn_permlane32_swap(__float_as_uint(u0), __float_as_uint(u1), false, false);
      const float v = __uint_as_float(sx[0]) + __uint_as_float(sx[1]);
      r4[j] = val ? v : 0.f;
    }
#pragma unroll
    for (int j = 0; j < 2; j++) {
      auto sx = __builtin_amdgcn_permlane16_swap(__float_as_uint(r4[j]), __float_as_uint(r4[j + 2]), false, false);
      const float v = __uint_as_float(sx[0]) + __uint_as_float(sx[1]);
      r2[j] = v + dpp<0x128>(v);
    }
    const bool lo8 = (l & 8) == 0;
    return f32x2{lo8 ? r2[0] : M, lo8 ? r2[1] : L};
  };
  auto publish = [&](f32x2 v, u64 tg, unsigned sto) {
    st16u_sc1(rs, sto, u32x4{__float_as_uint(v[0]), (unsigned)tg, __float_as_uint(v[1]), (unsigned)(tg >> 32)});
  };
  f32x2 seg0 = {0.f, 0.f};
#pragma unroll 1
  for (; x < 8 * RD; x += 8) {
    const int rd = x >> 3;
    if (rd == PRIOF) __builtin_amdgcn_s_setprio(0);
    if (!sw && x >= 256 - SS) do_switch();
    if (rd == rw + 2) {
#pragma unroll
      for (int p = 0; p < 8; p++) {
#pragma unroll
        for (int i = 0; i < 4; i++) {
          const unsigned u0 = wo[2 * p][i], u1 = wo[2 * p + 1][i];
          wo[2 * p][i] = __builtin_amdgcn_perm(u1, u0, 0x05040100u);
          wo[2 * p + 1][i] = __builtin_amdgcn_perm(u1, u0, 0x07060302u);
        }
        asm volatile("" : "+v"(wo[2 * p]), "+v"(wo[2 * p + 1]));
      }
    }
    if (rd == re) {
    for (unsigned spins = 0; __hip_atomic_load(cnt0, __ATOMIC_RELAXED, __HIP_MEMORY_SCOPE_WORKGROUP) != 8u && spins < SPIN_CAP; ++spins)
      __builtin_amdgcn_s_sleep(1);
    asm volatile("" ::: "memory");
    f32x4 q4[4];
#pragma unroll
    for (int i = 0; i < 4; i++) q4[i] = *(const f32x4*)(mlr0 + i * 4);
    const float M = mxd[1];
    float L = 0.f;
    f32x2 a2 = {0.f, 0.f};
#pragma unroll
    for (int i = 0; i < 8; i++) {
      const float fw = fexp2((q4[i >> 1][(i & 1) * 2] - M) * LOG2E);
      L = fmaf(q4[i >> 1][(i & 1) * 2 + 1], fw, L);
      a2 += *(const f32x2*)(rec0 + i * 128 + 2 * l) * fw;
    }
    const unsigned hi = cvtpk(a2[0], a2[1]);
    *(unsigned*)(prv + l * 4) = hi;
    *(unsigned*)(prv + 256 + l * 4) = cvtpk(a2[0] - bf_lo(hi), a2[1] - bf_hi(hi));
    seg0 = project(prv + apo, M, L, false);
    }
    if (rd == rw) {
      const __amdgpu_buffer_rsrc_t ro = mkrs(woB, HD * DM * 2);
      const unsigned vo = (unsigned)(((h * DH + rg * 16) * DM + w * 64 + cc * 8) * 2);
#pragma unroll
      for (int i = 0; i < 16; i++) wo[i] = __builtin_amdgcn_raw_buffer_load_b128(ro, vo, i * DM * 2, NT);
    }
    part0(kr);
    front(rk, rv, soff(x + 8));
    part2();
  }
  wave_partial(recd + w * RST, mld + w * 2, mxd);
  unsigned o_ml = 98304u + (l < NP ? l : 0) * 8, o_rv = 65536u + (g * RST + w * 16 + c) * 4, o_fw = 45056u + w * 512 + ((l & 3) * 16 + (l >> 2)) * 4;
  unsigned o_fr = 45056u + w * 512 + g * 64, o_oi = (g < 2 && (c & 1) == 0) ? 41216u + g * 256 + (w * 16 + c) * 2 : 45056u + w * 512 + 256 + l * 4;
  unsigned o_ap = 41216u + apo, o_st = store_off(nseg - 1);
  unsigned o_s0 = k == 1 ? store_off(0) : 0x7ffffff0u;
  asm volatile("" : "+v"(o_s0));
  asm volatile("" : "+v"(o_ml), "+v"(o_rv), "+v"(o_fw), "+v"(o_fr), "+v"(o_oi), "+v"(o_ap), "+v"(o_st));
  {
    {
#pragma unroll 1
      while (x < NI) {
        unsigned dr; u64 sv;
        asm volatile("s_mov_b64 %1, exec\n\ts_mov_b64 exec, 1\n\tds_add_rtn_u32 %0, %2, %3\n\ts_mov_b64 exec, %1"
                     : "=&v"(dr), "=&s"(sv) : "v"(cnta), "v"(1u), "v"(kr[0][0]) : "memory");
        __builtin_amdgcn_sched_barrier(0);
        part0(kr);
        asm volatile("s_waitcnt lgkmcnt(0)" : "+v"(dr) :: "memory");
        const int xn = __builtin_amdgcn_readfirstlane(dr);
        if (h >= 24 && j6 == 2 && xn == NI - GI && l == 0) st_u64(ws + WS_FP, next_tag(epa));
        const bool ok = xn < NI;
        front(mkrs(kc, ok ? KVB : 0u), mkrs(vc, ok ? KVB : 0u), ok ? soff(xn) : 0u);
        part2c(recd + (x - 8 * RD + 8) * RST, mld + (x - 8 * RD + 8) * 2);
        x = xn;
      }
      asm volatile("s_waitcnt lgkmcnt(0)" ::: "memory");
      __syncthreads();
    }
    {
      f32x2 mli = *(const f32x2*)(lds + o_ml);
      float rv_[NP4 / 4];
#pragma unroll
      for (int r = 0; r < NP4 / 4; r++) rv_[r] = *(const float*)(lds + o_rv + r * 4 * RST * 4);
      if (l >= NI - 8 * RD + 8) mli = f32x2{-INFINITY, 0.f};
      const float M = *mxd;
      const float fi = fexp2((mli[0] - M) * LOG2E);
      *(float*)(lds + o_fw) = fi;
      f32x4 f4[(NP4 / 4 + 3) / 4];
#pragma unroll
      for (int i = 0; i < (NP4 / 4 + 3) / 4; i++) f4[i] = *(const f32x4*)(lds + o_fr + i * 16);
      float a = 0.f;
#pragma unroll
      for (int r = 0; r < NP4 / 4; r++) a = fmaf(f4[r >> 2][r & 3], rv_[r], a);
      a = xrow_sum(a);
      const float an = dpp<0xB1>(a);
      const unsigned hi = cvtpk(a, an);
      const unsigned lw = cvtpk(a - bf_lo(hi), an - bf_hi(hi));
      *(unsigned*)(lds + o_oi) = g ? lw : hi;
      __syncthreads();
      publish(project(lds + o_ap, M, fi * mli[1], true), k == 1 ? tg1 : tg0, o_st);
      publish(seg0, tg0, o_s0);
    }
    if (w < nseg) {
      const int sg = w;
      const int b = b0 + (sg ? 1 - e0 : e0);
      const int j64 = h * 2 + sp;
      const u64 tg = sg ? tg1 : tg0;
      const unsigned ta = (unsigned)tg, tb = (unsigned)(tg >> 32);
      const unsigned oml = (unsigned)(WS_PBML + (size_t)(b * 64 + l) * 16);
      const unsigned om4 = (unsigned)(WS_PBML + (size_t)(b * 64 + (l >> 2)) * 16);
      const unsigned og = (unsigned)(WS_PBG + ((size_t)(b * 64 + (l >> 2)) * DM + j64 * 8) * 8 + (l & 3) * 16);
      u32x4 ml[4], p[4];
      __builtin_amdgcn_s_sleep(6);
#pragma unroll 1
      for (unsigned spins = 0; spins < SPIN_CAP; ++spins) {
        asm volatile("" ::: "memory");
#pragma unroll
        for (int i = 0; i < 4; i++) ml[i] = ld16u_sc1(rs, om4 + i * 256);
#pragma unroll
        for (int i = 0; i < 4; i++) p[i] = ld16u_sc1(rs, og + i * 16 * DM * 8);
        int n = 0;
#pragma unroll
        for (int i = 0; i < 4; i++) n += __popcll(__ballot(ml[i][1] == ta && ml[i][3] == tb && p[i][1] == ta && p[i][3] == tb));
        if (n == 256) break;
#pragma unroll 1
        for (; n < 4 * PTHR && spins < SPIN_CAP; ++spins) {
          __builtin_amdgcn_s_sleep(4);
          asm volatile("" ::: "memory");
          const u32x4 m1 = ld16u_sc1(rs, oml);
          n = 4 * __popcll(__ballot(m1[1] == ta && m1[3] == tb));
        }
      }
      float v0 = 0.f, v1 = 0.f;
#pragma unroll
      for (int i = 0; i < 4; i++) {
        const float mm = __uint_as_float(ml[i][0]);
        const float Mh = fmaxf(mm, dpp<0x141>(mm));
        const float fq = fexp2((mm - Mh) * LOG2E);
        float L = fq * __uint_as_float(ml[i][2]);
        L += dpp<0x141>(L);
        const float wgt = fq * __builtin_amdgcn_rcpf(L);
        v0 = fmaf(__uint_as_float(p[i][0]), wgt, v0);
        v1 = fmaf(__uint_as_float(p[i][2]), wgt, v1);
      }
      v0 += dpp<0x128>(v0); v0 += dpp<0x124>(v0); v0 = xrow_sum(v0);
      v1 += dpp<0x128>(v1); v1 += dpp<0x124>(v1); v1 = xrow_sum(v1);
      if (l < 4) {
        *(unsigned*)(out + b * 1024 + 512 + j64 * 8 + l * 2) = cvtpk(v0, v1);
        if (j64 == 0 && l == 0) st_u64(ws + WS_EPB + 256 * b, tg);
      }
    }
  }
}

__device__ __forceinline__ unsigned lds_off(int row, int ch) {
  return 256 * row + 16 * (ch ^ (((row & 3) << 2) | ((row >> 2) & 3)));
}

__device__ __forceinline__ void role_A1(int h, const __hip_bfloat16* __restrict__ xA, const __hip_bfloat16* __restrict__ wqA,
                                        const __hip_bfloat16* __restrict__ wuk, const __hip_bfloat16* __restrict__ wuv,
                                        const __hip_bfloat16* __restrict__ woA, char* __restrict__ ws,
                                        __amdgpu_buffer_rsrc_t rs, __hip_bfloat16* __restrict__ out, char* lds) {
  const int t = threadIdx.x, l = t & 63;
  const int w = __builtin_amdgcn_readfirstlane(t >> 6);
  const int g = l >> 4, c = l & 15;
  float* red = (float*)(lds + 16384);
  const unsigned vo = (unsigned)(((w * 64 + g * 16) * HD + h * DH + c * 8) * 2);
  u64 tag;
  {
    u32x4 xr[NB][2], wr[16], wk[16];
    const __amdgpu_buffer_rsrc_t rx = mkrs(xA, NB * DM * 2);
    const unsigned xo = (unsigned)((w * 64 + g * 16) * 2);
#pragma unroll
    for (int bb = 0; bb < NB; bb++)
#pragma unroll
      for (int i = 0; i < 2; i++) xr[bb][i] = __builtin_amdgcn_raw_buffer_load_b128(rx, xo + bb * DM * 2 + i * 16, 0, 0);
    const __amdgpu_buffer_rsrc_t rq = mkrs(wqA, DM * HD * 2);
#pragma unroll
    for (int i = 0; i < 16; i++) wr[i] = __builtin_amdgcn_raw_buffer_load_b128(rq, vo, i * HD * 2, NT);
    u64 epv = 0;
    if (t == 0) epv = ld_u64(ws + WS_EPA);
    unsigned* qs = (unsigned*)lds;
    unsigned short* qo = (unsigned short*)(lds + 4096);
    float acc[NB][8];
#pragma unroll
    for (int bb = 0; bb < NB; bb++)
#pragma unroll
      for (int j = 0; j < 8; j++) acc[bb][j] = 0.f;
#pragma unroll
    for (int i = 0; i < 16; i += 2) {
#pragma unroll
      for (int j = 0; j < 4; j++) {
        const unsigned pl = __builtin_amdgcn_perm(wr[i + 1][j], wr[i][j], 0x05040100u);
        const unsigned ph = __builtin_amdgcn_perm(wr[i + 1][j], wr[i][j], 0x07060302u);
#pragma unroll
        for (int bb = 0; bb < NB; bb++) {
          const unsigned x = xr[bb][i >> 3][(i >> 1) & 3];
          acc[bb][2 * j] = dot2(pl, x, acc[bb][2 * j]);
          acc[bb][2 * j + 1] = dot2(ph, x, acc[bb][2 * j + 1]);
        }
      }
    }
    float* rp = red + (w * 4 + g) * 512 + c * 8;
#pragma unroll
    for (int bb = 0; bb < NB; bb++) {
      *(f32x4*)(rp + bb * 128) = f32x4{acc[bb][0], acc[bb][1], acc[bb][2], acc[bb][3]};
      *(f32x4*)(rp + bb * 128 + 4) = f32x4{acc[bb][4], acc[bb][5], acc[bb][6], acc[bb][7]};
    }
    __syncthreads();
    {
      const __amdgpu_buffer_rsrc_t rku = mkrs(wuk, DC * HD * 2);
#pragma unroll
      for (int i = 0; i < 16; i++) wk[i] = __builtin_amdgcn_raw_buffer_load_b128(rku, vo, i * HD * 2, NT);
    }
    {
      float s = 0.f;
#pragma unroll
      for (int i = 0; i < 32; i++) s += red[i * 512 + t];
      const float sn = dpp<0xB1>(s);
      if ((t & 1) == 0) qs[t >> 1] = cvtpk(s, sn);
      if (t == 0) *(u64*)(lds + 2048) = epv;
    }
    __syncthreads();
    tag = next_tag(uni64(*(const u64*)(lds + 2048)));
    {
      u32x4 qq[NB];
#pragma unroll
      for (int bb = 0; bb < NB; bb++) qq[bb] = *(const u32x4*)(qs + bb * 64 + c * 4);
#pragma unroll
      for (int i = 0; i < 16; i++) {
        float s[NB];
#pragma unroll
        for (int bb = 0; bb < NB; bb++) {
          float v = dot2(wk[i][0], qq[bb][0], 0.f);
          v = dot2(wk[i][1], qq[bb][1], v);
          v = dot2(wk[i][2], qq[bb][2], v);
          v = dot2(wk[i][3], qq[bb][3], v);
          s[bb] = sum16(v);
        }
        const float v = (c & 2) ? ((c & 1) ? s[3] : s[2]) : ((c & 1) ? s[1] : s[0]);
        if (c < NB) qo[c * 512 + w * 64 + g * 16 + i] = (unsigned short)(cvtpk(v, v) & 0xffff);
      }
    }
    __syncthreads();
    if (t < 256) {
      const int bb = t >> 6, ch = t & 63;
      st16u_sc1(rs, (unsigned)(WS_QABS + ((size_t)(bb * NH + h) * DC + ch * 8) * 2), *(const u32x4*)(qo + bb * 512 + ch * 8));
    }
    signal_flag(ws + WS_F1 + h * 8, tag);
  }
  const unsigned ta = (unsigned)tag, tb = (unsigned)(tag >> 32);
  {
    u32x4 bv[8][2], bo[8][2];
    u32x4 pre[2][8], wo[16];
    {
      const __amdgpu_buffer_rsrc_t ruv = mkrs(wuv, DC * HD * 2);
      const __amdgpu_buffer_rsrc_t rwo = mkrs(woA, HD * DM * 2);
      const unsigned wvo = (unsigned)(((h * DH + (l >> 3) * 16) * DM + w * 64 + (l & 7) * 8) * 2);
#pragma unroll
      for (int i = 0; i < 16; i++) pre[i >> 3][i & 7] = __builtin_amdgcn_raw_buffer_load_b128(ruv, vo, i * HD * 2, NT);
#pragma unroll
      for (int i = 0; i < 16; i++) wo[i] = __builtin_amdgcn_raw_buffer_load_b128(rwo, wvo, i * DM * 2, NT);
    }
#pragma unroll
    for (int j = 0; j < 8; j++)
#pragma unroll
      for (int s = 0; s < 2; s++) {
#pragma unroll
        for (int e = 0; e < 4; e++)
          bv[j][s][e] = __builtin_amdgcn_perm(pre[s][2 * e + 1][j >> 1], pre[s][2 * e][j >> 1], (j & 1) ? 0x07060302u : 0x05040100u);
        asm volatile("" : "+v"(bv[j][s]));
      }
#pragma unroll
    for (int j = 0; j < 8; j++)
#pragma unroll
      for (int s = 0; s < 2; s++) {
#pragma unroll
        for (int e = 0; e < 4; e++)
          bo[j][s][e] = __builtin_amdgcn_perm(wo[8 * s + 2 * e + 1][j >> 1], wo[8 * s + 2 * e][j >> 1], (j & 1) ? 0x07060302u : 0x05040100u);
        asm volatile("" : "+v"(bo[j][s]));
      }
    constexpr int LAS = 1040;
    char* la = lds;
    unsigned short* cs = (unsigned short*)(lds + 16384);
    float* outs = (float*)(lds + 20480);
    float* cred = (float*)(lds + 32768);
    {
    {
    wait_gate(ws + WS_FP, tag);
    __syncthreads();
    }
    if (t < 256) {
      const int bb = t >> 6, ch = t & 63;
      u32x4 ov[SPLIT_A];
      float mm[SPLIT_A], ll[SPLIT_A];
      float M = -INFINITY;
      u32x2 mv[SPLIT_A];
      {
#pragma unroll
        for (int s = 0; s < SPLIT_A; s++) { asm volatile("" : "=v"(ov[s])); asm volatile("" : "=v"(mv[s])); }
        unsigned got = 0;
#pragma unroll 1
        for (unsigned spins = 0; got != 0xffu && spins < SPIN_CAP; ++spins) {
          const u64 f = ld_u64(ws + WS_F2 + (w * SPLIT_A + (l & 7)) * 8);
          const unsigned now = (unsigned)__ballot(f == tag) & 0xffu;
          const unsigned fresh = now & ~got;
#pragma unroll
          for (int s = 0; s < SPLIT_A; s++)
            if ((fresh >> s) & 1u) {
              ov[s] = ld16u_sc1(rs, (unsigned)(WS_PAO + (((size_t)(bb * SPLIT_A + s) * NH + h) * DC + ch * 8) * 2));
              mv[s] = __builtin_amdgcn_raw_buffer_load_b64(rs, (unsigned)(WS_PAML + ((size_t)(bb * SPLIT_A + s) * NH + h) * 8), 0, SC1);
            }
          got = now;
          if (got != 0xffu) __builtin_amdgcn_s_sleep(6);
        }
      }
#pragma unroll
      for (int s = 0; s < SPLIT_A; s++) {
        mm[s] = __uint_as_float(mv[s][0]); ll[s] = __uint_as_float(mv[s][1]);
        M = fmaxf(M, mm[s]);
      }
      float L = 0.f;
#pragma unroll
      for (int s = 0; s < SPLIT_A; s++) { mm[s] = fexp2((mm[s] - M) * LOG2E); L += mm[s] * ll[s]; }
      const float inv = 1.f / L;
      float av[8];
#pragma unroll
      for (int j = 0; j < 8; j++) av[j] = 0.f;
#pragma unroll
      for (int s = 0; s < SPLIT_A; s++)
#pragma unroll
        for (int j = 0; j < 4; j++) {
          av[2 * j] = fmaf(bf_lo(ov[s][j]), mm[s], av[2 * j]);
          av[2 * j + 1] = fmaf(bf_hi(ov[s][j]), mm[s], av[2 * j + 1]);
        }
      u32x4 hi, lo;
#pragma unroll
      for (int j = 0; j < 4; j++) {
        const float v0 = av[2 * j] * inv, v1 = av[2 * j + 1] * inv;
        hi[j] = cvtpk(v0, v1);
        lo[j] = cvtpk(v0 - bf_lo(hi[j]), v1 - bf_hi(hi[j]));
      }
      *(u32x4*)(la + bb * LAS + ch * 16) = hi;
      *(u32x4*)(la + (4 + bb) * LAS + ch * 16) = lo;
    }
    __syncthreads();
    {
      const char* ap = la + (c & 7) * LAS + (w * 64 + g * 16) * 2;
      const u32x4 a0 = *(const u32x4*)(ap), a1 = *(const u32x4*)(ap + 16);
      f32x4 dd[8];
#pragma unroll
      for (int j = 0; j < 8; j++)
        dd[j] = __builtin_amdgcn_mfma_f32_16x16x32_bf16(__builtin_bit_cast(bf16x8, a0), __builtin_bit_cast(bf16x8, bv[j][0]), f32x4{0.f, 0.f, 0.f, 0.f}, 0, 0, 0);
#pragma unroll
      for (int j = 0; j < 8; j++)
        dd[j] = __builtin_amdgcn_mfma_f32_16x16x32_bf16(__builtin_bit_cast(bf16x8, a1), __builtin_bit_cast(bf16x8, bv[j][1]), dd[j], 0, 0, 0);
#pragma unroll
      for (int r = 0; r < 4; r++) {
        f32x4 v;
#pragma unroll
        for (int j = 0; j < 4; j++) {
          auto sx = __builtin_amdgcn_permlane16_swap(__float_as_uint(dd[j][r]), __float_as_uint(dd[4 + j][r]), false, false);
          v[j] = __uint_as_float(sx[0]) + __uint_as_float(sx[1]);
        }
        if (g < 2) *(f32x4*)(cred + (w * 4 + r) * 128 + c * 8 + g * 4) = v;
      }
    }
    __syncthreads();
    {
      float s = 0.f;
#pragma unroll
      for (int i = 0; i < 8; i++) s += cred[i * 512 + t];
      const float sn = dpp<0xB1>(s);
      if ((t & 1) == 0) *(unsigned*)(cs + t) = cvtpk(s, sn);
    }
    __syncthreads();
    {
      const char* ap = (const char*)cs + ((c >> 1) & 3) * 256 + (g * 32 + (c & 1) * 16) * 2;
      const u32x4 a0 = *(const u32x4*)(ap), a1 = *(const u32x4*)(ap + 16);
      f32x4 dd[8];
#pragma unroll
      for (int j = 0; j < 8; j++)
        dd[j] = __builtin_amdgcn_mfma_f32_16x16x32_bf16(__builtin_bit_cast(bf16x8, a0), __builtin_bit_cast(bf16x8, bo[j][0]), f32x4{0.f, 0.f, 0.f, 0.f}, 0, 0, 0);
#pragma unroll
      for (int j = 0; j < 8; j++)
        dd[j] = __builtin_amdgcn_mfma_f32_16x16x32_bf16(__builtin_bit_cast(bf16x8, a1), __builtin_bit_cast(bf16x8, bo[j][1]), dd[j], 0, 0, 0);
      f32x4 v4[2];
#pragma unroll
      for (int j = 0; j < 8; j++) {
        const float u0 = (l & 8) ? dd[j][1] : dd[j][0], u1 = (l & 8) ? dd[j][3] : dd[j][2];
        const float keep = (l & 8) ? u1 : u0, give = (l & 8) ? u0 : u1;
        v4[j >> 2][j & 3] = keep + dpp<0x128>(give);
      }
      if (g < 2) {
        float* op = outs + (g * 2 + ((l >> 3) & 1)) * 512 + w * 64 + (l & 7) * 8;
        *(f32x4*)op = v4[0];
        *(f32x4*)(op + 4) = v4[1];
      }
    }
    __syncthreads();
    {
      const f32x2 va = *(const f32x2*)(outs + t * 2), vb = *(const f32x2*)(outs + 1024 + t * 2);
      const unsigned oo = (unsigned)(WS_PA + ((size_t)((t >> 8) * NH + h) * DM + (t & 255) * 2) * 8);
      st16u_sc1(rs, oo, u32x4{__float_as_uint(va[0]), ta, __float_as_uint(va[1]), tb});
      st16u_sc1(rs, oo + 2 * NH * DM * 8, u32x4{__float_as_uint(vb[0]), ta, __float_as_uint(vb[1]), tb});
    }
  if (w < 4) {
    const int bb = h >> 3, n0 = (h & 7) * 64 + w * 16;
    const unsigned og = (unsigned)(WS_PA + ((size_t)(bb * NH + (l >> 3)) * DM + n0) * 8 + (l & 7) * 16);
    u32x4 p[4];
    __builtin_amdgcn_s_sleep(10);
#pragma unroll 1
    for (unsigned spins = 0; spins < SPIN_CAP; ++spins) {
      asm volatile("" ::: "memory");
#pragma unroll
      for (int i = 0; i < 4; i++) p[i] = ld16u_sc1(rs, og + i * 8 * DM * 8);
      bool ok = true;
#pragma unroll
      for (int i = 0; i < 4; i++) ok = ok && p[i][1] == ta && p[i][3] == tb;
      if (__all(ok)) break;
      __builtin_amdgcn_s_sleep(4);
    }
    float v0 = (__uint_as_float(p[0][0]) + __uint_as_float(p[1][0])) + (__uint_as_float(p[2][0]) + __uint_as_float(p[3][0]));
    float v1 = (__uint_as_float(p[0][2]) + __uint_as_float(p[1][2])) + (__uint_as_float(p[2][2]) + __uint_as_float(p[3][2]));
    v0 += dpp<0x128>(v0); v0 = xrow_sum(v0);
    v1 += dpp<0x128>(v1); v1 = xrow_sum(v1);
    if (l < 8) {
      *(unsigned*)(out + bb * 1024 + n0 + l * 2) = cvtpk(v0, v1);
      if (h == 0 && t == 0) st_u64(ws + WS_EPA, tag);
    }
  }
    }
  }
}

__device__ __forceinline__ void role_A2(int a, const __hip_bfloat16* __restrict__ ckv, char* __restrict__ ws,
                                        __amdgpu_buffer_rsrc_t rs, char* lds) {
  const int t = threadIdx.x, l = t & 63;
  const int w = __builtin_amdgcn_readfirstlane(t >> 6);
  const int g = l >> 4, c = l & 15;
  u64 ep = 0;
  if (t == 0) ep = ld_u64(ws + WS_EPA);
  const int grp = w >> 2, wq = w & 3;
  const int b = a / SPLIT_A, sp = a % SPLIT_A;
  const int r = l & 31, hh = l >> 5;
  constexpr unsigned CB = (unsigned)((size_t)NB * SK * DC * 2);
  constexpr int NTL = KA / 64;
  const unsigned cvo = (unsigned)((g * DC + 128 * wq + c * 8) * 2);
  const unsigned cso = (unsigned)(((size_t)b * SK + sp * KA + grp * 32) * DC * 2);
  auto load_tile = [&](u32x4 (&pr)[8], int tile) {
    const bool ok = tile < NTL;
    const __amdgpu_buffer_rsrc_t rc = mkrs(ckv, ok ? CB : 0u);
    const unsigned so = ok ? cso + (unsigned)((tile + sp) & (NTL - 1)) * 64 * DC * 2 : 0u;
#pragma unroll
    for (int u = 0; u < 8; u++) pr[u] = __builtin_amdgcn_raw_buffer_load_b128(rc, cvo, so + u * 4 * DC * 2, NT);
  };
  char* img = lds + w * 8192;
  float* sred = (float*)(lds + 65536 + grp * 16384);
  float* mxs = (float*)(lds + 98304 + grp * 512);
  unsigned* pbuf = (unsigned*)(lds + 102400 + grp * 2048);
  float* lall = (float*)(lds + 106496);
  const unsigned imga = (unsigned)(uintptr_t)(__attribute__((address_space(3))) char*)img;
  auto put_tile = [&](u32x4 (&pr)[8]) {
#pragma unroll
    for (int u = 0; u < 8; u++) *(u32x4*)(img + lds_off(4 * u + g, c)) = pr[u];
  };
  u32x4 pre[2][8];
  load_tile(pre[0], 0);
  load_tile(pre[1], 1);
  const u64 tag = next_tag(uni64(ep));
  put_tile(pre[0]);
  __builtin_amdgcn_sched_barrier(0);
  load_tile(pre[0], 2);
  if (t < 64) {
#pragma unroll 1
    for (unsigned spins = 0; spins < SPIN_CAP; ++spins) {
      __builtin_amdgcn_s_sleep(32);
      if (__all(ld_u64(ws + WS_F1 + (l & 31) * 8) == tag)) break;
    }
  }
  __syncthreads();
  bf16x8 qf[8];
  {
    char* qi = lds + 65536;
    const unsigned qo = (unsigned)(WS_QABS + (size_t)b * NH * DC * 2 + t * 16);
    u32x4 qv[4];
#pragma unroll
    for (int i = 0; i < 4; i++) qv[i] = ld16u_sc1(rs, qo + i * 8192);
#pragma unroll
    for (int i = 0; i < 4; i++) *(u32x4*)(qi + (i * 8 + w) * 1040 + l * 16) = qv[i];
    __syncthreads();
#pragma unroll
    for (int s = 0; s < 8; s++) qf[s] = __builtin_bit_cast(bf16x8, *(const u32x4*)(qi + r * 1040 + (128 * wq + 16 * s + 8 * hh) * 2));
#pragma unroll
    for (int s = 0; s < 8; s++) asm volatile("" : "+v"(qf[s]));
    __syncthreads();
  }
  f32x16 o[4];
#pragma unroll
  for (int i = 0; i < 4; i++)
#pragma unroll
    for (int j = 0; j < 16; j++) o[i][j] = 0.f;
  float m = -INFINITY, lsum = 0.f;
  const int q4 = (l & 15) >> 2, p4 = l & 3, colh = g & 1;
  auto tile_body = [&](int tile, u32x4 (&nx)[8]) {
    f32x16 s;
#pragma unroll
    for (int j = 0; j < 16; j++) s[j] = 0.f;
    {
      u32x4 af[8];
#pragma unroll
      for (int ks = 0; ks < 8; ks++) af[ks] = *(const u32x4*)(img + lds_off(r, 2 * ks + hh));
      __builtin_amdgcn_sched_barrier(0);
#pragma unroll
      for (int ks = 0; ks < 8; ks++) s = __builtin_amdgcn_mfma_f32_32x32x16_bf16(__builtin_bit_cast(bf16x8, af[ks]), qf[ks], s, 0, 0, 0);
    }
    f32x4* sr = (f32x4*)sred;
#pragma unroll
    for (int k4 = 0; k4 < 4; k4++) sr[(k4 * 4 + wq) * 64 + l] = f32x4{s[4 * k4], s[4 * k4 + 1], s[4 * k4 + 2], s[4 * k4 + 3]};
    __syncthreads();
    float x[4];
    {
      const f32x4 v = (sr[(wq * 4) * 64 + l] + sr[(wq * 4 + 1) * 64 + l]) + (sr[(wq * 4 + 2) * 64 + l] + sr[(wq * 4 + 3) * 64 + l]);
#pragma unroll
      for (int e = 0; e < 4; e += 2) {
        const unsigned r1 = cvtpk(v[e], v[e + 1]);
        const unsigned r2 = cvtpk(bf_lo(r1) * SCALE, bf_hi(r1) * SCALE);
        x[e] = bf_lo(r2); x[e + 1] = bf_hi(r2);
      }
      float mq = fmaxf(fmaxf(x[0], x[1]), fmaxf(x[2], x[3]));
      auto sx = __builtin_amdgcn_permlane32_swap(__float_as_uint(mq), __float_as_uint(mq), false, false);
      mq = fmaxf(__uint_as_float(sx[0]), __uint_as_float(sx[1]));
      if (hh == 0) mxs[r * 4 + wq] = mq;
    }
    __syncthreads();
    const f32x4 m4 = *(const f32x4*)(mxs + r * 4);
    const float mn = fmaxf(fmaxf(m, fmaxf(m4[0], m4[1])), fmaxf(m4[2], m4[3]));
    const float alpha = fexp2((m - mn) * LOG2E);
    {
      const unsigned p0 = cvtpk(fexp2((x[0] - mn) * LOG2E), fexp2((x[1] - mn) * LOG2E));
      const unsigned p1 = cvtpk(fexp2((x[2] - mn) * LOG2E), fexp2((x[3] - mn) * LOG2E));
      *(u32x2*)(pbuf + l * 8 + wq * 2) = u32x2{p0, p1};
      lsum = lsum * alpha + ((bf_lo(p0) + bf_hi(p0)) + (bf_lo(p1) + bf_hi(p1)));
    }
    m = mn;
    __syncthreads();
    unsigned pk[8];
    {
      const u32x4 a0 = *(const u32x4*)(pbuf + l * 8), a1 = *(const u32x4*)(pbuf + l * 8 + 4);
      pk[0] = a0[0]; pk[1] = a0[1]; pk[2] = a0[2]; pk[3] = a0[3]; pk[4] = a1[0]; pk[5] = a1[1]; pk[6] = a1[2]; pk[7] = a1[3];
    }
    if (__any(alpha != 1.f)) {
#pragma unroll
      for (int i = 0; i < 4; i++)
#pragma unroll
        for (int j = 0; j < 16; j++) o[i][j] *= alpha;
    }
#pragma unroll
    for (int s2 = 0; s2 < 2; s2++) {
      u32x2 tv[4][2];
#pragma unroll
      for (int ct = 0; ct < 4; ct++) {
        const int ch = 4 * ct + 2 * colh + (p4 >> 1);
        const unsigned a0 = lds_off(4 * hh + q4, ch) + 8 * (p4 & 1);
        const unsigned a1 = lds_off(8 + 4 * hh + q4, ch) + 8 * (p4 & 1);
        if (s2 == 0) {
          asm volatile("ds_read_b64_tr_b16 %0, %1" : "=v"(tv[ct][0]) : "v"(imga + a0) : "memory");
          asm volatile("ds_read_b64_tr_b16 %0, %1" : "=v"(tv[ct][1]) : "v"(imga + a1) : "memory");
        } else {
          asm volatile("ds_read_b64_tr_b16 %0, %1 offset:4096" : "=v"(tv[ct][0]) : "v"(imga + a0) : "memory");
          asm volatile("ds_read_b64_tr_b16 %0, %1 offset:4096" : "=v"(tv[ct][1]) : "v"(imga + a1) : "memory");
        }
      }
      asm volatile("s_waitcnt lgkmcnt(0)" ::: "memory");
      __builtin_amdgcn_sched_barrier(0);
      u32x4 pb = {pk[4 * s2], pk[4 * s2 + 1], pk[4 * s2 + 2], pk[4 * s2 + 3]};
#pragma unroll
      for (int ct = 0; ct < 4; ct++) {
        u32x4 av = {tv[ct][0][0], tv[ct][0][1], tv[ct][1][0], tv[ct][1][1]};
        o[ct] = __builtin_amdgcn_mfma_f32_32x32x16_bf16(__builtin_bit_cast(bf16x8, av), __builtin_bit_cast(bf16x8, pb), o[ct], 0, 0, 0);
      }
    }
    put_tile(nx);
    __builtin_amdgcn_sched_barrier(0);
    load_tile(nx, tile + 3);
  };
#pragma unroll 1
  for (int tile = 0; tile < NTL; tile += 2) {
    tile_body(tile, pre[1]);
    tile_body(tile + 1, pre[0]);
  }
  constexpr int OST = 516;
  float* ob = (float*)lds;
  float* mlb = (float*)(lds + 32 * OST * 4);
  lall[w * 64 + l] = lsum;
  __syncthreads();
  {
    const float* lp = lall + grp * 256 + r;
    lsum = ((lp[0] + lp[32]) + (lp[64] + lp[96])) + ((lp[128] + lp[160]) + (lp[192] + lp[224]));
  }
  if (grp == 1) {
#pragma unroll
    for (int ct = 0; ct < 4; ct++)
#pragma unroll
      for (int j4 = 0; j4 < 4; j4++)
        *(f32x4*)(ob + r * OST + 128 * wq + 32 * ct + 8 * j4 + 4 * hh) =
            f32x4{o[ct][4 * j4], o[ct][4 * j4 + 1], o[ct][4 * j4 + 2], o[ct][4 * j4 + 3]};
    if (wq == 0 && l < 32) { mlb[2 * r] = m; mlb[2 * r + 1] = lsum; }
  }
  __syncthreads();
  if (grp == 0) {
    const float m1 = mlb[2 * r], l1 = mlb[2 * r + 1];
    const float M = fmaxf(m, m1);
    const float f0 = fexp2((m - M) * LOG2E), f1 = fexp2((m1 - M) * LOG2E);
#pragma unroll
    for (int ct = 0; ct < 4; ct++)
#pragma unroll
      for (int j4 = 0; j4 < 4; j4++) {
        float* p = ob + r * OST + 128 * wq + 32 * ct + 8 * j4 + 4 * hh;
        const f32x4 o1 = *(const f32x4*)p;
        *(f32x4*)p = f32x4{o[ct][4 * j4] * f0 + o1[0] * f1, o[ct][4 * j4 + 1] * f0 + o1[1] * f1,
                           o[ct][4 * j4 + 2] * f0 + o1[2] * f1, o[ct][4 * j4 + 3] * f0 + o1[3] * f1};
      }
    if (wq == 0 && l < 32) {
      f32x2 v = {M, lsum * f0 + l1 * f1};
      st8_sc1(ws + WS_PAML + ((size_t)(b * SPLIT_A + sp) * NH + r) * 8, v);
    }
  }
  __syncthreads();
  {
    const unsigned pao = (unsigned)(WS_PAO + (size_t)(b * SPLIT_A + sp) * NH * DC * 2);
    u32x4 pv[4];
#pragma unroll
    for (int i = 0; i < 4; i++) {
      const int idx = i * 512 + t;
      const int hd = idx >> 6, c8 = idx & 63;
      const f32x4 v0 = *(const f32x4*)(ob + hd * OST + c8 * 8), v1 = *(const f32x4*)(ob + hd * OST + c8 * 8 + 4);
      pv[i] = u32x4{cvtpk(v0[0], v0[1]), cvtpk(v0[2], v0[3]), cvtpk(v1[0], v1[1]), cvtpk(v1[2], v1[3])};
      asm volatile("" : "+v"(pv[i]));
    }
    wait_gate(ws + WS_FP, tag);
    __syncthreads();
#pragma unroll
    for (int i = 0; i < 4; i++) st16u_sc1(rs, pao + (unsigned)(i * 512 + t) * 16, pv[i]);
  }
  signal_flag(ws + WS_F2 + a * 8, tag);
}

__global__ __launch_bounds__(512, 2) void k_fused(const __hip_bfloat16* __restrict__ xA, const __hip_bfloat16* __restrict__ ckv,
                                                  const __hip_bfloat16* __restrict__ wqA, const __hip_bfloat16* __restrict__ wuk,
                                                  const __hip_bfloat16* __restrict__ wuv, const __hip_bfloat16* __restrict__ woA,
                                                  const __hip_bfloat16* __restrict__ xB, const __hip_bfloat16* __restrict__ kc,
                                                  const __hip_bfloat16* __restrict__ vc, const __hip_bfloat16* __restrict__ wqB,
                                                  const __hip_bfloat16* __restrict__ woB, char* __restrict__ ws,
                                                  __hip_bfloat16* __restrict__ out) {
  __shared__ __attribute__((aligned(16))) char lds[131072];
  u64 te_; asm volatile("s_memrealtime %0" : "=s"(te_));
  asm volatile("" :: "s"(xA), "s"(ckv), "s"(wqA), "s"(wuk), "s"(wuv), "s"(woA), "s"(xB), "s"(kc), "s"(vc), "s"(wqB), "s"(woB), "s"(ws), "s"(out));
  u64 tk_; asm volatile("s_waitcnt lgkmcnt(0)\n s_memrealtime %0\n s_waitcnt lgkmcnt(0)" : "=s"(tk_), "+s"(te_) : "s"(ws));
  const int blk = blockIdx.x, xcd = blk & 7, r = blk >> 3;
  unsigned pfv = 0;
  {
    u64 pc; unsigned len;
    asm volatile("s_getpc_b64 %0\n.Lcode_begin:\n\ts_mov_b32 %1, .Lcode_end-.Lcode_begin" : "=s"(pc), "=s"(len));
    const unsigned idx = (unsigned)(r * 8 + (threadIdx.x >> 6));
    const __amdgpu_buffer_rsrc_t rc = mkrs((const void*)pc, len);
    pfv = __builtin_amdgcn_raw_buffer_load_b32(rc, idx * 1024 + (threadIdx.x & 63) * 16, 0, 0);
  }
  const __amdgpu_buffer_rsrc_t rs = mkrs(ws, (unsigned)WS_END);
  if (r < 8) {
    if (r < 4) role_A1(xcd + 8 * r, xA, wqA, wuk, wuv, woA, ws, rs, out, lds);
    else role_A2(xcd * 4 + r - 4, ckv, ws, rs, lds);
  } else {
    const int rr = r - 8, hq = rr / 6;
    role_B(((0x76043215u >> (4 * xcd)) & 7) + 8 * hq, rr - 6 * hq, xB, kc, vc, wqB, woB, ws, rs, out, lds);
  }
  if (threadIdx.x < 64) while (__builtin_amdgcn_s_memrealtime() - te_ < 8000ull) __builtin_amdgcn_s_sleep(1);
  asm volatile(".Lcode_end:" :: "v"(pfv));
}

extern "C" void kernel_launch(void* const* d_in, const int* in_sizes, int n_in,
                              void* d_out, int out_size, void* d_ws, size_t ws_size,
                              hipStream_t stream) {
  k_fused<<<256, 512, 0, stream>>>((const __hip_bfloat16*)d_in[0], (const __hip_bfloat16*)d_in[1], (const __hip_bfloat16*)d_in[2],
                                   (const __hip_bfloat16*)d_in[3], (const __hip_bfloat16*)d_in[4], (const __hip_bfloat16*)d_in[5],
                                   (const __hip_bfloat16*)d_in[6], (const __hip_bfloat16*)d_in[7], (const __hip_bfloat16*)d_in[8],
                                   (const __hip_bfloat16*)d_in[9], (const __hip_bfloat16*)d_in[10], (char*)d_ws,
                                   (__hip_bfloat16*)d_out);
}
```

```cpp
#define TRACE
#define DEFER 1
#define WOAUX 0
#define WQAUX 0
#define ROTQ 0x203120
#define EXF_IDX 0
#define EXF_I0 0
#define EXF_I1 4
#define EXF_NW 8
#define EXF_XCC -1
#define EXF_MAX 1
#define EXF_OFF 0
#define EXF_MUL 4
#include <hip/hip_runtime.h>
#include <hip/hip_bf16.h>
#include <stdint.h>

typedef __attribute__((ext_vector_type(4))) unsigned u32x4;
typedef __attribute__((ext_vector_type(2))) unsigned u32x2;
typedef __attribute__((ext_vector_type(4))) float f32x4;
typedef __attribute__((ext_vector_type(2))) float f32x2;
typedef __attribute__((ext_vector_type(16))) float f32x16;
typedef __attribute__((ext_vector_type(8))) __bf16 bf16x8;
typedef __attribute__((ext_vector_type(2))) __bf16 bf16x2;
typedef __attribute__((address_space(1))) unsigned gu32;
typedef __attribute__((address_space(1))) unsigned long long gu64;
typedef unsigned long long u64;

#define ROTM 0
#define AROT1 0
#define AROT2 0
#define PERM 0x76043215u
#define PSL 4
#define HAB 0
#define NOC 0
#define WO0 3
#define WOS 4
#define TSY0 950
#define TSW 190
#define KVAUX 2
#define ABL 0
constexpr int NB = 4;
constexpr int NH = 32;
constexpr int SK = 4096;
constexpr int DH = 128;
constexpr int DM = 512;
constexpr int DC = 512;
constexpr int HD = NH * DH;

constexpr int SPLIT_A = 8;
constexpr int KA = SK / SPLIT_A;
constexpr int SS = 171;
constexpr int RD = 18;
constexpr int NP = SS - 8 * RD + 8;
constexpr int NP4 = (NP + 3) & ~3;
constexpr int RST = 144;
#define GID 6
constexpr int GI = GID;
constexpr int RE = 15;
#define PRIOFD 4
constexpr int PRIOF = PRIOFD;

constexpr float SCALE = 0.08838834764831845f;
constexpr float LOG2E = 1.4426950408889634f;
#define NTA 2
constexpr int NT = NTA;
constexpr int SC1 = 16;

constexpr size_t WS_EPA  = 0;
constexpr size_t WS_EPB  = 256;
constexpr size_t WS_F1   = 4096;
constexpr size_t WS_F2   = WS_F1 + 1024;
constexpr size_t WS_FP   = 8192;
constexpr size_t WS_QABS = 16384;
constexpr size_t WS_PAML = WS_QABS + (size_t)NB * NH * DC * 2;
constexpr size_t WS_PAO  = WS_PAML + (size_t)NB * SPLIT_A * NH * 2 * 4;
constexpr size_t WS_PA   = WS_PAO + (size_t)NB * SPLIT_A * NH * DC * 2;
constexpr size_t WS_PBML = WS_PA + (size_t)NB * NH * DM * 8;
constexpr size_t WS_PBG  = WS_PBML + (size_t)NB * 64 * 16;
constexpr size_t WS_END  = WS_PBG + (size_t)NB * 64 * DM * 8;

__device__ __forceinline__ float bf_lo(unsigned u) { return __uint_as_float(u << 16); }
__device__ __forceinline__ float bf_hi(unsigned u) { return __uint_as_float(u & 0xffff0000u); }
__device__ __forceinline__ unsigned cvtpk(float lo, float hi) {
  f32x2 v = {lo, hi};
  return __builtin_bit_cast(unsigned, __builtin_convertvector(v, bf16x2));
}
__device__ __forceinline__ float rbf(float x) { return __uint_as_float(cvtpk(x, x) << 16); }
__device__ __forceinline__ float dot2(unsigned k, unsigned q, float acc) {
  return __builtin_amdgcn_fdot2_f32_bf16(__builtin_bit_cast(bf16x2, k), __builtin_bit_cast(bf16x2, q), acc, false);
}
template <int CTRL> __device__ __forceinline__ float dpp(float x) {
  return __builtin_bit_cast(float, __builtin_amdgcn_mov_dpp(__builtin_bit_cast(int, x), CTRL, 0xf, 0xf, true));
}
__device__ __forceinline__ float sum16(float x) {
  x += dpp<0x128>(x);
  x += dpp<0x124>(x);
  x += dpp<0x4E>(x);
  x += dpp<0xB1>(x);
  return x;
}
__device__ __forceinline__ float xrow_max(float x) {
  auto s = __builtin_amdgcn_permlane16_swap(__float_as_uint(x), __float_as_uint(x), false, false);
  x = fmaxf(__uint_as_float(s[0]), __uint_as_float(s[1]));
  auto t = __builtin_amdgcn_permlane32_swap(__float_as_uint(x), __float_as_uint(x), false, false);
  return fmaxf(__uint_as_float(t[0]), __uint_as_float(t[1]));
}
__device__ __forceinline__ float xrow_sum(float x) {
  auto s = __builtin_amdgcn_permlane16_swap(__float_as_uint(x), __float_as_uint(x), false, false);
  x = __uint_as_float(s[0]) + __uint_as_float(s[1]);
  auto t = __builtin_amdgcn_permlane32_swap(__float_as_uint(x), __float_as_uint(x), false, false);
  return __uint_as_float(t[0]) + __uint_as_float(t[1]);
}
__device__ __forceinline__ float fexp2(float x) { return __builtin_amdgcn_exp2f(x); }
__device__ __forceinline__ __amdgpu_buffer_rsrc_t mkrs(const void* p, unsigned bytes) {
  return __builtin_amdgcn_make_buffer_rsrc((void*)p, 0, (int)bytes, 0x00020000);
}

constexpr size_t WS_TR = (size_t)64 << 20;
#define TR(i) do { __builtin_amdgcn_sched_barrier(0); { const unsigned ts_ = (unsigned)__builtin_amdgcn_s_memrealtime(); trv = (int)(threadIdx.x & 63) == (i) ? ts_ : trv; } __builtin_amdgcn_sched_barrier(0); } while (0)
#define TRDUMP() trv = (threadIdx.x & 63) == 61 ? (unsigned)__builtin_amdgcn_s_memtime() : trv; __hip_atomic_store((gu32*)(ws + WS_TR) + (blockIdx.x * 8 + (threadIdx.x >> 6)) * 64 + (threadIdx.x & 63), trv, __ATOMIC_RELAXED, __HIP_MEMORY_SCOPE_AGENT)
constexpr int PTHR = 60;
constexpr unsigned SPIN_CAP = 1u << 21;
__device__ __forceinline__ u64 ld_u64(const void* p) {
  return __hip_atomic_load((gu64*)p, __ATOMIC_RELAXED, __HIP_MEMORY_SCOPE_AGENT);
}
__device__ __forceinline__ void st_u64(void* p, u64 v) {
  __hip_atomic_store((gu64*)p, v, __ATOMIC_RELAXED, __HIP_MEMORY_SCOPE_AGENT);
}
__device__ __forceinline__ u64 next_tag(u64 e) { return e + 0x0000000100000001ull; }
__device__ __forceinline__ u64 uni64(u64 v) {
  const unsigned lo = __builtin_amdgcn_readfirstlane((unsigned)v), hi = __builtin_amdgcn_readfirstlane((unsigned)(v >> 32));
  return ((u64)hi << 32) | lo;
}
__device__ __forceinline__ void signal_flag(char* flag, u64 tag) {
  asm volatile("s_waitcnt vmcnt(0)" ::: "memory");
  __syncthreads();
  if (threadIdx.x == 0) st_u64(flag, tag);
}
__device__ __forceinline__ void wait_gate(const char* flag, u64 tag, unsigned trv) {
  if (threadIdx.x < 64) {
    for (unsigned spins = 0; spins < SPIN_CAP; ++spins) {
      if (ld_u64(flag) == tag) break;
      __builtin_amdgcn_s_sleep(4);
    }
  }
}
__device__ __forceinline__ void st16_sc1(__amdgpu_buffer_rsrc_t rs, unsigned off, f32x4 v) {
  __builtin_amdgcn_raw_buffer_store_b128(__builtin_bit_cast(u32x4, v), rs, off, 0, SC1);
}
__device__ __forceinline__ void st16u_sc1(__amdgpu_buffer_rsrc_t rs, unsigned off, u32x4 v) {
  __builtin_amdgcn_raw_buffer_store_b128(v, rs, off, 0, SC1);
}
__device__ __forceinline__ f32x4 ld16_sc1(__amdgpu_buffer_rsrc_t rs, unsigned off) {
  return __builtin_bit_cast(f32x4, __builtin_amdgcn_raw_buffer_load_b128(rs, off, 0, SC1));
}
__device__ __forceinline__ u32x4 ld16u_sc1(__amdgpu_buffer_rsrc_t rs, unsigned off) {
  return __builtin_amdgcn_raw_buffer_load_b128(rs, off, 0, SC1);
}
__device__ __forceinline__ void st8_sc1(void* p, f32x2 v) { st_u64(p, __builtin_bit_cast(u64, v)); }
__device__ __forceinline__ f32x2 ld8_sc1(const void* p) { return __builtin_bit_cast(f32x2, ld_u64(p)); }

__device__ __forceinline__ void item_dots(const u32x4 (&ks)[4], const u32x4 q, float (&s_)[4]) {
#pragma unroll
  for (int u = 0; u < 4; u++) {
    float a = dot2(ks[u][0], q[0], 0.f);
    a = dot2(ks[u][1], q[1], a);
    a = dot2(ks[u][2], q[2], a);
    a = dot2(ks[u][3], q[3], a);
    s_[u] = a;
    asm volatile("" : "+v"(s_[u]));
  }
}
__device__ __forceinline__ void item_unpack(const u32x4 (&vs)[4], f32x2 (&vf)[4][4]) {
#pragma unroll
  for (int u = 0; u < 4; u++)
#pragma unroll
    for (int j = 0; j < 4; j++) { vf[u][j] = f32x2{bf_lo(vs[u][j]), bf_hi(vs[u][j])}; asm volatile("" : "+v"(vf[u][j])); }
}
__device__ __forceinline__ f32x2 rows_sum(const float (&o)[8]) {
  float r4[4], r2[2];
#pragma unroll
  for (int j = 0; j < 4; j++) {
    auto sx = __builtin_amdgcn_permlane32_swap(__float_as_uint(o[j]), __float_as_uint(o[j + 4]), false, false);
    r4[j] = __uint_as_float(sx[0]) + __uint_as_float(sx[1]);
  }
#pragma unroll
  for (int j = 0; j < 2; j++) {
    auto sx = __builtin_amdgcn_permlane16_swap(__float_as_uint(r4[j]), __float_as_uint(r4[j + 2]), false, false);
    r2[j] = __uint_as_float(sx[0]) + __uint_as_float(sx[1]);
  }
  return f32x2{r2[0], r2[1]};
}
__device__ __forceinline__ f32x2 item_partial(const float (&s_)[4], const f32x2 (&vf)[4][4], float& mx, float& ll) {
  float s[4];
#pragma unroll
  for (int u = 0; u < 4; u++) s[u] = rbf(rbf(sum16(s_[u])) * SCALE);
  mx = xrow_max(fmaxf(fmaxf(s[0], s[1]), fmaxf(s[2], s[3])));
  float p[4];
#pragma unroll
  for (int u = 0; u < 4; u++) p[u] = fexp2((s[u] - mx) * LOG2E);
  ll = xrow_sum((p[0] + p[1]) + (p[2] + p[3]));
  float o[8];
#pragma unroll
  for (int j = 0; j < 4; j++) {
    f32x2 v = vf[0][j] * p[0];
#pragma unroll
    for (int u = 1; u < 4; u++) v = __builtin_elementwise_fma(vf[u][j], f32x2{p[u], p[u]}, v);
    o[2 * j] = v[0]; o[2 * j + 1] = v[1];
  }
  return rows_sum(o);
}
__device__ __forceinline__ void role_B(int h, int j6, const __hip_bfloat16* __restrict__ xB,
                                       const __hip_bfloat16* __restrict__ kc, const __hip_bfloat16* __restrict__ vc,
                                       const __hip_bfloat16* __restrict__ wqB, const __hip_bfloat16* __restrict__ woB,
                                       char* __restrict__ ws, __amdgpu_buffer_rsrc_t rs,
                                       __hip_bfloat16* __restrict__ out, char* lds, unsigned trv) {
  const int t = threadIdx.x, l = t & 63;
  const int w = __builtin_amdgcn_readfirstlane(t >> 6);
  const int g = l >> 4, c = l & 15;
  const int pr = j6 >= 3 ? 1 : 0, k = j6 - 3 * pr;
  const int b0 = 2 * pr;
  const int NI = k == 1 ? 2 * (256 - SS) : SS;
  const int e0 = k == 1 ? (h & 1) : (k >> 1);
  u32x4 xr[2][2], wr[16];
  {
    const __amdgpu_buffer_rsrc_t rx = mkrs(xB, NB * DM * 2);
    const int rq_ = (ROTQ >> (4 * j6)) & 3;
    const int cw = w ^ ((rq_ & 2) << 1), r0 = rq_ & 1;
    const unsigned xo = (unsigned)((b0 * DM + cw * 64 + g * 16) * 2);
#pragma unroll
    for (int bb = 0; bb < 2; bb++)
#pragma unroll
      for (int i = 0; i < 2; i++) xr[bb][i] = __builtin_amdgcn_raw_buffer_load_b128(rx, xo + bb * DM * 2, (i ^ r0) * 16, 0);
    const __amdgpu_buffer_rsrc_t rq = mkrs(wqB, DM * HD * 2);
    const unsigned vo = (unsigned)(((cw * 64 + g * 16) * HD + h * DH + c * 8) * 2);
    const unsigned vo0 = vo + r0 * 8 * HD * 2, vo1 = vo + (1 - r0) * 8 * HD * 2;
#pragma unroll
    for (int i = 0; i < 16; i++) wr[i] = __builtin_amdgcn_raw_buffer_load_b128(rq, i < 8 ? vo0 : vo1, (i & 7) * HD * 2, WQAUX);
  }
  constexpr unsigned KVB = (unsigned)((size_t)NB * NH * SK * DH * 2);
  const __amdgpu_buffer_rsrc_t rk = mkrs(kc, KVB), rv = mkrs(vc, KVB);
  const unsigned lo = (unsigned)(g * 256 + c * 16);
  const unsigned hb0 = ((unsigned)((b0 + e0) * NH + h) << 20) + (k == 1 ? (unsigned)SS << 12 : 0u);
  const unsigned hb1 = ((unsigned)((b0 + 1 - e0) * NH + h) << 20) + ((unsigned)(2 * SS - 256) << 12);
  auto soff = [&](int x) { return ((k == 1 && x >= 256 - SS) ? hb1 : hb0) + ((unsigned)x << 12); };
  auto ld4 = [&](u32x4 (&d)[4], __amdgpu_buffer_rsrc_t r, unsigned so) {
#pragma unroll
    for (int u = 0; u < 4; u++) d[u] = __builtin_amdgcn_raw_buffer_load_b128(r, lo + u * 1024, so, KVAUX);
  };
  u32x4 kr[4], vr[4], ke[4], ve[4];
  ld4(kr, rk, soff(w));
  ld4(vr, rv, soff(w));
  u64* eps = (u64*)(lds + 33808);
  u64 epv = 0;
  if (t < 3) epv = ld_u64(ws + (t == 0 ? WS_EPA : WS_EPB + 256 * (b0 + (t == 1 ? e0 : 1 - e0))));
  TR(56);
  float* red = (float*)lds;
  unsigned* qs = (unsigned*)(lds + 32768);
  unsigned* cnt0 = (unsigned*)(lds + 33792);
  unsigned* cntd = (unsigned*)(lds + 33796);
  float* rec0 = (float*)(lds + 36864);
  float* mlr0 = (float*)(lds + 40960);
  char* prv = lds + 45056 + w * 512;
  float* recd = (float*)(lds + 65536);
  float* mld = (float*)(lds + 98304);
  const unsigned cnta = (unsigned)(uintptr_t)(__attribute__((address_space(3))) char*)cntd;
  float* mxd = (float*)(lds + 33800);
  if (t == 0) { *cnt0 = 0u; *cntd = 8u * (RD + 1); mxd[0] = -INFINITY; mxd[1] = -INFINITY; }
  if (t < 64 * (NP4 - NP + 1)) *(f32x2*)(recd + (NP - 1 + (t >> 6)) * RST + 2 * l) = f32x2{0.f, 0.f};
  {
    float a0[8], a1[8];
#pragma unroll
    for (int j = 0; j < 8; j++) { a0[j] = 0.f; a1[j] = 0.f; }
#pragma unroll
    for (int i = 0; i < 16; i += 2) {
      const unsigned x0 = xr[0][i >> 3][(i >> 1) & 3], x1 = xr[1][i >> 3][(i >> 1) & 3];
#pragma unroll
      for (int j = 0; j < 4; j++) {
        const unsigned pl = __builtin_amdgcn_perm(wr[i + 1][j], wr[i][j], 0x05040100u);
        const unsigned ph = __builtin_amdgcn_perm(wr[i + 1][j], wr[i][j], 0x07060302u);
        a0[2 * j] = dot2(pl, x0, a0[2 * j]); a0[2 * j + 1] = dot2(ph, x0, a0[2 * j + 1]);
        a1[2 * j] = dot2(pl, x1, a1[2 * j]); a1[2 * j + 1] = dot2(ph, x1, a1[2 * j + 1]);
      }
    }
    TR(57);
#pragma unroll
    for (int j = 0; j < 8; j++) asm volatile("" : "+v"(a0[j]), "+v"(a1[j]));
    __builtin_amdgcn_sched_barrier(0);
    ld4(ke, rk, soff(w + 8));
    ld4(ve, rv, soff(w + 8));
    __builtin_amdgcn_sched_barrier(0);
    float* rp = red + (w * 4 + g) * 256 + c * 8;
    *(f32x4*)(rp) = f32x4{a0[0], a0[1], a0[2], a0[3]};
    *(f32x4*)(rp + 4) = f32x4{a0[4], a0[5], a0[6], a0[7]};
    *(f32x4*)(rp + 128) = f32x4{a1[0], a1[1], a1[2], a1[3]};
    *(f32x4*)(rp + 132) = f32x4{a1[4], a1[5], a1[6], a1[7]};
  }
  __syncthreads();
  {
    const int o = t >> 1, hf = t & 1;
    float s = 0.f;
#pragma unroll
    for (int i = 0; i < 16; i++) s += red[(hf * 16 + i) * 256 + o];
    s += dpp<0xB1>(s);
    const float sn = dpp<0x4E>(s);
    if ((t & 3) == 0) qs[o >> 1] = cvtpk(s, sn);
    if (t < 3) eps[t] = epv;
  }
  __syncthreads();
  const u64 epa = uni64(eps[0]), tg0 = next_tag(uni64(eps[1])), tg1 = next_tag(uni64(eps[2]));
  u32x4 q = *(const u32x4*)(qs + e0 * 64 + c * 4);
  const u32x4 q1 = *(const u32x4*)(qs + (1 - e0) * 64 + c * 4);
  TR(1);
  float m = -INFINITY, lsum = 0.f, acc[8];
#pragma unroll
  for (int j = 0; j < 8; j++) acc[j] = 0.f;
  float s_[4]; f32x2 vf[4][4];
  auto part0 = [&](u32x4 (&ks)[4]) { item_dots(ks, q, s_); };
  auto part1 = [&](u32x4 (&vs)[4]) { item_unpack(vs, vf); };
  auto part2 = [&]() {
    float s[4];
#pragma unroll
    for (int u = 0; u < 4; u++) s[u] = rbf(rbf(sum16(s_[u])) * SCALE);
    const float mx = fmaxf(fmaxf(m, fmaxf(s[0], s[1])), fmaxf(s[2], s[3]));
    const float alpha = fexp2((m - mx) * LOG2E);
    m = mx;
    float p[4];
#pragma unroll
    for (int u = 0; u < 4; u++) p[u] = fexp2((s[u] - mx) * LOG2E);
    lsum = lsum * alpha + ((p[0] + p[1]) + (p[2] + p[3]));
#pragma unroll
    for (int j = 0; j < 8; j++) acc[j] *= alpha;
#pragma unroll
    for (int u = 0; u < 4; u++)
#pragma unroll
      for (int j = 0; j < 4; j++) {
        acc[2 * j]     = fmaf(p[u], vf[u][j][0], acc[2 * j]);
        acc[2 * j + 1] = fmaf(p[u], vf[u][j][1], acc[2 * j + 1]);
      }
  };
  auto front = [&](__amdgpu_buffer_rsrc_t rk2, __amdgpu_buffer_rsrc_t rv2, unsigned so) {
    __builtin_amdgcn_sched_barrier(0);
    ld4(kr, rk2, so);
    __builtin_amdgcn_sched_barrier(0);
    part1(vr);
    __builtin_amdgcn_sched_barrier(0);
    ld4(vr, rv2, so);
    __builtin_amdgcn_sched_barrier(0);
  };
  auto put_partial = [&](f32x2 r2, float mw, float ll, float* rec_, float* ml_, float* mx_) {
    *(f32x2*)(rec_ + c * 8 + g * 2) = r2;
    if (l == 0) {
      *(f32x2*)ml_ = f32x2{mw, ll};
      asm volatile("ds_max_f32 %0, %1" :: "v"((unsigned)(uintptr_t)(__attribute__((address_space(3))) float*)mx_), "v"(mw) : "memory");
    }
  };
  auto wave_partial = [&](float* rec_, float* ml_, float* mx_) {
    const float mw = xrow_max(m);
    const float fq = fexp2((m - mw) * LOG2E);
    const float ll = xrow_sum(lsum * fq);
    float o[8];
#pragma unroll
    for (int j = 0; j < 8; j++) o[j] = acc[j] * fq;
    put_partial(rows_sum(o), mw, ll, rec_, ml_, mx_);
  };
  auto part2c = [&](float* rec_, float* ml_) {
    float mx, ll;
    const f32x2 r2 = item_partial(s_, vf, mx, ll);
    put_partial(r2, mx, ll, rec_, ml_, mxd);
  };
  bool sw = k != 1;
  auto do_switch = [&]() {
    wave_partial(rec0 + w * 128, mlr0 + w * 2, mxd + 1);
    asm volatile("s_waitcnt lgkmcnt(0)" ::: "memory");
    if (l == 0) asm volatile("ds_add_u32 %0, %1" :: "v"(cnta - 4u), "v"(1u) : "memory");
    m = -INFINITY; lsum = 0.f;
#pragma unroll
    for (int j = 0; j < 8; j++) acc[j] = 0.f;
    q = q1; sw = true;
  };
  if (w >= 4 && PRIOF > 0) __builtin_amdgcn_s_setprio(1);
  part0(kr);
  front(rk, rv, soff(w + 16));
  part2();
  part0(ke); part1(ve); part2();
  int x = w + 16;
  const int rg = l >> 3, cc = l & 7;
  const int wc = w, rw0 = 0;
  const int rw = WO0 + WOS * (h >> 3);
  const int re = k != 1 ? -1 : rw + 2 > RE ? rw + 2 : RE;
  u32x4 wo[16];
  __builtin_amdgcn_sched_barrier(0);
#pragma unroll
  for (int i = 0; i < 16; i++) asm volatile("" : "=v"(wo[i]));
  const int nseg = k == 1 ? 2 : 1, sp = k == 1 ? 1 : 0;
  auto store_off = [&](int sg) {
    const int b = b0 + (sg ? 1 - e0 : e0);
    const unsigned og_ = (unsigned)(WS_PBG + ((size_t)(b * 64 + h * 2 + sp) * DM + wc * 64 + cc * 8 + g * 2) * 8);
    const unsigned om_ = (unsigned)(WS_PBML + (size_t)(b * 64 + h * 2 + sp) * 16);
    return (l & 8) == 0 ? og_ : t == 8 ? om_ : 0x7ffffff0u;
  };
  const unsigned apo = (unsigned)((c >> 3) * 256 + (g * 32 + ((c >> 2) & 1) * 16) * 2);
  auto project = [&](const char* ap, float M, float L, bool lred) {
    const u32x4 a0 = *(const u32x4*)(ap + rw0 * 16), a1 = *(const u32x4*)(ap + 16 - rw0 * 16);
    f32x4 de = {0.f, 0.f, 0.f, 0.f}, dq = {0.f, 0.f, 0.f, 0.f};
#pragma unroll
    for (int p = 0; p < 8; p++) {
      const unsigned d = p < 4 ? a0[p & 3] : a1[p & 3];
      const u32x4 af = {(c & 3) == 0 ? d : 0u, (c & 3) == 1 ? d : 0u, (c & 3) == 2 ? d : 0u, (c & 3) == 3 ? d : 0u};
      de = __builtin_amdgcn_mfma_f32_16x16x32_bf16(__builtin_bit_cast(bf16x8, af), __builtin_bit_cast(bf16x8, wo[2 * p]), de, 0, 0, 0);
      dq = __builtin_amdgcn_mfma_f32_16x16x32_bf16(__builtin_bit_cast(bf16x8, af), __builtin_bit_cast(bf16x8, wo[2 * p + 1]), dq, 0, 0, 0);
    }
    if (lred && w == 0) L = xrow_sum(sum16(L));
    const bool val = (g & 1) == ((l >> 3) & 1);
    float r4[4], r2[2];
#pragma unroll
    for (int j = 0; j < 4; j++) {
      const float u0 = (j & 1) ? dq[j >> 1] : de[j >> 1], u1 = (j & 1) ? dq[2 + (j >> 1)] : de[2 + (j >> 1)];
      auto sx = __builtin_amdgcn_permlane32_swap(__float_as_uint(u0), __float_as_uint(u1), false, false);
      const float v = __uint_as_float(sx[0]) + __uint_as_float(sx[1]);
      r4[j] = val ? v : 0.f;
    }
#pragma unroll
    for (int j = 0; j < 2; j++) {
      auto sx = __builtin_amdgcn_permlane16_swap(__float_as_uint(r4[j]), __float_as_uint(r4[j + 2]), false, false);
      const float v = __uint_as_float(sx[0]) + __uint_as_float(sx[1]);
      r2[j] = v + dpp<0x128>(v);
    }
    const bool lo8 = (l & 8) == 0;
    return f32x2{lo8 ? r2[0] : M, lo8 ? r2[1] : L};
  };
  auto publish = [&](f32x2 v, u64 tg, unsigned sto) {
    st16u_sc1(rs, sto, u32x4{__float_as_uint(v[0]), (unsigned)tg, __float_as_uint(v[1]), (unsigned)(tg >> 32)});
  };
  f32x2 seg0 = {0.f, 0.f};
  auto events = [&](int rd) {
    if (rd == PRIOF) __builtin_amdgcn_s_setprio(0);
    if (!sw && x >= 256 - SS) do_switch();
    if (rd == rw + 2) {
#pragma unroll
      for (int p = 0; p < 8; p++) {
#pragma unroll
        for (int i = 0; i < 4; i++) {
          const unsigned u0 = wo[2 * p][i], u1 = wo[2 * p + 1][i];
          wo[2 * p][i] = __builtin_amdgcn_perm(u1, u0, 0x05040100u);
          wo[2 * p + 1][i] = __builtin_amdgcn_perm(u1, u0, 0x07060302u);
        }
        asm volatile("" : "+v"(wo[2 * p]), "+v"(wo[2 * p + 1]));
      }
    }
    if (rd == re) {
    for (unsigned spins = 0; __hip_atomic_load(cnt0, __ATOMIC_RELAXED, __HIP_MEMORY_SCOPE_WORKGROUP) != 8u && spins < SPIN_CAP; ++spins)
      __builtin_amdgcn_s_sleep(1);
    asm volatile("" ::: "memory");
    f32x4 q4[4];
#pragma unroll
    for (int i = 0; i < 4; i++) q4[i] = *(const f32x4*)(mlr0 + i * 4);
    const float M = mxd[1];
    float L = 0.f;
    f32x2 a2 = {0.f, 0.f};
#pragma unroll
    for (int i = 0; i < 8; i++) {
      const float fw = fexp2((q4[i >> 1][(i & 1) * 2] - M) * LOG2E);
      L = fmaf(q4[i >> 1][(i & 1) * 2 + 1], fw, L);
      a2 += *(const f32x2*)(rec0 + i * 128 + 2 * l) * fw;
    }
    const unsigned hi = cvtpk(a2[0], a2[1]);
    *(unsigned*)(prv + l * 4) = hi;
    *(unsigned*)(prv + 256 + l * 4) = cvtpk(a2[0] - bf_lo(hi), a2[1] - bf_hi(hi));
    seg0 = project(prv + apo, M, L, false);
    }
    if (rd == rw) {
      const __amdgpu_buffer_rsrc_t ro = mkrs(woB, (ABL & 1) ? 0 : HD * DM * 2);
      const unsigned vo = (unsigned)(((h * DH + rg * 16) * DM + wc * 64 + cc * 8) * 2);
      const unsigned vo0 = vo + rw0 * 8 * DM * 2, vo1 = vo + (1 - rw0) * 8 * DM * 2;
#pragma unroll
      for (int i = 0; i < 16; i++) wo[i] = __builtin_amdgcn_raw_buffer_load_b128(ro, i < 8 ? vo0 : vo1, (i & 7) * DM * 2, WOAUX);
    }
  };
#pragma unroll 1
  for (; x < 8 * RD; x += 8) {
    const int rd = x >> 3;
    events(rd);
    part0(kr);
    front(rk, rv, soff(x + 8));
    part2();
    TR(2 + rd);
  }
  int nit = RD;
  wave_partial(recd + w * RST, mld + w * 2, mxd);
  unsigned o_ml = 98304u + (l < NP ? l : 0) * 8, o_rv = 65536u + (g * RST + w * 16 + c) * 4, o_fw = 45056u + w * 512 + ((l & 3) * 16 + (l >> 2)) * 4;
  unsigned o_fr = 45056u + w * 512 + g * 64, o_oi = (g < 2 && (c & 1) == 0) ? 41216u + g * 256 + (w * 16 + c) * 2 : 45056u + w * 512 + 256 + l * 4;
  unsigned o_ap = 41216u + apo, o_st = store_off(nseg - 1);
  unsigned o_s0 = k == 1 ? store_off(0) : 0x7ffffff0u;
  asm volatile("" : "+v"(o_s0));
  asm volatile("" : "+v"(o_ml), "+v"(o_rv), "+v"(o_fw), "+v"(o_fr), "+v"(o_oi), "+v"(o_ap), "+v"(o_st));
  {
    {
#pragma unroll 1
      while (x < NI) {
        unsigned dr; u64 sv;
        asm volatile("s_mov_b64 %1, exec\n\ts_mov_b64 exec, 1\n\tds_add_rtn_u32 %0, %2, %3\n\ts_mov_b64 exec, %1"
                     : "=&v"(dr), "=&s"(sv) : "v"(cnta), "v"(1u), "v"(kr[0][0]) : "memory");
        __builtin_amdgcn_sched_barrier(0);
        part0(kr);
        asm volatile("s_waitcnt lgkmcnt(0)" : "+v"(dr) :: "memory");
        const int xn = __builtin_amdgcn_readfirstlane(dr);
        if (h >= 24 && j6 == 2 && xn == NI - GI && l == 0) st_u64(ws + WS_FP, next_tag(epa));
        const bool ok = xn < NI;
        front(mkrs(kc, ok ? KVB : 0u), mkrs(vc, ok ? KVB : 0u), ok ? soff(xn) : 0u);
        part2c(recd + (x - 8 * RD + 8) * RST, mld + (x - 8 * RD + 8) * 2);
        x = xn;
        TR(2 + nit); nit++;
      }
      TR(40);
      asm volatile("s_waitcnt lgkmcnt(0)" ::: "memory");
      __syncthreads();
      TR(41);
    }
    {
      f32x2 mli = *(const f32x2*)(lds + o_ml);
      float rv_[NP4 / 4];
#pragma unroll
      for (int r = 0; r < NP4 / 4; r++) rv_[r] = *(const float*)(lds + o_rv + r * 4 * RST * 4);
      if (l >= NI - 8 * RD + 8) mli = f32x2{-INFINITY, 0.f};
      const float M = *mxd;
      const float fi = fexp2((mli[0] - M) * LOG2E);
      *(float*)(lds + o_fw) = fi;
      f32x4 f4[(NP4 / 4 + 3) / 4];
#pragma unroll
      for (int i = 0; i < (NP4 / 4 + 3) / 4; i++) f4[i] = *(const f32x4*)(lds + o_fr + i * 16);
      float a = 0.f;
#pragma unroll
      for (int r = 0; r < NP4 / 4; r++) a = fmaf(f4[r >> 2][r & 3], rv_[r], a);
      a = xrow_sum(a);
      const float an = dpp<0xB1>(a);
      const unsigned hi = cvtpk(a, an);
      const unsigned lw = cvtpk(a - bf_lo(hi), an - bf_hi(hi));
      *(unsigned*)(lds + o_oi) = g ? lw : hi;
      __syncthreads();
      const f32x2 pv = project(lds + o_ap, M, fi * mli[1], true);
      publish(pv, k == 1 ? tg1 : tg0, o_st);
      publish(seg0, tg0, o_s0);
    }
    TR(42);
    if (w < nseg) {
      const int sg = w;
      const int b = b0 + (sg ? 1 - e0 : e0);
      const int j64 = h * 2 + sp;
      const u64 tg = sg ? tg1 : tg0;
      const unsigned ta = (unsigned)tg, tb = (unsigned)(tg >> 32);
      const unsigned oml = (unsigned)(WS_PBML + (size_t)(b * 64 + l) * 16);
      const unsigned om4 = (unsigned)(WS_PBML + (size_t)(b * 64 + (l >> 2)) * 16);
      const unsigned og = (unsigned)(WS_PBG + ((size_t)(b * 64 + (l >> 2)) * DM + j64 * 8) * 8 + (l & 3) * 16);
      u32x4 ml[4], p[4];
      __builtin_amdgcn_s_sleep(6);
#pragma unroll 1
      for (unsigned spins = 0; spins < SPIN_CAP; ++spins) {
        asm volatile("" ::: "memory");
#pragma unroll
        for (int i = 0; i < 4; i++) ml[i] = ld16u_sc1(rs, om4 + i * 256);
#pragma unroll
        for (int i = 0; i < 4; i++) p[i] = ld16u_sc1(rs, og + i * 16 * DM * 8);
        int n = 0;
#pragma unroll
        for (int i = 0; i < 4; i++) n += __popcll(__ballot(ml[i][1] == ta && ml[i][3] == tb && p[i][1] == ta && p[i][3] == tb));
        if (n == 256 || ((HAB & 64) && n >= 192)) break;
#pragma unroll 1
        for (; n < ((HAB & 64) ? 4 * 44 : 4 * PTHR) && spins < SPIN_CAP; ++spins) {
          __builtin_amdgcn_s_sleep(PSL);
          asm volatile("" ::: "memory");
          const u32x4 m1 = ld16u_sc1(rs, oml);
          n = 4 * __popcll(__ballot(m1[1] == ta && m1[3] == tb));
        }
      }
      float v0 = 0.f, v1 = 0.f;
#pragma unroll
      for (int i = 0; i < 4; i++) {
        const float mm = __uint_as_float(ml[i][0]);
        const float Mh = fmaxf(mm, dpp<0x141>(mm));
        const float fq = fexp2((mm - Mh) * LOG2E);
        float L = fq * __uint_as_float(ml[i][2]);
        L += dpp<0x141>(L);
        const float wgt = fq * __builtin_amdgcn_rcpf(L);
        v0 = fmaf(__uint_as_float(p[i][0]), wgt, v0);
        v1 = fmaf(__uint_as_float(p[i][2]), wgt, v1);
      }
      v0 += dpp<0x128>(v0); v0 += dpp<0x124>(v0); v0 = xrow_sum(v0);
      v1 += dpp<0x128>(v1); v1 += dpp<0x124>(v1); v1 = xrow_sum(v1);
      if (l < 4) {
        *(unsigned*)(out + b * 1024 + 512 + j64 * 8 + l * 2) = cvtpk(v0, v1);
        if (j64 == 0 && l == 0) st_u64(ws + WS_EPB + 256 * b, tg);
      }
    }
  }
  TR(43);
  TRDUMP();
}

__device__ __forceinline__ unsigned lds_off(int row, int ch) {
  return 256 * row + 16 * (ch ^ (((row & 3) << 2) | ((row >> 2) & 3)));
}

__device__ __forceinline__ void role_A1(int h, const __hip_bfloat16* __restrict__ xA, const __hip_bfloat16* __restrict__ wqA,
                                        const __hip_bfloat16* __restrict__ wuk, const __hip_bfloat16* __restrict__ wuv,
                                        const __hip_bfloat16* __restrict__ woA, char* __restrict__ ws,
                                        __amdgpu_buffer_rsrc_t rs, __hip_bfloat16* __restrict__ out, char* lds, unsigned trv) {
  const int t = threadIdx.x, l = t & 63;
  const int w = __builtin_amdgcn_readfirstlane(t >> 6);
  const int g = l >> 4, c = l & 15;
  float* red = (float*)(lds + 16384);
  const unsigned vo = (unsigned)(((w * 64 + g * 16) * HD + h * DH + c * 8) * 2);
  u64 tag;
  {
    u32x4 xr[NB][2], wr[16], wk[16];
    const __amdgpu_buffer_rsrc_t rx = mkrs(xA, NB * DM * 2);
    const unsigned xo = (unsigned)((w * 64 + g * 16) * 2);
#pragma unroll
    for (int bb = 0; bb < NB; bb++)
#pragma unroll
      for (int i = 0; i < 2; i++) xr[bb][i] = __builtin_amdgcn_raw_buffer_load_b128(rx, xo + bb * DM * 2 + i * 16, 0, 0);
    const __amdgpu_buffer_rsrc_t rq = mkrs(wqA, DM * HD * 2);
#pragma unroll
    for (int i = 0; i < 16; i++) wr[i] = __builtin_amdgcn_raw_buffer_load_b128(rq, vo, i * HD * 2, NT);
    u64 epv = 0;
    if (t == 0) epv = ld_u64(ws + WS_EPA);
    unsigned* qs = (unsigned*)lds;
    unsigned short* qo = (unsigned short*)(lds + 4096);
    float acc[NB][8];
#pragma unroll
    for (int bb = 0; bb < NB; bb++)
#pragma unroll
      for (int j = 0; j < 8; j++) acc[bb][j] = 0.f;
#pragma unroll
    for (int i = 0; i < 16; i += 2) {
#pragma unroll
      for (int j = 0; j < 4; j++) {
        const unsigned pl = __builtin_amdgcn_perm(wr[i + 1][j], wr[i][j], 0x05040100u);
        const unsigned ph = __builtin_amdgcn_perm(wr[i + 1][j], wr[i][j], 0x07060302u);
#pragma unroll
        for (int bb = 0; bb < NB; bb++) {
          const unsigned x = xr[bb][i >> 3][(i >> 1) & 3];
          acc[bb][2 * j] = dot2(pl, x, acc[bb][2 * j]);
          acc[bb][2 * j + 1] = dot2(ph, x, acc[bb][2 * j + 1]);
        }
      }
    }
    float* rp = red + (w * 4 + g) * 512 + c * 8;
#pragma unroll
    for (int bb = 0; bb < NB; bb++) {
      *(f32x4*)(rp + bb * 128) = f32x4{acc[bb][0], acc[bb][1], acc[bb][2], acc[bb][3]};
      *(f32x4*)(rp + bb * 128 + 4) = f32x4{acc[bb][4], acc[bb][5], acc[bb][6], acc[bb][7]};
    }
    __syncthreads();
    {
      const __amdgpu_buffer_rsrc_t rku = mkrs(wuk, DC * HD * 2);
#pragma unroll
      for (int i = 0; i < 16; i++) wk[i] = __builtin_amdgcn_raw_buffer_load_b128(rku, vo, i * HD * 2, NT);
    }
    {
      float s = 0.f;
#pragma unroll
      for (int i = 0; i < 32; i++) s += red[i * 512 + t];
      const float sn = dpp<0xB1>(s);
      if ((t & 1) == 0) qs[t >> 1] = cvtpk(s, sn);
      if (t == 0) *(u64*)(lds + 2048) = epv;
    }
    __syncthreads();
    tag = next_tag(uni64(*(const u64*)(lds + 2048)));
    {
      u32x4 qq[NB];
#pragma unroll
      for (int bb = 0; bb < NB; bb++) qq[bb] = *(const u32x4*)(qs + bb * 64 + c * 4);
#pragma unroll
      for (int i = 0; i < 16; i++) {
        float s[NB];
#pragma unroll
        for (int bb = 0; bb < NB; bb++) {
          float v = dot2(wk[i][0], qq[bb][0], 0.f);
          v = dot2(wk[i][1], qq[bb][1], v);
          v = dot2(wk[i][2], qq[bb][2], v);
          v = dot2(wk[i][3], qq[bb][3], v);
          s[bb] = sum16(v);
        }
        const float v = (c & 2) ? ((c & 1) ? s[3] : s[2]) : ((c & 1) ? s[1] : s[0]);
        if (c < NB) qo[c * 512 + w * 64 + g * 16 + i] = (unsigned short)(cvtpk(v, v) & 0xffff);
      }
    }
    __syncthreads();
    if (t < 256) {
      const int bb = t >> 6, ch = t & 63;
      st16u_sc1(rs, ((HAB & 16) ? 0x70000000u : 0u) + (unsigned)(WS_QABS + ((size_t)(bb * NH + h) * DC + ch * 8) * 2), *(const u32x4*)(qo + bb * 512 + ch * 8));
    }
    TR(1);
    signal_flag(ws + WS_F1 + h * 8, tag);
    TR(2);
  }
  const unsigned ta = (unsigned)tag, tb = (unsigned)(tag >> 32);
  {
    u32x4 bv[8][2], bo[8][2];
    u32x4 pre[2][8], wo[16];
    {
      const __amdgpu_buffer_rsrc_t ruv = mkrs(wuv, (ABL & 4) ? 0 : DC * HD * 2);
      const __amdgpu_buffer_rsrc_t rwo = mkrs(woA, (ABL & 4) ? 0 : HD * DM * 2);
      const unsigned wvo = (unsigned)(((h * DH + (l >> 3) * 16) * DM + w * 64 + (l & 7) * 8) * 2);
#pragma unroll
      for (int i = 0; i < 16; i++) pre[i >> 3][i & 7] = __builtin_amdgcn_raw_buffer_load_b128(ruv, vo, i * HD * 2, NT);
#pragma unroll
      for (int i = 0; i < 16; i++) wo[i] = __builtin_amdgcn_raw_buffer_load_b128(rwo, wvo, i * DM * 2, NT);
    }
#pragma unroll
    for (int j = 0; j < 8; j++)
#pragma unroll
      for (int s = 0; s < 2; s++) {
#pragma unroll
        for (int e = 0; e < 4; e++)
          bv[j][s][e] = __builtin_amdgcn_perm(pre[s][2 * e + 1][j >> 1], pre[s][2 * e][j >> 1], (j & 1) ? 0x07060302u : 0x05040100u);
        asm volatile("" : "+v"(bv[j][s]));
      }
#pragma unroll
    for (int j = 0; j < 8; j++)
#pragma unroll
      for (int s = 0; s < 2; s++) {
#pragma unroll
        for (int e = 0; e < 4; e++)
          bo[j][s][e] = __builtin_amdgcn_perm(wo[8 * s + 2 * e + 1][j >> 1], wo[8 * s + 2 * e][j >> 1], (j & 1) ? 0x07060302u : 0x05040100u);
        asm volatile("" : "+v"(bo[j][s]));
      }
    constexpr int LAS = 1040;
    char* la = lds;
    unsigned short* cs = (unsigned short*)(lds + 16384);
    float* outs = (float*)(lds + 20480);
    float* cred = (float*)(lds + 32768);
    {
    {
    TR(3);
    wait_gate(ws + WS_FP, tag, trv);
    __syncthreads();
    TR(4);
    }
    if (t < 256) {
      const int bb = t >> 6, ch = t & 63;
      u32x4 ov[SPLIT_A];
      float mm[SPLIT_A], ll[SPLIT_A];
      float M = -INFINITY;
      u32x2 mv[SPLIT_A];
      {
#pragma unroll
        for (int s = 0; s < SPLIT_A; s++) { asm volatile("" : "=v"(ov[s])); asm volatile("" : "=v"(mv[s])); }
        unsigned got = 0;
#pragma unroll 1
        for (unsigned spins = 0; got != 0xffu && spins < SPIN_CAP; ++spins) {
          const u64 f = ld_u64(ws + WS_F2 + (w * SPLIT_A + (l & 7)) * 8);
          const unsigned now = (unsigned)__ballot(f == tag) & 0xffu;
          const unsigned fresh = now & ~got;
#pragma unroll
          for (int s = 0; s < SPLIT_A; s++)
            if ((fresh >> s) & 1u) {
              ov[s] = ld16u_sc1(rs, ((HAB & 2) ? 0x70000000u : 0u) + (unsigned)(WS_PAO + (((size_t)(bb * SPLIT_A + s) * NH + h) * DC + ch * 8) * 2));
              mv[s] = __builtin_amdgcn_raw_buffer_load_b64(rs, (unsigned)(WS_PAML + ((size_t)(bb * SPLIT_A + s) * NH + h) * 8), 0, SC1);
            }
          got = now;
          if (got != 0xffu) __builtin_amdgcn_s_sleep(6);
        }
        TR(5);
      }
#pragma unroll
      for (int s = 0; s < SPLIT_A; s++) {
        mm[s] = __uint_as_float(mv[s][0]); ll[s] = __uint_as_float(mv[s][1]);
        M = fmaxf(M, mm[s]);
      }
      float L = 0.f;
#pragma unroll
      for (int s = 0; s < SPLIT_A; s++) { mm[s] = fexp2((mm[s] - M) * LOG2E); L += mm[s] * ll[s]; }
      const float inv = 1.f / L;
      float av[8];
#pragma unroll
      for (int j = 0; j < 8; j++) av[j] = 0.f;
#pragma unroll
      for (int s = 0; s < SPLIT_A; s++)
#pragma unroll
        for (int j = 0; j < 4; j++) {
          av[2 * j] = fmaf(bf_lo(ov[s][j]), mm[s], av[2 * j]);
          av[2 * j + 1] = fmaf(bf_hi(ov[s][j]), mm[s], av[2 * j + 1]);
        }
      u32x4 hi, lo;
#pragma unroll
      for (int j = 0; j < 4; j++) {
        const float v0 = av[2 * j] * inv, v1 = av[2 * j + 1] * inv;
        hi[j] = cvtpk(v0, v1);
        lo[j] = cvtpk(v0 - bf_lo(hi[j]), v1 - bf_hi(hi[j]));
      }
      *(u32x4*)(la + bb * LAS + ch * 16) = hi;
      *(u32x4*)(la + (4 + bb) * LAS + ch * 16) = lo;
    }
    __syncthreads();
    {
      const char* ap = la + (c & 7) * LAS + (w * 64 + g * 16) * 2;
      const u32x4 a0 = *(const u32x4*)(ap), a1 = *(const u32x4*)(ap + 16);
      f32x4 dd[8];
#pragma unroll
      for (int j = 0; j < 8; j++)
        dd[j] = __builtin_amdgcn_mfma_f32_16x16x32_bf16(__builtin_bit_cast(bf16x8, a0), __builtin_bit_cast(bf16x8, bv[j][0]), f32x4{0.f, 0.f, 0.f, 0.f}, 0, 0, 0);
#pragma unroll
      for (int j = 0; j < 8; j++)
        dd[j] = __builtin_amdgcn_mfma_f32_16x16x32_bf16(__builtin_bit_cast(bf16x8, a1), __builtin_bit_cast(bf16x8, bv[j][1]), dd[j], 0, 0, 0);
#pragma unroll
      for (int r = 0; r < 4; r++) {
        f32x4 v;
#pragma unroll
        for (int j = 0; j < 4; j++) {
          auto sx = __builtin_amdgcn_permlane16_swap(__float_as_uint(dd[j][r]), __float_as_uint(dd[4 + j][r]), false, false);
          v[j] = __uint_as_float(sx[0]) + __uint_as_float(sx[1]);
        }
        if (g < 2) *(f32x4*)(cred + (w * 4 + r) * 128 + c * 8 + g * 4) = v;
      }
    }
    __syncthreads();
    {
      float s = 0.f;
#pragma unroll
      for (int i = 0; i < 8; i++) s += cred[i * 512 + t];
      const float sn = dpp<0xB1>(s);
      if ((t & 1) == 0) *(unsigned*)(cs + t) = cvtpk(s, sn);
    }
    __syncthreads();
    {
      const char* ap = (const char*)cs + ((c >> 1) & 3) * 256 + (g * 32 + (c & 1) * 16) * 2;
      const u32x4 a0 = *(const u32x4*)(ap), a1 = *(const u32x4*)(ap + 16);
      f32x4 dd[8];
#pragma unroll
      for (int j = 0; j < 8; j++)
        dd[j] = __builtin_amdgcn_mfma_f32_16x16x32_bf16(__builtin_bit_cast(bf16x8, a0), __builtin_bit_cast(bf16x8, bo[j][0]), f32x4{0.f, 0.f, 0.f, 0.f}, 0, 0, 0);
#pragma unroll
      for (int j = 0; j < 8; j++)
        dd[j] = __builtin_amdgcn_mfma_f32_16x16x32_bf16(__builtin_bit_cast(bf16x8, a1), __builtin_bit_cast(bf16x8, bo[j][1]), dd[j], 0, 0, 0);
      f32x4 v4[2];
#pragma unroll
      for (int j = 0; j < 8; j++) {
        const float u0 = (l & 8) ? dd[j][1] : dd[j][0], u1 = (l & 8) ? dd[j][3] : dd[j][2];
        const float keep = (l & 8) ? u1 : u0, give = (l & 8) ? u0 : u1;
        v4[j >> 2][j & 3] = keep + dpp<0x128>(give);
      }
      if (g < 2) {
        float* op = outs + (g * 2 + ((l >> 3) & 1)) * 512 + w * 64 + (l & 7) * 8;
        *(f32x4*)op = v4[0];
        *(f32x4*)(op + 4) = v4[1];
      }
    }
    __syncthreads();
    {
      const f32x2 va = *(const f32x2*)(outs + t * 2), vb = *(const f32x2*)(outs + 1024 + t * 2);
      const unsigned oo = ((HAB & 4) ? 0x70000000u : 0u) + (unsigned)(WS_PA + ((size_t)((t >> 8) * NH + h) * DM + (t & 255) * 2) * 8);
      st16u_sc1(rs, oo, u32x4{__float_as_uint(va[0]), ta, __float_as_uint(va[1]), tb});
      st16u_sc1(rs, oo + 2 * NH * DM * 8, u32x4{__float_as_uint(vb[0]), ta, __float_as_uint(vb[1]), tb});
    }
  TR(6);
  if (w < 4) {
    const int bb = h >> 3, n0 = (h & 7) * 64 + w * 16;
    const unsigned og = ((HAB & 4) ? 0x70000000u : 0u) + (unsigned)(WS_PA + ((size_t)(bb * NH + (l >> 3)) * DM + n0) * 8 + (l & 7) * 16);
    u32x4 p[4];
    __builtin_amdgcn_s_sleep(10);
#pragma unroll 1
    for (unsigned spins = 0; spins < SPIN_CAP; ++spins) {
      asm volatile("" ::: "memory");
#pragma unroll
      for (int i = 0; i < 4; i++) p[i] = ld16u_sc1(rs, og + i * 8 * DM * 8);
      bool ok = true;
#pragma unroll
      for (int i = 0; i < 4; i++) ok = ok && p[i][1] == ta && p[i][3] == tb;
      if (__all(ok) || (HAB & 4)) break;
      __builtin_amdgcn_s_sleep(4);
    }
    float v0 = (__uint_as_float(p[0][0]) + __uint_as_float(p[1][0])) + (__uint_as_float(p[2][0]) + __uint_as_float(p[3][0]));
    float v1 = (__uint_as_float(p[0][2]) + __uint_as_float(p[1][2])) + (__uint_as_float(p[2][2]) + __uint_as_float(p[3][2]));
    v0 += dpp<0x128>(v0); v0 = xrow_sum(v0);
    v1 += dpp<0x128>(v1); v1 = xrow_sum(v1);
    if (l < 8) {
      *(unsigned*)(out + bb * 1024 + n0 + l * 2) = cvtpk(v0, v1);
      if (h == 0 && t == 0) st_u64(ws + WS_EPA, tag);
    }
  }
    }
  }
  TR(43);
  TRDUMP();
}

__device__ __forceinline__ void role_A2(int a, const __hip_bfloat16* __restrict__ ckv, char* __restrict__ ws,
                                        __amdgpu_buffer_rsrc_t rs, char* lds, unsigned trv) {
  const int t = threadIdx.x, l = t & 63;
  const int w = __builtin_amdgcn_readfirstlane(t >> 6);
  const int g = l >> 4, c = l & 15;
  u64 ep = 0;
  if (t == 0) ep = ld_u64(ws + WS_EPA);
  const int grp = w >> 2, wq = w & 3;
  const int b = a / SPLIT_A, sp = a % SPLIT_A;
  const int r = l & 31, hh = l >> 5;
  constexpr unsigned CB = (unsigned)((size_t)NB * SK * DC * 2);
  constexpr int NTL = KA / 64;
  const unsigned cvo = (unsigned)((g * DC + 128 * wq + c * 8) * 2);
  const unsigned cso = (unsigned)(((size_t)b * SK + sp * KA + grp * 32) * DC * 2);
  auto load_tile = [&](u32x4 (&pr)[8], int tile) {
    const bool ok = tile < NTL;
    const __amdgpu_buffer_rsrc_t rc = mkrs(ckv, (ok && !(ABL & 2)) ? CB : 0u);
    const unsigned so = ok ? cso + (unsigned)((tile + sp) & (NTL - 1)) * 64 * DC * 2 : 0u;
#pragma unroll
    for (int u = 0; u < 8; u++) pr[u] = __builtin_amdgcn_raw_buffer_load_b128(rc, cvo, so + u * 4 * DC * 2, NT);
  };
  char* img = lds + w * 8192;
  float* sred = (float*)(lds + 65536 + grp * 16384);
  float* mxs = (float*)(lds + 98304 + grp * 512);
  unsigned* pbuf = (unsigned*)(lds + 102400 + grp * 2048);
  float* lall = (float*)(lds + 106496);
  const unsigned imga = (unsigned)(uintptr_t)(__attribute__((address_space(3))) char*)img;
  auto put_tile = [&](u32x4 (&pr)[8]) {
#pragma unroll
    for (int u = 0; u < 8; u++) *(u32x4*)(img + lds_off(4 * u + g, c)) = pr[u];
  };
  u32x4 pre[2][8];
  load_tile(pre[0], 0);
  load_tile(pre[1], 1);
  const u64 tag = next_tag(uni64(ep));
  put_tile(pre[0]);
  __builtin_amdgcn_sched_barrier(0);
  load_tile(pre[0], 2);
  TR(1);
  if (t < 64) {
#pragma unroll 1
    for (unsigned spins = 0; spins < SPIN_CAP; ++spins) {
      __builtin_amdgcn_s_sleep(32);
      if (__all(ld_u64(ws + WS_F1 + (l & 31) * 8) == tag)) break;
    }
  }
  __syncthreads();
  TR(3);
  bf16x8 qf[8];
  {
    char* qi = lds + 65536;
    const unsigned qo = ((HAB & 16) ? 0x70000000u : 0u) + (unsigned)(WS_QABS + (size_t)b * NH * DC * 2 + t * 16);
    u32x4 qv[4];
#pragma unroll
    for (int i = 0; i < 4; i++) qv[i] = ld16u_sc1(rs, qo + i * 8192);
#pragma unroll
    for (int i = 0; i < 4; i++) *(u32x4*)(qi + (i * 8 + w) * 1040 + l * 16) = qv[i];
    __syncthreads();
#pragma unroll
    for (int s = 0; s < 8; s++) qf[s] = __builtin_bit_cast(bf16x8, *(const u32x4*)(qi + r * 1040 + (128 * wq + 16 * s + 8 * hh) * 2));
#pragma unroll
    for (int s = 0; s < 8; s++) asm volatile("" : "+v"(qf[s]));
    __syncthreads();
  }
  f32x16 o[4];
#pragma unroll
  for (int i = 0; i < 4; i++)
#pragma unroll
    for (int j = 0; j < 16; j++) o[i][j] = 0.f;
  float m = -INFINITY, lsum = 0.f;
  const int q4 = (l & 15) >> 2, p4 = l & 3, colh = g & 1;
  auto tile_body = [&](int tile, u32x4 (&nx)[8]) {
    f32x16 s;
#pragma unroll
    for (int j = 0; j < 16; j++) s[j] = 0.f;
    {
      u32x4 af[8];
#pragma unroll
      for (int ks = 0; ks < 8; ks++) af[ks] = *(const u32x4*)(img + lds_off(r, 2 * ks + hh));
      __builtin_amdgcn_sched_barrier(0);
#pragma unroll
      for (int ks = 0; ks < 8; ks++) s = __builtin_amdgcn_mfma_f32_32x32x16_bf16(__builtin_bit_cast(bf16x8, af[ks]), qf[ks], s, 0, 0, 0);
    }
    f32x4* sr = (f32x4*)sred;
#pragma unroll
    for (int k4 = 0; k4 < 4; k4++) sr[(k4 * 4 + wq) * 64 + l] = f32x4{s[4 * k4], s[4 * k4 + 1], s[4 * k4 + 2], s[4 * k4 + 3]};
    __syncthreads();
    float x[4];
    {
      const f32x4 v = (sr[(wq * 4) * 64 + l] + sr[(wq * 4 + 1) * 64 + l]) + (sr[(wq * 4 + 2) * 64 + l] + sr[(wq * 4 + 3) * 64 + l]);
#pragma unroll
      for (int e = 0; e < 4; e += 2) {
        const unsigned r1 = cvtpk(v[e], v[e + 1]);
        const unsigned r2 = cvtpk(bf_lo(r1) * SCALE, bf_hi(r1) * SCALE);
        x[e] = bf_lo(r2); x[e + 1] = bf_hi(r2);
      }
      float mq = fmaxf(fmaxf(x[0], x[1]), fmaxf(x[2], x[3]));
      auto sx = __builtin_amdgcn_permlane32_swap(__float_as_uint(mq), __float_as_uint(mq), false, false);
      mq = fmaxf(__uint_as_float(sx[0]), __uint_as_float(sx[1]));
      if (hh == 0) mxs[r * 4 + wq] = mq;
    }
    __syncthreads();
    const f32x4 m4 = *(const f32x4*)(mxs + r * 4);
    const float mn = fmaxf(fmaxf(m, fmaxf(m4[0], m4[1])), fmaxf(m4[2], m4[3]));
    const float alpha = fexp2((m - mn) * LOG2E);
    {
      const unsigned p0 = cvtpk(fexp2((x[0] - mn) * LOG2E), fexp2((x[1] - mn) * LOG2E));
      const unsigned p1 = cvtpk(fexp2((x[2] - mn) * LOG2E), fexp2((x[3] - mn) * LOG2E));
      *(u32x2*)(pbuf + l * 8 + wq * 2) = u32x2{p0, p1};
      lsum = lsum * alpha + ((bf_lo(p0) + bf_hi(p0)) + (bf_lo(p1) + bf_hi(p1)));
    }
    m = mn;
    __syncthreads();
    unsigned pk[8];
    {
      const u32x4 a0 = *(const u32x4*)(pbuf + l * 8), a1 = *(const u32x4*)(pbuf + l * 8 + 4);
      pk[0] = a0[0]; pk[1] = a0[1]; pk[2] = a0[2]; pk[3] = a0[3]; pk[4] = a1[0]; pk[5] = a1[1]; pk[6] = a1[2]; pk[7] = a1[3];
    }
    if (__any(alpha != 1.f)) {
#pragma unroll
      for (int i = 0; i < 4; i++)
#pragma unroll
        for (int j = 0; j < 16; j++) o[i][j] *= alpha;
    }
#pragma unroll
    for (int s2 = 0; s2 < 2; s2++) {
      u32x2 tv[4][2];
#pragma unroll
      for (int ct = 0; ct < 4; ct++) {
        const int ch = 4 * ct + 2 * colh + (p4 >> 1);
        const unsigned a0 = lds_off(4 * hh + q4, ch) + 8 * (p4 & 1);
        const unsigned a1 = lds_off(8 + 4 * hh + q4, ch) + 8 * (p4 & 1);
        if (s2 == 0) {
          asm volatile("ds_read_b64_tr_b16 %0, %1" : "=v"(tv[ct][0]) : "v"(imga + a0) : "memory");
          asm volatile("ds_read_b64_tr_b16 %0, %1" : "=v"(tv[ct][1]) : "v"(imga + a1) : "memory");
        } else {
          asm volatile("ds_read_b64_tr_b16 %0, %1 offset:4096" : "=v"(tv[ct][0]) : "v"(imga + a0) : "memory");
          asm volatile("ds_read_b64_tr_b16 %0, %1 offset:4096" : "=v"(tv[ct][1]) : "v"(imga + a1) : "memory");
        }
      }
      asm volatile("s_waitcnt lgkmcnt(0)" ::: "memory");
      __builtin_amdgcn_sched_barrier(0);
      u32x4 pb = {pk[4 * s2], pk[4 * s2 + 1], pk[4 * s2 + 2], pk[4 * s2 + 3]};
#pragma unroll
      for (int ct = 0; ct < 4; ct++) {
        u32x4 av = {tv[ct][0][0], tv[ct][0][1], tv[ct][1][0], tv[ct][1][1]};
        o[ct] = __builtin_amdgcn_mfma_f32_32x32x16_bf16(__builtin_bit_cast(bf16x8, av), __builtin_bit_cast(bf16x8, pb), o[ct], 0, 0, 0);
      }
    }
    put_tile(nx);
    __builtin_amdgcn_sched_barrier(0);
    load_tile(nx, tile + 3);
    TR(4 + tile);
  };
#pragma unroll 1
  for (int tile = 0; tile < NTL; tile += 2) {
    tile_body(tile, pre[1]);
    tile_body(tile + 1, pre[0]);
  }
  constexpr int OST = 516;
  float* ob = (float*)lds;
  float* mlb = (float*)(lds + 32 * OST * 4);
  lall[w * 64 + l] = lsum;
  __syncthreads();
  {
    const float* lp = lall + grp * 256 + r;
    lsum = ((lp[0] + lp[32]) + (lp[64] + lp[96])) + ((lp[128] + lp[160]) + (lp[192] + lp[224]));
  }
  if (grp == 1) {
#pragma unroll
    for (int ct = 0; ct < 4; ct++)
#pragma unroll
      for (int j4 = 0; j4 < 4; j4++)
        *(f32x4*)(ob + r * OST + 128 * wq + 32 * ct + 8 * j4 + 4 * hh) =
            f32x4{o[ct][4 * j4], o[ct][4 * j4 + 1], o[ct][4 * j4 + 2], o[ct][4 * j4 + 3]};
    if (wq == 0 && l < 32) { mlb[2 * r] = m; mlb[2 * r + 1] = lsum; }
  }
  __syncthreads();
  f32x2 mlv = {0.f, 0.f};
  if (grp == 0) {
    const float m1 = mlb[2 * r], l1 = mlb[2 * r + 1];
    const float M = fmaxf(m, m1);
    const float f0 = fexp2((m - M) * LOG2E), f1 = fexp2((m1 - M) * LOG2E);
#pragma unroll
    for (int ct = 0; ct < 4; ct++)
#pragma unroll
      for (int j4 = 0; j4 < 4; j4++) {
        float* p = ob + r * OST + 128 * wq + 32 * ct + 8 * j4 + 4 * hh;
        const f32x4 o1 = *(const f32x4*)p;
        *(f32x4*)p = f32x4{o[ct][4 * j4] * f0 + o1[0] * f1, o[ct][4 * j4 + 1] * f0 + o1[1] * f1,
                           o[ct][4 * j4 + 2] * f0 + o1[2] * f1, o[ct][4 * j4 + 3] * f0 + o1[3] * f1};
      }
    if (wq == 0 && l < 32) {
      f32x2 v = {M, lsum * f0 + l1 * f1};
      st8_sc1(ws + WS_PAML + ((size_t)(b * SPLIT_A + sp) * NH + r) * 8, v);
    }
  }
  __syncthreads();
  {
    const unsigned pao = (unsigned)(WS_PAO + (size_t)(b * SPLIT_A + sp) * NH * DC * 2);
    u32x4 pv[4];
#pragma unroll
    for (int i = 0; i < 4; i++) {
      const int idx = i * 512 + t;
      const int hd = idx >> 6, c8 = idx & 63;
      const f32x4 v0 = *(const f32x4*)(ob + hd * OST + c8 * 8), v1 = *(const f32x4*)(ob + hd * OST + c8 * 8 + 4);
      pv[i] = u32x4{cvtpk(v0[0], v0[1]), cvtpk(v0[2], v0[3]), cvtpk(v1[0], v1[1]), cvtpk(v1[2], v1[3])};
      asm volatile("" : "+v"(pv[i]));
    }
    TR(19);
    wait_gate(ws + WS_FP, tag, trv);
    __syncthreads();
    TR(18);
#pragma unroll
    for (int i = 0; i < 4; i++) st16u_sc1(rs, (((HAB & 1) || ((HAB & 8) && i >= 2)) ? 0x70000000u : 0u) + pao + (unsigned)(i * 512 + t) * 16, pv[i]);
  }
  TR(20);
  signal_flag(ws + WS_F2 + a * 8, tag);
  TR(43);
  TRDUMP();
}

__global__ __launch_bounds__(512, 2) void k_fused(const __hip_bfloat16* __restrict__ xA, const __hip_bfloat16* __restrict__ ckv,
                                                  const __hip_bfloat16* __restrict__ wqA, const __hip_bfloat16* __restrict__ wuk,
                                                  const __hip_bfloat16* __restrict__ wuv, const __hip_bfloat16* __restrict__ woA,
                                                  const __hip_bfloat16* __restrict__ xB, const __hip_bfloat16* __restrict__ kc,
                                                  const __hip_bfloat16* __restrict__ vc, const __hip_bfloat16* __restrict__ wqB,
                                                  const __hip_bfloat16* __restrict__ woB, char* __restrict__ ws,
                                                  __hip_bfloat16* __restrict__ out) {
  __shared__ __attribute__((aligned(16))) char lds[131072];
  unsigned trv = 0;
  u64 tfirst; asm volatile("s_memrealtime %0" : "=s"(tfirst));
  TR(0);
  asm volatile("s_waitcnt lgkmcnt(0)" : "+s"(tfirst));
  trv = (threadIdx.x & 63) == 59 ? (unsigned)tfirst : trv;
  { unsigned xid; asm volatile("s_getreg_b32 %0, hwreg(HW_REG_XCC_ID)" : "=s"(xid)); trv = (threadIdx.x & 63) == 63 ? (xid & 15) + 1 : trv; trv = (threadIdx.x & 63) == 60 ? (unsigned)__builtin_amdgcn_s_memtime() : trv; }
  asm volatile("" :: "s"(xA), "s"(ckv), "s"(wqA), "s"(wuk), "s"(wuv), "s"(woA), "s"(xB), "s"(kc), "s"(vc), "s"(wqB), "s"(woB), "s"(ws), "s"(out));
  TR(58);
  const int blk = blockIdx.x, xcd = blk & 7, r = blk >> 3;
  unsigned pfv = 0;
  {
    u64 pc; unsigned len;
    asm volatile("s_getpc_b64 %0\n.Lcode_begin:\n\ts_mov_b32 %1, .Lcode_end-.Lcode_begin" : "=s"(pc), "=s"(len));
    const unsigned idx = (unsigned)(r * 8 + (threadIdx.x >> 6));
    const __amdgpu_buffer_rsrc_t rc = mkrs((const void*)pc, len);
    pfv = __builtin_amdgcn_raw_buffer_load_b32(rc, idx * 1024 + (threadIdx.x & 63) * 16, 0, 0);
  }
  const __amdgpu_buffer_rsrc_t rs = mkrs(ws, (unsigned)WS_END);
  if (r < 8) {
    if (r < 4) role_A1(((xcd + AROT1) & 7) + 8 * r, xA, wqA, wuk, wuv, woA, ws, rs, out, lds, trv);
    else role_A2(((xcd + AROT2) & 7) * 4 + r - 4, ckv, ws, rs, lds, trv);
  } else {
    const int rr = r - 8, hq = rr / 6;
    role_B(((PERM >> (4 * xcd)) & 7) + 8 * hq, rr - 6 * hq, xB, kc, vc, wqB, woB, ws, rs, out, lds, trv);
  }

  if (blockIdx.x == 0 && threadIdx.x < 64) {
    const unsigned t00 = __builtin_amdgcn_readfirstlane(trv);
    while ((unsigned)__builtin_amdgcn_s_memrealtime() - t00 < 7500u) __builtin_amdgcn_s_sleep(1);
    const int l = threadIdx.x;
    auto ld = [&](int blk, int wv, int idx) { return __hip_atomic_load((gu32*)(ws + WS_TR) + (blk * 8 + wv) * 64 + idx, __ATOMIC_RELAXED, __HIP_MEMORY_SCOPE_AGENT); };
    int mn = 1 << 30;
    for (int i = 0; i < 4; i++) { const int d = (int)(ld(l + 64 * i, 0, 0) - t00); mn = d < mn ? d : mn; }
    for (int o = 32; o > 0; o >>= 1) { const int v = __shfl_xor(mn, o); mn = v < mn ? v : mn; }
    const unsigned T0 = t00 + (unsigned)mn;
    int sum = 0, cnt = 0, mx = -(1 << 30), suma = 0, cnta = 0;
    for (int i = EXF_I0; i < 4; i++) {
      const int blk = l + 64 * i;
      const int xcc = (int)ld(blk, 0, 63) - 1;
      int v = -(1 << 30);
      for (int wv = 0; wv < EXF_NW; wv++) { const unsigned u = ld(blk, wv, EXF_IDX); if (u) { const int d = (int)(u - T0); v = d > v ? d : v; } }
      if ((EXF_XCC < 0 || xcc == EXF_XCC || (EXF_XCC >= 100 && (blk & 7) == EXF_XCC - 100)) && v > -(1 << 29) && i < EXF_I1) { sum += v; cnt++; mx = v > mx ? v : mx; }
      if (v > -(1 << 29) && i < EXF_I1) { suma += v; cnta++; }
    }
    for (int o = 32; o > 0; o >>= 1) { sum += __shfl_xor(sum, o); cnt += __shfl_xor(cnt, o); suma += __shfl_xor(suma, o); cnta += __shfl_xor(cnta, o); const int v = __shfl_xor(mx, o); mx = v > mx ? v : mx; }
    int f = EXF_MAX ? mx : sum / (cnt ? cnt : 1);
    if (EXF_XCC == -2) {
      int ma = -(1 << 30), mb = -(1 << 30);
      for (int i = 0; i < 4; i++) { const int blk = l + 64 * i; int v = -(1 << 30);
        for (int wv = 0; wv < 8; wv++) { const unsigned u = ld(blk, wv, EXF_IDX); if (u) { const int d = (int)(u - T0); v = d > v ? d : v; } }
        if (i == 0) ma = v > ma ? v : ma; else mb = v > mb ? v : mb; }
      for (int o = 32; o > 0; o >>= 1) { int v = __shfl_xor(ma, o); ma = v > ma ? v : ma; v = __shfl_xor(mb, o); mb = v > mb ? v : mb; }
      f = ma - mb;
    }
    if (EXF_OFF < 0 && EXF_XCC != -2) f -= suma / (cnta ? cnta : 1);
    f -= EXF_OFF;
    if (f < 0) f = 0;
    if (f > 2000) f = 2000;
    while ((unsigned)__builtin_amdgcn_s_memrealtime() - T0 < 10000u + (unsigned)(EXF_MUL * f)) __builtin_amdgcn_s_sleep(1);
  }
  asm volatile(".Lcode_end:" :: "v"(pfv));
}

extern "C" void kernel_launch(void* const* d_in, const int* in_sizes, int n_in,
                              void* d_out, int out_size, void* d_ws, size_t ws_size,
                              hipStream_t stream) {
  k_fused<<<256, 512, 0, stream>>>((const __hip_bfloat16*)d_in[0], (const __hip_bfloat16*)d_in[1], (const __hip_bfloat16*)d_in[2],
                                   (const __hip_bfloat16*)d_in[3], (const __hip_bfloat16*)d_in[4], (const __hip_bfloat16*)d_in[5],
                                   (const __hip_bfloat16*)d_in[6], (const __hip_bfloat16*)d_in[7], (const __hip_bfloat16*)d_in[8],
                                   (const __hip_bfloat16*)d_in[9], (const __hip_bfloat16*)d_in[10], (char*)d_ws,
                                   (__hip_bfloat16*)d_out);
}
```

```cpp
#define TRACE
#define DEFER 1
#define WOAUX 0
#define WQAUX 0
#define ROTQ 0x203120
#define DML
#define GID 10
#define EX_IA 42
#define EX_A0 64
#define EX_A1 256
#define EX_GA 1
#define EX_IB 40
#define EX_B0 64
#define EX_B1 256
#define EX_GB 1
#define EX_OFF 0
#define EX_MUL 10
#include <hip/hip_runtime.h>
#include <hip/hip_bf16.h>
#include <stdint.h>

typedef __attribute__((ext_vector_type(4))) unsigned u32x4;
typedef __attribute__((ext_vector_type(2))) unsigned u32x2;
typedef __attribute__((ext_vector_type(4))) float f32x4;
typedef __attribute__((ext_vector_type(2))) float f32x2;
typedef __attribute__((ext_vector_type(16))) float f32x16;
typedef __attribute__((ext_vector_type(8))) __bf16 bf16x8;
typedef __attribute__((ext_vector_type(2))) __bf16 bf16x2;
typedef __attribute__((address_space(1))) unsigned gu32;
typedef __attribute__((address_space(1))) unsigned long long gu64;
typedef unsigned long long u64;

#define ROTM 0
#define AROT1 0
#define AROT2 0
#define PERM 0x76043215u
#define PSL 4
#define HAB 0
#define NOC 0
#define WO0 3
#define WOS 4
#define TSY0 950
#define TSW 190
#define KVAUX 2
#define ABL 0
constexpr int NB = 4;
constexpr int NH = 32;
constexpr int SK = 4096;
constexpr int DH = 128;
constexpr int DM = 512;
constexpr int DC = 512;
constexpr int HD = NH * DH;

constexpr int SPLIT_A = 8;
constexpr int KA = SK / SPLIT_A;
constexpr int SS = 171;
constexpr int RD = 18;
constexpr int NP = SS - 8 * RD + 8;
constexpr int NP4 = (NP + 3) & ~3;
constexpr int RST = 144;
constexpr int GI = GID;
constexpr int RE = 15;
#define PRIOFD 4
constexpr int PRIOF = PRIOFD;

constexpr float SCALE = 0.08838834764831845f;
constexpr float LOG2E = 1.4426950408889634f;
#define NTA 2
constexpr int NT = NTA;
constexpr int SC1 = 16;

constexpr size_t WS_EPA  = 0;
constexpr size_t WS_EPB  = 256;
constexpr size_t WS_F1   = 4096;
constexpr size_t WS_F2   = WS_F1 + 1024;
constexpr size_t WS_FP   = 8192;
constexpr size_t WS_QABS = 16384;
constexpr size_t WS_PAML = WS_QABS + (size_t)NB * NH * DC * 2;
constexpr size_t WS_PAO  = WS_PAML + (size_t)NB * SPLIT_A * NH * 2 * 4;
constexpr size_t WS_PA   = WS_PAO + (size_t)NB * SPLIT_A * NH * DC * 2;
constexpr size_t WS_PBML = WS_PA + (size_t)NB * NH * DM * 8;
constexpr size_t WS_PBG  = WS_PBML + (size_t)NB * 64 * 16;
constexpr size_t WS_END  = WS_PBG + (size_t)NB * 64 * DM * 8;

__device__ __forceinline__ float bf_lo(unsigned u) { return __uint_as_float(u << 16); }
__device__ __forceinline__ float bf_hi(unsigned u) { return __uint_as_float(u & 0xffff0000u); }
__device__ __forceinline__ unsigned cvtpk(float lo, float hi) {
  f32x2 v = {lo, hi};
  return __builtin_bit_cast(unsigned, __builtin_convertvector(v, bf16x2));
}
__device__ __forceinline__ float rbf(float x) { return __uint_as_float(cvtpk(x, x) << 16); }
__device__ __forceinline__ float dot2(unsigned k, unsigned q, float acc) {
  return __builtin_amdgcn_fdot2_f32_bf16(__builtin_bit_cast(bf16x2, k), __builtin_bit_cast(bf16x2, q), acc, false);
}
template <int CTRL> __device__ __forceinline__ float dpp(float x) {
  return __builtin_bit_cast(float, __builtin_amdgcn_mov_dpp(__builtin_bit_cast(int, x), CTRL, 0xf, 0xf, true));
}
__device__ __forceinline__ float sum16(float x) {
  x += dpp<0x128>(x);
  x += dpp<0x124>(x);
  x += dpp<0x4E>(x);
  x += dpp<0xB1>(x);
  return x;
}
__device__ __forceinline__ float xrow_max(float x) {
  auto s = __builtin_amdgcn_permlane16_swap(__float_as_uint(x), __float_as_uint(x), false, false);
  x = fmaxf(__uint_as_float(s[0]), __uint_as_float(s[1]));
  auto t = __builtin_amdgcn_permlane32_swap(__float_as_uint(x), __float_as_uint(x), false, false);
  return fmaxf(__uint_as_float(t[0]), __uint_as_float(t[1]));
}
__device__ __forceinline__ float xrow_sum(float x) {
  auto s = __builtin_amdgcn_permlane16_swap(__float_as_uint(x), __float_as_uint(x), false, false);
  x = __uint_as_float(s[0]) + __uint_as_float(s[1]);
  auto t = __builtin_amdgcn_permlane32_swap(__float_as_uint(x), __float_as_uint(x), false, false);
  return __uint_as_float(t[0]) + __uint_as_float(t[1]);
}
__device__ __forceinline__ float fexp2(float x) { return __builtin_amdgcn_exp2f(x); }
__device__ __forceinline__ __amdgpu_buffer_rsrc_t mkrs(const void* p, unsigned bytes) {
  return __builtin_amdgcn_make_buffer_rsrc((void*)p, 0, (int)bytes, 0x00020000);
}

constexpr size_t WS_TR = (size_t)64 << 20;
#define TR(i) do { __builtin_amdgcn_sched_barrier(0); { const unsigned ts_ = (unsigned)__builtin_amdgcn_s_memrealtime(); trv = (int)(threadIdx.x & 63) == (i) ? ts_ : trv; } __builtin_amdgcn_sched_barrier(0); } while (0)
#define TRDUMP() trv = (threadIdx.x & 63) == 61 ? (unsigned)__builtin_amdgcn_s_memtime() : trv; __hip_atomic_store((gu32*)(ws + WS_TR) + (blockIdx.x * 8 + (threadIdx.x >> 6)) * 64 + (threadIdx.x & 63), trv, __ATOMIC_RELAXED, __HIP_MEMORY_SCOPE_AGENT)
constexpr int PTHR = 60;
constexpr unsigned SPIN_CAP = 1u << 21;
__device__ __forceinline__ u64 ld_u64(const void* p) {
  return __hip_atomic_load((gu64*)p, __ATOMIC_RELAXED, __HIP_MEMORY_SCOPE_AGENT);
}
__device__ __forceinline__ void st_u64(void* p, u64 v) {
  __hip_atomic_store((gu64*)p, v, __ATOMIC_RELAXED, __HIP_MEMORY_SCOPE_AGENT);
}
__device__ __forceinline__ u64 next_tag(u64 e) { return e + 0x0000000100000001ull; }
__device__ __forceinline__ u64 uni64(u64 v) {
  const unsigned lo = __builtin_amdgcn_readfirstlane((unsigned)v), hi = __builtin_amdgcn_readfirstlane((unsigned)(v >> 32));
  return ((u64)hi << 32) | lo;
}
__device__ __forceinline__ void signal_flag(char* flag, u64 tag) {
  asm volatile("s_waitcnt vmcnt(0)" ::: "memory");
  __syncthreads();
  if (threadIdx.x == 0) st_u64(flag, tag);
}
__device__ __forceinline__ void wait_gate(const char* flag, u64 tag, unsigned trv) {
  if (threadIdx.x < 64) {
    for (unsigned spins = 0; spins < SPIN_CAP; ++spins) {
      if (ld_u64(flag) == tag) break;
      __builtin_amdgcn_s_sleep(4);
    }
  }
}
__device__ __forceinline__ void st16_sc1(__amdgpu_buffer_rsrc_t rs, unsigned off, f32x4 v) {
  __builtin_amdgcn_raw_buffer_store_b128(__builtin_bit_cast(u32x4, v), rs, off, 0, SC1);
}
__device__ __forceinline__ void st16u_sc1(__amdgpu_buffer_rsrc_t rs, unsigned off, u32x4 v) {
  __builtin_amdgcn_raw_buffer_store_b128(v, rs, off, 0, SC1);
}
__device__ __forceinline__ f32x4 ld16_sc1(__amdgpu_buffer_rsrc_t rs, unsigned off) {
  return __builtin_bit_cast(f32x4, __builtin_amdgcn_raw_buffer_load_b128(rs, off, 0, SC1));
}
__device__ __forceinline__ u32x4 ld16u_sc1(__amdgpu_buffer_rsrc_t rs, unsigned off) {
  return __builtin_amdgcn_raw_buffer_load_b128(rs, off, 0, SC1);
}
__device__ __forceinline__ void st8_sc1(void* p, f32x2 v) { st_u64(p, __builtin_bit_cast(u64, v)); }
__device__ __forceinline__ f32x2 ld8_sc1(const void* p) { return __builtin_bit_cast(f32x2, ld_u64(p)); }

__device__ __forceinline__ void item_dots(const u32x4 (&ks)[4], const u32x4 q, float (&s_)[4]) {
#pragma unroll
  for (int u = 0; u < 4; u++) {
    float a = dot2(ks[u][0], q[0], 0.f);
    a = dot2(ks[u][1], q[1], a);
    a = dot2(ks[u][2], q[2], a);
    a = dot2(ks[u][3], q[3], a);
    s_[u] = a;
    asm volatile("" : "+v"(s_[u]));
  }
}
__device__ __forceinline__ void item_unpack(const u32x4 (&vs)[4], f32x2 (&vf)[4][4]) {
#pragma unroll
  for (int u = 0; u < 4; u++)
#pragma unroll
    for (int j = 0; j < 4; j++) { vf[u][j] = f32x2{bf_lo(vs[u][j]), bf_hi(vs[u][j])}; asm volatile("" : "+v"(vf[u][j])); }
}
__device__ __forceinline__ f32x2 rows_sum(const float (&o)[8]) {
  float r4[4], r2[2];
#pragma unroll
  for (int j = 0; j < 4; j++) {
    auto sx = __builtin_amdgcn_permlane32_swap(__float_as_uint(o[j]), __float_as_uint(o[j + 4]), false, false);
    r4[j] = __uint_as_float(sx[0]) + __uint_as_float(sx[1]);
  }
#pragma unroll
  for (int j = 0; j < 2; j++) {
    auto sx = __builtin_amdgcn_permlane16_swap(__float_as_uint(r4[j]), __float_as_uint(r4[j + 2]), false, false);
    r2[j] = __uint_as_float(sx[0]) + __uint_as_float(sx[1]);
  }
  return f32x2{r2[0], r2[1]};
}
__device__ __forceinline__ f32x2 item_partial(const float (&s_)[4], const f32x2 (&vf)[4][4], float& mx, float& ll) {
  float s[4];
#pragma unroll
  for (int u = 0; u < 4; u++) s[u] = rbf(rbf(sum16(s_[u])) * SCALE);
  mx = xrow_max(fmaxf(fmaxf(s[0], s[1]), fmaxf(s[2], s[3])));
  float p[4];
#pragma unroll
  for (int u = 0; u < 4; u++) p[u] = fexp2((s[u] - mx) * LOG2E);
  ll = xrow_sum((p[0] + p[1]) + (p[2] + p[3]));
  float o[8];
#pragma unroll
  for (int j = 0; j < 4; j++) {
    f32x2 v = vf[0][j] * p[0];
#pragma unroll
    for (int u = 1; u < 4; u++) v = __builtin_elementwise_fma(vf[u][j], f32x2{p[u], p[u]}, v);
    o[2 * j] = v[0]; o[2 * j + 1] = v[1];
  }
  return rows_sum(o);
}
__device__ __forceinline__ void role_B(int h, int j6, const __hip_bfloat16* __restrict__ xB,
                                       const __hip_bfloat16* __restrict__ kc, const __hip_bfloat16* __restrict__ vc,
                                       const __hip_bfloat16* __restrict__ wqB, const __hip_bfloat16* __restrict__ woB,
                                       char* __restrict__ ws, __amdgpu_buffer_rsrc_t rs,
                                       __hip_bfloat16* __restrict__ out, char* lds, unsigned trv) {
  const int t = threadIdx.x, l = t & 63;
  const int w = __builtin_amdgcn_readfirstlane(t >> 6);
  const int g = l >> 4, c = l & 15;
  const int pr = j6 >= 3 ? 1 : 0, k = j6 - 3 * pr;
  const int b0 = 2 * pr;
  const int NI = k == 1 ? 2 * (256 - SS) : SS;
  const int e0 = k == 1 ? (h & 1) : (k >> 1);
  u32x4 xr[2][2], wr[16];
  {
    const __amdgpu_buffer_rsrc_t rx = mkrs(xB, NB * DM * 2);
    const int rq_ = (ROTQ >> (4 * j6)) & 3;
    const int cw = w ^ ((rq_ & 2) << 1), r0 = rq_ & 1;
    const unsigned xo = (unsigned)((b0 * DM + cw * 64 + g * 16) * 2);
#pragma unroll
    for (int bb = 0; bb < 2; bb++)
#pragma unroll
      for (int i = 0; i < 2; i++) xr[bb][i] = __builtin_amdgcn_raw_buffer_load_b128(rx, xo + bb * DM * 2, (i ^ r0) * 16, 0);
    const __amdgpu_buffer_rsrc_t rq = mkrs(wqB, DM * HD * 2);
    const unsigned vo = (unsigned)(((cw * 64 + g * 16) * HD + h * DH + c * 8) * 2);
    const unsigned vo0 = vo + r0 * 8 * HD * 2, vo1 = vo + (1 - r0) * 8 * HD * 2;
#pragma unroll
    for (int i = 0; i < 16; i++) wr[i] = __builtin_amdgcn_raw_buffer_load_b128(rq, i < 8 ? vo0 : vo1, (i & 7) * HD * 2, WQAUX);
  }
  constexpr unsigned KVB = (unsigned)((size_t)NB * NH * SK * DH * 2);
  const __amdgpu_buffer_rsrc_t rk = mkrs(kc, KVB), rv = mkrs(vc, KVB);
  const unsigned lo = (unsigned)(g * 256 + c * 16);
  const unsigned hb0 = ((unsigned)((b0 + e0) * NH + h) << 20) + (k == 1 ? (unsigned)SS << 12 : 0u);
  const unsigned hb1 = ((unsigned)((b0 + 1 - e0) * NH + h) << 20) + ((unsigned)(2 * SS - 256) << 12);
  auto soff = [&](int x) { return ((k == 1 && x >= 256 - SS) ? hb1 : hb0) + ((unsigned)x << 12); };
  auto ld4 = [&](u32x4 (&d)[4], __amdgpu_buffer_rsrc_t r, unsigned so) {
#pragma unroll
    for (int u = 0; u < 4; u++) d[u] = __builtin_amdgcn_raw_buffer_load_b128(r, lo + u * 1024, so, KVAUX);
  };
  u32x4 kr[4], vr[4], ke[4], ve[4];
  ld4(kr, rk, soff(w));
  ld4(vr, rv, soff(w));
  u64* eps = (u64*)(lds + 33808);
  u64 epv = 0;
  if (t < 3) epv = ld_u64(ws + (t == 0 ? WS_EPA : WS_EPB + 256 * (b0 + (t == 1 ? e0 : 1 - e0))));
  TR(56);
  float* red = (float*)lds;
  unsigned* qs = (unsigned*)(lds + 32768);
  unsigned* cnt0 = (unsigned*)(lds + 33792);
  unsigned* cntd = (unsigned*)(lds + 33796);
  float* rec0 = (float*)(lds + 36864);
  float* mlr0 = (float*)(lds + 40960);
  char* prv = lds + 45056 + w * 512;
  float* recd = (float*)(lds + 65536);
  float* mld = (float*)(lds + 98304);
  const unsigned cnta = (unsigned)(uintptr_t)(__attribute__((address_space(3))) char*)cntd;
  float* mxd = (float*)(lds + 33800);
  if (t == 0) { *cnt0 = 0u; *cntd = 8u * (RD + 1); mxd[0] = -INFINITY; mxd[1] = -INFINITY; }
  if (t < 64 * (NP4 - NP + 1)) *(f32x2*)(recd + (NP - 1 + (t >> 6)) * RST + 2 * l) = f32x2{0.f, 0.f};
  {
    float a0[8], a1[8];
#pragma unroll
    for (int j = 0; j < 8; j++) { a0[j] = 0.f; a1[j] = 0.f; }
#pragma unroll
    for (int i = 0; i < 16; i += 2) {
      const unsigned x0 = xr[0][i >> 3][(i >> 1) & 3], x1 = xr[1][i >> 3][(i >> 1) & 3];
#pragma unroll
      for (int j = 0; j < 4; j++) {
        const unsigned pl = __builtin_amdgcn_perm(wr[i + 1][j], wr[i][j], 0x05040100u);
        const unsigned ph = __builtin_amdgcn_perm(wr[i + 1][j], wr[i][j], 0x07060302u);
        a0[2 * j] = dot2(pl, x0, a0[2 * j]); a0[2 * j + 1] = dot2(ph, x0, a0[2 * j + 1]);
        a1[2 * j] = dot2(pl, x1, a1[2 * j]); a1[2 * j + 1] = dot2(ph, x1, a1[2 * j + 1]);
      }
    }
    TR(57);
#pragma unroll
    for (int j = 0; j < 8; j++) asm volatile("" : "+v"(a0[j]), "+v"(a1[j]));
    __builtin_amdgcn_sched_barrier(0);
    ld4(ke, rk, soff(w + 8));
    ld4(ve, rv, soff(w + 8));
    __builtin_amdgcn_sched_barrier(0);
    float* rp = red + (w * 4 + g) * 256 + c * 8;
    *(f32x4*)(rp) = f32x4{a0[0], a0[1], a0[2], a0[3]};
    *(f32x4*)(rp + 4) = f32x4{a0[4], a0[5], a0[6], a0[7]};
    *(f32x4*)(rp + 128) = f32x4{a1[0], a1[1], a1[2], a1[3]};
    *(f32x4*)(rp + 132) = f32x4{a1[4], a1[5], a1[6], a1[7]};
  }
  __syncthreads();
  {
    const int o = t >> 1, hf = t & 1;
    float s = 0.f;
#pragma unroll
    for (int i = 0; i < 16; i++) s += red[(hf * 16 + i) * 256 + o];
    s += dpp<0xB1>(s);
    const float sn = dpp<0x4E>(s);
    if ((t & 3) == 0) qs[o >> 1] = cvtpk(s, sn);
    if (t < 3) eps[t] = epv;
  }
  __syncthreads();
  const u64 epa = uni64(eps[0]), tg0 = next_tag(uni64(eps[1])), tg1 = next_tag(uni64(eps[2]));
  u32x4 q = *(const u32x4*)(qs + e0 * 64 + c * 4);
  const u32x4 q1 = *(const u32x4*)(qs + (1 - e0) * 64 + c * 4);
  TR(1);
  float m = -INFINITY, lsum = 0.f, acc[8];
#pragma unroll
  for (int j = 0; j < 8; j++) acc[j] = 0.f;
  float s_[4]; f32x2 vf[4][4];
  auto part0 = [&](u32x4 (&ks)[4]) { item_dots(ks, q, s_); };
  auto part1 = [&](u32x4 (&vs)[4]) { item_unpack(vs, vf); };
  auto part2 = [&]() {
    float s[4];
#pragma unroll
    for (int u = 0; u < 4; u++) s[u] = rbf(rbf(sum16(s_[u])) * SCALE);
    const float mx = fmaxf(fmaxf(m, fmaxf(s[0], s[1])), fmaxf(s[2], s[3]));
    const float alpha = fexp2((m - mx) * LOG2E);
    m = mx;
    float p[4];
#pragma unroll
    for (int u = 0; u < 4; u++) p[u] = fexp2((s[u] - mx) * LOG2E);
    lsum = lsum * alpha + ((p[0] + p[1]) + (p[2] + p[3]));
#pragma unroll
    for (int j = 0; j < 8; j++) acc[j] *= alpha;
#pragma unroll
    for (int u = 0; u < 4; u++)
#pragma unroll
      for (int j = 0; j < 4; j++) {
        acc[2 * j]     = fmaf(p[u], vf[u][j][0], acc[2 * j]);
        acc[2 * j + 1] = fmaf(p[u], vf[u][j][1], acc[2 * j + 1]);
      }
  };
  auto front = [&](__amdgpu_buffer_rsrc_t rk2, __amdgpu_buffer_rsrc_t rv2, unsigned so) {
    __builtin_amdgcn_sched_barrier(0);
    ld4(kr, rk2, so);
    __builtin_amdgcn_sched_barrier(0);
    part1(vr);
    __builtin_amdgcn_sched_barrier(0);
    ld4(vr, rv2, so);
    __builtin_amdgcn_sched_barrier(0);
  };
  auto put_partial = [&](f32x2 r2, float mw, float ll, float* rec_, float* ml_, float* mx_) {
    *(f32x2*)(rec_ + c * 8 + g * 2) = r2;
    if (l == 0) {
      *(f32x2*)ml_ = f32x2{mw, ll};
      asm volatile("ds_max_f32 %0, %1" :: "v"((unsigned)(uintptr_t)(__attribute__((address_space(3))) float*)mx_), "v"(mw) : "memory");
    }
  };
  auto wave_partial = [&](float* rec_, float* ml_, float* mx_) {
    const float mw = xrow_max(m);
    const float fq = fexp2((m - mw) * LOG2E);
    const float ll = xrow_sum(lsum * fq);
    float o[8];
#pragma unroll
    for (int j = 0; j < 8; j++) o[j] = acc[j] * fq;
    put_partial(rows_sum(o), mw, ll, rec_, ml_, mx_);
  };
  auto part2c = [&](float* rec_, float* ml_) {
    float mx, ll;
    const f32x2 r2 = item_partial(s_, vf, mx, ll);
    put_partial(r2, mx, ll, rec_, ml_, mxd);
  };
  bool sw = k != 1;
  auto do_switch = [&]() {
    wave_partial(rec0 + w * 128, mlr0 + w * 2, mxd + 1);
    asm volatile("s_waitcnt lgkmcnt(0)" ::: "memory");
    if (l == 0) asm volatile("ds_add_u32 %0, %1" :: "v"(cnta - 4u), "v"(1u) : "memory");
    m = -INFINITY; lsum = 0.f;
#pragma unroll
    for (int j = 0; j < 8; j++) acc[j] = 0.f;
    q = q1; sw = true;
  };
  if (w >= 4 && PRIOF > 0) __builtin_amdgcn_s_setprio(1);
  part0(kr);
  front(rk, rv, soff(w + 16));
  part2();
  part0(ke); part1(ve); part2();
  int x = w + 16;
  const int rg = l >> 3, cc = l & 7;
  const int wc = w, rw0 = 0;
  const int rw = WO0 + WOS * (h >> 3);
  const int re = k != 1 ? -1 : rw + 2 > RE ? rw + 2 : RE;
  u32x4 wo[16];
  __builtin_amdgcn_sched_barrier(0);
#pragma unroll
  for (int i = 0; i < 16; i++) asm volatile("" : "=v"(wo[i]));
  const int nseg = k == 1 ? 2 : 1, sp = k == 1 ? 1 : 0;
  auto store_off = [&](int sg) {
    const int b = b0 + (sg ? 1 - e0 : e0);
    const unsigned og_ = (unsigned)(WS_PBG + ((size_t)(b * 64 + h * 2 + sp) * DM + wc * 64 + cc * 8 + g * 2) * 8);
    const unsigned om_ = (unsigned)(WS_PBML + (size_t)(b * 64 + h * 2 + sp) * 16);
    return (l & 8) == 0 ? og_ : t == 8 ? om_ : 0x7ffffff0u;
  };
  const unsigned apo = (unsigned)((c >> 3) * 256 + (g * 32 + ((c >> 2) & 1) * 16) * 2);
  auto project = [&](const char* ap, float M, float L, bool lred) {
    const u32x4 a0 = *(const u32x4*)(ap + rw0 * 16), a1 = *(const u32x4*)(ap + 16 - rw0 * 16);
    f32x4 de = {0.f, 0.f, 0.f, 0.f}, dq = {0.f, 0.f, 0.f, 0.f};
#pragma unroll
    for (int p = 0; p < 8; p++) {
      const unsigned d = p < 4 ? a0[p & 3] : a1[p & 3];
      const u32x4 af = {(c & 3) == 0 ? d : 0u, (c & 3) == 1 ? d : 0u, (c & 3) == 2 ? d : 0u, (c & 3) == 3 ? d : 0u};
      de = __builtin_amdgcn_mfma_f32_16x16x32_bf16(__builtin_bit_cast(bf16x8, af), __builtin_bit_cast(bf16x8, wo[2 * p]), de, 0, 0, 0);
      dq = __builtin_amdgcn_mfma_f32_16x16x32_bf16(__builtin_bit_cast(bf16x8, af), __builtin_bit_cast(bf16x8, wo[2 * p + 1]), dq, 0, 0, 0);
    }
    if (lred && w == 0) L = xrow_sum(sum16(L));
    const bool val = (g & 1) == ((l >> 3) & 1);
    float r4[4], r2[2];
#pragma unroll
    for (int j = 0; j < 4; j++) {
      const float u0 = (j & 1) ? dq[j >> 1] : de[j >> 1], u1 = (j & 1) ? dq[2 + (j >> 1)] : de[2 + (j >> 1)];
      auto sx = __builtin_amdgcn_permlane32_swap(__float_as_uint(u0), __float_as_uint(u1), false, false);
      const float v = __uint_as_float(sx[0]) + __uint_as_float(sx[1]);
      r4[j] = val ? v : 0.f;
    }
#pragma unroll
    for (int j = 0; j < 2; j++) {
      auto sx = __builtin_amdgcn_permlane16_swap(__float_as_uint(r4[j]), __float_as_uint(r4[j + 2]), false, false);
      const float v = __uint_as_float(sx[0]) + __uint_as_float(sx[1]);
      r2[j] = v + dpp<0x128>(v);
    }
    const bool lo8 = (l & 8) == 0;
    return f32x2{lo8 ? r2[0] : M, lo8 ? r2[1] : L};
  };
  auto publish = [&](f32x2 v, u64 tg, unsigned sto) {
    st16u_sc1(rs, sto, u32x4{__float_as_uint(v[0]), (unsigned)tg, __float_as_uint(v[1]), (unsigned)(tg >> 32)});
  };
  f32x2 seg0 = {0.f, 0.f};
  auto events = [&](int rd) {
    if (rd == PRIOF) __builtin_amdgcn_s_setprio(0);
    if (!sw && x >= 256 - SS) do_switch();
    if (rd == rw + 2) {
#pragma unroll
      for (int p = 0; p < 8; p++) {
#pragma unroll
        for (int i = 0; i < 4; i++) {
          const unsigned u0 = wo[2 * p][i], u1 = wo[2 * p + 1][i];
          wo[2 * p][i] = __builtin_amdgcn_perm(u1, u0, 0x05040100u);
          wo[2 * p + 1][i] = __builtin_amdgcn_perm(u1, u0, 0x07060302u);
        }
        asm volatile("" : "+v"(wo[2 * p]), "+v"(wo[2 * p + 1]));
      }
    }
    if (rd == re) {
    for (unsigned spins = 0; __hip_atomic_load(cnt0, __ATOMIC_RELAXED, __HIP_MEMORY_SCOPE_WORKGROUP) != 8u && spins < SPIN_CAP; ++spins)
      __builtin_amdgcn_s_sleep(1);
    asm volatile("" ::: "memory");
    f32x4 q4[4];
#pragma unroll
    for (int i = 0; i < 4; i++) q4[i] = *(const f32x4*)(mlr0 + i * 4);
    const float M = mxd[1];
    float L = 0.f;
    f32x2 a2 = {0.f, 0.f};
#pragma unroll
    for (int i = 0; i < 8; i++) {
      const float fw = fexp2((q4[i >> 1][(i & 1) * 2] - M) * LOG2E);
      L = fmaf(q4[i >> 1][(i & 1) * 2 + 1], fw, L);
      a2 += *(const f32x2*)(rec0 + i * 128 + 2 * l) * fw;
    }
    const unsigned hi = cvtpk(a2[0], a2[1]);
    *(unsigned*)(prv + l * 4) = hi;
    *(unsigned*)(prv + 256 + l * 4) = cvtpk(a2[0] - bf_lo(hi), a2[1] - bf_hi(hi));
    seg0 = project(prv + apo, M, L, false);
    }
    if (rd == rw) {
      const __amdgpu_buffer_rsrc_t ro = mkrs(woB, (ABL & 1) ? 0 : HD * DM * 2);
      const unsigned vo = (unsigned)(((h * DH + rg * 16) * DM + wc * 64 + cc * 8) * 2);
      const unsigned vo0 = vo + rw0 * 8 * DM * 2, vo1 = vo + (1 - rw0) * 8 * DM * 2;
#pragma unroll
      for (int i = 0; i < 16; i++) wo[i] = __builtin_amdgcn_raw_buffer_load_b128(ro, i < 8 ? vo0 : vo1, (i & 7) * DM * 2, WOAUX);
    }
  };
#pragma unroll 1
  for (; x < 8 * RD; x += 8) {
    const int rd = x >> 3;
    events(rd);
    part0(kr);
    front(rk, rv, soff(x + 8));
    part2();
    TR(2 + rd);
  }
  int nit = RD;
  wave_partial(recd + w * RST, mld + w * 2, mxd);
  unsigned o_ml = 98304u + (l < NP ? l : 0) * 8, o_rv = 65536u + (g * RST + w * 16 + c) * 4, o_fw = 45056u + w * 512 + ((l & 3) * 16 + (l >> 2)) * 4;
  unsigned o_fr = 45056u + w * 512 + g * 64, o_oi = (g < 2 && (c & 1) == 0) ? 41216u + g * 256 + (w * 16 + c) * 2 : 45056u + w * 512 + 256 + l * 4;
  unsigned o_ap = 41216u + apo, o_st = store_off(nseg - 1);
  unsigned o_s0 = k == 1 ? store_off(0) : 0x7ffffff0u;
  asm volatile("" : "+v"(o_s0));
  asm volatile("" : "+v"(o_ml), "+v"(o_rv), "+v"(o_fw), "+v"(o_fr), "+v"(o_oi), "+v"(o_ap), "+v"(o_st));
  {
    {
#pragma unroll 1
      while (x < NI) {
        unsigned dr; u64 sv;
        asm volatile("s_mov_b64 %1, exec\n\ts_mov_b64 exec, 1\n\tds_add_rtn_u32 %0, %2, %3\n\ts_mov_b64 exec, %1"
                     : "=&v"(dr), "=&s"(sv) : "v"(cnta), "v"(1u), "v"(kr[0][0]) : "memory");
        __builtin_amdgcn_sched_barrier(0);
        part0(kr);
        asm volatile("s_waitcnt lgkmcnt(0)" : "+v"(dr) :: "memory");
        const int xn = __builtin_amdgcn_readfirstlane(dr);
        if (h >= 24 && j6 == 2 && xn == NI - GI && l == 0) st_u64(ws + WS_FP, next_tag(epa));
        const bool ok = xn < NI;
        front(mkrs(kc, ok ? KVB : 0u), mkrs(vc, ok ? KVB : 0u), ok ? soff(xn) : 0u);
        part2c(recd + (x - 8 * RD + 8) * RST, mld + (x - 8 * RD + 8) * 2);
        x = xn;
        TR(2 + nit); nit++;
      }
      TR(40);
      asm volatile("s_waitcnt lgkmcnt(0)" ::: "memory");
      __syncthreads();
      TR(41);
    }
    {
      f32x2 mli = *(const f32x2*)(lds + o_ml);
      float rv_[NP4 / 4];
#pragma unroll
      for (int r = 0; r < NP4 / 4; r++) rv_[r] = *(const float*)(lds + o_rv + r * 4 * RST * 4);
      if (l >= NI - 8 * RD + 8) mli = f32x2{-INFINITY, 0.f};
      const float M = *mxd;
      const float fi = fexp2((mli[0] - M) * LOG2E);
      *(float*)(lds + o_fw) = fi;
      f32x4 f4[(NP4 / 4 + 3) / 4];
#pragma unroll
      for (int i = 0; i < (NP4 / 4 + 3) / 4; i++) f4[i] = *(const f32x4*)(lds + o_fr + i * 16);
      float a = 0.f;
#pragma unroll
      for (int r = 0; r < NP4 / 4; r++) a = fmaf(f4[r >> 2][r & 3], rv_[r], a);
      a = xrow_sum(a);
      const float an = dpp<0xB1>(a);
      const unsigned hi = cvtpk(a, an);
      const unsigned lw = cvtpk(a - bf_lo(hi), an - bf_hi(hi));
      *(unsigned*)(lds + o_oi) = g ? lw : hi;
      __syncthreads();
      const f32x2 pv = project(lds + o_ap, M, fi * mli[1], true);
      publish(pv, k == 1 ? tg1 : tg0, o_st);
      publish(seg0, tg0, o_s0);
    }
    TR(42);
    if (w < nseg) {
      const int sg = w;
      const int b = b0 + (sg ? 1 - e0 : e0);
      const int j64 = h * 2 + sp;
      const u64 tg = sg ? tg1 : tg0;
      const unsigned ta = (unsigned)tg, tb = (unsigned)(tg >> 32);
      const unsigned oml = (unsigned)(WS_PBML + (size_t)(b * 64 + l) * 16);
      const unsigned om4 = (unsigned)(WS_PBML + (size_t)(b * 64 + (l >> 2)) * 16);
      const unsigned og = (unsigned)(WS_PBG + ((size_t)(b * 64 + (l >> 2)) * DM + j64 * 8) * 8 + (l & 3) * 16);
      u32x4 ml[4], p[4];
      __builtin_amdgcn_s_sleep(6);
#pragma unroll 1
      for (unsigned spins = 0; spins < SPIN_CAP; ++spins) {
        asm volatile("" ::: "memory");
#pragma unroll
        for (int i = 0; i < 4; i++) ml[i] = ld16u_sc1(rs, om4 + i * 256);
#pragma unroll
        for (int i = 0; i < 4; i++) p[i] = ld16u_sc1(rs, og + i * 16 * DM * 8);
        int n = 0;
#pragma unroll
        for (int i = 0; i < 4; i++) n += __popcll(__ballot(ml[i][1] == ta && ml[i][3] == tb && p[i][1] == ta && p[i][3] == tb));
        if (n == 256 || ((HAB & 64) && n >= 192)) break;
#pragma unroll 1
        for (; n < ((HAB & 64) ? 4 * 44 : 4 * PTHR) && spins < SPIN_CAP; ++spins) {
          __builtin_amdgcn_s_sleep(PSL);
          asm volatile("" ::: "memory");
          const u32x4 m1 = ld16u_sc1(rs, oml);
          n = 4 * __popcll(__ballot(m1[1] == ta && m1[3] == tb));
        }
      }
      float v0 = 0.f, v1 = 0.f;
#pragma unroll
      for (int i = 0; i < 4; i++) {
        const float mm = __uint_as_float(ml[i][0]);
        const float Mh = fmaxf(mm, dpp<0x141>(mm));
        const float fq = fexp2((mm - Mh) * LOG2E);
        float L = fq * __uint_as_float(ml[i][2]);
        L += dpp<0x141>(L);
        const float wgt = fq * __builtin_amdgcn_rcpf(L);
        v0 = fmaf(__uint_as_float(p[i][0]), wgt, v0);
        v1 = fmaf(__uint_as_float(p[i][2]), wgt, v1);
      }
      v0 += dpp<0x128>(v0); v0 += dpp<0x124>(v0); v0 = xrow_sum(v0);
      v1 += dpp<0x128>(v1); v1 += dpp<0x124>(v1); v1 = xrow_sum(v1);
      if (l < 4) {
        *(unsigned*)(out + b * 1024 + 512 + j64 * 8 + l * 2) = cvtpk(v0, v1);
        if (j64 == 0 && l == 0) st_u64(ws + WS_EPB + 256 * b, tg);
      }
    }
  }
  TR(43);
  TRDUMP();
}

__device__ __forceinline__ unsigned lds_off(int row, int ch) {
  return 256 * row + 16 * (ch ^ (((row & 3) << 2) | ((row >> 2) & 3)));
}

__device__ __forceinline__ void role_A1(int h, const __hip_bfloat16* __restrict__ xA, const __hip_bfloat16* __restrict__ wqA,
                                        const __hip_bfloat16* __restrict__ wuk, const __hip_bfloat16* __restrict__ wuv,
                                        const __hip_bfloat16* __restrict__ woA, char* __restrict__ ws,
                                        __amdgpu_buffer_rsrc_t rs, __hip_bfloat16* __restrict__ out, char* lds, unsigned trv) {
  const int t = threadIdx.x, l = t & 63;
  const int w = __builtin_amdgcn_readfirstlane(t >> 6);
  const int g = l >> 4, c = l & 15;
  float* red = (float*)(lds + 16384);
  const unsigned vo = (unsigned)(((w * 64 + g * 16) * HD + h * DH + c * 8) * 2);
  u64 tag;
  {
    u32x4 xr[NB][2], wr[16], wk[16];
    const __amdgpu_buffer_rsrc_t rx = mkrs(xA, NB * DM * 2);
    const unsigned xo = (unsigned)((w * 64 + g * 16) * 2);
#pragma unroll
    for (int bb = 0; bb < NB; bb++)
#pragma unroll
      for (int i = 0; i < 2; i++) xr[bb][i] = __builtin_amdgcn_raw_buffer_load_b128(rx, xo + bb * DM * 2 + i * 16, 0, 0);
    const __amdgpu_buffer_rsrc_t rq = mkrs(wqA, DM * HD * 2);
#pragma unroll
    for (int i = 0; i < 16; i++) wr[i] = __builtin_amdgcn_raw_buffer_load_b128(rq, vo, i * HD * 2, NT);
    u64 epv = 0;
    if (t == 0) epv = ld_u64(ws + WS_EPA);
    unsigned* qs = (unsigned*)lds;
    unsigned short* qo = (unsigned short*)(lds + 4096);
    float acc[NB][8];
#pragma unroll
    for (int bb = 0; bb < NB; bb++)
#pragma unroll
      for (int j = 0; j < 8; j++) acc[bb][j] = 0.f;
#pragma unroll
    for (int i = 0; i < 16; i += 2) {
#pragma unroll
      for (int j = 0; j < 4; j++) {
        const unsigned pl = __builtin_amdgcn_perm(wr[i + 1][j], wr[i][j], 0x05040100u);
        const unsigned ph = __builtin_amdgcn_perm(wr[i + 1][j], wr[i][j], 0x07060302u);
#pragma unroll
        for (int bb = 0; bb < NB; bb++) {
          const unsigned x = xr[bb][i >> 3][(i >> 1) & 3];
          acc[bb][2 * j] = dot2(pl, x, acc[bb][2 * j]);
          acc[bb][2 * j + 1] = dot2(ph, x, acc[bb][2 * j + 1]);
        }
      }
    }
    float* rp = red + (w * 4 + g) * 512 + c * 8;
#pragma unroll
    for (int bb = 0; bb < NB; bb++) {
      *(f32x4*)(rp + bb * 128) = f32x4{acc[bb][0], acc[bb][1], acc[bb][2], acc[bb][3]};
      *(f32x4*)(rp + bb * 128 + 4) = f32x4{acc[bb][4], acc[bb][5], acc[bb][6], acc[bb][7]};
    }
    __syncthreads();
    {
      const __amdgpu_buffer_rsrc_t rku = mkrs(wuk, DC * HD * 2);
#pragma unroll
      for (int i = 0; i < 16; i++) wk[i] = __builtin_amdgcn_raw_buffer_load_b128(rku, vo, i * HD * 2, NT);
    }
    {
      float s = 0.f;
#pragma unroll
      for (int i = 0; i < 32; i++) s += red[i * 512 + t];
      const float sn = dpp<0xB1>(s);
      if ((t & 1) == 0) qs[t >> 1] = cvtpk(s, sn);
      if (t == 0) *(u64*)(lds + 2048) = epv;
    }
    __syncthreads();
    tag = next_tag(uni64(*(const u64*)(lds + 2048)));
    {
      u32x4 qq[NB];
#pragma unroll
      for (int bb = 0; bb < NB; bb++) qq[bb] = *(const u32x4*)(qs + bb * 64 + c * 4);
#pragma unroll
      for (int i = 0; i < 16; i++) {
        float s[NB];
#pragma unroll
        for (int bb = 0; bb < NB; bb++) {
          float v = dot2(wk[i][0], qq[bb][0], 0.f);
          v = dot2(wk[i][1], qq[bb][1], v);
          v = dot2(wk[i][2], qq[bb][2], v);
          v = dot2(wk[i][3], qq[bb][3], v);
          s[bb] = sum16(v);
        }
        const float v = (c & 2) ? ((c & 1) ? s[3] : s[2]) : ((c & 1) ? s[1] : s[0]);
        if (c < NB) qo[c * 512 + w * 64 + g * 16 + i] = (unsigned short)(cvtpk(v, v) & 0xffff);
      }
    }
    __syncthreads();
    if (t < 256) {
      const int bb = t >> 6, ch = t & 63;
      st16u_sc1(rs, ((HAB & 16) ? 0x70000000u : 0u) + (unsigned)(WS_QABS + ((size_t)(bb * NH + h) * DC + ch * 8) * 2), *(const u32x4*)(qo + bb * 512 + ch * 8));
    }
    TR(1);
    signal_flag(ws + WS_F1 + h * 8, tag);
    TR(2);
  }
  const unsigned ta = (unsigned)tag, tb = (unsigned)(tag >> 32);
  {
    u32x4 bv[8][2], bo[8][2];
    u32x4 pre[2][8], wo[16];
    {
      const __amdgpu_buffer_rsrc_t ruv = mkrs(wuv, (ABL & 4) ? 0 : DC * HD * 2);
      const __amdgpu_buffer_rsrc_t rwo = mkrs(woA, (ABL & 4) ? 0 : HD * DM * 2);
      const unsigned wvo = (unsigned)(((h * DH + (l >> 3) * 16) * DM + w * 64 + (l & 7) * 8) * 2);
#pragma unroll
      for (int i = 0; i < 16; i++) pre[i >> 3][i & 7] = __builtin_amdgcn_raw_buffer_load_b128(ruv, vo, i * HD * 2, NT);
#pragma unroll
      for (int i = 0; i < 16; i++) wo[i] = __builtin_amdgcn_raw_buffer_load_b128(rwo, wvo, i * DM * 2, NT);
    }
#pragma unroll
    for (int j = 0; j < 8; j++)
#pragma unroll
      for (int s = 0; s < 2; s++) {
#pragma unroll
        for (int e = 0; e < 4; e++)
          bv[j][s][e] = __builtin_amdgcn_perm(pre[s][2 * e + 1][j >> 1], pre[s][2 * e][j >> 1], (j & 1) ? 0x07060302u : 0x05040100u);
        asm volatile("" : "+v"(bv[j][s]));
      }
#pragma unroll
    for (int j = 0; j < 8; j++)
#pragma unroll
      for (int s = 0; s < 2; s++) {
#pragma unroll
        for (int e = 0; e < 4; e++)
          bo[j][s][e] = __builtin_amdgcn_perm(wo[8 * s + 2 * e + 1][j >> 1], wo[8 * s + 2 * e][j >> 1], (j & 1) ? 0x07060302u : 0x05040100u);
        asm volatile("" : "+v"(bo[j][s]));
      }
    constexpr int LAS = 1040;
    char* la = lds;
    unsigned short* cs = (unsigned short*)(lds + 16384);
    float* outs = (float*)(lds + 20480);
    float* cred = (float*)(lds + 32768);
    {
    {
    TR(3);
    wait_gate(ws + WS_FP, tag, trv);
    __syncthreads();
    TR(4);
    }
    if (t < 256) {
      const int bb = t >> 6, ch = t & 63;
      u32x4 ov[SPLIT_A];
      float mm[SPLIT_A], ll[SPLIT_A];
      float M = -INFINITY;
      u32x2 mv[SPLIT_A];
      {
#pragma unroll
        for (int s = 0; s < SPLIT_A; s++) { asm volatile("" : "=v"(ov[s])); asm volatile("" : "=v"(mv[s])); }
        unsigned got = 0;
#pragma unroll 1
        for (unsigned spins = 0; got != 0xffu && spins < SPIN_CAP; ++spins) {
          const u64 f = ld_u64(ws + WS_F2 + (w * SPLIT_A + (l & 7)) * 8);
          const unsigned now = (unsigned)__ballot(f == tag) & 0xffu;
          const unsigned fresh = now & ~got;
#pragma unroll
          for (int s = 0; s < SPLIT_A; s++)
            if ((fresh >> s) & 1u) {
              ov[s] = ld16u_sc1(rs, ((HAB & 2) ? 0x70000000u : 0u) + (unsigned)(WS_PAO + (((size_t)(bb * SPLIT_A + s) * NH + h) * DC + ch * 8) * 2));
              mv[s] = __builtin_amdgcn_raw_buffer_load_b64(rs, (unsigned)(WS_PAML + ((size_t)(bb * SPLIT_A + s) * NH + h) * 8), 0, SC1);
            }
          got = now;
          if (got != 0xffu) __builtin_amdgcn_s_sleep(6);
        }
        TR(5);
      }
#pragma unroll
      for (int s = 0; s < SPLIT_A; s++) {
        mm[s] = __uint_as_float(mv[s][0]); ll[s] = __uint_as_float(mv[s][1]);
        M = fmaxf(M, mm[s]);
      }
      float L = 0.f;
#pragma unroll
      for (int s = 0; s < SPLIT_A; s++) { mm[s] = fexp2((mm[s] - M) * LOG2E); L += mm[s] * ll[s]; }
      const float inv = 1.f / L;
      float av[8];
#pragma unroll
      for (int j = 0; j < 8; j++) av[j] = 0.f;
#pragma unroll
      for (int s = 0; s < SPLIT_A; s++)
#pragma unroll
        for (int j = 0; j < 4; j++) {
          av[2 * j] = fmaf(bf_lo(ov[s][j]), mm[s], av[2 * j]);
          av[2 * j + 1] = fmaf(bf_hi(ov[s][j]), mm[s], av[2 * j + 1]);
        }
      u32x4 hi, lo;
#pragma unroll
      for (int j = 0; j < 4; j++) {
        const float v0 = av[2 * j] * inv, v1 = av[2 * j + 1] * inv;
        hi[j] = cvtpk(v0, v1);
        lo[j] = cvtpk(v0 - bf_lo(hi[j]), v1 - bf_hi(hi[j]));
      }
      *(u32x4*)(la + bb * LAS + ch * 16) = hi;
      *(u32x4*)(la + (4 + bb) * LAS + ch * 16) = lo;
    }
    __syncthreads();
    {
      const char* ap = la + (c & 7) * LAS + (w * 64 + g * 16) * 2;
      const u32x4 a0 = *(const u32x4*)(ap), a1 = *(const u32x4*)(ap + 16);
      f32x4 dd[8];
#pragma unroll
      for (int j = 0; j < 8; j++)
        dd[j] = __builtin_amdgcn_mfma_f32_16x16x32_bf16(__builtin_bit_cast(bf16x8, a0), __builtin_bit_cast(bf16x8, bv[j][0]), f32x4{0.f, 0.f, 0.f, 0.f}, 0, 0, 0);
#pragma unroll
      for (int j = 0; j < 8; j++)
        dd[j] = __builtin_amdgcn_mfma_f32_16x16x32_bf16(__builtin_bit_cast(bf16x8, a1), __builtin_bit_cast(bf16x8, bv[j][1]), dd[j], 0, 0, 0);
#pragma unroll
      for (int r = 0; r < 4; r++) {
        f32x4 v;
#pragma unroll
        for (int j = 0; j < 4; j++) {
          auto sx = __builtin_amdgcn_permlane16_swap(__float_as_uint(dd[j][r]), __float_as_uint(dd[4 + j][r]), false, false);
          v[j] = __uint_as_float(sx[0]) + __uint_as_float(sx[1]);
        }
        if (g < 2) *(f32x4*)(cred + (w * 4 + r) * 128 + c * 8 + g * 4) = v;
      }
    }
    __syncthreads();
    {
      float s = 0.f;
#pragma unroll
      for (int i = 0; i < 8; i++) s += cred[i * 512 + t];
      const float sn = dpp<0xB1>(s);
      if ((t & 1) == 0) *(unsigned*)(cs + t) = cvtpk(s, sn);
    }
    __syncthreads();
    {
      const char* ap = (const char*)cs + ((c >> 1) & 3) * 256 + (g * 32 + (c & 1) * 16) * 2;
      const u32x4 a0 = *(const u32x4*)(ap), a1 = *(const u32x4*)(ap + 16);
      f32x4 dd[8];
#pragma unroll
      for (int j = 0; j < 8; j++)
        dd[j] = __builtin_amdgcn_mfma_f32_16x16x32_bf16(__builtin_bit_cast(bf16x8, a0), __builtin_bit_cast(bf16x8, bo[j][0]), f32x4{0.f, 0.f, 0.f, 0.f}, 0, 0, 0);
#pragma unroll
      for (int j = 0; j < 8; j++)
        dd[j] = __builtin_amdgcn_mfma_f32_16x16x32_bf16(__builtin_bit_cast(bf16x8, a1), __builtin_bit_cast(bf16x8, bo[j][1]), dd[j], 0, 0, 0);
      f32x4 v4[2];
#pragma unroll
      for (int j = 0; j < 8; j++) {
        const float u0 = (l & 8) ? dd[j][1] : dd[j][0], u1 = (l & 8) ? dd[j][3] : dd[j][2];
        const float keep = (l & 8) ? u1 : u0, give = (l & 8) ? u0 : u1;
        v4[j >> 2][j & 3] = keep + dpp<0x128>(give);
      }
      if (g < 2) {
        float* op = outs + (g * 2 + ((l >> 3) & 1)) * 512 + w * 64 + (l & 7) * 8;
        *(f32x4*)op = v4[0];
        *(f32x4*)(op + 4) = v4[1];
      }
    }
    __syncthreads();
    {
      const f32x2 va = *(const f32x2*)(outs + t * 2), vb = *(const f32x2*)(outs + 1024 + t * 2);
      const unsigned oo = ((HAB & 4) ? 0x70000000u : 0u) + (unsigned)(WS_PA + ((size_t)((t >> 8) * NH + h) * DM + (t & 255) * 2) * 8);
      st16u_sc1(rs, oo, u32x4{__float_as_uint(va[0]), ta, __float_as_uint(va[1]), tb});
      st16u_sc1(rs, oo + 2 * NH * DM * 8, u32x4{__float_as_uint(vb[0]), ta, __float_as_uint(vb[1]), tb});
    }
  TR(6);
  if (w < 4) {
    const int bb = h >> 3, n0 = (h & 7) * 64 + w * 16;
    const unsigned og = ((HAB & 4) ? 0x70000000u : 0u) + (unsigned)(WS_PA + ((size_t)(bb * NH + (l >> 3)) * DM + n0) * 8 + (l & 7) * 16);
    u32x4 p[4];
    __builtin_amdgcn_s_sleep(10);
#pragma unroll 1
    for (unsigned spins = 0; spins < SPIN_CAP; ++spins) {
      asm volatile("" ::: "memory");
#pragma unroll
      for (int i = 0; i < 4; i++) p[i] = ld16u_sc1(rs, og + i * 8 * DM * 8);
      bool ok = true;
#pragma unroll
      for (int i = 0; i < 4; i++) ok = ok && p[i][1] == ta && p[i][3] == tb;
      if (__all(ok) || (HAB & 4)) break;
      __builtin_amdgcn_s_sleep(4);
    }
    float v0 = (__uint_as_float(p[0][0]) + __uint_as_float(p[1][0])) + (__uint_as_float(p[2][0]) + __uint_as_float(p[3][0]));
    float v1 = (__uint_as_float(p[0][2]) + __uint_as_float(p[1][2])) + (__uint_as_float(p[2][2]) + __uint_as_float(p[3][2]));
    v0 += dpp<0x128>(v0); v0 = xrow_sum(v0);
    v1 += dpp<0x128>(v1); v1 = xrow_sum(v1);
    if (l < 8) {
      *(unsigned*)(out + bb * 1024 + n0 + l * 2) = cvtpk(v0, v1);
      if (h == 0 && t == 0) st_u64(ws + WS_EPA, tag);
    }
  }
    }
  }
  TR(43);
  TRDUMP();
}

__device__ __forceinline__ void role_A2(int a, const __hip_bfloat16* __restrict__ ckv, char* __restrict__ ws,
                                        __amdgpu_buffer_rsrc_t rs, char* lds, unsigned trv) {
  const int t = threadIdx.x, l = t & 63;
  const int w = __builtin_amdgcn_readfirstlane(t >> 6);
  const int g = l >> 4, c = l & 15;
  u64 ep = 0;
  if (t == 0) ep = ld_u64(ws + WS_EPA);
  const int grp = w >> 2, wq = w & 3;
  const int b = a / SPLIT_A, sp = a % SPLIT_A;
  const int r = l & 31, hh = l >> 5;
  constexpr unsigned CB = (unsigned)((size_t)NB * SK * DC * 2);
  constexpr int NTL = KA / 64;
  const unsigned cvo = (unsigned)((g * DC + 128 * wq + c * 8) * 2);
  const unsigned cso = (unsigned)(((size_t)b * SK + sp * KA + grp * 32) * DC * 2);
  auto load_tile = [&](u32x4 (&pr)[8], int tile) {
    const bool ok = tile < NTL;
    const __amdgpu_buffer_rsrc_t rc = mkrs(ckv, (ok && !(ABL & 2)) ? CB : 0u);
    const unsigned so = ok ? cso + (unsigned)((tile + sp) & (NTL - 1)) * 64 * DC * 2 : 0u;
#pragma unroll
    for (int u = 0; u < 8; u++) pr[u] = __builtin_amdgcn_raw_buffer_load_b128(rc, cvo, so + u * 4 * DC * 2, NT);
  };
  char* img = lds + w * 8192;
  float* sred = (float*)(lds + 65536 + grp * 16384);
  float* mxs = (float*)(lds + 98304 + grp * 512);
  unsigned* pbuf = (unsigned*)(lds + 102400 + grp * 2048);
  float* lall = (float*)(lds + 106496);
  const unsigned imga = (unsigned)(uintptr_t)(__attribute__((address_space(3))) char*)img;
  auto put_tile = [&](u32x4 (&pr)[8]) {
#pragma unroll
    for (int u = 0; u < 8; u++) *(u32x4*)(img + lds_off(4 * u + g, c)) = pr[u];
  };
  u32x4 pre[2][8];
  load_tile(pre[0], 0);
  load_tile(pre[1], 1);
  const u64 tag = next_tag(uni64(ep));
  put_tile(pre[0]);
  __builtin_amdgcn_sched_barrier(0);
  load_tile(pre[0], 2);
  TR(1);
  if (t < 64) {
#pragma unroll 1
    for (unsigned spins = 0; spins < SPIN_CAP; ++spins) {
      __builtin_amdgcn_s_sleep(32);
      if (__all(ld_u64(ws + WS_F1 + (l & 31) * 8) == tag)) break;
    }
  }
  __syncthreads();
  TR(3);
  bf16x8 qf[8];
  {
    char* qi = lds + 65536;
    const unsigned qo = ((HAB & 16) ? 0x70000000u : 0u) + (unsigned)(WS_QABS + (size_t)b * NH * DC * 2 + t * 16);
    u32x4 qv[4];
#pragma unroll
    for (int i = 0; i < 4; i++) qv[i] = ld16u_sc1(rs, qo + i * 8192);
#pragma unroll
    for (int i = 0; i < 4; i++) *(u32x4*)(qi + (i * 8 + w) * 1040 + l * 16) = qv[i];
    __syncthreads();
#pragma unroll
    for (int s = 0; s < 8; s++) qf[s] = __builtin_bit_cast(bf16x8, *(const u32x4*)(qi + r * 1040 + (128 * wq + 16 * s + 8 * hh) * 2));
#pragma unroll
    for (int s = 0; s < 8; s++) asm volatile("" : "+v"(qf[s]));
    __syncthreads();
  }
  f32x16 o[4];
#pragma unroll
  for (int i = 0; i < 4; i++)
#pragma unroll
    for (int j = 0; j < 16; j++) o[i][j] = 0.f;
  float m = -INFINITY, lsum = 0.f;
  const int q4 = (l & 15) >> 2, p4 = l & 3, colh = g & 1;
  auto tile_body = [&](int tile, u32x4 (&nx)[8]) {
    f32x16 s;
#pragma unroll
    for (int j = 0; j < 16; j++) s[j] = 0.f;
    {
      u32x4 af[8];
#pragma unroll
      for (int ks = 0; ks < 8; ks++) af[ks] = *(const u32x4*)(img + lds_off(r, 2 * ks + hh));
      __builtin_amdgcn_sched_barrier(0);
#pragma unroll
      for (int ks = 0; ks < 8; ks++) s = __builtin_amdgcn_mfma_f32_32x32x16_bf16(__builtin_bit_cast(bf16x8, af[ks]), qf[ks], s, 0, 0, 0);
    }
    f32x4* sr = (f32x4*)sred;
#pragma unroll
    for (int k4 = 0; k4 < 4; k4++) sr[(k4 * 4 + wq) * 64 + l] = f32x4{s[4 * k4], s[4 * k4 + 1], s[4 * k4 + 2], s[4 * k4 + 3]};
    __syncthreads();
    float x[4];
    {
      const f32x4 v = (sr[(wq * 4) * 64 + l] + sr[(wq * 4 + 1) * 64 + l]) + (sr[(wq * 4 + 2) * 64 + l] + sr[(wq * 4 + 3) * 64 + l]);
#pragma unroll
      for (int e = 0; e < 4; e += 2) {
        const unsigned r1 = cvtpk(v[e], v[e + 1]);
        const unsigned r2 = cvtpk(bf_lo(r1) * SCALE, bf_hi(r1) * SCALE);
        x[e] = bf_lo(r2); x[e + 1] = bf_hi(r2);
      }
      float mq = fmaxf(fmaxf(x[0], x[1]), fmaxf(x[2], x[3]));
      auto sx = __builtin_amdgcn_permlane32_swap(__float_as_uint(mq), __float_as_uint(mq), false, false);
      mq = fmaxf(__uint_as_float(sx[0]), __uint_as_float(sx[1]));
      if (hh == 0) mxs[r * 4 + wq] = mq;
    }
    __syncthreads();
    const f32x4 m4 = *(const f32x4*)(mxs + r * 4);
    const float mn = fmaxf(fmaxf(m, fmaxf(m4[0], m4[1])), fmaxf(m4[2], m4[3]));
    const float alpha = fexp2((m - mn) * LOG2E);
    {
      const unsigned p0 = cvtpk(fexp2((x[0] - mn) * LOG2E), fexp2((x[1] - mn) * LOG2E));
      const unsigned p1 = cvtpk(fexp2((x[2] - mn) * LOG2E), fexp2((x[3] - mn) * LOG2E));
      *(u32x2*)(pbuf + l * 8 + wq * 2) = u32x2{p0, p1};
      lsum = lsum * alpha + ((bf_lo(p0) + bf_hi(p0)) + (bf_lo(p1) + bf_hi(p1)));
    }
    m = mn;
    __syncthreads();
    unsigned pk[8];
    {
      const u32x4 a0 = *(const u32x4*)(pbuf + l * 8), a1 = *(const u32x4*)(pbuf + l * 8 + 4);
      pk[0] = a0[0]; pk[1] = a0[1]; pk[2] = a0[2]; pk[3] = a0[3]; pk[4] = a1[0]; pk[5] = a1[1]; pk[6] = a1[2]; pk[7] = a1[3];
    }
    if (__any(alpha != 1.f)) {
#pragma unroll
      for (int i = 0; i < 4; i++)
#pragma unroll
        for (int j = 0; j < 16; j++) o[i][j] *= alpha;
    }
#pragma unroll
    for (int s2 = 0; s2 < 2; s2++) {
      u32x2 tv[4][2];
#pragma unroll
      for (int ct = 0; ct < 4; ct++) {
        const int ch = 4 * ct + 2 * colh + (p4 >> 1);
        const unsigned a0 = lds_off(4 * hh + q4, ch) + 8 * (p4 & 1);
        const unsigned a1 = lds_off(8 + 4 * hh + q4, ch) + 8 * (p4 & 1);
        if (s2 == 0) {
          asm volatile("ds_read_b64_tr_b16 %0, %1" : "=v"(tv[ct][0]) : "v"(imga + a0) : "memory");
          asm volatile("ds_read_b64_tr_b16 %0, %1" : "=v"(tv[ct][1]) : "v"(imga + a1) : "memory");
        } else {
          asm volatile("ds_read_b64_tr_b16 %0, %1 offset:4096" : "=v"(tv[ct][0]) : "v"(imga + a0) : "memory");
          asm volatile("ds_read_b64_tr_b16 %0, %1 offset:4096" : "=v"(tv[ct][1]) : "v"(imga + a1) : "memory");
        }
      }
      asm volatile("s_waitcnt lgkmcnt(0)" ::: "memory");
      __builtin_amdgcn_sched_barrier(0);
      u32x4 pb = {pk[4 * s2], pk[4 * s2 + 1], pk[4 * s2 + 2], pk[4 * s2 + 3]};
#pragma unroll
      for (int ct = 0; ct < 4; ct++) {
        u32x4 av = {tv[ct][0][0], tv[ct][0][1], tv[ct][1][0], tv[ct][1][1]};
        o[ct] = __builtin_amdgcn_mfma_f32_32x32x16_bf16(__builtin_bit_cast(bf16x8, av), __builtin_bit_cast(bf16x8, pb), o[ct], 0, 0, 0);
      }
    }
    put_tile(nx);
    __builtin_amdgcn_sched_barrier(0);
    load_tile(nx, tile + 3);
    TR(4 + tile);
  };
#pragma unroll 1
  for (int tile = 0; tile < NTL; tile += 2) {
    tile_body(tile, pre[1]);
    tile_body(tile + 1, pre[0]);
  }
  constexpr int OST = 516;
  float* ob = (float*)lds;
  float* mlb = (float*)(lds + 32 * OST * 4);
  lall[w * 64 + l] = lsum;
  __syncthreads();
  {
    const float* lp = lall + grp * 256 + r;
    lsum = ((lp[0] + lp[32]) + (lp[64] + lp[96])) + ((lp[128] + lp[160]) + (lp[192] + lp[224]));
  }
  if (grp == 1) {
#pragma unroll
    for (int ct = 0; ct < 4; ct++)
#pragma unroll
      for (int j4 = 0; j4 < 4; j4++)
        *(f32x4*)(ob + r * OST + 128 * wq + 32 * ct + 8 * j4 + 4 * hh) =
            f32x4{o[ct][4 * j4], o[ct][4 * j4 + 1], o[ct][4 * j4 + 2], o[ct][4 * j4 + 3]};
    if (wq == 0 && l < 32) { mlb[2 * r] = m; mlb[2 * r + 1] = lsum; }
  }
  __syncthreads();
  f32x2 mlv = {0.f, 0.f};
  if (grp == 0) {
    const float m1 = mlb[2 * r], l1 = mlb[2 * r + 1];
    const float M = fmaxf(m, m1);
    const float f0 = fexp2((m - M) * LOG2E), f1 = fexp2((m1 - M) * LOG2E);
#pragma unroll
    for (int ct = 0; ct < 4; ct++)
#pragma unroll
      for (int j4 = 0; j4 < 4; j4++) {
        float* p = ob + r * OST + 128 * wq + 32 * ct + 8 * j4 + 4 * hh;
        const f32x4 o1 = *(const f32x4*)p;
        *(f32x4*)p = f32x4{o[ct][4 * j4] * f0 + o1[0] * f1, o[ct][4 * j4 + 1] * f0 + o1[1] * f1,
                           o[ct][4 * j4 + 2] * f0 + o1[2] * f1, o[ct][4 * j4 + 3] * f0 + o1[3] * f1};
      }
    mlv = f32x2{M, lsum * f0 + l1 * f1};
  }
  __syncthreads();
  {
    const unsigned pao = (unsigned)(WS_PAO + (size_t)(b * SPLIT_A + sp) * NH * DC * 2);
    u32x4 pv[4];
#pragma unroll
    for (int i = 0; i < 4; i++) {
      const int idx = i * 512 + t;
      const int hd = idx >> 6, c8 = idx & 63;
      const f32x4 v0 = *(const f32x4*)(ob + hd * OST + c8 * 8), v1 = *(const f32x4*)(ob + hd * OST + c8 * 8 + 4);
      pv[i] = u32x4{cvtpk(v0[0], v0[1]), cvtpk(v0[2], v0[3]), cvtpk(v1[0], v1[1]), cvtpk(v1[2], v1[3])};
      asm volatile("" : "+v"(pv[i]));
    }
    TR(19);
    wait_gate(ws + WS_FP, tag, trv);
    __syncthreads();
    TR(18);
#pragma unroll
    for (int i = 0; i < 4; i++) st16u_sc1(rs, (((HAB & 1) || ((HAB & 8) && i >= 2)) ? 0x70000000u : 0u) + pao + (unsigned)(i * 512 + t) * 16, pv[i]);
    if (t < 32) st8_sc1(ws + WS_PAML + ((size_t)(b * SPLIT_A + sp) * NH + r) * 8, mlv);
  }
  TR(20);
  signal_flag(ws + WS_F2 + a * 8, tag);
  TR(43);
  TRDUMP();
}

__global__ __launch_bounds__(512, 2) void k_fused(const __hip_bfloat16* __restrict__ xA, const __hip_bfloat16* __restrict__ ckv,
                                                  const __hip_bfloat16* __restrict__ wqA, const __hip_bfloat16* __restrict__ wuk,
                                                  const __hip_bfloat16* __restrict__ wuv, const __hip_bfloat16* __restrict__ woA,
                                                  const __hip_bfloat16* __restrict__ xB, const __hip_bfloat16* __restrict__ kc,
                                                  const __hip_bfloat16* __restrict__ vc, const __hip_bfloat16* __restrict__ wqB,
                                                  const __hip_bfloat16* __restrict__ woB, char* __restrict__ ws,
                                                  __hip_bfloat16* __restrict__ out) {
  __shared__ __attribute__((aligned(16))) char lds[131072];
  unsigned trv = 0;
  u64 tfirst; asm volatile("s_memrealtime %0" : "=s"(tfirst));
  TR(0);
  asm volatile("s_waitcnt lgkmcnt(0)" : "+s"(tfirst));
  trv = (threadIdx.x & 63) == 59 ? (unsigned)tfirst : trv;
  { unsigned xid; asm volatile("s_getreg_b32 %0, hwreg(HW_REG_XCC_ID)" : "=s"(xid)); trv = (threadIdx.x & 63) == 63 ? (xid & 15) + 1 : trv; trv = (threadIdx.x & 63) == 60 ? (unsigned)__builtin_amdgcn_s_memtime() : trv; }
  asm volatile("" :: "s"(xA), "s"(ckv), "s"(wqA), "s"(wuk), "s"(wuv), "s"(woA), "s"(xB), "s"(kc), "s"(vc), "s"(wqB), "s"(woB), "s"(ws), "s"(out));
  TR(58);
  const int blk = blockIdx.x, xcd = blk & 7, r = blk >> 3;
  unsigned pfv = 0;
  {
    u64 pc; unsigned len;
    asm volatile("s_getpc_b64 %0\n.Lcode_begin:\n\ts_mov_b32 %1, .Lcode_end-.Lcode_begin" : "=s"(pc), "=s"(len));
    const unsigned idx = (unsigned)(r * 8 + (threadIdx.x >> 6));
    const __amdgpu_buffer_rsrc_t rc = mkrs((const void*)pc, len);
    pfv = __builtin_amdgcn_raw_buffer_load_b32(rc, idx * 1024 + (threadIdx.x & 63) * 16, 0, 0);
  }
  const __amdgpu_buffer_rsrc_t rs = mkrs(ws, (unsigned)WS_END);
  if (r < 8) {
    if (r < 4) role_A1(((xcd + AROT1) & 7) + 8 * r, xA, wqA, wuk, wuv, woA, ws, rs, out, lds, trv);
    else role_A2(((xcd + AROT2) & 7) * 4 + r - 4, ckv, ws, rs, lds, trv);
  } else {
    const int rr = r - 8, hq = rr / 6;
    role_B(((PERM >> (4 * xcd)) & 7) + 8 * hq, rr - 6 * hq, xB, kc, vc, wqB, woB, ws, rs, out, lds, trv);
  }

  if (blockIdx.x == 0 && threadIdx.x < 64) {
    const unsigned t00 = __builtin_amdgcn_readfirstlane(trv);
    while ((unsigned)__builtin_amdgcn_s_memrealtime() - t00 < 7500u) __builtin_amdgcn_s_sleep(1);
    const int l = threadIdx.x;
    auto ld = [&](int blk, int wv, int idx) { return __hip_atomic_load((gu32*)(ws + WS_TR) + (blk * 8 + wv) * 64 + idx, __ATOMIC_RELAXED, __HIP_MEMORY_SCOPE_AGENT); };
    auto agg = [&](int idx, int b0, int b1, int kind) {
      int sum = 0, cnt = 0, mx = -(1 << 30), mn = 1 << 30;
      for (int i = 0; i < 4; i++) {
        const int blk = l + 64 * i;
        if (blk < b0 || blk >= b1) continue;
        int v = -(1 << 30);
        for (int wv = 0; wv < 8; wv++) { const unsigned u = ld(blk, wv, idx); if (u) { const int d = (int)(u - t00); v = d > v ? d : v; } }
        if (v > -(1 << 29)) { sum += v; cnt++; mx = v > mx ? v : mx; mn = v < mn ? v : mn; }
      }
      for (int o = 32; o > 0; o >>= 1) { sum += __shfl_xor(sum, o); cnt += __shfl_xor(cnt, o); int v = __shfl_xor(mx, o); mx = v > mx ? v : mx; v = __shfl_xor(mn, o); mn = v < mn ? v : mn; }
      return kind == 0 ? sum / (cnt ? cnt : 1) : kind == 1 ? mx : mn;
    };
    int f = agg(EX_IA, EX_A0, EX_A1, EX_GA) - agg(EX_IB, EX_B0, EX_B1, EX_GB) + EX_OFF;
    if (f < 0) f = 0;
    if (f > 2000) f = 2000;
    while ((unsigned)__builtin_amdgcn_s_memrealtime() - t00 < 10000u + (unsigned)(EX_MUL * f)) __builtin_amdgcn_s_sleep(1);
  }
  asm volatile(".Lcode_end:" :: "v"(pfv));
}

extern "C" void kernel_launch(void* const* d_in, const int* in_sizes, int n_in,
                              void* d_out, int out_size, void* d_ws, size_t ws_size,
                              hipStream_t stream) {
  k_fused<<<256, 512, 0, stream>>>((const __hip_bfloat16*)d_in[0], (const __hip_bfloat16*)d_in[1], (const __hip_bfloat16*)d_in[2],
                                   (const __hip_bfloat16*)d_in[3], (const __hip_bfloat16*)d_in[4], (const __hip_bfloat16*)d_in[5],
                                   (const __hip_bfloat16*)d_in[6], (const __hip_bfloat16*)d_in[7], (const __hip_bfloat16*)d_in[8],
                                   (const __hip_bfloat16*)d_in[9], (const __hip_bfloat16*)d_in[10], (char*)d_ws,
                                   (__hip_bfloat16*)d_out);
}
```

```cpp
#define GC 430
#define GD 90
#include <hip/hip_runtime.h>
#include <hip/hip_bf16.h>
#include <stdint.h>

typedef __attribute__((ext_vector_type(4))) unsigned u32x4;
typedef __attribute__((ext_vector_type(2))) unsigned u32x2;
typedef __attribute__((ext_vector_type(4))) float f32x4;
typedef __attribute__((ext_vector_type(2))) float f32x2;
typedef __attribute__((ext_vector_type(16))) float f32x16;
typedef __attribute__((ext_vector_type(8))) __bf16 bf16x8;
typedef __attribute__((ext_vector_type(2))) __bf16 bf16x2;
typedef __attribute__((address_space(1))) unsigned gu32;
typedef __attribute__((address_space(1))) unsigned long long gu64;
typedef unsigned long long u64;

#define ROTM 0
#define AROT1 0
#define AROT2 0
#define PERM 0x76043215u
#define PSL 4
#define HAB 0
#define NOC 0
#define DEFER 1
#define WO0 3
#define WOS 4
#define WQAUX 0
#define ROTQ 0x203120
#define TSY0 950
#define TSW 190
#define WOAUX 0
#define GMW 2
#define T4D 10
#define T4S 4
#define RSL 2
#define KVAUX 2
#define ABL 0
constexpr int NB = 4;
constexpr int NH = 32;
constexpr int SK = 4096;
constexpr int DH = 128;
constexpr int DM = 512;
constexpr int DC = 512;
constexpr int HD = NH * DH;

constexpr int SPLIT_A = 8;
constexpr int KA = SK / SPLIT_A;
constexpr int SS = 171;
constexpr int RD = 18;
constexpr int NP = SS - 8 * RD + 8;
constexpr int NP4 = (NP + 3) & ~3;
constexpr int RST = 144;
#define GID 18
constexpr int GI = GID;
constexpr int RE = 15;
#define PRIOFD 4
constexpr int PRIOF = PRIOFD;

constexpr float SCALE = 0.08838834764831845f;
constexpr float LOG2E = 1.4426950408889634f;
#define NTA 2
constexpr int NT = NTA;
constexpr int SC1 = 16;

constexpr size_t WS_EPA  = 0;
constexpr size_t WS_EPB  = 256;
constexpr size_t WS_F1   = 4096;
constexpr size_t WS_F2   = WS_F1 + 1024;
constexpr size_t WS_FP   = 8192;
constexpr size_t WS_QABS = 16384;
constexpr size_t WS_PAML = WS_QABS + (size_t)NB * NH * DC * 2;
constexpr size_t WS_PAC  = WS_PAML + (size_t)NB * SPLIT_A * NH * 16;
constexpr size_t WS_PAO  = WS_PAC + (size_t)NB * SPLIT_A * NH * 8;
constexpr size_t WS_PA   = WS_PAO + (size_t)NB * SPLIT_A * NH * DC * 2;
constexpr size_t WS_PBML = WS_PA + (size_t)NB * NH * DM * 8;
constexpr size_t WS_PBG  = WS_PBML + (size_t)NB * 64 * 16;
constexpr size_t WS_END  = WS_PBG + (size_t)NB * 64 * DM * 8;

__device__ __forceinline__ float bf_lo(unsigned u) { return __uint_as_float(u << 16); }
__device__ __forceinline__ float bf_hi(unsigned u) { return __uint_as_float(u & 0xffff0000u); }
__device__ __forceinline__ unsigned cvtpk(float lo, float hi) {
  f32x2 v = {lo, hi};
  return __builtin_bit_cast(unsigned, __builtin_convertvector(v, bf16x2));
}
__device__ __forceinline__ float rbf(float x) { return __uint_as_float(cvtpk(x, x) << 16); }
__device__ __forceinline__ float dot2(unsigned k, unsigned q, float acc) {
  return __builtin_amdgcn_fdot2_f32_bf16(__builtin_bit_cast(bf16x2, k), __builtin_bit_cast(bf16x2, q), acc, false);
}
template <int CTRL> __device__ __forceinline__ float dpp(float x) {
  return __builtin_bit_cast(float, __builtin_amdgcn_mov_dpp(__builtin_bit_cast(int, x), CTRL, 0xf, 0xf, true));
}
__device__ __forceinline__ float sum16(float x) {
  x += dpp<0x128>(x);
  x += dpp<0x124>(x);
  x += dpp<0x4E>(x);
  x += dpp<0xB1>(x);
  return x;
}
__device__ __forceinline__ float xrow_max(float x) {
  auto s = __builtin_amdgcn_permlane16_swap(__float_as_uint(x), __float_as_uint(x), false, false);
  x = fmaxf(__uint_as_float(s[0]), __uint_as_float(s[1]));
  auto t = __builtin_amdgcn_permlane32_swap(__float_as_uint(x), __float_as_uint(x), false, false);
  return fmaxf(__uint_as_float(t[0]), __uint_as_float(t[1]));
}
__device__ __forceinline__ float xrow_sum(float x) {
  auto s = __builtin_amdgcn_permlane16_swap(__float_as_uint(x), __float_as_uint(x), false, false);
  x = __uint_as_float(s[0]) + __uint_as_float(s[1]);
  auto t = __builtin_amdgcn_permlane32_swap(__float_as_uint(x), __float_as_uint(x), false, false);
  return __uint_as_float(t[0]) + __uint_as_float(t[1]);
}
__device__ __forceinline__ float fexp2(float x) { return __builtin_amdgcn_exp2f(x); }
__device__ __forceinline__ __amdgpu_buffer_rsrc_t mkrs(const void* p, unsigned bytes) {
  return __builtin_amdgcn_make_buffer_rsrc((void*)p, 0, (int)bytes, 0x00020000);
}

#define TR(i) do {} while (0)
#define TRDUMP() do {} while (0)
constexpr int PTHR = 60;
constexpr unsigned SPIN_CAP = 1u << 21;
__device__ __forceinline__ u64 ld_u64(const void* p) {
  return __hip_atomic_load((gu64*)p, __ATOMIC_RELAXED, __HIP_MEMORY_SCOPE_AGENT);
}
__device__ __forceinline__ void st_u64(void* p, u64 v) {
  __hip_atomic_store((gu64*)p, v, __ATOMIC_RELAXED, __HIP_MEMORY_SCOPE_AGENT);
}
__device__ __forceinline__ u64 next_tag(u64 e) { return e + 0x0000000100000001ull; }
__device__ __forceinline__ u64 uni64(u64 v) {
  const unsigned lo = __builtin_amdgcn_readfirstlane((unsigned)v), hi = __builtin_amdgcn_readfirstlane((unsigned)(v >> 32));
  return ((u64)hi << 32) | lo;
}
__device__ __forceinline__ void signal_flag(char* flag, u64 tag) {
  asm volatile("s_waitcnt vmcnt(0)" ::: "memory");
  __syncthreads();
  if (threadIdx.x == 0) st_u64(flag, tag);
}
template <int CTRL> __device__ __forceinline__ int dppi(int x) { return __builtin_amdgcn_mov_dpp(x, CTRL, 0xf, 0xf, true); }
__device__ __forceinline__ int sum8i(int x) { x += dppi<0xB1>(x); x += dppi<0x4E>(x); x += dppi<0x141>(x); return x; }
__device__ __forceinline__ unsigned row_sums(const u32x4 (&p)[4]) {
  unsigned c4[4];
#pragma unroll
  for (int i = 0; i < 4; i++) c4[i] = (p[i][0] + p[i][1]) + (p[i][2] + p[i][3]);
  auto s0 = __builtin_amdgcn_permlane32_swap(c4[0], c4[2], false, false);
  auto s1 = __builtin_amdgcn_permlane32_swap(c4[1], c4[3], false, false);
  auto s2 = __builtin_amdgcn_permlane16_swap(s0[0] + s0[1], s1[0] + s1[1], false, false);
  int x = (int)(s2[0] + s2[1]);
  x += dppi<0x128>(x);
  return (unsigned)sum8i(x);
}
__device__ __forceinline__ unsigned clk() { return (unsigned)__builtin_amdgcn_s_memrealtime(); }
__device__ __forceinline__ unsigned wait_gate(const char* flag, unsigned ta, unsigned* slot) {
  if (threadIdx.x < 64) {
    u64 v = 0;
#pragma unroll 1
    for (unsigned spins = 0; spins < SPIN_CAP; ++spins) {
      v = ld_u64(flag + (threadIdx.x & 7) * 8);
      if (__all((unsigned)(v >> 32) == ta)) break;
      __builtin_amdgcn_s_sleep(8);
    }
    const unsigned t0 = __builtin_amdgcn_readfirstlane((unsigned)v);
    const int d = (int)((unsigned)v - t0);
    int mx = max(d, dppi<0xB1>(d)); mx = max(mx, dppi<0x4E>(mx)); mx = max(mx, dppi<0x141>(mx));
    const int mean = sum8i(d) >> 3;
    if (threadIdx.x == 0) *slot = t0 + (unsigned)(mean + (((mx - mean) * GMW) >> 2) + GC);
  }
  __syncthreads();
  return __builtin_amdgcn_readfirstlane(*slot);
}
__device__ __forceinline__ void wait_until(unsigned t) {
#pragma unroll 1
  for (int spins = 0; spins < 4096 && (int)(t - clk()) > 0; ++spins) __builtin_amdgcn_s_sleep(1);
}
__device__ __forceinline__ void st16_sc1(__amdgpu_buffer_rsrc_t rs, unsigned off, f32x4 v) {
  __builtin_amdgcn_raw_buffer_store_b128(__builtin_bit_cast(u32x4, v), rs, off, 0, SC1);
}
__device__ __forceinline__ void st16u_sc1(__amdgpu_buffer_rsrc_t rs, unsigned off, u32x4 v) {
  __builtin_amdgcn_raw_buffer_store_b128(v, rs, off, 0, SC1);
}
__device__ __forceinline__ f32x4 ld16_sc1(__amdgpu_buffer_rsrc_t rs, unsigned off) {
  return __builtin_bit_cast(f32x4, __builtin_amdgcn_raw_buffer_load_b128(rs, off, 0, SC1));
}
__device__ __forceinline__ u32x4 ld16u_sc1(__amdgpu_buffer_rsrc_t rs, unsigned off) {
  return __builtin_amdgcn_raw_buffer_load_b128(rs, off, 0, SC1);
}
__device__ __forceinline__ void st8_sc1(void* p, f32x2 v) { st_u64(p, __builtin_bit_cast(u64, v)); }
__device__ __forceinline__ f32x2 ld8_sc1(const void* p) { return __builtin_bit_cast(f32x2, ld_u64(p)); }

__device__ __forceinline__ void item_dots(const u32x4 (&ks)[4], const u32x4 q, float (&s_)[4]) {
#pragma unroll
  for (int u = 0; u < 4; u++) {
    float a = dot2(ks[u][0], q[0], 0.f);
    a = dot2(ks[u][1], q[1], a);
    a = dot2(ks[u][2], q[2], a);
    a = dot2(ks[u][3], q[3], a);
    s_[u] = a;
    asm volatile("" : "+v"(s_[u]));
  }
}
__device__ __forceinline__ void item_unpack(const u32x4 (&vs)[4], f32x2 (&vf)[4][4]) {
#pragma unroll
  for (int u = 0; u < 4; u++)
#pragma unroll
    for (int j = 0; j < 4; j++) { vf[u][j] = f32x2{bf_lo(vs[u][j]), bf_hi(vs[u][j])}; asm volatile("" : "+v"(vf[u][j])); }
}
__device__ __forceinline__ f32x2 rows_sum(const float (&o)[8]) {
  float r4[4], r2[2];
#pragma unroll
  for (int j = 0; j < 4; j++) {
    auto sx = __builtin_amdgcn_permlane32_swap(__float_as_uint(o[j]), __float_as_uint(o[j + 4]), false, false);
    r4[j] = __uint_as_float(sx[0]) + __uint_as_float(sx[1]);
  }
#pragma unroll
  for (int j = 0; j < 2; j++) {
    auto sx = __builtin_amdgcn_permlane16_swap(__float_as_uint(r4[j]), __float_as_uint(r4[j + 2]), false, false);
    r2[j] = __uint_as_float(sx[0]) + __uint_as_float(sx[1]);
  }
  return f32x2{r2[0], r2[1]};
}
__device__ __forceinline__ f32x2 item_partial(const float (&s_)[4], const f32x2 (&vf)[4][4], float& mx, float& ll) {
  float s[4];
#pragma unroll
  for (int u = 0; u < 4; u++) s[u] = rbf(rbf(sum16(s_[u])) * SCALE);
  mx = xrow_max(fmaxf(fmaxf(s[0], s[1]), fmaxf(s[2], s[3])));
  float p[4];
#pragma unroll
  for (int u = 0; u < 4; u++) p[u] = fexp2((s[u] - mx) * LOG2E);
  ll = xrow_sum((p[0] + p[1]) + (p[2] + p[3]));
  float o[8];
#pragma unroll
  for (int j = 0; j < 4; j++) {
    f32x2 v = vf[0][j] * p[0];
#pragma unroll
    for (int u = 1; u < 4; u++) v = __builtin_elementwise_fma(vf[u][j], f32x2{p[u], p[u]}, v);
    o[2 * j] = v[0]; o[2 * j + 1] = v[1];
  }
  return rows_sum(o);
}
__device__ __forceinline__ void role_B(int h, int j6, const __hip_bfloat16* __restrict__ xB,
                                       const __hip_bfloat16* __restrict__ kc, const __hip_bfloat16* __restrict__ vc,
                                       const __hip_bfloat16* __restrict__ wqB, const __hip_bfloat16* __restrict__ woB,
                                       char* __restrict__ ws, __amdgpu_buffer_rsrc_t rs,
                                       __hip_bfloat16* __restrict__ out, char* lds, unsigned trv) {
  const int t = threadIdx.x, l = t & 63;
  const int w = __builtin_amdgcn_readfirstlane(t >> 6);
  const int g = l >> 4, c = l & 15;
  const int pr = j6 >= 3 ? 1 : 0, k = j6 - 3 * pr;
  const int b0 = 2 * pr;
  const int NI = k == 1 ? 2 * (256 - SS) : SS;
  const int e0 = k == 1 ? (h & 1) : (k >> 1);
  u32x4 xr[2][2], wr[16];
  {
    const __amdgpu_buffer_rsrc_t rx = mkrs(xB, NB * DM * 2);
    const int rq_ = (ROTQ >> (4 * j6)) & 3;
    const int cw = w ^ ((rq_ & 2) << 1), r0 = rq_ & 1;
    const unsigned xo = (unsigned)((b0 * DM + cw * 64 + g * 16) * 2);
#pragma unroll
    for (int bb = 0; bb < 2; bb++)
#pragma unroll
      for (int i = 0; i < 2; i++) xr[bb][i] = __builtin_amdgcn_raw_buffer_load_b128(rx, xo + bb * DM * 2, (i ^ r0) * 16, 0);
    const __amdgpu_buffer_rsrc_t rq = mkrs(wqB, DM * HD * 2);
    const unsigned vo = (unsigned)(((cw * 64 + g * 16) * HD + h * DH + c * 8) * 2);
    const unsigned vo0 = vo + r0 * 8 * HD * 2, vo1 = vo + (1 - r0) * 8 * HD * 2;
#pragma unroll
    for (int i = 0; i < 16; i++) wr[i] = __builtin_amdgcn_raw_buffer_load_b128(rq, i < 8 ? vo0 : vo1, (i & 7) * HD * 2, WQAUX);
  }
  constexpr unsigned KVB = (unsigned)((size_t)NB * NH * SK * DH * 2);
  const __amdgpu_buffer_rsrc_t rk = mkrs(kc, KVB), rv = mkrs(vc, KVB);
  const unsigned lo = (unsigned)(g * 256 + c * 16);
  const unsigned hb0 = ((unsigned)((b0 + e0) * NH + h) << 20) + (k == 1 ? (unsigned)SS << 12 : 0u);
  const unsigned hb1 = ((unsigned)((b0 + 1 - e0) * NH + h) << 20) + ((unsigned)(2 * SS - 256) << 12);
  auto soff = [&](int x) { return ((k == 1 && x >= 256 - SS) ? hb1 : hb0) + ((unsigned)x << 12); };
  auto ld4 = [&](u32x4 (&d)[4], __amdgpu_buffer_rsrc_t r, unsigned so) {
#pragma unroll
    for (int u = 0; u < 4; u++) d[u] = __builtin_amdgcn_raw_buffer_load_b128(r, lo + u * 1024, so, KVAUX);
  };
  u32x4 kr[4], vr[4], ke[4], ve[4];
  ld4(kr, rk, soff(w));
  ld4(vr, rv, soff(w));
  u64* eps = (u64*)(lds + 33808);
  u64 epv = 0;
  if (t < 3) epv = ld_u64(ws + (t == 0 ? WS_EPA : WS_EPB + 256 * (b0 + (t == 1 ? e0 : 1 - e0))));
  TR(56);
  float* red = (float*)lds;
  unsigned* qs = (unsigned*)(lds + 32768);
  unsigned* cnt0 = (unsigned*)(lds + 33792);
  unsigned* cntd = (unsigned*)(lds + 33796);
  float* rec0 = (float*)(lds + 36864);
  float* mlr0 = (float*)(lds + 40960);
  char* prv = lds + 45056 + w * 512;
  float* recd = (float*)(lds + 65536);
  float* mld = (float*)(lds + 98304);
  const unsigned cnta = (unsigned)(uintptr_t)(__attribute__((address_space(3))) char*)cntd;
  float* mxd = (float*)(lds + 33800);
  if (t == 0) { *cnt0 = 0u; *cntd = 8u * (RD + 1); mxd[0] = -INFINITY; mxd[1] = -INFINITY; }
  if (t < 64 * (NP4 - NP + 1)) *(f32x2*)(recd + (NP - 1 + (t >> 6)) * RST + 2 * l) = f32x2{0.f, 0.f};
  {
    float a0[8], a1[8];
#pragma unroll
    for (int j = 0; j < 8; j++) { a0[j] = 0.f; a1[j] = 0.f; }
#pragma unroll
    for (int i = 0; i < 16; i += 2) {
      const unsigned x0 = xr[0][i >> 3][(i >> 1) & 3], x1 = xr[1][i >> 3][(i >> 1) & 3];
#pragma unroll
      for (int j = 0; j < 4; j++) {
        const unsigned pl = __builtin_amdgcn_perm(wr[i + 1][j], wr[i][j], 0x05040100u);
        const unsigned ph = __builtin_amdgcn_perm(wr[i + 1][j], wr[i][j], 0x07060302u);
        a0[2 * j] = dot2(pl, x0, a0[2 * j]); a0[2 * j + 1] = dot2(ph, x0, a0[2 * j + 1]);
        a1[2 * j] = dot2(pl, x1, a1[2 * j]); a1[2 * j + 1] = dot2(ph, x1, a1[2 * j + 1]);
      }
    }
    TR(57);
#pragma unroll
    for (int j = 0; j < 8; j++) asm volatile("" : "+v"(a0[j]), "+v"(a1[j]));
    __builtin_amdgcn_sched_barrier(0);
    ld4(ke, rk, soff(w + 8));
    ld4(ve, rv, soff(w + 8));
    __builtin_amdgcn_sched_barrier(0);
    float* rp = red + (w * 4 + g) * 256 + c * 8;
    *(f32x4*)(rp) = f32x4{a0[0], a0[1], a0[2], a0[3]};
    *(f32x4*)(rp + 4) = f32x4{a0[4], a0[5], a0[6], a0[7]};
    *(f32x4*)(rp + 128) = f32x4{a1[0], a1[1], a1[2], a1[3]};
    *(f32x4*)(rp + 132) = f32x4{a1[4], a1[5], a1[6], a1[7]};
  }
  __syncthreads();
  {
    const int o = t >> 1, hf = t & 1;
    float s = 0.f;
#pragma unroll
    for (int i = 0; i < 16; i++) s += red[(hf * 16 + i) * 256 + o];
    s += dpp<0xB1>(s);
    const float sn = dpp<0x4E>(s);
    if ((t & 3) == 0) qs[o >> 1] = cvtpk(s, sn);
    if (t < 3) eps[t] = epv;
  }
  __syncthreads();
  const u64 epa = uni64(eps[0]), tg0 = next_tag(uni64(eps[1])), tg1 = next_tag(uni64(eps[2]));
  u32x4 q = *(const u32x4*)(qs + e0 * 64 + c * 4);
  const u32x4 q1 = *(const u32x4*)(qs + (1 - e0) * 64 + c * 4);
  TR(1);
  float m = -INFINITY, lsum = 0.f, acc[8];
#pragma unroll
  for (int j = 0; j < 8; j++) acc[j] = 0.f;
  float s_[4]; f32x2 vf[4][4];
  auto part0 = [&](u32x4 (&ks)[4]) { item_dots(ks, q, s_); };
  auto part1 = [&](u32x4 (&vs)[4]) { item_unpack(vs, vf); };
  auto part2 = [&]() {
    float s[4];
#pragma unroll
    for (int u = 0; u < 4; u++) s[u] = rbf(rbf(sum16(s_[u])) * SCALE);
    const float mx = fmaxf(fmaxf(m, fmaxf(s[0], s[1])), fmaxf(s[2], s[3]));
    const float alpha = fexp2((m - mx) * LOG2E);
    m = mx;
    float p[4];
#pragma unroll
    for (int u = 0; u < 4; u++) p[u] = fexp2((s[u] - mx) * LOG2E);
    lsum = lsum * alpha + ((p[0] + p[1]) + (p[2] + p[3]));
#pragma unroll
    for (int j = 0; j < 8; j++) acc[j] *= alpha;
#pragma unroll
    for (int u = 0; u < 4; u++)
#pragma unroll
      for (int j = 0; j < 4; j++) {
        acc[2 * j]     = fmaf(p[u], vf[u][j][0], acc[2 * j]);
        acc[2 * j + 1] = fmaf(p[u], vf[u][j][1], acc[2 * j + 1]);
      }
  };
  auto front = [&](__amdgpu_buffer_rsrc_t rk2, __amdgpu_buffer_rsrc_t rv2, unsigned so) {
    __builtin_amdgcn_sched_barrier(0);
    ld4(kr, rk2, so);
    __builtin_amdgcn_sched_barrier(0);
    part1(vr);
    __builtin_amdgcn_sched_barrier(0);
    ld4(vr, rv2, so);
    __builtin_amdgcn_sched_barrier(0);
  };
  auto put_partial = [&](f32x2 r2, float mw, float ll, float* rec_, float* ml_, float* mx_) {
    *(f32x2*)(rec_ + c * 8 + g * 2) = r2;
    if (l == 0) {
      *(f32x2*)ml_ = f32x2{mw, ll};
      asm volatile("ds_max_f32 %0, %1" :: "v"((unsigned)(uintptr_t)(__attribute__((address_space(3))) float*)mx_), "v"(mw) : "memory");
    }
  };
  auto wave_partial = [&](float* rec_, float* ml_, float* mx_) {
    const float mw = xrow_max(m);
    const float fq = fexp2((m - mw) * LOG2E);
    const float ll = xrow_sum(lsum * fq);
    float o[8];
#pragma unroll
    for (int j = 0; j < 8; j++) o[j] = acc[j] * fq;
    put_partial(rows_sum(o), mw, ll, rec_, ml_, mx_);
  };
  auto part2c = [&](float* rec_, float* ml_) {
    float mx, ll;
    const f32x2 r2 = item_partial(s_, vf, mx, ll);
    put_partial(r2, mx, ll, rec_, ml_, mxd);
  };
  bool sw = k != 1;
  auto do_switch = [&]() {
    wave_partial(rec0 + w * 128, mlr0 + w * 2, mxd + 1);
    asm volatile("s_waitcnt lgkmcnt(0)" ::: "memory");
    if (l == 0) asm volatile("ds_add_u32 %0, %1" :: "v"(cnta - 4u), "v"(1u) : "memory");
    m = -INFINITY; lsum = 0.f;
#pragma unroll
    for (int j = 0; j < 8; j++) acc[j] = 0.f;
    q = q1; sw = true;
  };
  if (w >= 4 && PRIOF > 0) __builtin_amdgcn_s_setprio(1);
  part0(kr);
  front(rk, rv, soff(w + 16));
  part2();
  part0(ke); part1(ve); part2();
  int x = w + 16;
  const int rg = l >> 3, cc = l & 7;
  const int wc = w, rw0 = 0;
  const int rw = WO0 + WOS * (h >> 3);
  const int re = k != 1 ? -1 : rw + 2 > RE ? rw + 2 : RE;
  u32x4 wo[16];
  __builtin_amdgcn_sched_barrier(0);
#pragma unroll
  for (int i = 0; i < 16; i++) asm volatile("" : "=v"(wo[i]));
  const int nseg = k == 1 ? 2 : 1, sp = k == 1 ? 1 : 0;
  auto store_off = [&](int sg) {
    const int b = b0 + (sg ? 1 - e0 : e0);
    const unsigned og_ = (unsigned)(WS_PBG + ((size_t)(b * 64 + h * 2 + sp) * DM + wc * 64 + cc * 8 + g * 2) * 8);
    const unsigned om_ = (unsigned)(WS_PBML + (size_t)(b * 64 + h * 2 + sp) * 16);
    return (l & 8) == 0 ? og_ : t == 8 ? om_ : 0x7ffffff0u;
  };
  const unsigned apo = (unsigned)((c >> 3) * 256 + (g * 32 + ((c >> 2) & 1) * 16) * 2);
  auto project = [&](const char* ap, float M, float L, bool lred) {
    const u32x4 a0 = *(const u32x4*)(ap + rw0 * 16), a1 = *(const u32x4*)(ap + 16 - rw0 * 16);
    f32x4 de = {0.f, 0.f, 0.f, 0.f}, dq = {0.f, 0.f, 0.f, 0.f};
#pragma unroll
    for (int p = 0; p < 8; p++) {
      const unsigned d = p < 4 ? a0[p & 3] : a1[p & 3];
      const u32x4 af = {(c & 3) == 0 ? d : 0u, (c & 3) == 1 ? d : 0u, (c & 3) == 2 ? d : 0u, (c & 3) == 3 ? d : 0u};
      de = __builtin_amdgcn_mfma_f32_16x16x32_bf16(__builtin_bit_cast(bf16x8, af), __builtin_bit_cast(bf16x8, wo[2 * p]), de, 0, 0, 0);
      dq = __builtin_amdgcn_mfma_f32_16x16x32_bf16(__builtin_bit_cast(bf16x8, af), __builtin_bit_cast(bf16x8, wo[2 * p + 1]), dq, 0, 0, 0);
    }
    if (lred && w == 0) L = xrow_sum(sum16(L));
    const bool val = (g & 1) == ((l >> 3) & 1);
    float r4[4], r2[2];
#pragma unroll
    for (int j = 0; j < 4; j++) {
      const float u0 = (j & 1) ? dq[j >> 1] : de[j >> 1], u1 = (j & 1) ? dq[2 + (j >> 1)] : de[2 + (j >> 1)];
      auto sx = __builtin_amdgcn_permlane32_swap(__float_as_uint(u0), __float_as_uint(u1), false, false);
      const float v = __uint_as_float(sx[0]) + __uint_as_float(sx[1]);
      r4[j] = val ? v : 0.f;
    }
#pragma unroll
    for (int j = 0; j < 2; j++) {
      auto sx = __builtin_amdgcn_permlane16_swap(__float_as_uint(r4[j]), __float_as_uint(r4[j + 2]), false, false);
      const float v = __uint_as_float(sx[0]) + __uint_as_float(sx[1]);
      r2[j] = v + dpp<0x128>(v);
    }
    const bool lo8 = (l & 8) == 0;
    return f32x2{lo8 ? r2[0] : M, lo8 ? r2[1] : L};
  };
  auto publish = [&](f32x2 v, u64 tg, unsigned sto) {
    st16u_sc1(rs, sto, u32x4{__float_as_uint(v[0]), (unsigned)tg, __float_as_uint(v[1]), (unsigned)(tg >> 32)});
  };
  f32x2 seg0 = {0.f, 0.f};
  auto events = [&](int rd) {
    if (rd == PRIOF) __builtin_amdgcn_s_setprio(0);
    if (!sw && x >= 256 - SS) do_switch();
    if (rd == rw + 2) {
#pragma unroll
      for (int p = 0; p < 8; p++) {
#pragma unroll
        for (int i = 0; i < 4; i++) {
          const unsigned u0 = wo[2 * p][i], u1 = wo[2 * p + 1][i];
          wo[2 * p][i] = __builtin_amdgcn_perm(u1, u0, 0x05040100u);
          wo[2 * p + 1][i] = __builtin_amdgcn_perm(u1, u0, 0x07060302u);
        }
        asm volatile("" : "+v"(wo[2 * p]), "+v"(wo[2 * p + 1]));
      }
    }
    if (rd == re) {
    for (unsigned spins = 0; __hip_atomic_load(cnt0, __ATOMIC_RELAXED, __HIP_MEMORY_SCOPE_WORKGROUP) != 8u && spins < SPIN_CAP; ++spins)
      __builtin_amdgcn_s_sleep(1);
    asm volatile("" ::: "memory");
    f32x4 q4[4];
#pragma unroll
    for (int i = 0; i < 4; i++) q4[i] = *(const f32x4*)(mlr0 + i * 4);
    const float M = mxd[1];
    float L = 0.f;
    f32x2 a2 = {0.f, 0.f};
#pragma unroll
    for (int i = 0; i < 8; i++) {
      const float fw = fexp2((q4[i >> 1][(i & 1) * 2] - M) * LOG2E);
      L = fmaf(q4[i >> 1][(i & 1) * 2 + 1], fw, L);
      a2 += *(const f32x2*)(rec0 + i * 128 + 2 * l) * fw;
    }
    const unsigned hi = cvtpk(a2[0], a2[1]);
    *(unsigned*)(prv + l * 4) = hi;
    *(unsigned*)(prv + 256 + l * 4) = cvtpk(a2[0] - bf_lo(hi), a2[1] - bf_hi(hi));
    seg0 = project(prv + apo, M, L, false);
    }
    if (rd == rw) {
      const __amdgpu_buffer_rsrc_t ro = mkrs(woB, (ABL & 1) ? 0 : HD * DM * 2);
      const unsigned vo = (unsigned)(((h * DH + rg * 16) * DM + wc * 64 + cc * 8) * 2);
      const unsigned vo0 = vo + rw0 * 8 * DM * 2, vo1 = vo + (1 - rw0) * 8 * DM * 2;
#pragma unroll
      for (int i = 0; i < 16; i++) wo[i] = __builtin_amdgcn_raw_buffer_load_b128(ro, i < 8 ? vo0 : vo1, (i & 7) * DM * 2, WOAUX);
    }
  };
#pragma unroll 1
  for (; x < 8 * RD; x += 8) {
    const int rd = x >> 3;
    events(rd);
    part0(kr);
    front(rk, rv, soff(x + 8));
    part2();
    TR(2 + rd);
  }
  int nit = RD;
  wave_partial(recd + w * RST, mld + w * 2, mxd);
  unsigned o_ml = 98304u + (l < NP ? l : 0) * 8, o_rv = 65536u + (g * RST + w * 16 + c) * 4, o_fw = 45056u + w * 512 + ((l & 3) * 16 + (l >> 2)) * 4;
  unsigned o_fr = 45056u + w * 512 + g * 64, o_oi = (g < 2 && (c & 1) == 0) ? 41216u + g * 256 + (w * 16 + c) * 2 : 45056u + w * 512 + 256 + l * 4;
  unsigned o_ap = 41216u + apo, o_st = store_off(nseg - 1);
  unsigned o_s0 = k == 1 ? store_off(0) : 0x7ffffff0u;
  asm volatile("" : "+v"(o_s0));
  asm volatile("" : "+v"(o_ml), "+v"(o_rv), "+v"(o_fw), "+v"(o_fr), "+v"(o_oi), "+v"(o_ap), "+v"(o_st));
  {
    {
#pragma unroll 1
      while (x < NI) {
        unsigned dr; u64 sv;
        asm volatile("s_mov_b64 %1, exec\n\ts_mov_b64 exec, 1\n\tds_add_rtn_u32 %0, %2, %3\n\ts_mov_b64 exec, %1"
                     : "=&v"(dr), "=&s"(sv) : "v"(cnta), "v"(1u), "v"(kr[0][0]) : "memory");
        __builtin_amdgcn_sched_barrier(0);
        part0(kr);
        asm volatile("s_waitcnt lgkmcnt(0)" : "+v"(dr) :: "memory");
        const int xn = __builtin_amdgcn_readfirstlane(dr);
        if (h >= 24 && j6 == 2 && xn == NI - GI) { TR(45); if (l == 0) st_u64(ws + WS_FP + (h - 24) * 8, (next_tag(epa) << 32) | clk()); }
        const bool ok = xn < NI;
        front(mkrs(kc, ok ? KVB : 0u), mkrs(vc, ok ? KVB : 0u), ok ? soff(xn) : 0u);
        part2c(recd + (x - 8 * RD + 8) * RST, mld + (x - 8 * RD + 8) * 2);
        x = xn;
        TR(2 + nit); nit++;
      }
      TR(40);
      asm volatile("s_waitcnt lgkmcnt(0)" ::: "memory");
      __syncthreads();
      TR(41);
    }
    {
      f32x2 mli = *(const f32x2*)(lds + o_ml);
      float rv_[NP4 / 4];
#pragma unroll
      for (int r = 0; r < NP4 / 4; r++) rv_[r] = *(const float*)(lds + o_rv + r * 4 * RST * 4);
      if (l >= NI - 8 * RD + 8) mli = f32x2{-INFINITY, 0.f};
      const float M = *mxd;
      const float fi = fexp2((mli[0] - M) * LOG2E);
      *(float*)(lds + o_fw) = fi;
      f32x4 f4[(NP4 / 4 + 3) / 4];
#pragma unroll
      for (int i = 0; i < (NP4 / 4 + 3) / 4; i++) f4[i] = *(const f32x4*)(lds + o_fr + i * 16);
      float a = 0.f;
#pragma unroll
      for (int r = 0; r < NP4 / 4; r++) a = fmaf(f4[r >> 2][r & 3], rv_[r], a);
      a = xrow_sum(a);
      const float an = dpp<0xB1>(a);
      const unsigned hi = cvtpk(a, an);
      const unsigned lw = cvtpk(a - bf_lo(hi), an - bf_hi(hi));
      *(unsigned*)(lds + o_oi) = g ? lw : hi;
      __syncthreads();
      const f32x2 pv = project(lds + o_ap, M, fi * mli[1], true);
      publish(pv, k == 1 ? tg1 : tg0, o_st);
      publish(seg0, tg0, o_s0);
    }
    TR(42);
    if (w < nseg) {
      const int sg = w;
      const int b = b0 + (sg ? 1 - e0 : e0);
      const int j64 = h * 2 + sp;
      const u64 tg = sg ? tg1 : tg0;
      const unsigned ta = (unsigned)tg, tb = (unsigned)(tg >> 32);
      const unsigned oml = (unsigned)(WS_PBML + (size_t)(b * 64 + l) * 16);
      const unsigned om4 = (unsigned)(WS_PBML + (size_t)(b * 64 + (l >> 2)) * 16);
      const unsigned og = (unsigned)(WS_PBG + ((size_t)(b * 64 + (l >> 2)) * DM + j64 * 8) * 8 + (l & 3) * 16);
      u32x4 ml[4], p[4];
      __builtin_amdgcn_s_sleep(6);
#pragma unroll 1
      for (unsigned spins = 0; spins < SPIN_CAP; ++spins) {
        asm volatile("" ::: "memory");
#pragma unroll
        for (int i = 0; i < 4; i++) ml[i] = ld16u_sc1(rs, om4 + i * 256);
#pragma unroll
        for (int i = 0; i < 4; i++) p[i] = ld16u_sc1(rs, og + i * 16 * DM * 8);
        int n = 0;
#pragma unroll
        for (int i = 0; i < 4; i++) n += __popcll(__ballot(ml[i][1] == ta && ml[i][3] == tb && p[i][1] == ta && p[i][3] == tb));
        if (n == 256 || ((HAB & 64) && n >= 192)) break;
#pragma unroll 1
        for (; n < ((HAB & 64) ? 4 * 44 : 4 * PTHR) && spins < SPIN_CAP; ++spins) {
          __builtin_amdgcn_s_sleep(PSL);
          asm volatile("" ::: "memory");
          const u32x4 m1 = ld16u_sc1(rs, oml);
          n = 4 * __popcll(__ballot(m1[1] == ta && m1[3] == tb));
        }
      }
      float v0 = 0.f, v1 = 0.f;
#pragma unroll
      for (int i = 0; i < 4; i++) {
        const float mm = __uint_as_float(ml[i][0]);
        const float Mh = fmaxf(mm, dpp<0x141>(mm));
        const float fq = fexp2((mm - Mh) * LOG2E);
        float L = fq * __uint_as_float(ml[i][2]);
        L += dpp<0x141>(L);
        const float wgt = fq * __builtin_amdgcn_rcpf(L);
        v0 = fmaf(__uint_as_float(p[i][0]), wgt, v0);
        v1 = fmaf(__uint_as_float(p[i][2]), wgt, v1);
      }
      v0 += dpp<0x128>(v0); v0 += dpp<0x124>(v0); v0 = xrow_sum(v0);
      v1 += dpp<0x128>(v1); v1 += dpp<0x124>(v1); v1 = xrow_sum(v1);
      if (l < 4) {
        *(unsigned*)(out + b * 1024 + 512 + j64 * 8 + l * 2) = cvtpk(v0, v1);
        if (j64 == 0 && l == 0) st_u64(ws + WS_EPB + 256 * b, tg);
      }
    }
  }
  TR(43);
  TRDUMP();
}

__device__ __forceinline__ unsigned lds_off(int row, int ch) {
  return 256 * row + 16 * (ch ^ (((row & 3) << 2) | ((row >> 2) & 3)));
}

__device__ __forceinline__ void role_A1(int h, const __hip_bfloat16* __restrict__ xA, const __hip_bfloat16* __restrict__ wqA,
                                        const __hip_bfloat16* __restrict__ wuk, const __hip_bfloat16* __restrict__ wuv,
                                        const __hip_bfloat16* __restrict__ woA, char* __restrict__ ws,
                                        __amdgpu_buffer_rsrc_t rs, __hip_bfloat16* __restrict__ out, char* lds, unsigned trv) {
  const int t = threadIdx.x, l = t & 63;
  const int w = __builtin_amdgcn_readfirstlane(t >> 6);
  const int g = l >> 4, c = l & 15;
  float* red = (float*)(lds + 16384);
  const unsigned vo = (unsigned)(((w * 64 + g * 16) * HD + h * DH + c * 8) * 2);
  u64 tag;
  {
    u32x4 xr[NB][2], wr[16], wk[16];
    const __amdgpu_buffer_rsrc_t rx = mkrs(xA, NB * DM * 2);
    const unsigned xo = (unsigned)((w * 64 + g * 16) * 2);
#pragma unroll
    for (int bb = 0; bb < NB; bb++)
#pragma unroll
      for (int i = 0; i < 2; i++) xr[bb][i] = __builtin_amdgcn_raw_buffer_load_b128(rx, xo + bb * DM * 2 + i * 16, 0, 0);
    const __amdgpu_buffer_rsrc_t rq = mkrs(wqA, DM * HD * 2);
#pragma unroll
    for (int i = 0; i < 16; i++) wr[i] = __builtin_amdgcn_raw_buffer_load_b128(rq, vo, i * HD * 2, NT);
    u64 epv = 0;
    if (t == 0) epv = ld_u64(ws + WS_EPA);
    unsigned* qs = (unsigned*)lds;
    unsigned short* qo = (unsigned short*)(lds + 4096);
    float acc[NB][8];
#pragma unroll
    for (int bb = 0; bb < NB; bb++)
#pragma unroll
      for (int j = 0; j < 8; j++) acc[bb][j] = 0.f;
#pragma unroll
    for (int i = 0; i < 16; i += 2) {
#pragma unroll
      for (int j = 0; j < 4; j++) {
        const unsigned pl = __builtin_amdgcn_perm(wr[i + 1][j], wr[i][j], 0x05040100u);
        const unsigned ph = __builtin_amdgcn_perm(wr[i + 1][j], wr[i][j], 0x07060302u);
#pragma unroll
        for (int bb = 0; bb < NB; bb++) {
          const unsigned x = xr[bb][i >> 3][(i >> 1) & 3];
          acc[bb][2 * j] = dot2(pl, x, acc[bb][2 * j]);
          acc[bb][2 * j + 1] = dot2(ph, x, acc[bb][2 * j + 1]);
        }
      }
    }
    float* rp = red + (w * 4 + g) * 512 + c * 8;
#pragma unroll
    for (int bb = 0; bb < NB; bb++) {
      *(f32x4*)(rp + bb * 128) = f32x4{acc[bb][0], acc[bb][1], acc[bb][2], acc[bb][3]};
      *(f32x4*)(rp + bb * 128 + 4) = f32x4{acc[bb][4], acc[bb][5], acc[bb][6], acc[bb][7]};
    }
    __syncthreads();
    {
      const __amdgpu_buffer_rsrc_t rku = mkrs(wuk, DC * HD * 2);
#pragma unroll
      for (int i = 0; i < 16; i++) wk[i] = __builtin_amdgcn_raw_buffer_load_b128(rku, vo, i * HD * 2, NT);
    }
    {
      float s = 0.f;
#pragma unroll
      for (int i = 0; i < 32; i++) s += red[i * 512 + t];
      const float sn = dpp<0xB1>(s);
      if ((t & 1) == 0) qs[t >> 1] = cvtpk(s, sn);
      if (t == 0) *(u64*)(lds + 2048) = epv;
    }
    __syncthreads();
    tag = next_tag(uni64(*(const u64*)(lds + 2048)));
    {
      u32x4 qq[NB];
#pragma unroll
      for (int bb = 0; bb < NB; bb++) qq[bb] = *(const u32x4*)(qs + bb * 64 + c * 4);
#pragma unroll
      for (int i = 0; i < 16; i++) {
        float s[NB];
#pragma unroll
        for (int bb = 0; bb < NB; bb++) {
          float v = dot2(wk[i][0], qq[bb][0], 0.f);
          v = dot2(wk[i][1], qq[bb][1], v);
          v = dot2(wk[i][2], qq[bb][2], v);
          v = dot2(wk[i][3], qq[bb][3], v);
          s[bb] = sum16(v);
        }
        const float v = (c & 2) ? ((c & 1) ? s[3] : s[2]) : ((c & 1) ? s[1] : s[0]);
        if (c < NB) qo[c * 512 + w * 64 + g * 16 + i] = (unsigned short)(cvtpk(v, v) & 0xffff);
      }
    }
    __syncthreads();
    if (t < 256) {
      const int bb = t >> 6, ch = t & 63;
      st16u_sc1(rs, ((HAB & 16) ? 0x70000000u : 0u) + (unsigned)(WS_QABS + ((size_t)(bb * NH + h) * DC + ch * 8) * 2), *(const u32x4*)(qo + bb * 512 + ch * 8));
    }
    TR(1);
    signal_flag(ws + WS_F1 + h * 8, tag);
    TR(2);
  }
  const unsigned ta = (unsigned)tag, tb = (unsigned)(tag >> 32);
  {
    u32x4 bv[8][2], bo[8][2];
    u32x4 pre[2][8], wo[16];
    {
      const __amdgpu_buffer_rsrc_t ruv = mkrs(wuv, (ABL & 4) ? 0 : DC * HD * 2);
      const __amdgpu_buffer_rsrc_t rwo = mkrs(woA, (ABL & 4) ? 0 : HD * DM * 2);
      const unsigned wvo = (unsigned)(((h * DH + (l >> 3) * 16) * DM + w * 64 + (l & 7) * 8) * 2);
#pragma unroll
      for (int i = 0; i < 16; i++) pre[i >> 3][i & 7] = __builtin_amdgcn_raw_buffer_load_b128(ruv, vo - g * 8 * HD * 2, ((i >> 3) * 32 + (i & 7)) * HD * 2, NT);
#pragma unroll
      for (int i = 0; i < 16; i++) wo[i] = __builtin_amdgcn_raw_buffer_load_b128(rwo, wvo, i * DM * 2, NT);
    }
#pragma unroll
    for (int j = 0; j < 8; j++)
#pragma unroll
      for (int s = 0; s < 2; s++) {
#pragma unroll
        for (int e = 0; e < 4; e++)
          bv[j][s][e] = __builtin_amdgcn_perm(pre[s][2 * e + 1][j >> 1], pre[s][2 * e][j >> 1], (j & 1) ? 0x07060302u : 0x05040100u);
        asm volatile("" : "+v"(bv[j][s]));
      }
#pragma unroll
    for (int j = 0; j < 8; j++)
#pragma unroll
      for (int s = 0; s < 2; s++) {
#pragma unroll
        for (int e = 0; e < 4; e++)
          bo[j][s][e] = __builtin_amdgcn_perm(wo[8 * s + 2 * e + 1][j >> 1], wo[8 * s + 2 * e][j >> 1], (j & 1) ? 0x07060302u : 0x05040100u);
        asm volatile("" : "+v"(bo[j][s]));
      }
    constexpr int LAS = 1040;
    char* la = lds;
    unsigned short* cs = (unsigned short*)(lds + 16384);
    float* outs = (float*)(lds + 20480);
    float* cred = (float*)(lds + 32768);
    {
    {
    TR(3);
    }
    unsigned po = (unsigned)(WS_PAO + ((size_t)(w * 4 * NH + h) * DC + l * 8) * 2);
    unsigned pc = (unsigned)(WS_PAC + (size_t)((w * 4 + g) * NH + h) * 8);
    unsigned lw_ = (unsigned)(65536 + ((w & 1) * 16 + (w >> 1) * 4) * LAS + l * 16), lr_ = (unsigned)(65536 + c * LAS + (w * 64 + g * 8) * 2);
    asm volatile("" : "+v"(po), "+v"(pc), "+v"(lw_), "+v"(lr_));
    const unsigned tw = (h & 1) ? tb : ta;
    const unsigned tg = wait_gate(ws + WS_FP, ta, (unsigned*)(lds + 8192));
    TR(4);
    wait_until(tg + GD);
    TR(7);
    {
      u32x4 pr[4];
#pragma unroll 1
      for (unsigned spins = 0; spins < SPIN_CAP; ++spins) {
        asm volatile("" ::: "memory");
        const u32x2 ck = __builtin_amdgcn_raw_buffer_load_b64(rs, pc, 0, SC1);
#pragma unroll
        for (int i = 0; i < 4; i++) pr[i] = __builtin_amdgcn_raw_buffer_load_b128(rs, po, i * NH * DC * 2, SC1);
#pragma unroll
        for (int i = 0; i < 4; i++) *(u32x4*)(lds + lw_ + i * LAS) = pr[i];
        if (__all(row_sums(pr) == ck[0] && ck[1] == tw)) break;
        __builtin_amdgcn_s_sleep(RSL);
      }
      TR(5);
      __syncthreads();
      u32x4 af[2][2];
#pragma unroll
      for (int tl = 0; tl < 2; tl++)
#pragma unroll
        for (int s = 0; s < 2; s++) af[tl][s] = *(const u32x4*)(lds + lr_ + tl * 16 * LAS + s * 64);
      f32x4 dd[8];
#pragma unroll
      for (int j = 0; j < 8; j++)
        dd[j] = __builtin_amdgcn_mfma_f32_16x16x32_bf16(__builtin_bit_cast(bf16x8, af[0][0]), __builtin_bit_cast(bf16x8, bv[j][0]), f32x4{0.f, 0.f, 0.f, 0.f}, 0, 0, 0);
#pragma unroll
      for (int j = 0; j < 8; j++)
        dd[j] = __builtin_amdgcn_mfma_f32_16x16x32_bf16(__builtin_bit_cast(bf16x8, af[0][1]), __builtin_bit_cast(bf16x8, bv[j][1]), dd[j], 0, 0, 0);
#pragma unroll
      for (int j = 0; j < 8; j++)
        dd[j] = __builtin_amdgcn_mfma_f32_16x16x32_bf16(__builtin_bit_cast(bf16x8, af[1][0]), __builtin_bit_cast(bf16x8, bv[j][0]), dd[j], 0, 0, 0);
#pragma unroll
      for (int j = 0; j < 8; j++)
        dd[j] = __builtin_amdgcn_mfma_f32_16x16x32_bf16(__builtin_bit_cast(bf16x8, af[1][1]), __builtin_bit_cast(bf16x8, bv[j][1]), dd[j], 0, 0, 0);
      f32x4 v[2];
#pragma unroll
      for (int j = 0; j < 8; j++) v[j >> 2][j & 3] = (dd[j][0] + dd[j][1]) + (dd[j][2] + dd[j][3]);
      *(f32x4*)(cred + (w * 4 + g) * 128 + c * 8) = v[0];
      *(f32x4*)(cred + (w * 4 + g) * 128 + c * 8 + 4) = v[1];
    }
    __syncthreads();
    {
      float s = 0.f;
#pragma unroll
      for (int i = 0; i < 8; i++) s += cred[i * 512 + t];
      const float sn = dpp<0xB1>(s);
      if ((t & 1) == 0) *(unsigned*)(cs + t) = cvtpk(s, sn);
    }
    __syncthreads();
    {
      const char* ap = (const char*)cs + ((c >> 1) & 3) * 256 + (g * 32 + (c & 1) * 16) * 2;
      const u32x4 a0 = *(const u32x4*)(ap), a1 = *(const u32x4*)(ap + 16);
      f32x4 dd[8];
#pragma unroll
      for (int j = 0; j < 8; j++)
        dd[j] = __builtin_amdgcn_mfma_f32_16x16x32_bf16(__builtin_bit_cast(bf16x8, a0), __builtin_bit_cast(bf16x8, bo[j][0]), f32x4{0.f, 0.f, 0.f, 0.f}, 0, 0, 0);
#pragma unroll
      for (int j = 0; j < 8; j++)
        dd[j] = __builtin_amdgcn_mfma_f32_16x16x32_bf16(__builtin_bit_cast(bf16x8, a1), __builtin_bit_cast(bf16x8, bo[j][1]), dd[j], 0, 0, 0);
      f32x4 v4[2];
#pragma unroll
      for (int j = 0; j < 8; j++) {
        const float u0 = (l & 8) ? dd[j][1] : dd[j][0], u1 = (l & 8) ? dd[j][3] : dd[j][2];
        const float keep = (l & 8) ? u1 : u0, give = (l & 8) ? u0 : u1;
        v4[j >> 2][j & 3] = keep + dpp<0x128>(give);
      }
      if (g < 2) {
        float* op = outs + (g * 2 + ((l >> 3) & 1)) * 512 + w * 64 + (l & 7) * 8;
        *(f32x4*)op = v4[0];
        *(f32x4*)(op + 4) = v4[1];
      }
    }
    __syncthreads();
    {
      const f32x2 va = *(const f32x2*)(outs + t * 2), vb = *(const f32x2*)(outs + 1024 + t * 2);
      const unsigned oo = ((HAB & 4) ? 0x70000000u : 0u) + (unsigned)(WS_PA + ((size_t)((t >> 8) * NH + h) * DM + (t & 255) * 2) * 8);
      st16u_sc1(rs, oo, u32x4{__float_as_uint(va[0]), ta, __float_as_uint(va[1]), tb});
      st16u_sc1(rs, oo + 2 * NH * DM * 8, u32x4{__float_as_uint(vb[0]), ta, __float_as_uint(vb[1]), tb});
    }
  TR(6);
  if (w < 4) {
    const int bb = h >> 3, n0 = (h & 7) * 64 + w * 16;
    const unsigned og = ((HAB & 4) ? 0x70000000u : 0u) + (unsigned)(WS_PA + ((size_t)(bb * NH + (l >> 3)) * DM + n0) * 8 + (l & 7) * 16);
    u32x4 p[4];
    __builtin_amdgcn_s_sleep(T4D);
#pragma unroll 1
    for (unsigned spins = 0; spins < SPIN_CAP; ++spins) {
      asm volatile("" ::: "memory");
#pragma unroll
      for (int i = 0; i < 4; i++) p[i] = ld16u_sc1(rs, og + i * 8 * DM * 8);
      bool ok = true;
#pragma unroll
      for (int i = 0; i < 4; i++) ok = ok && p[i][1] == ta && p[i][3] == tb;
      if (__all(ok)) break;
      __builtin_amdgcn_s_sleep(T4S);
    }
    float v0 = (__uint_as_float(p[0][0]) + __uint_as_float(p[1][0])) + (__uint_as_float(p[2][0]) + __uint_as_float(p[3][0]));
    float v1 = (__uint_as_float(p[0][2]) + __uint_as_float(p[1][2])) + (__uint_as_float(p[2][2]) + __uint_as_float(p[3][2]));
    v0 += dpp<0x128>(v0); v0 = xrow_sum(v0);
    v1 += dpp<0x128>(v1); v1 = xrow_sum(v1);
    if (l < 8) {
      *(unsigned*)(out + bb * 1024 + n0 + l * 2) = cvtpk(v0, v1);
      if (h == 0 && t == 0) st_u64(ws + WS_EPA, tag);
    }
  }
    }
  }
  TR(43);
  TRDUMP();
}

__device__ __forceinline__ void role_A2(int a, const __hip_bfloat16* __restrict__ ckv, char* __restrict__ ws,
                                        __amdgpu_buffer_rsrc_t rs, char* lds, unsigned trv) {
  const int t = threadIdx.x, l = t & 63;
  const int w = __builtin_amdgcn_readfirstlane(t >> 6);
  const int g = l >> 4, c = l & 15;
  u64 ep = 0;
  if (t == 0) ep = ld_u64(ws + WS_EPA);
  const int grp = w >> 2, wq = w & 3;
  const int b = a / SPLIT_A, sp = a % SPLIT_A;
  const int r = l & 31, hh = l >> 5;
  constexpr unsigned CB = (unsigned)((size_t)NB * SK * DC * 2);
  constexpr int NTL = KA / 64;
  const unsigned cvo = (unsigned)((g * DC + 128 * wq + c * 8) * 2);
  const unsigned cso = (unsigned)(((size_t)b * SK + sp * KA + grp * 32) * DC * 2);
  auto load_tile = [&](u32x4 (&pr)[8], int tile) {
    const bool ok = tile < NTL;
    const __amdgpu_buffer_rsrc_t rc = mkrs(ckv, (ok && !(ABL & 2)) ? CB : 0u);
    const unsigned so = ok ? cso + (unsigned)((tile + sp) & (NTL - 1)) * 64 * DC * 2 : 0u;
#pragma unroll
    for (int u = 0; u < 8; u++) pr[u] = __builtin_amdgcn_raw_buffer_load_b128(rc, cvo, so + u * 4 * DC * 2, NT);
  };
  char* img = lds + w * 8192;
  float* sred = (float*)(lds + 65536 + grp * 16384);
  float* mxs = (float*)(lds + 98304 + grp * 512);
  unsigned* pbuf = (unsigned*)(lds + 102400 + grp * 2048);
  float* lall = (float*)(lds + 106496);
  const unsigned imga = (unsigned)(uintptr_t)(__attribute__((address_space(3))) char*)img;
  auto put_tile = [&](u32x4 (&pr)[8]) {
#pragma unroll
    for (int u = 0; u < 8; u++) *(u32x4*)(img + lds_off(4 * u + g, c)) = pr[u];
  };
  u32x4 pre[2][8];
  load_tile(pre[0], 0);
  load_tile(pre[1], 1);
  const u64 tag0 = next_tag(uni64(ep));
  put_tile(pre[0]);
  __builtin_amdgcn_sched_barrier(0);
  load_tile(pre[0], 2);
  TR(1);
  if (t < 64) {
#pragma unroll 1
    for (unsigned spins = 0; spins < SPIN_CAP; ++spins) {
      __builtin_amdgcn_s_sleep(32);
      if (__all(ld_u64(ws + WS_F1 + (l & 31) * 8) == tag0)) break;
    }
  }
  if (t == 0) *(u64*)(lds + 110592) = tag0;
  __syncthreads();
  const u64 tag = uni64(*(const u64*)(lds + 110592));
  const unsigned ta = (unsigned)tag, tb = (unsigned)(tag >> 32);
  TR(3);
  bf16x8 qf[8];
  {
    char* qi = lds + 65536;
    const unsigned qo = ((HAB & 16) ? 0x70000000u : 0u) + (unsigned)(WS_QABS + (size_t)b * NH * DC * 2 + t * 16);
    u32x4 qv[4];
#pragma unroll
    for (int i = 0; i < 4; i++) qv[i] = ld16u_sc1(rs, qo + i * 8192);
#pragma unroll
    for (int i = 0; i < 4; i++) *(u32x4*)(qi + (i * 8 + w) * 1040 + l * 16) = qv[i];
    __syncthreads();
#pragma unroll
    for (int s = 0; s < 8; s++) qf[s] = __builtin_bit_cast(bf16x8, *(const u32x4*)(qi + r * 1040 + (128 * wq + 16 * s + 8 * hh) * 2));
#pragma unroll
    for (int s = 0; s < 8; s++) asm volatile("" : "+v"(qf[s]));
    __syncthreads();
  }
  f32x16 o[4];
#pragma unroll
  for (int i = 0; i < 4; i++)
#pragma unroll
    for (int j = 0; j < 16; j++) o[i][j] = 0.f;
  float m = -INFINITY, lsum = 0.f;
  const int q4 = (l & 15) >> 2, p4 = l & 3, colh = g & 1;
  auto tile_body = [&](int tile, u32x4 (&nx)[8]) {
    f32x16 s;
#pragma unroll
    for (int j = 0; j < 16; j++) s[j] = 0.f;
    {
      u32x4 af[8];
#pragma unroll
      for (int ks = 0; ks < 8; ks++) af[ks] = *(const u32x4*)(img + lds_off(r, 2 * ks + hh));
      __builtin_amdgcn_sched_barrier(0);
#pragma unroll
      for (int ks = 0; ks < 8; ks++) s = __builtin_amdgcn_mfma_f32_32x32x16_bf16(__builtin_bit_cast(bf16x8, af[ks]), qf[ks], s, 0, 0, 0);
    }
    f32x4* sr = (f32x4*)sred;
#pragma unroll
    for (int k4 = 0; k4 < 4; k4++) sr[(k4 * 4 + wq) * 64 + l] = f32x4{s[4 * k4], s[4 * k4 + 1], s[4 * k4 + 2], s[4 * k4 + 3]};
    __syncthreads();
    float x[4];
    {
      const f32x4 v = (sr[(wq * 4) * 64 + l] + sr[(wq * 4 + 1) * 64 + l]) + (sr[(wq * 4 + 2) * 64 + l] + sr[(wq * 4 + 3) * 64 + l]);
#pragma unroll
      for (int e = 0; e < 4; e += 2) {
        const unsigned r1 = cvtpk(v[e], v[e + 1]);
        const unsigned r2 = cvtpk(bf_lo(r1) * SCALE, bf_hi(r1) * SCALE);
        x[e] = bf_lo(r2); x[e + 1] = bf_hi(r2);
      }
      float mq = fmaxf(fmaxf(x[0], x[1]), fmaxf(x[2], x[3]));
      auto sx = __builtin_amdgcn_permlane32_swap(__float_as_uint(mq), __float_as_uint(mq), false, false);
      mq = fmaxf(__uint_as_float(sx[0]), __uint_as_float(sx[1]));
      if (hh == 0) mxs[r * 4 + wq] = mq;
    }
    __syncthreads();
    const f32x4 m4 = *(const f32x4*)(mxs + r * 4);
    const float mn = fmaxf(fmaxf(m, fmaxf(m4[0], m4[1])), fmaxf(m4[2], m4[3]));
    const float alpha = fexp2((m - mn) * LOG2E);
    {
      const unsigned p0 = cvtpk(fexp2((x[0] - mn) * LOG2E), fexp2((x[1] - mn) * LOG2E));
      const unsigned p1 = cvtpk(fexp2((x[2] - mn) * LOG2E), fexp2((x[3] - mn) * LOG2E));
      *(u32x2*)(pbuf + l * 8 + wq * 2) = u32x2{p0, p1};
      lsum = lsum * alpha + ((bf_lo(p0) + bf_hi(p0)) + (bf_lo(p1) + bf_hi(p1)));
    }
    m = mn;
    __syncthreads();
    unsigned pk[8];
    {
      const u32x4 a0 = *(const u32x4*)(pbuf + l * 8), a1 = *(const u32x4*)(pbuf + l * 8 + 4);
      pk[0] = a0[0]; pk[1] = a0[1]; pk[2] = a0[2]; pk[3] = a0[3]; pk[4] = a1[0]; pk[5] = a1[1]; pk[6] = a1[2]; pk[7] = a1[3];
    }
    if (__any(alpha != 1.f)) {
#pragma unroll
      for (int i = 0; i < 4; i++)
#pragma unroll
        for (int j = 0; j < 16; j++) o[i][j] *= alpha;
    }
#pragma unroll
    for (int s2 = 0; s2 < 2; s2++) {
      u32x2 tv[4][2];
#pragma unroll
      for (int ct = 0; ct < 4; ct++) {
        const int ch = 4 * ct + 2 * colh + (p4 >> 1);
        const unsigned a0 = lds_off(4 * hh + q4, ch) + 8 * (p4 & 1);
        const unsigned a1 = lds_off(8 + 4 * hh + q4, ch) + 8 * (p4 & 1);
        if (s2 == 0) {
          asm volatile("ds_read_b64_tr_b16 %0, %1" : "=v"(tv[ct][0]) : "v"(imga + a0) : "memory");
          asm volatile("ds_read_b64_tr_b16 %0, %1" : "=v"(tv[ct][1]) : "v"(imga + a1) : "memory");
        } else {
          asm volatile("ds_read_b64_tr_b16 %0, %1 offset:4096" : "=v"(tv[ct][0]) : "v"(imga + a0) : "memory");
          asm volatile("ds_read_b64_tr_b16 %0, %1 offset:4096" : "=v"(tv[ct][1]) : "v"(imga + a1) : "memory");
        }
      }
      asm volatile("s_waitcnt lgkmcnt(0)" ::: "memory");
      __builtin_amdgcn_sched_barrier(0);
      u32x4 pb = {pk[4 * s2], pk[4 * s2 + 1], pk[4 * s2 + 2], pk[4 * s2 + 3]};
#pragma unroll
      for (int ct = 0; ct < 4; ct++) {
        u32x4 av = {tv[ct][0][0], tv[ct][0][1], tv[ct][1][0], tv[ct][1][1]};
        o[ct] = __builtin_amdgcn_mfma_f32_32x32x16_bf16(__builtin_bit_cast(bf16x8, av), __builtin_bit_cast(bf16x8, pb), o[ct], 0, 0, 0);
      }
    }
    put_tile(nx);
    __builtin_amdgcn_sched_barrier(0);
    load_tile(nx, tile + 3);
    TR(4 + tile);
  };
#pragma unroll 1
  for (int tile = 0; tile < NTL; tile += 2) {
    tile_body(tile, pre[1]);
    tile_body(tile + 1, pre[0]);
  }
  constexpr int OST = 516;
  float* ob = (float*)lds;
  float* mlb = (float*)(lds + 32 * OST * 4);
  lall[w * 64 + l] = lsum;
  __syncthreads();
  {
    const float* lp = lall + grp * 256 + r;
    lsum = ((lp[0] + lp[32]) + (lp[64] + lp[96])) + ((lp[128] + lp[160]) + (lp[192] + lp[224]));
  }
  if (grp == 1) {
#pragma unroll
    for (int ct = 0; ct < 4; ct++)
#pragma unroll
      for (int j4 = 0; j4 < 4; j4++)
        *(f32x4*)(ob + r * OST + 128 * wq + 32 * ct + 8 * j4 + 4 * hh) =
            f32x4{o[ct][4 * j4], o[ct][4 * j4 + 1], o[ct][4 * j4 + 2], o[ct][4 * j4 + 3]};
    if (wq == 0 && l < 32) { mlb[2 * r] = m; mlb[2 * r + 1] = lsum; }
  }
  __syncthreads();
  if (grp == 0) {
    const float m1 = mlb[2 * r], l1 = mlb[2 * r + 1];
    const float M = fmaxf(m, m1);
    const float f0 = fexp2((m - M) * LOG2E), f1 = fexp2((m1 - M) * LOG2E);
#pragma unroll
    for (int ct = 0; ct < 4; ct++)
#pragma unroll
      for (int j4 = 0; j4 < 4; j4++) {
        float* p = ob + r * OST + 128 * wq + 32 * ct + 8 * j4 + 4 * hh;
        const f32x4 o1 = *(const f32x4*)p;
        *(f32x4*)p = f32x4{o[ct][4 * j4] * f0 + o1[0] * f1, o[ct][4 * j4 + 1] * f0 + o1[1] * f1,
                           o[ct][4 * j4 + 2] * f0 + o1[2] * f1, o[ct][4 * j4 + 3] * f0 + o1[3] * f1};
      }
    if (wq == 0 && l < 32)
      st16u_sc1(rs, (unsigned)(WS_PAML + ((size_t)(b * SPLIT_A + sp) * NH + r) * 16),
                u32x4{__float_as_uint(M), ta, __float_as_uint(lsum * f0 + l1 * f1), tb});
  }
  float* wgt = mlb + 64;
  unsigned* pcs = (unsigned*)(mlb + 128);
  if (t < 256) {
    const unsigned mo = (unsigned)(WS_PAML + ((size_t)(b * SPLIT_A + (t & 7)) * NH + (t >> 3)) * 16);
    u32x4 mv;
#pragma unroll 1
    for (unsigned spins = 0; spins < SPIN_CAP; ++spins) {
      __builtin_amdgcn_s_sleep(16);
      asm volatile("" ::: "memory");
      mv = ld16u_sc1(rs, mo);
      if (__all(mv[1] == ta && mv[3] == tb)) break;
    }
    const float mm = __uint_as_float(mv[0]);
    float M = fmaxf(mm, dpp<0xB1>(mm)); M = fmaxf(M, dpp<0x4E>(M)); M = fmaxf(M, dpp<0x141>(M));
    const float f = fexp2((mm - M) * LOG2E);
    float L = f * __uint_as_float(mv[2]);
    L += dpp<0xB1>(L); L += dpp<0x4E>(L); L += dpp<0x141>(L);
    if ((t & 7) == sp) wgt[t >> 3] = f / L;
  }
  __syncthreads();
  {
    const unsigned pao = (unsigned)(WS_PAO + (size_t)(b * SPLIT_A + sp) * NH * DC * 2);
    u32x4 pv[4];
#pragma unroll
    for (int i = 0; i < 4; i++) {
      const int hd = i * 8 + w;
      const float f = wgt[hd];
      const f32x4 v0 = *(const f32x4*)(ob + hd * OST + l * 8) * f, v1 = *(const f32x4*)(ob + hd * OST + l * 8 + 4) * f;
      pv[i] = u32x4{cvtpk(v0[0], v0[1]), cvtpk(v0[2], v0[3]), cvtpk(v1[0], v1[1]), cvtpk(v1[2], v1[3])};
      asm volatile("" : "+v"(pv[i]));
    }
    {
      const unsigned cs = row_sums(pv);
      if (c == 0) pcs[g * 8 + w] = cs;
    }
    TR(19);
    const unsigned tg = wait_gate(ws + WS_FP, ta, (unsigned*)(mlb + 96));
    TR(18);
    const u32x2 c2 = *(const u32x2*)(pcs + (t & 15) * 2);
    const unsigned co = t < 16 ? (unsigned)(WS_PAC + (size_t)(b * SPLIT_A + sp) * NH * 8 + t * 16) : 0x7ffffff0u;
    wait_until(tg);
    TR(17);
#pragma unroll
    for (int i = 0; i < 4; i++) st16u_sc1(rs, pao + (unsigned)(i * 512 + t) * 16, pv[i]);
    if (w == 0) st16u_sc1(rs, co, u32x4{c2[0], ta, c2[1], tb});
  }
  TR(20);
  TR(43);
  TRDUMP();
}

__global__ __launch_bounds__(512, 2) void k_fused(const __hip_bfloat16* __restrict__ xA, const __hip_bfloat16* __restrict__ ckv,
                                                  const __hip_bfloat16* __restrict__ wqA, const __hip_bfloat16* __restrict__ wuk,
                                                  const __hip_bfloat16* __restrict__ wuv, const __hip_bfloat16* __restrict__ woA,
                                                  const __hip_bfloat16* __restrict__ xB, const __hip_bfloat16* __restrict__ kc,
                                                  const __hip_bfloat16* __restrict__ vc, const __hip_bfloat16* __restrict__ wqB,
                                                  const __hip_bfloat16* __restrict__ woB, char* __restrict__ ws,
                                                  __hip_bfloat16* __restrict__ out) {
  __shared__ __attribute__((aligned(16))) char lds[131072];
  unsigned trv = 0;
  TR(0);
  asm volatile("" :: "s"(xA), "s"(ckv), "s"(wqA), "s"(wuk), "s"(wuv), "s"(woA), "s"(xB), "s"(kc), "s"(vc), "s"(wqB), "s"(woB), "s"(ws), "s"(out));
  TR(58);
  const int blk = blockIdx.x, xcd = blk & 7, r = blk >> 3;
  unsigned pfv = 0;
  {
    u64 pc; unsigned len;
    asm volatile("s_getpc_b64 %0\n.Lcode_begin:\n\ts_mov_b32 %1, .Lcode_end-.Lcode_begin" : "=s"(pc), "=s"(len));
    const unsigned idx = (unsigned)(r * 8 + (threadIdx.x >> 6));
    const __amdgpu_buffer_rsrc_t rc = mkrs((const void*)pc, len);
    pfv = __builtin_amdgcn_raw_buffer_load_b32(rc, idx * 1024 + (threadIdx.x & 63) * 16, 0, 0);
  }
  const __amdgpu_buffer_rsrc_t rs = mkrs(ws, (unsigned)WS_END);
  if (r < 8) {
    if (r < 4) role_A1(((xcd + AROT1) & 7) + 8 * r, xA, wqA, wuk, wuv, woA, ws, rs, out, lds, trv);
    else role_A2(((xcd + AROT2) & 7) * 4 + r - 4, ckv, ws, rs, lds, trv);
  } else {
    const int rr = r - 8, hq = rr / 6;
    role_B(((PERM >> (4 * xcd)) & 7) + 8 * hq, rr - 6 * hq, xB, kc, vc, wqB, woB, ws, rs, out, lds, trv);
  }
  asm volatile(".Lcode_end:" :: "v"(pfv));
}

extern "C" void kernel_launch(void* const* d_in, const int* in_sizes, int n_in,
                              void* d_out, int out_size, void* d_ws, size_t ws_size,
                              hipStream_t stream) {
  k_fused<<<256, 512, 0, stream>>>((const __hip_bfloat16*)d_in[0], (const __hip_bfloat16*)d_in[1], (const __hip_bfloat16*)d_in[2],
                                   (const __hip_bfloat16*)d_in[3], (const __hip_bfloat16*)d_in[4], (const __hip_bfloat16*)d_in[5],
                                   (const __hip_bfloat16*)d_in[6], (const __hip_bfloat16*)d_in[7], (const __hip_bfloat16*)d_in[8],
                                   (const __hip_bfloat16*)d_in[9], (const __hip_bfloat16*)d_in[10], (char*)d_ws,
                                   (__hip_bfloat16*)d_out);
}
```

```cpp
#include <hip/hip_runtime.h>
#include <hip/hip_bf16.h>
#include <stdint.h>

typedef __attribute__((ext_vector_type(4))) unsigned u32x4;
typedef __attribute__((ext_vector_type(2))) unsigned u32x2;
typedef __attribute__((ext_vector_type(4))) float f32x4;
typedef __attribute__((ext_vector_type(2))) float f32x2;
typedef __attribute__((ext_vector_type(3))) float f32x3;
typedef __attribute__((ext_vector_type(16))) float f32x16;
typedef __attribute__((ext_vector_type(8))) __bf16 bf16x8;
typedef __attribute__((ext_vector_type(2))) __bf16 bf16x2;
typedef __attribute__((address_space(1))) unsigned gu32;
typedef __attribute__((address_space(1))) unsigned long long gu64;
typedef unsigned long long u64;

constexpr int NB = 4;
constexpr int NH = 32;
constexpr int SK = 4096;
constexpr int DH = 128;
constexpr int DM = 512;
constexpr int DC = 512;
constexpr int HD = NH * DH;

constexpr int SPLIT_A = 8;
constexpr int KA = SK / SPLIT_A;
constexpr int SS = 171;
constexpr int RD = 18;
constexpr int NP = SS - 8 * RD + 8;
constexpr int NP4 = (NP + 3) & ~3;
constexpr int RST = 144;
constexpr int GI = 18;
constexpr int RE = 15;
constexpr int PRIOF = 4;

constexpr float SCALE = 0.08838834764831845f;
constexpr float LOG2E = 1.4426950408889634f;
constexpr int NT = 2;
constexpr int SC1 = 16;

constexpr size_t WS_EPA  = 0;
constexpr size_t WS_EPB  = 256;
constexpr size_t WS_F1   = 4096;
constexpr size_t WS_F2   = WS_F1 + 1024;
constexpr size_t WS_FP   = 8192;
constexpr size_t WS_QABS = 16384;
constexpr size_t WS_PAML = WS_QABS + (size_t)NB * NH * DC * 2;
constexpr size_t WS_PAC  = WS_PAML + (size_t)NB * SPLIT_A * NH * 16;
constexpr size_t WS_PAO  = WS_PAC + (size_t)NB * SPLIT_A * NH * 8;
constexpr size_t WS_PA   = WS_PAO + (size_t)NB * SPLIT_A * NH * DC * 2;
constexpr size_t WS_PBML = WS_PA + (size_t)NB * NH * DM * 8;
constexpr size_t WS_PBG  = WS_PBML + (size_t)NB * 64 * 16;
constexpr size_t WS_END  = WS_PBG + (size_t)NB * 64 * DM * 8;

__device__ __forceinline__ float bf_lo(unsigned u) { return __uint_as_float(u << 16); }
__device__ __forceinline__ float bf_hi(unsigned u) { return __uint_as_float(u & 0xffff0000u); }
__device__ __forceinline__ unsigned cvtpk(float lo, float hi) {
  f32x2 v = {lo, hi};
  return __builtin_bit_cast(unsigned, __builtin_convertvector(v, bf16x2));
}
__device__ __forceinline__ float rbf(float x) { return __uint_as_float(cvtpk(x, x) << 16); }
__device__ __forceinline__ float dot2(unsigned k, unsigned q, float acc) {
  return __builtin_amdgcn_fdot2_f32_bf16(__builtin_bit_cast(bf16x2, k), __builtin_bit_cast(bf16x2, q), acc, false);
}
template <int CTRL> __device__ __forceinline__ float dpp(float x) {
  return __builtin_bit_cast(float, __builtin_amdgcn_mov_dpp(__builtin_bit_cast(int, x), CTRL, 0xf, 0xf, true));
}
__device__ __forceinline__ float sum16(float x) {
  x += dpp<0x128>(x);
  x += dpp<0x124>(x);
  x += dpp<0x4E>(x);
  x += dpp<0xB1>(x);
  return x;
}
__device__ __forceinline__ float xrow_max(float x) {
  auto s = __builtin_amdgcn_permlane16_swap(__float_as_uint(x), __float_as_uint(x), false, false);
  x = fmaxf(__uint_as_float(s[0]), __uint_as_float(s[1]));
  auto t = __builtin_amdgcn_permlane32_swap(__float_as_uint(x), __float_as_uint(x), false, false);
  return fmaxf(__uint_as_float(t[0]), __uint_as_float(t[1]));
}
__device__ __forceinline__ float xrow_sum(float x) {
  auto s = __builtin_amdgcn_permlane16_swap(__float_as_uint(x), __float_as_uint(x), false, false);
  x = __uint_as_float(s[0]) + __uint_as_float(s[1]);
  auto t = __builtin_amdgcn_permlane32_swap(__float_as_uint(x), __float_as_uint(x), false, false);
  return __uint_as_float(t[0]) + __uint_as_float(t[1]);
}
__device__ __forceinline__ float fexp2(float x) { return __builtin_amdgcn_exp2f(x); }
__device__ __forceinline__ __amdgpu_buffer_rsrc_t mkrs(const void* p, unsigned bytes) {
  return __builtin_amdgcn_make_buffer_rsrc((void*)p, 0, (int)bytes, 0x00020000);
}


constexpr int PTHR = 60;
constexpr unsigned SPIN_CAP = 1u << 21;
__device__ __forceinline__ u64 ld_u64(const void* p) {
  return __hip_atomic_load((gu64*)p, __ATOMIC_RELAXED, __HIP_MEMORY_SCOPE_AGENT);
}
__device__ __forceinline__ void st_u64(void* p, u64 v) {
  __hip_atomic_store((gu64*)p, v, __ATOMIC_RELAXED, __HIP_MEMORY_SCOPE_AGENT);
}
__device__ __forceinline__ u64 next_tag(u64 e) { return e + 0x0000000100000001ull; }
__device__ __forceinline__ u64 uni64(u64 v) {
  const unsigned lo = __builtin_amdgcn_readfirstlane((unsigned)v), hi = __builtin_amdgcn_readfirstlane((unsigned)(v >> 32));
  return ((u64)hi << 32) | lo;
}
__device__ __forceinline__ void signal_flag(char* flag, u64 tag) {
  asm volatile("s_waitcnt vmcnt(0)" ::: "memory");
  __syncthreads();
  if (threadIdx.x == 0) st_u64(flag, tag);
}
template <int CTRL> __device__ __forceinline__ int dppi(int x) { return __builtin_amdgcn_mov_dpp(x, CTRL, 0xf, 0xf, true); }
__device__ __forceinline__ int sum8i(int x) { x += dppi<0xB1>(x); x += dppi<0x4E>(x); x += dppi<0x141>(x); return x; }
__device__ __forceinline__ unsigned row_sums(const u32x4 (&p)[4]) {
  unsigned c4[4];
#pragma unroll
  for (int i = 0; i < 4; i++) c4[i] = (p[i][0] + p[i][1]) + (p[i][2] + p[i][3]);
  auto s0 = __builtin_amdgcn_permlane32_swap(c4[0], c4[2], false, false);
  auto s1 = __builtin_amdgcn_permlane32_swap(c4[1], c4[3], false, false);
  auto s2 = __builtin_amdgcn_permlane16_swap(s0[0] + s0[1], s1[0] + s1[1], false, false);
  int x = (int)(s2[0] + s2[1]);
  x += dppi<0x128>(x);
  return (unsigned)sum8i(x);
}
__device__ __forceinline__ unsigned clk() { return (unsigned)__builtin_amdgcn_s_memrealtime(); }
__device__ __forceinline__ unsigned wait_gate(const char* flag, unsigned ta, unsigned* slot) {
  if (threadIdx.x < 64) {
    u64 v = 0;
#pragma unroll 1
    for (unsigned spins = 0; spins < SPIN_CAP; ++spins) {
      v = ld_u64(flag + (threadIdx.x & 7) * 8);
      if (__all((unsigned)(v >> 32) == ta)) break;
      __builtin_amdgcn_s_sleep(8);
    }
    const unsigned t0 = __builtin_amdgcn_readfirstlane((unsigned)v);
    const int d = (int)((unsigned)v - t0);
    int mx = max(d, dppi<0xB1>(d)); mx = max(mx, dppi<0x4E>(mx)); mx = max(mx, dppi<0x141>(mx));
    const int mean = sum8i(d) >> 3;
    if (threadIdx.x == 0) *slot = t0 + (unsigned)(mean + (((mx - mean) * 2) >> 2) + 450);
  }
  __syncthreads();
  return __builtin_amdgcn_readfirstlane(*slot);
}
__device__ __forceinline__ void wait_until(unsigned t) {
#pragma unroll 1
  for (int spins = 0; spins < 4096 && (int)(t - clk()) > 0; ++spins) __builtin_amdgcn_s_sleep(1);
}
__device__ __forceinline__ void st16_sc1(__amdgpu_buffer_rsrc_t rs, unsigned off, f32x4 v) {
  __builtin_amdgcn_raw_buffer_store_b128(__builtin_bit_cast(u32x4, v), rs, off, 0, SC1);
}
__device__ __forceinline__ void st16u_sc1(__amdgpu_buffer_rsrc_t rs, unsigned off, u32x4 v) {
  __builtin_amdgcn_raw_buffer_store_b128(v, rs, off, 0, SC1);
}
__device__ __forceinline__ f32x4 ld16_sc1(__amdgpu_buffer_rsrc_t rs, unsigned off) {
  return __builtin_bit_cast(f32x4, __builtin_amdgcn_raw_buffer_load_b128(rs, off, 0, SC1));
}
__device__ __forceinline__ u32x4 ld16u_sc1(__amdgpu_buffer_rsrc_t rs, unsigned off) {
  return __builtin_amdgcn_raw_buffer_load_b128(rs, off, 0, SC1);
}
__device__ __forceinline__ void st8_sc1(void* p, f32x2 v) { st_u64(p, __builtin_bit_cast(u64, v)); }
__device__ __forceinline__ f32x2 ld8_sc1(const void* p) { return __builtin_bit_cast(f32x2, ld_u64(p)); }


__device__ __forceinline__ void item_dots(const u32x4 (&ks)[4], const u32x4 q, float (&s_)[4]) {
#pragma unroll
  for (int u = 0; u < 4; u++) {
    float a = dot2(ks[u][0], q[0], 0.f);
    a = dot2(ks[u][1], q[1], a);
    a = dot2(ks[u][2], q[2], a);
    a = dot2(ks[u][3], q[3], a);
    s_[u] = a;
    asm volatile("" : "+v"(s_[u]));
  }
}
__device__ __forceinline__ void item_unpack(const u32x4 (&vs)[4], f32x2 (&vf)[4][4]) {
#pragma unroll
  for (int u = 0; u < 4; u++)
#pragma unroll
    for (int j = 0; j < 4; j++) { vf[u][j] = f32x2{bf_lo(vs[u][j]), bf_hi(vs[u][j])}; asm volatile("" : "+v"(vf[u][j])); }
}
__device__ __forceinline__ f32x2 rows_sum(const float (&o)[8]) {
  float r4[4], r2[2];
#pragma unroll
  for (int j = 0; j < 4; j++) {
    auto sx = __builtin_amdgcn_permlane32_swap(__float_as_uint(o[j]), __float_as_uint(o[j + 4]), false, false);
    r4[j] = __uint_as_float(sx[0]) + __uint_as_float(sx[1]);
  }
#pragma unroll
  for (int j = 0; j < 2; j++) {
    auto sx = __builtin_amdgcn_permlane16_swap(__float_as_uint(r4[j]), __float_as_uint(r4[j + 2]), false, false);
    r2[j] = __uint_as_float(sx[0]) + __uint_as_float(sx[1]);
  }
  return f32x2{r2[0], r2[1]};
}
__device__ __forceinline__ f32x2 item_partial(const float (&s_)[4], const f32x2 (&vf)[4][4], float& mx, float& ll) {
  float s[4];
#pragma unroll
  for (int u = 0; u < 4; u++) s[u] = rbf(rbf(sum16(s_[u])) * SCALE);
  mx = xrow_max(fmaxf(fmaxf(s[0], s[1]), fmaxf(s[2], s[3])));
  float p[4];
#pragma unroll
  for (int u = 0; u < 4; u++) p[u] = fexp2((s[u] - mx) * LOG2E);
  ll = xrow_sum((p[0] + p[1]) + (p[2] + p[3]));
  float o[8];
#pragma unroll
  for (int j = 0; j < 4; j++) {
    f32x2 v = vf[0][j] * p[0];
#pragma unroll
    for (int u = 1; u < 4; u++) v = __builtin_elementwise_fma(vf[u][j], f32x2{p[u], p[u]}, v);
    o[2 * j] = v[0]; o[2 * j + 1] = v[1];
  }
  return rows_sum(o);
}
__device__ __forceinline__ void role_B(int h, int j6, const __hip_bfloat16* __restrict__ xB,
                                       const __hip_bfloat16* __restrict__ kc, const __hip_bfloat16* __restrict__ vc,
                                       const __hip_bfloat16* __restrict__ wqB, const __hip_bfloat16* __restrict__ woB,
                                       char* __restrict__ ws, __amdgpu_buffer_rsrc_t rs,
                                       __hip_bfloat16* __restrict__ out, char* lds) {
  const int t = threadIdx.x, l = t & 63;
  const int w = __builtin_amdgcn_readfirstlane(t >> 6);
  const int g = l >> 4, c = l & 15;
  const int pr = j6 >= 3 ? 1 : 0, k = j6 - 3 * pr;
  const int b0 = 2 * pr;
  const int NI = k == 1 ? 2 * (256 - SS) : SS;
  const int e0 = k == 1 ? (h & 1) : (k >> 1);
  u32x4 xr[2][2], wr[16];
  {
    const __amdgpu_buffer_rsrc_t rx = mkrs(xB, NB * DM * 2);
    const int rq_ = (0x203120 >> (4 * j6)) & 3;
    const int cw = w ^ ((rq_ & 2) << 1), r0 = rq_ & 1;
    const unsigned xo = (unsigned)((b0 * DM + cw * 64 + g * 16) * 2);
#pragma unroll
    for (int bb = 0; bb < 2; bb++)
#pragma unroll
      for (int i = 0; i < 2; i++) xr[bb][i] = __builtin_amdgcn_raw_buffer_load_b128(rx, xo + bb * DM * 2, (i ^ r0) * 16, 0);
    const __amdgpu_buffer_rsrc_t rq = mkrs(wqB, DM * HD * 2);
    const unsigned vo = (unsigned)(((cw * 64 + g * 16) * HD + h * DH + c * 8) * 2);
    const unsigned vo0 = vo + r0 * 8 * HD * 2, vo1 = vo + (1 - r0) * 8 * HD * 2;
#pragma unroll
    for (int i = 0; i < 16; i++) wr[i] = __builtin_amdgcn_raw_buffer_load_b128(rq, i < 8 ? vo0 : vo1, (i & 7) * HD * 2, 0);
  }
  constexpr unsigned KVB = (unsigned)((size_t)NB * NH * SK * DH * 2);
  const __amdgpu_buffer_rsrc_t rk = mkrs(kc, KVB), rv = mkrs(vc, KVB);
  const unsigned lo = (unsigned)(g * 256 + c * 16);
  const unsigned hb0 = ((unsigned)((b0 + e0) * NH + h) << 20) + (k == 1 ? (unsigned)SS << 12 : 0u);
  const unsigned hb1 = ((unsigned)((b0 + 1 - e0) * NH + h) << 20) + ((unsigned)(2 * SS - 256) << 12);
  auto soff = [&](int x) { return ((k == 1 && x >= 256 - SS) ? hb1 : hb0) + ((unsigned)x << 12); };
  auto ld4 = [&](u32x4 (&d)[4], __amdgpu_buffer_rsrc_t r, unsigned so) {
#pragma unroll
    for (int u = 0; u < 4; u++) d[u] = __builtin_amdgcn_raw_buffer_load_b128(r, lo + u * 1024, so, 2);
  };
  u32x4 kr[4], vr[4], ke[4], ve[4];
  ld4(kr, rk, soff(w));
  ld4(vr, rv, soff(w));
  u64* eps = (u64*)(lds + 33808);
  u64 epv = 0;
  if (t < 3) epv = ld_u64(ws + (t == 0 ? WS_EPA : WS_EPB + 256 * (b0 + (t == 1 ? e0 : 1 - e0))));
  float* red = (float*)lds;
  unsigned* qs = (unsigned*)(lds + 32768);
  unsigned* cnt0 = (unsigned*)(lds + 33792);
  unsigned* cntd = (unsigned*)(lds + 33796);
  float* rec0 = (float*)(lds + 36864);
  float* mlr0 = (float*)(lds + 40960);
  char* prv = lds + 45056 + w * 1024;
  float* recd = (float*)(lds + 65536);
  float* mld = (float*)(lds + 98304);
  const unsigned cnta = (unsigned)(uintptr_t)(__attribute__((address_space(3))) char*)cntd;
  float* mxd = (float*)(lds + 33800);
  if (t == 0) { *cnt0 = 0u; *cntd = 8u * (RD + 1); mxd[0] = -INFINITY; mxd[1] = -INFINITY; }
  if (t < 64 * (NP4 - NP + 1)) *(f32x2*)(recd + (NP - 1 + (t >> 6)) * RST + 2 * l) = f32x2{0.f, 0.f};
  {
    float a0[8], a1[8];
#pragma unroll
    for (int j = 0; j < 8; j++) { a0[j] = 0.f; a1[j] = 0.f; }
#pragma unroll
    for (int i = 0; i < 16; i += 2) {
      const unsigned x0 = xr[0][i >> 3][(i >> 1) & 3], x1 = xr[1][i >> 3][(i >> 1) & 3];
#pragma unroll
      for (int j = 0; j < 4; j++) {
        const unsigned pl = __builtin_amdgcn_perm(wr[i + 1][j], wr[i][j], 0x05040100u);
        const unsigned ph = __builtin_amdgcn_perm(wr[i + 1][j], wr[i][j], 0x07060302u);
        a0[2 * j] = dot2(pl, x0, a0[2 * j]); a0[2 * j + 1] = dot2(ph, x0, a0[2 * j + 1]);
        a1[2 * j] = dot2(pl, x1, a1[2 * j]); a1[2 * j + 1] = dot2(ph, x1, a1[2 * j + 1]);
      }
    }
#pragma unroll
    for (int j = 0; j < 8; j++) asm volatile("" : "+v"(a0[j]), "+v"(a1[j]));
    __builtin_amdgcn_sched_barrier(0);
    ld4(ke, rk, soff(w + 8));
    ld4(ve, rv, soff(w + 8));
    __builtin_amdgcn_sched_barrier(0);
    float* rp = red + (w * 4 + g) * 256 + c * 8;
    *(f32x4*)(rp) = f32x4{a0[0], a0[1], a0[2], a0[3]};
    *(f32x4*)(rp + 4) = f32x4{a0[4], a0[5], a0[6], a0[7]};
    *(f32x4*)(rp + 128) = f32x4{a1[0], a1[1], a1[2], a1[3]};
    *(f32x4*)(rp + 132) = f32x4{a1[4], a1[5], a1[6], a1[7]};
  }
  __syncthreads();
  {
    const int o = t >> 1, hf = t & 1;
    float s = 0.f;
#pragma unroll
    for (int i = 0; i < 16; i++) s += red[(hf * 16 + i) * 256 + o];
    s += dpp<0xB1>(s);
    const float sn = dpp<0x4E>(s);
    if ((t & 3) == 0) qs[o >> 1] = cvtpk(s, sn);
    if (t < 3) eps[t] = epv;
  }
  __syncthreads();
  const u64 epa = uni64(eps[0]), tg0 = next_tag(uni64(eps[1])), tg1 = next_tag(uni64(eps[2]));
  u32x4 q = *(const u32x4*)(qs + e0 * 64 + c * 4);
  const u32x4 q1 = *(const u32x4*)(qs + (1 - e0) * 64 + c * 4);
  float m = -INFINITY, lsum = 0.f, acc[8];
#pragma unroll
  for (int j = 0; j < 8; j++) acc[j] = 0.f;
  float s_[4]; f32x2 vf[4][4];
  auto part0 = [&](u32x4 (&ks)[4]) { item_dots(ks, q, s_); };
  auto part1 = [&](u32x4 (&vs)[4]) { item_unpack(vs, vf); };
  auto part2 = [&]() {
    float s[4];
#pragma unroll
    for (int u = 0; u < 4; u++) s[u] = rbf(rbf(sum16(s_[u])) * SCALE);
    const float mx = fmaxf(fmaxf(m, fmaxf(s[0], s[1])), fmaxf(s[2], s[3]));
    const float alpha = fexp2((m - mx) * LOG2E);
    m = mx;
    float p[4];
#pragma unroll
    for (int u = 0; u < 4; u++) p[u] = fexp2((s[u] - mx) * LOG2E);
    lsum = lsum * alpha + ((p[0] + p[1]) + (p[2] + p[3]));
#pragma unroll
    for (int j = 0; j < 8; j++) acc[j] *= alpha;
#pragma unroll
    for (int u = 0; u < 4; u++)
#pragma unroll
      for (int j = 0; j < 4; j++) {
        acc[2 * j]     = fmaf(p[u], vf[u][j][0], acc[2 * j]);
        acc[2 * j + 1] = fmaf(p[u], vf[u][j][1], acc[2 * j + 1]);
      }
  };
  auto front = [&](__amdgpu_buffer_rsrc_t rk2, __amdgpu_buffer_rsrc_t rv2, unsigned so) {
    __builtin_amdgcn_sched_barrier(0);
    ld4(kr, rk2, so);
    __builtin_amdgcn_sched_barrier(0);
    part1(vr);
    __builtin_amdgcn_sched_barrier(0);
    ld4(vr, rv2, so);
    __builtin_amdgcn_sched_barrier(0);
  };
  auto put_partial = [&](f32x2 r2, float mw, float ll, float* rec_, float* ml_, float* mx_) {
    *(f32x2*)(rec_ + c * 8 + g * 2) = r2;
    if (l == 0) {
      *(f32x2*)ml_ = f32x2{mw, ll};
      asm volatile("ds_max_f32 %0, %1" :: "v"((unsigned)(uintptr_t)(__attribute__((address_space(3))) float*)mx_), "v"(mw) : "memory");
    }
  };
  auto wave_partial = [&](float* rec_, float* ml_, float* mx_) {
    const float mw = xrow_max(m);
    const float fq = fexp2((m - mw) * LOG2E);
    const float ll = xrow_sum(lsum * fq);
    float o[8];
#pragma unroll
    for (int j = 0; j < 8; j++) o[j] = acc[j] * fq;
    put_partial(rows_sum(o), mw, ll, rec_, ml_, mx_);
  };
  auto part2c = [&](float* rec_, float* ml_) {
    float mx, ll;
    const f32x2 r2 = item_partial(s_, vf, mx, ll);
    put_partial(r2, mx, ll, rec_, ml_, mxd);
  };
  bool sw = k != 1;
  auto do_switch = [&]() {
    wave_partial(rec0 + w * 128, mlr0 + w * 2, mxd + 1);
    asm volatile("s_waitcnt lgkmcnt(0)" ::: "memory");
    if (l == 0) asm volatile("ds_add_u32 %0, %1" :: "v"(cnta - 4u), "v"(1u) : "memory");
    m = -INFINITY; lsum = 0.f;
#pragma unroll
    for (int j = 0; j < 8; j++) acc[j] = 0.f;
    q = q1; sw = true;
  };
  if (w >= 4 && PRIOF > 0) __builtin_amdgcn_s_setprio(1);
  part0(kr);
  front(rk, rv, soff(w + 16));
  part2();
  part0(ke); part1(ve); part2();
  int x = w + 16;
  const int rw = 3 + 4 * (h >> 3);
  const int re = k != 1 ? -1 : rw + 2 > RE ? rw + 2 : RE;
  u32x4 wo[16];
  __builtin_amdgcn_sched_barrier(0);
#pragma unroll
  for (int i = 0; i < 16; i++) asm volatile("" : "=v"(wo[i]));
  const int nseg = k == 1 ? 2 : 1, sp = k == 1 ? 1 : 0;
  auto store_off = [&](int sg) {
    const int b = b0 + (sg ? 1 - e0 : e0);
    const unsigned og_ = (unsigned)(WS_PBG + ((size_t)(b * 64 + h * 2 + sp) * DM + w * 64 + c * 4 + g) * 8);
    const unsigned om_ = (unsigned)(WS_PBML + (size_t)(b * 64 + h * 2 + sp) * 16);
    return (g & 1) == 0 ? og_ : t == 16 ? om_ : 0x7ffffff0u;
  };
  const unsigned apo = (unsigned)((c & 1) * 272 + g * 64);
  auto project = [&](const char* ap, float M, float L, bool lred) {
    u32x4 a[4];
#pragma unroll
    for (int s = 0; s < 4; s++) a[s] = *(const u32x4*)(ap + s * 16);
    __builtin_amdgcn_sched_barrier(0);
    if (lred) L = xrow_sum(sum16(L));
    __builtin_amdgcn_sched_barrier(0);
    f32x4 d[4];
#pragma unroll
    for (int s = 0; s < 4; s++)
#pragma unroll
      for (int j = 0; j < 4; j++)
        d[j] = __builtin_amdgcn_mfma_f32_16x16x32_bf16(__builtin_bit_cast(bf16x8, a[s]), __builtin_bit_cast(bf16x8, wo[4 * s + j]),
                                                       s ? d[j] : f32x4{0.f, 0.f, 0.f, 0.f}, 0, 0, 0);
    return f32x3{g < 2 ? d[0][0] + d[0][1] : d[3][0] + d[3][1], g < 2 ? d[1][0] + d[1][1] : M, g < 2 ? d[2][0] + d[2][1] : L};
  };
  f32x3 seg0 = {0.f, 0.f, 0.f};
  auto events = [&](int rd) {
    if (rd == PRIOF) __builtin_amdgcn_s_setprio(0);
    if (!sw && x >= 256 - SS) do_switch();
    if (rd == rw + 2) {
#pragma unroll
      for (int s = 0; s < 4; s++) {
        u32x4 o[4];
#pragma unroll
        for (int e = 0; e < 4; e++)
#pragma unroll
          for (int j = 0; j < 4; j++) o[j][e] = __builtin_amdgcn_perm(wo[4 * s + e][2 + (j >> 1)], wo[4 * s + e][j >> 1], (j & 1) ? 0x07060302u : 0x05040100u);
#pragma unroll
        for (int j = 0; j < 4; j++) wo[4 * s + j] = o[j];
        asm volatile("" : "+v"(wo[4 * s]), "+v"(wo[4 * s + 1]), "+v"(wo[4 * s + 2]), "+v"(wo[4 * s + 3]));
      }
    }
    if (rd == re) {
    for (unsigned spins = 0; __hip_atomic_load(cnt0, __ATOMIC_RELAXED, __HIP_MEMORY_SCOPE_WORKGROUP) != 8u && spins < SPIN_CAP; ++spins)
      __builtin_amdgcn_s_sleep(1);
    asm volatile("" ::: "memory");
    f32x4 q4[4];
#pragma unroll
    for (int i = 0; i < 4; i++) q4[i] = *(const f32x4*)(mlr0 + i * 4);
    const float M = mxd[1];
    float L = 0.f;
    f32x2 a2 = {0.f, 0.f};
#pragma unroll
    for (int i = 0; i < 8; i++) {
      const float fw = fexp2((q4[i >> 1][(i & 1) * 2] - M) * LOG2E);
      L = fmaf(q4[i >> 1][(i & 1) * 2 + 1], fw, L);
      a2 += *(const f32x2*)(rec0 + i * 128 + 2 * l) * fw;
    }
    const unsigned hi = cvtpk(a2[0], a2[1]);
    *(unsigned*)(prv + l * 4) = hi;
    *(unsigned*)(prv + 272 + l * 4) = cvtpk(a2[0] - bf_lo(hi), a2[1] - bf_hi(hi));
    seg0 = project(prv + apo, M, L, false);
    }
    if (rd == rw) {
      const __amdgpu_buffer_rsrc_t ro = mkrs(woB, HD * DM * 2);
      const unsigned vo = (unsigned)(((h * DH + g * 32) * DM + w * 64 + c * 4) * 2);
#pragma unroll
      for (int i = 0; i < 16; i++) {
        const u32x2 u0 = __builtin_amdgcn_raw_buffer_load_b64(ro, vo + (i & 1) * 2 * DM * 2, (i >> 1) * 4 * DM * 2, 0);
        const u32x2 u1 = __builtin_amdgcn_raw_buffer_load_b64(ro, vo + ((i & 1) * 2 + 1) * DM * 2, (i >> 1) * 4 * DM * 2, 0);
        wo[i] = u32x4{u0[0], u0[1], u1[0], u1[1]};
      }
    }
  };
#pragma unroll 1
  for (; x < 8 * RD; x += 8) {
    const int rd = x >> 3;
    events(rd);
    part0(kr);
    front(rk, rv, soff(x + 8));
    part2();
  }
  wave_partial(recd + w * RST, mld + w * 2, mxd);
  unsigned o_ml = 98304u + (l < NP ? l : 0) * 8, o_rv = 65536u + (g * RST + w * 16 + c) * 4, o_fw = 45056u + w * 1024 + ((l & 3) * 16 + (l >> 2)) * 4;
  unsigned o_fr = 45056u + w * 1024 + g * 64, o_oi = (g < 2 && (c & 1) == 0) ? 41216u + g * 272 + (w * 16 + c) * 2 : 45056u + w * 1024 + 256 + l * 4;
  unsigned o_ap = 41216u + apo, o_st = store_off(nseg - 1);
  unsigned o_s0 = k == 1 ? store_off(0) : 0x7ffffff0u;
  unsigned tl0 = (unsigned)(k == 1 ? tg1 : tg0);
  u32x4 sg0 = {__float_as_uint(seg0[0]), __float_as_uint(seg0[1]), __float_as_uint(seg0[2]), (unsigned)tg0};
  asm volatile("" : "+v"(o_s0), "+v"(tl0), "+v"(sg0));
  asm volatile("" : "+v"(o_ml), "+v"(o_rv), "+v"(o_fw), "+v"(o_fr), "+v"(o_oi), "+v"(o_ap), "+v"(o_st));
  {
    {
#pragma unroll 1
      while (x < NI) {
        unsigned dr; u64 sv;
        asm volatile("s_mov_b64 %1, exec\n\ts_mov_b64 exec, 1\n\tds_add_rtn_u32 %0, %2, %3\n\ts_mov_b64 exec, %1"
                     : "=&v"(dr), "=&s"(sv) : "v"(cnta), "v"(1u), "v"(kr[0][0]) : "memory");
        __builtin_amdgcn_sched_barrier(0);
        part0(kr);
        asm volatile("s_waitcnt lgkmcnt(0)" : "+v"(dr) :: "memory");
        const int xn = __builtin_amdgcn_readfirstlane(dr);
        if (h >= 24 && j6 == 2 && xn == NI - GI && l == 0) st_u64(ws + WS_FP + (h - 24) * 8, (next_tag(epa) << 32) | clk());
        const bool ok = xn < NI;
        front(mkrs(kc, ok ? KVB : 0u), mkrs(vc, ok ? KVB : 0u), ok ? soff(xn) : 0u);
        part2c(recd + (x - 8 * RD + 8) * RST, mld + (x - 8 * RD + 8) * 2);
        x = xn;
      }
      asm volatile("s_waitcnt lgkmcnt(0)" ::: "memory");
      __syncthreads();
    }
    {
      f32x2 mli = *(const f32x2*)(lds + o_ml);
      float rv_[NP4 / 4];
#pragma unroll
      for (int r = 0; r < NP4 / 4; r++) rv_[r] = *(const float*)(lds + o_rv + r * 4 * RST * 4);
      if (l >= NI - 8 * RD + 8) mli = f32x2{-INFINITY, 0.f};
      const float M = *mxd;
      const float fi = fexp2((mli[0] - M) * LOG2E);
      *(float*)(lds + o_fw) = fi;
      f32x4 f4[(NP4 / 4 + 3) / 4];
#pragma unroll
      for (int i = 0; i < (NP4 / 4 + 3) / 4; i++) f4[i] = *(const f32x4*)(lds + o_fr + i * 16);
      float a = 0.f;
#pragma unroll
      for (int r = 0; r < NP4 / 4; r++) a = fmaf(f4[r >> 2][r & 3], rv_[r], a);
      a = xrow_sum(a);
      const float an = dpp<0xB1>(a);
      const unsigned hi = cvtpk(a, an);
      const unsigned lw = cvtpk(a - bf_lo(hi), an - bf_hi(hi));
      *(unsigned*)(lds + o_oi) = g ? lw : hi;
      __syncthreads();
      const f32x3 pv = project(lds + o_ap, M, fi * mli[1], true);
      st16u_sc1(rs, o_st, u32x4{__float_as_uint(pv[0]), __float_as_uint(pv[1]), __float_as_uint(pv[2]), tl0});
      st16u_sc1(rs, o_s0, sg0);
    }
    if (w < nseg) {
      const int sg = w;
      const int b = b0 + (sg ? 1 - e0 : e0);
      const int j64 = h * 2 + sp;
      const u64 tg = sg ? tg1 : tg0;
      const unsigned ta = (unsigned)tg;
      const unsigned oml = (unsigned)(WS_PBML + (size_t)(b * 64 + l) * 16 + 12);
      const unsigned og = (unsigned)(WS_PBG + ((size_t)(b * 64 + (l >> 2)) * DM + j64 * 8) * 8 + (l & 3) * 16);
      u32x4 p[4];
      __builtin_amdgcn_s_sleep(4);
#pragma unroll 1
      for (unsigned spins = 0; spins < SPIN_CAP; ++spins) {
        asm volatile("" ::: "memory");
#pragma unroll
        for (int i = 0; i < 4; i++) p[i] = ld16u_sc1(rs, og + i * 16 * DM * 8);
        if (__all(p[0][3] == ta && p[1][3] == ta && p[2][3] == ta && p[3][3] == ta)) break;
        int n = 0;
#pragma unroll
        for (int i = 0; i < 4; i++) n += __popcll(__ballot(p[i][3] == ta));
        __builtin_amdgcn_s_sleep(0);
#pragma unroll 1
        for (; n < 4 * PTHR && spins < SPIN_CAP; ++spins) {
          __builtin_amdgcn_s_sleep(4);
          asm volatile("" ::: "memory");
          n = 4 * __popcll(__ballot(__builtin_amdgcn_raw_buffer_load_b32(rs, oml, 0, SC1) == ta));
        }
      }
      float v0 = 0.f, v1 = 0.f, v2 = 0.f;
#pragma unroll
      for (int i = 0; i < 4; i++) {
        const float mm = dpp<0xF5>(__uint_as_float(p[i][1]));
        const float Mh = fmaxf(mm, dpp<0x141>(mm));
        const float fq = fexp2((mm - Mh) * LOG2E);
        float L = fq * dpp<0xF5>(__uint_as_float(p[i][2]));
        L += dpp<0x141>(L);
        const float wgt = fq * __builtin_amdgcn_rcpf(L);
        v0 = fmaf(__uint_as_float(p[i][0]), wgt, v0);
        v1 = fmaf(__uint_as_float(p[i][1]), wgt, v1);
        v2 = fmaf(__uint_as_float(p[i][2]), wgt, v2);
      }
      const float u2 = dpp<0xA0>(v2);
      v1 = (l & 1) ? v0 : v1;
      v0 = (l & 1) ? u2 : v0;
      v0 += dpp<0x128>(v0); v0 += dpp<0x124>(v0); v0 = xrow_sum(v0);
      v1 += dpp<0x128>(v1); v1 += dpp<0x124>(v1); v1 = xrow_sum(v1);
      if (l < 4) {
        *(unsigned*)(out + b * 1024 + 512 + j64 * 8 + l * 2) = cvtpk(v0, v1);
        if (j64 == 0 && l == 0) st_u64(ws + WS_EPB + 256 * b, tg);
      }
    }
  }
}

__device__ __forceinline__ unsigned lds_off(int row, int ch) {
  return 256 * row + 16 * (ch ^ (((row & 3) << 2) | ((row >> 2) & 3)));
}

__device__ __forceinline__ void role_A1(int h, const __hip_bfloat16* __restrict__ xA, const __hip_bfloat16* __restrict__ wqA,
                                        const __hip_bfloat16* __restrict__ wuk, const __hip_bfloat16* __restrict__ wuv,
                                        const __hip_bfloat16* __restrict__ woA, char* __restrict__ ws,
                                        __amdgpu_buffer_rsrc_t rs, __hip_bfloat16* __restrict__ out, char* lds) {
  const int t = threadIdx.x, l = t & 63;
  const int w = __builtin_amdgcn_readfirstlane(t >> 6);
  const int g = l >> 4, c = l & 15;
  float* red = (float*)(lds + 16384);
  const unsigned vo = (unsigned)(((w * 64 + g * 16) * HD + h * DH + c * 8) * 2);
  u64 tag;
  {
    u32x4 xr[NB][2], wr[16], wk[16];
    const __amdgpu_buffer_rsrc_t rx = mkrs(xA, NB * DM * 2);
    const unsigned xo = (unsigned)((w * 64 + g * 16) * 2);
#pragma unroll
    for (int bb = 0; bb < NB; bb++)
#pragma unroll
      for (int i = 0; i < 2; i++) xr[bb][i] = __builtin_amdgcn_raw_buffer_load_b128(rx, xo + bb * DM * 2 + i * 16, 0, 0);
    const __amdgpu_buffer_rsrc_t rq = mkrs(wqA, DM * HD * 2);
#pragma unroll
    for (int i = 0; i < 16; i++) wr[i] = __builtin_amdgcn_raw_buffer_load_b128(rq, vo, i * HD * 2, NT);
    u64 epv = 0;
    if (t == 0) epv = ld_u64(ws + WS_EPA);
    unsigned* qs = (unsigned*)lds;
    unsigned short* qo = (unsigned short*)(lds + 4096);
    float acc[NB][8];
#pragma unroll
    for (int bb = 0; bb < NB; bb++)
#pragma unroll
      for (int j = 0; j < 8; j++) acc[bb][j] = 0.f;
#pragma unroll
    for (int i = 0; i < 16; i += 2) {
#pragma unroll
      for (int j = 0; j < 4; j++) {
        const unsigned pl = __builtin_amdgcn_perm(wr[i + 1][j], wr[i][j], 0x05040100u);
        const unsigned ph = __builtin_amdgcn_perm(wr[i + 1][j], wr[i][j], 0x07060302u);
#pragma unroll
        for (int bb = 0; bb < NB; bb++) {
          const unsigned x = xr[bb][i >> 3][(i >> 1) & 3];
          acc[bb][2 * j] = dot2(pl, x, acc[bb][2 * j]);
          acc[bb][2 * j + 1] = dot2(ph, x, acc[bb][2 * j + 1]);
        }
      }
    }
    float* rp = red + (w * 4 + g) * 512 + c * 8;
#pragma unroll
    for (int bb = 0; bb < NB; bb++) {
      *(f32x4*)(rp + bb * 128) = f32x4{acc[bb][0], acc[bb][1], acc[bb][2], acc[bb][3]};
      *(f32x4*)(rp + bb * 128 + 4) = f32x4{acc[bb][4], acc[bb][5], acc[bb][6], acc[bb][7]};
    }
    __syncthreads();
    {
      const __amdgpu_buffer_rsrc_t rku = mkrs(wuk, DC * HD * 2);
#pragma unroll
      for (int i = 0; i < 16; i++) wk[i] = __builtin_amdgcn_raw_buffer_load_b128(rku, vo, i * HD * 2, NT);
    }
    {
      float s = 0.f;
#pragma unroll
      for (int i = 0; i < 32; i++) s += red[i * 512 + t];
      const float sn = dpp<0xB1>(s);
      if ((t & 1) == 0) qs[t >> 1] = cvtpk(s, sn);
      if (t == 0) *(u64*)(lds + 2048) = epv;
    }
    __syncthreads();
    tag = next_tag(uni64(*(const u64*)(lds + 2048)));
    {
      u32x4 qq[NB];
#pragma unroll
      for (int bb = 0; bb < NB; bb++) qq[bb] = *(const u32x4*)(qs + bb * 64 + c * 4);
#pragma unroll
      for (int i = 0; i < 16; i++) {
        float s[NB];
#pragma unroll
        for (int bb = 0; bb < NB; bb++) {
          float v = dot2(wk[i][0], qq[bb][0], 0.f);
          v = dot2(wk[i][1], qq[bb][1], v);
          v = dot2(wk[i][2], qq[bb][2], v);
          v = dot2(wk[i][3], qq[bb][3], v);
          s[bb] = sum16(v);
        }
        const float v = (c & 2) ? ((c & 1) ? s[3] : s[2]) : ((c & 1) ? s[1] : s[0]);
        if (c < NB) qo[c * 512 + w * 64 + g * 16 + i] = (unsigned short)(cvtpk(v, v) & 0xffff);
      }
    }
    __syncthreads();
    if (t < 256) {
      const int bb = t >> 6, ch = t & 63;
      st16u_sc1(rs, (unsigned)(WS_QABS + ((size_t)(bb * NH + h) * DC + ch * 8) * 2), *(const u32x4*)(qo + bb * 512 + ch * 8));
    }
    signal_flag(ws + WS_F1 + h * 8, tag);
  }
  const unsigned ta = (unsigned)tag, tb = (unsigned)(tag >> 32);
  {
    u32x4 bv[8][2], bo[8][2];
    u32x4 pre[2][8], wo[16];
    {
      const __amdgpu_buffer_rsrc_t ruv = mkrs(wuv, DC * HD * 2);
      const __amdgpu_buffer_rsrc_t rwo = mkrs(woA, HD * DM * 2);
      const unsigned wvo = (unsigned)(((h * DH + (l >> 3) * 16) * DM + w * 64 + (l & 7) * 8) * 2);
#pragma unroll
      for (int i = 0; i < 16; i++) pre[i >> 3][i & 7] = __builtin_amdgcn_raw_buffer_load_b128(ruv, vo - g * 8 * HD * 2, ((i >> 3) * 32 + (i & 7)) * HD * 2, NT);
#pragma unroll
      for (int i = 0; i < 16; i++) wo[i] = __builtin_amdgcn_raw_buffer_load_b128(rwo, wvo, i * DM * 2, NT);
    }
#pragma unroll
    for (int j = 0; j < 8; j++)
#pragma unroll
      for (int s = 0; s < 2; s++) {
#pragma unroll
        for (int e = 0; e < 4; e++)
          bv[j][s][e] = __builtin_amdgcn_perm(pre[s][2 * e + 1][j >> 1], pre[s][2 * e][j >> 1], (j & 1) ? 0x07060302u : 0x05040100u);
        asm volatile("" : "+v"(bv[j][s]));
      }
#pragma unroll
    for (int j = 0; j < 8; j++)
#pragma unroll
      for (int s = 0; s < 2; s++) {
#pragma unroll
        for (int e = 0; e < 4; e++)
          bo[j][s][e] = __builtin_amdgcn_perm(wo[8 * s + 2 * e + 1][j >> 1], wo[8 * s + 2 * e][j >> 1], (j & 1) ? 0x07060302u : 0x05040100u);
        asm volatile("" : "+v"(bo[j][s]));
      }
    constexpr int LAS = 1040;
    char* la = lds;
    unsigned short* cs = (unsigned short*)(lds + 16384);
    float* outs = (float*)(lds + 20480);
    float* cred = (float*)(lds + 32768);
    {
    {
    }
    unsigned po = (unsigned)(WS_PAO + ((size_t)(w * 4 * NH + h) * DC + l * 8) * 2);
    unsigned pc = (unsigned)(WS_PAC + (size_t)((w * 4 + g) * NH + h) * 8);
    unsigned lw_ = (unsigned)(65536 + ((w & 1) * 16 + (w >> 1) * 4) * LAS + l * 16), lr_ = (unsigned)(65536 + c * LAS + (w * 64 + g * 8) * 2);
    asm volatile("" : "+v"(po), "+v"(pc), "+v"(lw_), "+v"(lr_));
    const unsigned tw = (h & 1) ? tb : ta;
    const unsigned tg = wait_gate(ws + WS_FP, ta, (unsigned*)(lds + 8192));
    wait_until(tg + 90);
    {
      u32x4 pr[4];
#pragma unroll 1
      for (unsigned spins = 0; spins < SPIN_CAP; ++spins) {
        asm volatile("" ::: "memory");
        const u32x2 ck = __builtin_amdgcn_raw_buffer_load_b64(rs, pc, 0, SC1);
#pragma unroll
        for (int i = 0; i < 4; i++) pr[i] = __builtin_amdgcn_raw_buffer_load_b128(rs, po, i * NH * DC * 2, SC1);
#pragma unroll
        for (int i = 0; i < 4; i++) *(u32x4*)(lds + lw_ + i * LAS) = pr[i];
        if (__all(row_sums(pr) == ck[0] && ck[1] == tw)) break;
        __builtin_amdgcn_s_sleep(2);
      }
      __syncthreads();
      u32x4 af[2][2];
#pragma unroll
      for (int tl = 0; tl < 2; tl++)
#pragma unroll
        for (int s = 0; s < 2; s++) af[tl][s] = *(const u32x4*)(lds + lr_ + tl * 16 * LAS + s * 64);
      f32x4 dd[8];
#pragma unroll
      for (int j = 0; j < 8; j++)
        dd[j] = __builtin_amdgcn_mfma_f32_16x16x32_bf16(__builtin_bit_cast(bf16x8, af[0][0]), __builtin_bit_cast(bf16x8, bv[j][0]), f32x4{0.f, 0.f, 0.f, 0.f}, 0, 0, 0);
#pragma unroll
      for (int j = 0; j < 8; j++)
        dd[j] = __builtin_amdgcn_mfma_f32_16x16x32_bf16(__builtin_bit_cast(bf16x8, af[0][1]), __builtin_bit_cast(bf16x8, bv[j][1]), dd[j], 0, 0, 0);
#pragma unroll
      for (int j = 0; j < 8; j++)
        dd[j] = __builtin_amdgcn_mfma_f32_16x16x32_bf16(__builtin_bit_cast(bf16x8, af[1][0]), __builtin_bit_cast(bf16x8, bv[j][0]), dd[j], 0, 0, 0);
#pragma unroll
      for (int j = 0; j < 8; j++)
        dd[j] = __builtin_amdgcn_mfma_f32_16x16x32_bf16(__builtin_bit_cast(bf16x8, af[1][1]), __builtin_bit_cast(bf16x8, bv[j][1]), dd[j], 0, 0, 0);
      f32x4 v[2];
#pragma unroll
      for (int j = 0; j < 8; j++) v[j >> 2][j & 3] = (dd[j][0] + dd[j][1]) + (dd[j][2] + dd[j][3]);
      *(f32x4*)(cred + (w * 4 + g) * 128 + c * 8) = v[0];
      *(f32x4*)(cred + (w * 4 + g) * 128 + c * 8 + 4) = v[1];
    }
    __syncthreads();
    {
      float s = 0.f;
#pragma unroll
      for (int i = 0; i < 8; i++) s += cred[i * 512 + t];
      const float sn = dpp<0xB1>(s);
      if ((t & 1) == 0) *(unsigned*)(cs + t) = cvtpk(s, sn);
    }
    __syncthreads();
    {
      const char* ap = (const char*)cs + ((c >> 1) & 3) * 256 + (g * 32 + (c & 1) * 16) * 2;
      const u32x4 a0 = *(const u32x4*)(ap), a1 = *(const u32x4*)(ap + 16);
      f32x4 dd[8];
#pragma unroll
      for (int j = 0; j < 8; j++)
        dd[j] = __builtin_amdgcn_mfma_f32_16x16x32_bf16(__builtin_bit_cast(bf16x8, a0), __builtin_bit_cast(bf16x8, bo[j][0]), f32x4{0.f, 0.f, 0.f, 0.f}, 0, 0, 0);
#pragma unroll
      for (int j = 0; j < 8; j++)
        dd[j] = __builtin_amdgcn_mfma_f32_16x16x32_bf16(__builtin_bit_cast(bf16x8, a1), __builtin_bit_cast(bf16x8, bo[j][1]), dd[j], 0, 0, 0);
      f32x4 v4[2];
#pragma unroll
      for (int j = 0; j < 8; j++) {
        const float u0 = (l & 8) ? dd[j][1] : dd[j][0], u1 = (l & 8) ? dd[j][3] : dd[j][2];
        const float keep = (l & 8) ? u1 : u0, give = (l & 8) ? u0 : u1;
        v4[j >> 2][j & 3] = keep + dpp<0x128>(give);
      }
      if (g < 2) {
        float* op = outs + (g * 2 + ((l >> 3) & 1)) * 512 + w * 64 + (l & 7) * 8;
        *(f32x4*)op = v4[0];
        *(f32x4*)(op + 4) = v4[1];
      }
    }
    __syncthreads();
    {
      const f32x2 va = *(const f32x2*)(outs + t * 2), vb = *(const f32x2*)(outs + 1024 + t * 2);
      const unsigned oo = (unsigned)(WS_PA + ((size_t)((t >> 8) * NH + h) * DM + (t & 255) * 2) * 8);
      st16u_sc1(rs, oo, u32x4{__float_as_uint(va[0]), ta, __float_as_uint(va[1]), tb});
      st16u_sc1(rs, oo + 2 * NH * DM * 8, u32x4{__float_as_uint(vb[0]), ta, __float_as_uint(vb[1]), tb});
    }
  if (w < 4) {
    const int bb = h >> 3, n0 = (h & 7) * 64 + w * 16;
    const unsigned og = (unsigned)(WS_PA + ((size_t)(bb * NH + (l >> 3)) * DM + n0) * 8 + (l & 7) * 16);
    u32x4 p[4];
    __builtin_amdgcn_s_sleep(10);
#pragma unroll 1
    for (unsigned spins = 0; spins < SPIN_CAP; ++spins) {
      asm volatile("" ::: "memory");
#pragma unroll
      for (int i = 0; i < 4; i++) p[i] = ld16u_sc1(rs, og + i * 8 * DM * 8);
      bool ok = true;
#pragma unroll
      for (int i = 0; i < 4; i++) ok = ok && p[i][1] == ta && p[i][3] == tb;
      if (__all(ok)) break;
      __builtin_amdgcn_s_sleep(4);
    }
    float v0 = (__uint_as_float(p[0][0]) + __uint_as_float(p[1][0])) + (__uint_as_float(p[2][0]) + __uint_as_float(p[3][0]));
    float v1 = (__uint_as_float(p[0][2]) + __uint_as_float(p[1][2])) + (__uint_as_float(p[2][2]) + __uint_as_float(p[3][2]));
    v0 += dpp<0x128>(v0); v0 = xrow_sum(v0);
    v1 += dpp<0x128>(v1); v1 = xrow_sum(v1);
    if (l < 8) {
      *(unsigned*)(out + bb * 1024 + n0 + l * 2) = cvtpk(v0, v1);
      if (h == 0 && t == 0) st_u64(ws + WS_EPA, tag);
    }
  }
    }
  }
}

__device__ __forceinline__ void role_A2(int a, const __hip_bfloat16* __restrict__ ckv, char* __restrict__ ws,
                                        __amdgpu_buffer_rsrc_t rs, char* lds) {
  const int t = threadIdx.x, l = t & 63;
  const int w = __builtin_amdgcn_readfirstlane(t >> 6);
  const int g = l >> 4, c = l & 15;
  u64 ep = 0;
  if (t == 0) ep = ld_u64(ws + WS_EPA);
  const int grp = w >> 2, wq = w & 3;
  const int b = a / SPLIT_A, sp = a % SPLIT_A;
  const int r = l & 31, hh = l >> 5;
  constexpr unsigned CB = (unsigned)((size_t)NB * SK * DC * 2);
  constexpr int NTL = KA / 64;
  const unsigned cvo = (unsigned)((g * DC + 128 * wq + c * 8) * 2);
  const unsigned cso = (unsigned)(((size_t)b * SK + sp * KA + grp * 32) * DC * 2);
  auto load_tile = [&](u32x4 (&pr)[8], int tile) {
    const bool ok = tile < NTL;
    const __amdgpu_buffer_rsrc_t rc = mkrs(ckv, ok ? CB : 0u);
    const unsigned so = ok ? cso + (unsigned)((tile + sp) & (NTL - 1)) * 64 * DC * 2 : 0u;
#pragma unroll
    for (int u = 0; u < 8; u++) pr[u] = __builtin_amdgcn_raw_buffer_load_b128(rc, cvo, so + u * 4 * DC * 2, NT);
  };
  char* img = lds + w * 8192;
  float* sred = (float*)(lds + 65536 + grp * 16384);
  float* mxs = (float*)(lds + 98304 + grp * 512);
  unsigned* pbuf = (unsigned*)(lds + 102400 + grp * 2048);
  float* lall = (float*)(lds + 106496);
  const unsigned imga = (unsigned)(uintptr_t)(__attribute__((address_space(3))) char*)img;
  auto put_tile = [&](u32x4 (&pr)[8]) {
#pragma unroll
    for (int u = 0; u < 8; u++) *(u32x4*)(img + lds_off(4 * u + g, c)) = pr[u];
  };
  u32x4 pre[2][8];
  load_tile(pre[0], 0);
  load_tile(pre[1], 1);
  const u64 tag0 = next_tag(uni64(ep));
  put_tile(pre[0]);
  __builtin_amdgcn_sched_barrier(0);
  load_tile(pre[0], 2);
  if (t < 64) {
#pragma unroll 1
    for (unsigned spins = 0; spins < SPIN_CAP; ++spins) {
      __builtin_amdgcn_s_sleep(32);
      if (__all(ld_u64(ws + WS_F1 + (l & 31) * 8) == tag0)) break;
    }
  }
  if (t == 0) *(u64*)(lds + 110592) = tag0;
  __syncthreads();
  const u64 tag = uni64(*(const u64*)(lds + 110592));
  const unsigned ta = (unsigned)tag, tb = (unsigned)(tag >> 32);
  bf16x8 qf[8];
  {
    char* qi = lds + 65536;
    const unsigned qo = (unsigned)(WS_QABS + (size_t)b * NH * DC * 2 + t * 16);
    u32x4 qv[4];
#pragma unroll
    for (int i = 0; i < 4; i++) qv[i] = ld16u_sc1(rs, qo + i * 8192);
#pragma unroll
    for (int i = 0; i < 4; i++) *(u32x4*)(qi + (i * 8 + w) * 1040 + l * 16) = qv[i];
    __syncthreads();
#pragma unroll
    for (int s = 0; s < 8; s++) qf[s] = __builtin_bit_cast(bf16x8, *(const u32x4*)(qi + r * 1040 + (128 * wq + 16 * s + 8 * hh) * 2));
#pragma unroll
    for (int s = 0; s < 8; s++) asm volatile("" : "+v"(qf[s]));
    __syncthreads();
  }
  f32x16 o[4];
#pragma unroll
  for (int i = 0; i < 4; i++)
#pragma unroll
    for (int j = 0; j < 16; j++) o[i][j] = 0.f;
  float m = -INFINITY, lsum = 0.f;
  const int q4 = (l & 15) >> 2, p4 = l & 3, colh = g & 1;
  auto tile_body = [&](int tile, u32x4 (&nx)[8]) {
    f32x16 s;
#pragma unroll
    for (int j = 0; j < 16; j++) s[j] = 0.f;
    {
      u32x4 af[8];
#pragma unroll
      for (int ks = 0; ks < 8; ks++) af[ks] = *(const u32x4*)(img + lds_off(r, 2 * ks + hh));
      __builtin_amdgcn_sched_barrier(0);
#pragma unroll
      for (int ks = 0; ks < 8; ks++) s = __builtin_amdgcn_mfma_f32_32x32x16_bf16(__builtin_bit_cast(bf16x8, af[ks]), qf[ks], s, 0, 0, 0);
    }
    f32x4* sr = (f32x4*)sred;
#pragma unroll
    for (int k4 = 0; k4 < 4; k4++) sr[(k4 * 4 + wq) * 64 + l] = f32x4{s[4 * k4], s[4 * k4 + 1], s[4 * k4 + 2], s[4 * k4 + 3]};
    __syncthreads();
    float x[4];
    {
      const f32x4 v = (sr[(wq * 4) * 64 + l] + sr[(wq * 4 + 1) * 64 + l]) + (sr[(wq * 4 + 2) * 64 + l] + sr[(wq * 4 + 3) * 64 + l]);
#pragma unroll
      for (int e = 0; e < 4; e += 2) {
        const unsigned r1 = cvtpk(v[e], v[e + 1]);
        const unsigned r2 = cvtpk(bf_lo(r1) * SCALE, bf_hi(r1) * SCALE);
        x[e] = bf_lo(r2); x[e + 1] = bf_hi(r2);
      }
      float mq = fmaxf(fmaxf(x[0], x[1]), fmaxf(x[2], x[3]));
      auto sx = __builtin_amdgcn_permlane32_swap(__float_as_uint(mq), __float_as_uint(mq), false, false);
      mq = fmaxf(__uint_as_float(sx[0]), __uint_as_float(sx[1]));
      if (hh == 0) mxs[r * 4 + wq] = mq;
    }
    __syncthreads();
    const f32x4 m4 = *(const f32x4*)(mxs + r * 4);
    const float mn = fmaxf(fmaxf(m, fmaxf(m4[0], m4[1])), fmaxf(m4[2], m4[3]));
    const float alpha = fexp2((m - mn) * LOG2E);
    {
      const unsigned p0 = cvtpk(fexp2((x[0] - mn) * LOG2E), fexp2((x[1] - mn) * LOG2E));
      const unsigned p1 = cvtpk(fexp2((x[2] - mn) * LOG2E), fexp2((x[3] - mn) * LOG2E));
      *(u32x2*)(pbuf + l * 8 + wq * 2) = u32x2{p0, p1};
      lsum = lsum * alpha + ((bf_lo(p0) + bf_hi(p0)) + (bf_lo(p1) + bf_hi(p1)));
    }
    m = mn;
    __syncthreads();
    unsigned pk[8];
    {
      const u32x4 a0 = *(const u32x4*)(pbuf + l * 8), a1 = *(const u32x4*)(pbuf + l * 8 + 4);
      pk[0] = a0[0]; pk[1] = a0[1]; pk[2] = a0[2]; pk[3] = a0[3]; pk[4] = a1[0]; pk[5] = a1[1]; pk[6] = a1[2]; pk[7] = a1[3];
    }
    if (__any(alpha != 1.f)) {
#pragma unroll
      for (int i = 0; i < 4; i++)
#pragma unroll
        for (int j = 0; j < 16; j++) o[i][j] *= alpha;
    }
#pragma unroll
    for (int s2 = 0; s2 < 2; s2++) {
      u32x2 tv[4][2];
#pragma unroll
      for (int ct = 0; ct < 4; ct++) {
        const int ch = 4 * ct + 2 * colh + (p4 >> 1);
        const unsigned a0 = lds_off(4 * hh + q4, ch) + 8 * (p4 & 1);
        const unsigned a1 = lds_off(8 + 4 * hh + q4, ch) + 8 * (p4 & 1);
        if (s2 == 0) {
          asm volatile("ds_read_b64_tr_b16 %0, %1" : "=v"(tv[ct][0]) : "v"(imga + a0) : "memory");
          asm volatile("ds_read_b64_tr_b16 %0, %1" : "=v"(tv[ct][1]) : "v"(imga + a1) : "memory");
        } else {
          asm volatile("ds_read_b64_tr_b16 %0, %1 offset:4096" : "=v"(tv[ct][0]) : "v"(imga + a0) : "memory");
          asm volatile("ds_read_b64_tr_b16 %0, %1 offset:4096" : "=v"(tv[ct][1]) : "v"(imga + a1) : "memory");
        }
      }
      asm volatile("s_waitcnt lgkmcnt(0)" ::: "memory");
      __builtin_amdgcn_sched_barrier(0);
      u32x4 pb = {pk[4 * s2], pk[4 * s2 + 1], pk[4 * s2 + 2], pk[4 * s2 + 3]};
#pragma unroll
      for (int ct = 0; ct < 4; ct++) {
        u32x4 av = {tv[ct][0][0], tv[ct][0][1], tv[ct][1][0], tv[ct][1][1]};
        o[ct] = __builtin_amdgcn_mfma_f32_32x32x16_bf16(__builtin_bit_cast(bf16x8, av), __builtin_bit_cast(bf16x8, pb), o[ct], 0, 0, 0);
      }
    }
    put_tile(nx);
    __builtin_amdgcn_sched_barrier(0);
    load_tile(nx, tile + 3);
  };
#pragma unroll 1
  for (int tile = 0; tile < NTL; tile += 2) {
    tile_body(tile, pre[1]);
    tile_body(tile + 1, pre[0]);
  }
  constexpr int OST = 516;
  float* ob = (float*)lds;
  float* mlb = (float*)(lds + 32 * OST * 4);
  lall[w * 64 + l] = lsum;
  __syncthreads();
  {
    const float* lp = lall + grp * 256 + r;
    lsum = ((lp[0] + lp[32]) + (lp[64] + lp[96])) + ((lp[128] + lp[160]) + (lp[192] + lp[224]));
  }
  if (grp == 1) {
#pragma unroll
    for (int ct = 0; ct < 4; ct++)
#pragma unroll
      for (int j4 = 0; j4 < 4; j4++)
        *(f32x4*)(ob + r * OST + 128 * wq + 32 * ct + 8 * j4 + 4 * hh) =
            f32x4{o[ct][4 * j4], o[ct][4 * j4 + 1], o[ct][4 * j4 + 2], o[ct][4 * j4 + 3]};
    if (wq == 0 && l < 32) { mlb[2 * r] = m; mlb[2 * r + 1] = lsum; }
  }
  __syncthreads();
  if (grp == 0) {
    const float m1 = mlb[2 * r], l1 = mlb[2 * r + 1];
    const float M = fmaxf(m, m1);
    const float f0 = fexp2((m - M) * LOG2E), f1 = fexp2((m1 - M) * LOG2E);
#pragma unroll
    for (int ct = 0; ct < 4; ct++)
#pragma unroll
      for (int j4 = 0; j4 < 4; j4++) {
        float* p = ob + r * OST + 128 * wq + 32 * ct + 8 * j4 + 4 * hh;
        const f32x4 o1 = *(const f32x4*)p;
        *(f32x4*)p = f32x4{o[ct][4 * j4] * f0 + o1[0] * f1, o[ct][4 * j4 + 1] * f0 + o1[1] * f1,
                           o[ct][4 * j4 + 2] * f0 + o1[2] * f1, o[ct][4 * j4 + 3] * f0 + o1[3] * f1};
      }
    if (wq == 0 && l < 32)
      st16u_sc1(rs, (unsigned)(WS_PAML + ((size_t)(b * SPLIT_A + sp) * NH + r) * 16),
                u32x4{__float_as_uint(M), ta, __float_as_uint(lsum * f0 + l1 * f1), tb});
  }
  float* wgt = mlb + 64;
  unsigned* pcs = (unsigned*)(mlb + 128);
  if (t < 256) {
    const unsigned mo = (unsigned)(WS_PAML + ((size_t)(b * SPLIT_A + (t & 7)) * NH + (t >> 3)) * 16);
    u32x4 mv;
#pragma unroll 1
    for (unsigned spins = 0; spins < SPIN_CAP; ++spins) {
      __builtin_amdgcn_s_sleep(16);
      asm volatile("" ::: "memory");
      mv = ld16u_sc1(rs, mo);
      if (__all(mv[1] == ta && mv[3] == tb)) break;
    }
    const float mm = __uint_as_float(mv[0]);
    float M = fmaxf(mm, dpp<0xB1>(mm)); M = fmaxf(M, dpp<0x4E>(M)); M = fmaxf(M, dpp<0x141>(M));
    const float f = fexp2((mm - M) * LOG2E);
    float L = f * __uint_as_float(mv[2]);
    L += dpp<0xB1>(L); L += dpp<0x4E>(L); L += dpp<0x141>(L);
    if ((t & 7) == sp) wgt[t >> 3] = f / L;
  }
  __syncthreads();
  {
    const unsigned pao = (unsigned)(WS_PAO + (size_t)(b * SPLIT_A + sp) * NH * DC * 2);
    u32x4 pv[4];
#pragma unroll
    for (int i = 0; i < 4; i++) {
      const int hd = i * 8 + w;
      const float f = wgt[hd];
      const f32x4 v0 = *(const f32x4*)(ob + hd * OST + l * 8) * f, v1 = *(const f32x4*)(ob + hd * OST + l * 8 + 4) * f;
      pv[i] = u32x4{cvtpk(v0[0], v0[1]), cvtpk(v0[2], v0[3]), cvtpk(v1[0], v1[1]), cvtpk(v1[2], v1[3])};
      asm volatile("" : "+v"(pv[i]));
    }
    {
      const unsigned cs = row_sums(pv);
      if (c == 0) pcs[g * 8 + w] = cs;
    }
    const unsigned tg = wait_gate(ws + WS_FP, ta, (unsigned*)(mlb + 96));
    const u32x2 c2 = *(const u32x2*)(pcs + (t & 15) * 2);
    const unsigned co = t < 16 ? (unsigned)(WS_PAC + (size_t)(b * SPLIT_A + sp) * NH * 8 + t * 16) : 0x7ffffff0u;
    wait_until(tg);
#pragma unroll
    for (int i = 0; i < 4; i++) st16u_sc1(rs, pao + (unsigned)(i * 512 + t) * 16, pv[i]);
    if (w == 0) st16u_sc1(rs, co, u32x4{c2[0], ta, c2[1], tb});
  }
}

__global__ __launch_bounds__(512, 2) void k_fused(const __hip_bfloat16* __restrict__ xA, const __hip_bfloat16* __restrict__ ckv,
                                                  const __hip_bfloat16* __restrict__ wqA, const __hip_bfloat16* __restrict__ wuk,
                                                  const __hip_bfloat16* __restrict__ wuv, const __hip_bfloat16* __restrict__ woA,
                                                  const __hip_bfloat16* __restrict__ xB, const __hip_bfloat16* __restrict__ kc,
                                                  const __hip_bfloat16* __restrict__ vc, const __hip_bfloat16* __restrict__ wqB,
                                                  const __hip_bfloat16* __restrict__ woB, char* __restrict__ ws,
                                                  __hip_bfloat16* __restrict__ out) {
  __shared__ __attribute__((aligned(16))) char lds[131072];
  asm volatile("" :: "s"(xA), "s"(ckv), "s"(wqA), "s"(wuk), "s"(wuv), "s"(woA), "s"(xB), "s"(kc), "s"(vc), "s"(wqB), "s"(woB), "s"(ws), "s"(out));
  const int blk = blockIdx.x, xcd = blk & 7, r = blk >> 3;
  unsigned pfv = 0;
  {
    u64 pc; unsigned len;
    asm volatile("s_getpc_b64 %0\n.Lcode_begin:\n\ts_mov_b32 %1, .Lcode_end-.Lcode_begin" : "=s"(pc), "=s"(len));
    const unsigned idx = (unsigned)(r * 8 + (threadIdx.x >> 6));
    const __amdgpu_buffer_rsrc_t rc = mkrs((const void*)pc, len);
    pfv = __builtin_amdgcn_raw_buffer_load_b32(rc, idx * 1024 + (threadIdx.x & 63) * 16, 0, 0);
  }
  const __amdgpu_buffer_rsrc_t rs = mkrs(ws, (unsigned)WS_END);
  if (r < 8) {
    if (r < 4) role_A1(xcd + 8 * r, xA, wqA, wuk, wuv, woA, ws, rs, out, lds);
    else role_A2(xcd * 4 + r - 4, ckv, ws, rs, lds);
  } else {
    const int rr = r - 8, hq = rr / 6;
    role_B(((0x75643120u >> (4 * xcd)) & 7) + 8 * hq, rr - 6 * hq, xB, kc, vc, wqB, woB, ws, rs, out, lds);
  }
  asm volatile(".Lcode_end:" :: "v"(pfv));
}

extern "C" void kernel_launch(void* const* d_in, const int* in_sizes, int n_in,
                              void* d_out, int out_size, void* d_ws, size_t ws_size,
                              hipStream_t stream) {
  k_fused<<<256, 512, 0, stream>>>((const __hip_bfloat16*)d_in[0], (const __hip_bfloat16*)d_in[1], (const __hip_bfloat16*)d_in[2],
                                   (const __hip_bfloat16*)d_in[3], (const __hip_bfloat16*)d_in[4], (const __hip_bfloat16*)d_in[5],
                                   (const __hip_bfloat16*)d_in[6], (const __hip_bfloat16*)d_in[7], (const __hip_bfloat16*)d_in[8],
                                   (const __hip_bfloat16*)d_in[9], (const __hip_bfloat16*)d_in[10], (char*)d_ws,
                                   (__hip_bfloat16*)d_out);
}
```

```cpp
#include <hip/hip_runtime.h>
#include <hip/hip_bf16.h>
#include <stdint.h>

typedef __attribute__((ext_vector_type(4))) unsigned u32x4;
typedef __attribute__((ext_vector_type(2))) unsigned u32x2;
typedef __attribute__((ext_vector_type(4))) float f32x4;
typedef __attribute__((ext_vector_type(2))) float f32x2;
typedef __attribute__((ext_vector_type(3))) float f32x3;
typedef __attribute__((ext_vector_type(16))) float f32x16;
typedef __attribute__((ext_vector_type(8))) __bf16 bf16x8;
typedef __attribute__((ext_vector_type(2))) __bf16 bf16x2;
typedef __attribute__((address_space(1))) unsigned gu32;
typedef __attribute__((address_space(1))) unsigned long long gu64;
typedef unsigned long long u64;

constexpr int NB = 4;
constexpr int NH = 32;
constexpr int SK = 4096;
constexpr int DH = 128;
constexpr int DM = 512;
constexpr int DC = 512;
constexpr int HD = NH * DH;

constexpr int SPLIT_A = 8;
constexpr int KA = SK / SPLIT_A;
constexpr int SS = 171;
constexpr int RD = 18;
constexpr int NP = SS - 8 * RD + 8;
constexpr int NP4 = (NP + 3) & ~3;
constexpr int RST = 144;
constexpr int GI = 18;
constexpr int RE = 15;
constexpr int PRIOF = 4;

constexpr unsigned TM_F1 = 1600;
constexpr unsigned TM_FC = 3650;
constexpr unsigned TM_PUT = 430;
constexpr unsigned TM_GET = 85;
constexpr unsigned TM_T4 = 400;
constexpr unsigned TM_SW = 40;

constexpr float SCALE = 0.08838834764831845f;
constexpr float LOG2E = 1.4426950408889634f;
constexpr int NT = 2;
constexpr int SC1 = 16;

constexpr size_t WS_EPA  = 0;
constexpr size_t WS_EPB  = 256;
constexpr int LINE = 256;
constexpr size_t WS_F1   = 4096;
constexpr size_t WS_FP   = 12288;
constexpr size_t WS_QABS = 16384;
constexpr size_t WS_PAML = WS_QABS + (size_t)NB * NH * DC * 2;
constexpr size_t WS_PAC  = WS_PAML + (size_t)NB * SPLIT_A * NH * 16;
constexpr size_t WS_PAO  = WS_PAC + (size_t)NB * SPLIT_A * NH * 8;
constexpr size_t WS_PA   = WS_PAO + (size_t)NB * SPLIT_A * NH * DC * 2;
constexpr size_t WS_PBG  = WS_PA + (size_t)NB * NH * DM * 8;
constexpr size_t WS_END  = WS_PBG + (size_t)NB * 64 * DM * 8;

__device__ __forceinline__ float bf_lo(unsigned u) { return __uint_as_float(u << 16); }
__device__ __forceinline__ float bf_hi(unsigned u) { return __uint_as_float(u & 0xffff0000u); }
__device__ __forceinline__ unsigned cvtpk(float lo, float hi) {
  f32x2 v = {lo, hi};
  return __builtin_bit_cast(unsigned, __builtin_convertvector(v, bf16x2));
}
__device__ __forceinline__ float rbf(float x) { return __uint_as_float(cvtpk(x, x) << 16); }
__device__ __forceinline__ float dot2(unsigned k, unsigned q, float acc) {
  return __builtin_amdgcn_fdot2_f32_bf16(__builtin_bit_cast(bf16x2, k), __builtin_bit_cast(bf16x2, q), acc, false);
}
template <int CTRL> __device__ __forceinline__ float dpp(float x) {
  return __builtin_bit_cast(float, __builtin_amdgcn_mov_dpp(__builtin_bit_cast(int, x), CTRL, 0xf, 0xf, true));
}
__device__ __forceinline__ float sum16(float x) {
  x += dpp<0x128>(x);
  x += dpp<0x124>(x);
  x += dpp<0x4E>(x);
  x += dpp<0xB1>(x);
  return x;
}
__device__ __forceinline__ float xrow_max(float x) {
  auto s = __builtin_amdgcn_permlane16_swap(__float_as_uint(x), __float_as_uint(x), false, false);
  x = fmaxf(__uint_as_float(s[0]), __uint_as_float(s[1]));
  auto t = __builtin_amdgcn_permlane32_swap(__float_as_uint(x), __float_as_uint(x), false, false);
  return fmaxf(__uint_as_float(t[0]), __uint_as_float(t[1]));
}
__device__ __forceinline__ float xrow_sum(float x) {
  auto s = __builtin_amdgcn_permlane16_swap(__float_as_uint(x), __float_as_uint(x), false, false);
  x = __uint_as_float(s[0]) + __uint_as_float(s[1]);
  auto t = __builtin_amdgcn_permlane32_swap(__float_as_uint(x), __float_as_uint(x), false, false);
  return __uint_as_float(t[0]) + __uint_as_float(t[1]);
}
__device__ __forceinline__ float fexp2(float x) { return __builtin_amdgcn_exp2f(x); }
__device__ __forceinline__ __amdgpu_buffer_rsrc_t mkrs(const void* p, unsigned bytes) {
  return __builtin_amdgcn_make_buffer_rsrc((void*)p, 0, (int)bytes, 0x00020000);
}


constexpr unsigned SPIN_CAP = 1u << 21;
__device__ __forceinline__ u64 ld_u64(const void* p) {
  return __hip_atomic_load((gu64*)p, __ATOMIC_RELAXED, __HIP_MEMORY_SCOPE_AGENT);
}
__device__ __forceinline__ void st_u64(void* p, u64 v) {
  __hip_atomic_store((gu64*)p, v, __ATOMIC_RELAXED, __HIP_MEMORY_SCOPE_AGENT);
}
__device__ __forceinline__ u64 next_tag(u64 e) { return e + 0x0000000100000001ull; }
__device__ __forceinline__ u64 uni64(u64 v) {
  const unsigned lo = __builtin_amdgcn_readfirstlane((unsigned)v), hi = __builtin_amdgcn_readfirstlane((unsigned)(v >> 32));
  return ((u64)hi << 32) | lo;
}
__device__ __forceinline__ void signal_flag(char* flag, u64 tag) {
  asm volatile("s_waitcnt vmcnt(0)" ::: "memory");
  __syncthreads();
  if (threadIdx.x == 0) st_u64(flag, tag);
}
template <int CTRL> __device__ __forceinline__ int dppi(int x) { return __builtin_amdgcn_mov_dpp(x, CTRL, 0xf, 0xf, true); }
__device__ __forceinline__ int sum8i(int x) { x += dppi<0xB1>(x); x += dppi<0x4E>(x); x += dppi<0x141>(x); return x; }
__device__ __forceinline__ unsigned row_sums(const u32x4 (&p)[4]) {
  unsigned c4[4];
#pragma unroll
  for (int i = 0; i < 4; i++) c4[i] = (p[i][0] + p[i][1]) + (p[i][2] + p[i][3]);
  auto s0 = __builtin_amdgcn_permlane32_swap(c4[0], c4[2], false, false);
  auto s1 = __builtin_amdgcn_permlane32_swap(c4[1], c4[3], false, false);
  auto s2 = __builtin_amdgcn_permlane16_swap(s0[0] + s0[1], s1[0] + s1[1], false, false);
  int x = (int)(s2[0] + s2[1]);
  x += dppi<0x128>(x);
  return (unsigned)sum8i(x);
}
__device__ __forceinline__ unsigned clk() { return (unsigned)__builtin_amdgcn_s_memrealtime(); }
__device__ __forceinline__ void wait_until(unsigned t) {
#pragma unroll 1
  for (int spins = 0; spins < 4096 && (int)(t - clk()) > 0; ++spins) __builtin_amdgcn_s_sleep(1);
}
__device__ __forceinline__ unsigned ref_time(unsigned v) {
  const unsigned t0 = __builtin_amdgcn_readfirstlane(v);
  const int d = (int)(v - t0);
  int mx = max(d, dppi<0xB1>(d)); mx = max(mx, dppi<0x4E>(mx)); mx = max(mx, dppi<0x141>(mx));
  const int mean = sum8i(d) >> 3;
  return t0 + (unsigned)__builtin_amdgcn_readfirstlane((mean + mx) >> 1);
}
__device__ __forceinline__ unsigned wait_gate(const char* flag, unsigned ta, unsigned* slot, unsigned tent) {
  if (threadIdx.x < 64) {
    u64 v = 0;
    wait_until(tent + TM_FC);
#pragma unroll 1
    for (unsigned spins = 0; spins < SPIN_CAP; ++spins) {
      v = ld_u64(flag + (threadIdx.x & 7) * LINE);
      if (__all((unsigned)(v >> 32) == ta)) break;
      __builtin_amdgcn_s_sleep(12);
    }
    const unsigned tr = ref_time((unsigned)v);
    if (threadIdx.x == 0) *slot = tr - TM_PUT;
  }
  __syncthreads();
  return __builtin_amdgcn_readfirstlane(*slot);
}
__device__ __forceinline__ void st16u_sc1(__amdgpu_buffer_rsrc_t rs, unsigned off, u32x4 v) {
  __builtin_amdgcn_raw_buffer_store_b128(v, rs, off, 0, SC1);
}
__device__ __forceinline__ u32x4 ld16u_sc1(__amdgpu_buffer_rsrc_t rs, unsigned off) {
  return __builtin_amdgcn_raw_buffer_load_b128(rs, off, 0, SC1);
}


__device__ __forceinline__ void item_dots(const u32x4 (&ks)[4], const u32x4 q, float (&s_)[4]) {
#pragma unroll
  for (int u = 0; u < 4; u++) {
    float a = dot2(ks[u][0], q[0], 0.f);
    a = dot2(ks[u][1], q[1], a);
    a = dot2(ks[u][2], q[2], a);
    a = dot2(ks[u][3], q[3], a);
    s_[u] = a;
    asm volatile("" : "+v"(s_[u]));
  }
}
__device__ __forceinline__ void item_unpack(const u32x4 (&vs)[4], f32x2 (&vf)[4][4]) {
#pragma unroll
  for (int u = 0; u < 4; u++)
#pragma unroll
    for (int j = 0; j < 4; j++) { vf[u][j] = f32x2{bf_lo(vs[u][j]), bf_hi(vs[u][j])}; asm volatile("" : "+v"(vf[u][j])); }
}
__device__ __forceinline__ f32x2 rows_sum(const float (&o)[8]) {
  float r4[4], r2[2];
#pragma unroll
  for (int j = 0; j < 4; j++) {
    auto sx = __builtin_amdgcn_permlane32_swap(__float_as_uint(o[j]), __float_as_uint(o[j + 4]), false, false);
    r4[j] = __uint_as_float(sx[0]) + __uint_as_float(sx[1]);
  }
#pragma unroll
  for (int j = 0; j < 2; j++) {
    auto sx = __builtin_amdgcn_permlane16_swap(__float_as_uint(r4[j]), __float_as_uint(r4[j + 2]), false, false);
    r2[j] = __uint_as_float(sx[0]) + __uint_as_float(sx[1]);
  }
  return f32x2{r2[0], r2[1]};
}
__device__ __forceinline__ f32x2 item_partial(const float (&s_)[4], const f32x2 (&vf)[4][4], float& mx, float& ll) {
  float s[4];
#pragma unroll
  for (int u = 0; u < 4; u++) s[u] = rbf(rbf(sum16(s_[u])) * SCALE);
  mx = xrow_max(fmaxf(fmaxf(s[0], s[1]), fmaxf(s[2], s[3])));
  float p[4];
#pragma unroll
  for (int u = 0; u < 4; u++) p[u] = fexp2((s[u] - mx) * LOG2E);
  ll = xrow_sum((p[0] + p[1]) + (p[2] + p[3]));
  float o[8];
#pragma unroll
  for (int j = 0; j < 4; j++) {
    f32x2 v = vf[0][j] * p[0];
#pragma unroll
    for (int u = 1; u < 4; u++) v = __builtin_elementwise_fma(vf[u][j], f32x2{p[u], p[u]}, v);
    o[2 * j] = v[0]; o[2 * j + 1] = v[1];
  }
  return rows_sum(o);
}
__device__ __forceinline__ void role_B(int h, int j6, const __hip_bfloat16* __restrict__ xB,
                                       const __hip_bfloat16* __restrict__ kc, const __hip_bfloat16* __restrict__ vc,
                                       const __hip_bfloat16* __restrict__ wqB, const __hip_bfloat16* __restrict__ woB,
                                       char* __restrict__ ws, __amdgpu_buffer_rsrc_t rs,
                                       __hip_bfloat16* __restrict__ out, char* lds) {
  const int t = threadIdx.x, l = t & 63;
  const int w = __builtin_amdgcn_readfirstlane(t >> 6);
  const int g = l >> 4, c = l & 15;
  const int pr = j6 >= 3 ? 1 : 0, k = j6 - 3 * pr;
  const unsigned tent = clk();
  const int b0 = 2 * pr;
  const int NI = k == 1 ? 2 * (256 - SS) : SS;
  const int e0 = k == 1 ? (h & 1) : (k >> 1);
  u32x4 xr[2][2], wr[16];
  {
    const __amdgpu_buffer_rsrc_t rx = mkrs(xB, NB * DM * 2);
    const int rq_ = (0x203120 >> (4 * j6)) & 3;
    const int cw = w ^ ((rq_ & 2) << 1), r0 = rq_ & 1;
    const unsigned xo = (unsigned)((b0 * DM + cw * 64 + g * 16) * 2);
#pragma unroll
    for (int bb = 0; bb < 2; bb++)
#pragma unroll
      for (int i = 0; i < 2; i++) xr[bb][i] = __builtin_amdgcn_raw_buffer_load_b128(rx, xo + bb * DM * 2, (i ^ r0) * 16, 0);
    const __amdgpu_buffer_rsrc_t rq = mkrs(wqB, DM * HD * 2);
    const unsigned vo = (unsigned)(((cw * 64 + g * 16) * HD + h * DH + c * 8) * 2);
    const unsigned vo0 = vo + r0 * 8 * HD * 2, vo1 = vo + (1 - r0) * 8 * HD * 2;
#pragma unroll
    for (int i = 0; i < 16; i++) wr[i] = __builtin_amdgcn_raw_buffer_load_b128(rq, i < 8 ? vo0 : vo1, (i & 7) * HD * 2, 0);
  }
  constexpr unsigned KVB = (unsigned)((size_t)NB * NH * SK * DH * 2);
  const __amdgpu_buffer_rsrc_t rk = mkrs(kc, KVB), rv = mkrs(vc, KVB);
  const unsigned lo = (unsigned)(g * 256 + c * 16);
  const unsigned hb0 = ((unsigned)((b0 + e0) * NH + h) << 20) + (k == 1 ? (unsigned)SS << 12 : 0u);
  const unsigned hb1 = ((unsigned)((b0 + 1 - e0) * NH + h) << 20) + ((unsigned)(2 * SS - 256) << 12);
  auto soff = [&](int x) { return ((k == 1 && x >= 256 - SS) ? hb1 : hb0) + ((unsigned)x << 12); };
  auto ld4 = [&](u32x4 (&d)[4], __amdgpu_buffer_rsrc_t r, unsigned so) {
#pragma unroll
    for (int u = 0; u < 4; u++) d[u] = __builtin_amdgcn_raw_buffer_load_b128(r, lo + u * 1024, so, NT);
  };
  u32x4 kr[4], vr[4], ke[4], ve[4];
  ld4(kr, rk, soff(w));
  ld4(vr, rv, soff(w));
  u64* eps = (u64*)(lds + 33808);
  u64 epv = 0;
  if (t < 3) epv = ld_u64(ws + (t == 0 ? WS_EPA : WS_EPB + 256 * (b0 + (t == 1 ? e0 : 1 - e0))));
  float* red = (float*)lds;
  unsigned* qs = (unsigned*)(lds + 32768);
  unsigned* cnt0 = (unsigned*)(lds + 33792);
  unsigned* cntd = (unsigned*)(lds + 33796);
  float* rec0 = (float*)(lds + 36864);
  float* mlr0 = (float*)(lds + 40960);
  char* prv = lds + 45056 + w * 1024;
  float* recd = (float*)(lds + 65536);
  float* mld = (float*)(lds + 98304);
  const unsigned cnta = (unsigned)(uintptr_t)(__attribute__((address_space(3))) char*)cntd;
  float* mxd = (float*)(lds + 33800);
  if (t == 0) { *cnt0 = 0u; *cntd = 8u * (RD + 1); mxd[0] = -INFINITY; mxd[1] = -INFINITY; }
  if (t < 64 * (NP4 - NP + 1)) *(f32x2*)(recd + (NP - 1 + (t >> 6)) * RST + 2 * l) = f32x2{0.f, 0.f};
  {
    float a0[8], a1[8];
#pragma unroll
    for (int j = 0; j < 8; j++) { a0[j] = 0.f; a1[j] = 0.f; }
#pragma unroll
    for (int i = 0; i < 16; i += 2) {
      const unsigned x0 = xr[0][i >> 3][(i >> 1) & 3], x1 = xr[1][i >> 3][(i >> 1) & 3];
#pragma unroll
      for (int j = 0; j < 4; j++) {
        const unsigned pl = __builtin_amdgcn_perm(wr[i + 1][j], wr[i][j], 0x05040100u);
        const unsigned ph = __builtin_amdgcn_perm(wr[i + 1][j], wr[i][j], 0x07060302u);
        a0[2 * j] = dot2(pl, x0, a0[2 * j]); a0[2 * j + 1] = dot2(ph, x0, a0[2 * j + 1]);
        a1[2 * j] = dot2(pl, x1, a1[2 * j]); a1[2 * j + 1] = dot2(ph, x1, a1[2 * j + 1]);
      }
    }
#pragma unroll
    for (int j = 0; j < 8; j++) asm volatile("" : "+v"(a0[j]), "+v"(a1[j]));
    __builtin_amdgcn_sched_barrier(0);
    ld4(ke, rk, soff(w + 8));
    ld4(ve, rv, soff(w + 8));
    __builtin_amdgcn_sched_barrier(0);
    float* rp = red + (w * 4 + g) * 256 + c * 8;
    *(f32x4*)(rp) = f32x4{a0[0], a0[1], a0[2], a0[3]};
    *(f32x4*)(rp + 4) = f32x4{a0[4], a0[5], a0[6], a0[7]};
    *(f32x4*)(rp + 128) = f32x4{a1[0], a1[1], a1[2], a1[3]};
    *(f32x4*)(rp + 132) = f32x4{a1[4], a1[5], a1[6], a1[7]};
  }
  __syncthreads();
  {
    const int o = t >> 1, hf = t & 1;
    float s = 0.f;
#pragma unroll
    for (int i = 0; i < 16; i++) s += red[(hf * 16 + i) * 256 + o];
    s += dpp<0xB1>(s);
    const float sn = dpp<0x4E>(s);
    if ((t & 3) == 0) qs[o >> 1] = cvtpk(s, sn);
    if (t < 3) eps[t] = epv;
  }
  __syncthreads();
  const u64 epa = uni64(eps[0]), tg0 = next_tag(uni64(eps[1])), tg1 = next_tag(uni64(eps[2]));
  u32x4 q = *(const u32x4*)(qs + e0 * 64 + c * 4);
  const u32x4 q1 = *(const u32x4*)(qs + (1 - e0) * 64 + c * 4);
  float m = -INFINITY, lsum = 0.f, acc[8];
#pragma unroll
  for (int j = 0; j < 8; j++) acc[j] = 0.f;
  float s_[4]; f32x2 vf[4][4];
  auto part0 = [&](u32x4 (&ks)[4]) { item_dots(ks, q, s_); };
  auto part1 = [&](u32x4 (&vs)[4]) { item_unpack(vs, vf); };
  auto part2 = [&]() {
    float s[4];
#pragma unroll
    for (int u = 0; u < 4; u++) s[u] = rbf(rbf(sum16(s_[u])) * SCALE);
    const float mx = fmaxf(fmaxf(m, fmaxf(s[0], s[1])), fmaxf(s[2], s[3]));
    const float alpha = fexp2((m - mx) * LOG2E);
    m = mx;
    float p[4];
#pragma unroll
    for (int u = 0; u < 4; u++) p[u] = fexp2((s[u] - mx) * LOG2E);
    lsum = lsum * alpha + ((p[0] + p[1]) + (p[2] + p[3]));
#pragma unroll
    for (int j = 0; j < 8; j++) acc[j] *= alpha;
#pragma unroll
    for (int u = 0; u < 4; u++)
#pragma unroll
      for (int j = 0; j < 4; j++) {
        acc[2 * j]     = fmaf(p[u], vf[u][j][0], acc[2 * j]);
        acc[2 * j + 1] = fmaf(p[u], vf[u][j][1], acc[2 * j + 1]);
      }
  };
  auto front = [&](__amdgpu_buffer_rsrc_t rk2, __amdgpu_buffer_rsrc_t rv2, unsigned so) {
    __builtin_amdgcn_sched_barrier(0);
    ld4(kr, rk2, so);
    __builtin_amdgcn_sched_barrier(0);
    part1(vr);
    __builtin_amdgcn_sched_barrier(0);
    ld4(vr, rv2, so);
    __builtin_amdgcn_sched_barrier(0);
  };
  auto put_partial = [&](f32x2 r2, float mw, float ll, float* rec_, float* ml_, float* mx_) {
    *(f32x2*)(rec_ + c * 8 + g * 2) = r2;
    if (l == 0) {
      *(f32x2*)ml_ = f32x2{mw, ll};
      asm volatile("ds_max_f32 %0, %1" :: "v"((unsigned)(uintptr_t)(__attribute__((address_space(3))) float*)mx_), "v"(mw) : "memory");
    }
  };
  auto wave_partial = [&](float* rec_, float* ml_, float* mx_) {
    const float mw = xrow_max(m);
    const float fq = fexp2((m - mw) * LOG2E);
    const float ll = xrow_sum(lsum * fq);
    float o[8];
#pragma unroll
    for (int j = 0; j < 8; j++) o[j] = acc[j] * fq;
    put_partial(rows_sum(o), mw, ll, rec_, ml_, mx_);
  };
  auto part2c = [&](float* rec_, float* ml_) {
    float mx, ll;
    const f32x2 r2 = item_partial(s_, vf, mx, ll);
    put_partial(r2, mx, ll, rec_, ml_, mxd);
  };
  bool sw = k != 1;
  auto do_switch = [&]() {
    wave_partial(rec0 + w * 128, mlr0 + w * 2, mxd + 1);
    asm volatile("s_waitcnt lgkmcnt(0)" ::: "memory");
    if (l == 0) asm volatile("ds_add_u32 %0, %1" :: "v"(cnta - 4u), "v"(1u) : "memory");
    m = -INFINITY; lsum = 0.f;
#pragma unroll
    for (int j = 0; j < 8; j++) acc[j] = 0.f;
    q = q1; sw = true;
  };
  if (w >= 4) __builtin_amdgcn_s_setprio(1);
  part0(kr);
  front(rk, rv, soff(w + 16));
  part2();
  part0(ke); part1(ve); part2();
  int x = w + 16;
  const int rw = 3 + 4 * (h >> 3);
  const int re = k != 1 ? -1 : rw + 2 > RE ? rw + 2 : RE;
  u32x4 wo[16];
  __builtin_amdgcn_sched_barrier(0);
#pragma unroll
  for (int i = 0; i < 16; i++) asm volatile("" : "=v"(wo[i]));
  const int nseg = k == 1 ? 2 : 1, sp = k == 1 ? 1 : 0;
  auto store_off = [&](int sg) {
    const int b = b0 + (sg ? 1 - e0 : e0);
    const unsigned og_ = (unsigned)(WS_PBG + ((size_t)(b * 64 + h * 2 + sp) * DM + w * 64 + c * 4 + g) * 8);
    return (g & 1) == 0 ? og_ : 0x7ffffff0u;
  };
  const unsigned apo = (unsigned)((c & 1) * 272 + g * 64);
  auto project = [&](const char* ap, float M, float L, bool lred) {
    u32x4 a[4];
#pragma unroll
    for (int s = 0; s < 4; s++) a[s] = *(const u32x4*)(ap + s * 16);
    __builtin_amdgcn_sched_barrier(0);
    if (lred) L = xrow_sum(sum16(L));
    __builtin_amdgcn_sched_barrier(0);
    f32x4 d[4];
#pragma unroll
    for (int s = 0; s < 4; s++)
#pragma unroll
      for (int j = 0; j < 4; j++)
        d[j] = __builtin_amdgcn_mfma_f32_16x16x32_bf16(__builtin_bit_cast(bf16x8, a[s]), __builtin_bit_cast(bf16x8, wo[4 * s + j]),
                                                       s ? d[j] : f32x4{0.f, 0.f, 0.f, 0.f}, 0, 0, 0);
    return f32x3{g < 2 ? d[0][0] + d[0][1] : d[3][0] + d[3][1], g < 2 ? d[1][0] + d[1][1] : M, g < 2 ? d[2][0] + d[2][1] : L};
  };
  f32x3 seg0 = {0.f, 0.f, 0.f};
  auto events = [&](int rd) {
    if (rd == PRIOF) __builtin_amdgcn_s_setprio(0);
    if (!sw && x >= 256 - SS) do_switch();
    if (rd == rw + 2) {
#pragma unroll
      for (int s = 0; s < 4; s++) {
        u32x4 o[4];
#pragma unroll
        for (int e = 0; e < 4; e++)
#pragma unroll
          for (int j = 0; j < 4; j++) o[j][e] = __builtin_amdgcn_perm(wo[4 * s + e][2 + (j >> 1)], wo[4 * s + e][j >> 1], (j & 1) ? 0x07060302u : 0x05040100u);
#pragma unroll
        for (int j = 0; j < 4; j++) wo[4 * s + j] = o[j];
        asm volatile("" : "+v"(wo[4 * s]), "+v"(wo[4 * s + 1]), "+v"(wo[4 * s + 2]), "+v"(wo[4 * s + 3]));
      }
    }
    if (rd == re) {
    for (unsigned spins = 0; __hip_atomic_load(cnt0, __ATOMIC_RELAXED, __HIP_MEMORY_SCOPE_WORKGROUP) != 8u && spins < SPIN_CAP; ++spins)
      __builtin_amdgcn_s_sleep(1);
    asm volatile("" ::: "memory");
    f32x4 q4[4];
#pragma unroll
    for (int i = 0; i < 4; i++) q4[i] = *(const f32x4*)(mlr0 + i * 4);
    const float M = mxd[1];
    float L = 0.f;
    f32x2 a2 = {0.f, 0.f};
#pragma unroll
    for (int i = 0; i < 8; i++) {
      const float fw = fexp2((q4[i >> 1][(i & 1) * 2] - M) * LOG2E);
      L = fmaf(q4[i >> 1][(i & 1) * 2 + 1], fw, L);
      a2 += *(const f32x2*)(rec0 + i * 128 + 2 * l) * fw;
    }
    const unsigned hi = cvtpk(a2[0], a2[1]);
    *(unsigned*)(prv + l * 4) = hi;
    *(unsigned*)(prv + 272 + l * 4) = cvtpk(a2[0] - bf_lo(hi), a2[1] - bf_hi(hi));
    seg0 = project(prv + apo, M, L, false);
    }
    if (rd == rw) {
      const __amdgpu_buffer_rsrc_t ro = mkrs(woB, HD * DM * 2);
      const unsigned vo = (unsigned)(((h * DH + g * 32) * DM + w * 64 + c * 4) * 2);
#pragma unroll
      for (int i = 0; i < 16; i++) {
        const u32x2 u0 = __builtin_amdgcn_raw_buffer_load_b64(ro, vo + (i & 1) * 2 * DM * 2, (i >> 1) * 4 * DM * 2, 0);
        const u32x2 u1 = __builtin_amdgcn_raw_buffer_load_b64(ro, vo + ((i & 1) * 2 + 1) * DM * 2, (i >> 1) * 4 * DM * 2, 0);
        wo[i] = u32x4{u0[0], u0[1], u1[0], u1[1]};
      }
    }
  };
#pragma unroll 1
  for (; x < 8 * RD; x += 8) {
    const int rd = x >> 3;
    events(rd);
    part0(kr);
    front(rk, rv, soff(x + 8));
    part2();
  }
  wave_partial(recd + w * RST, mld + w * 2, mxd);
  unsigned o_ml = 98304u + (l < NP ? l : 0) * 8, o_rv = 65536u + (g * RST + w * 16 + c) * 4, o_fw = 45056u + w * 1024 + ((l & 3) * 16 + (l >> 2)) * 4;
  unsigned o_fr = 45056u + w * 1024 + g * 64, o_oi = (g < 2 && (c & 1) == 0) ? 41216u + g * 272 + (w * 16 + c) * 2 : 45056u + w * 1024 + 256 + l * 4;
  unsigned o_ap = 41216u + apo, o_st = store_off(nseg - 1);
  unsigned o_s0 = k == 1 ? store_off(0) : 0x7ffffff0u;
  unsigned tl0 = (unsigned)(k == 1 ? tg1 : tg0);
  u32x4 sg0 = {__float_as_uint(seg0[0]), __float_as_uint(seg0[1]), __float_as_uint(seg0[2]), (unsigned)tg0};
  asm volatile("" : "+v"(o_s0), "+v"(tl0), "+v"(sg0));
  asm volatile("" : "+v"(o_ml), "+v"(o_rv), "+v"(o_fw), "+v"(o_fr), "+v"(o_oi), "+v"(o_ap), "+v"(o_st));
  u32x2 rep = {0u, 0u};
  {
    {
#pragma unroll 1
      while (x < NI) {
        unsigned dr; u64 sv;
        asm volatile("s_mov_b64 %1, exec\n\ts_mov_b64 exec, 1\n\tds_add_rtn_u32 %0, %2, %3\n\ts_mov_b64 exec, %1"
                     : "=&v"(dr), "=&s"(sv) : "v"(cnta), "v"(1u), "v"(kr[0][0]) : "memory");
        __builtin_amdgcn_sched_barrier(0);
        part0(kr);
        asm volatile("s_waitcnt lgkmcnt(0)" : "+v"(dr) :: "memory");
        const int xn = __builtin_amdgcn_readfirstlane(dr);
        if (h >= 24 && j6 == 2 && xn == NI - GI) {
          const unsigned now = clk();
          if (l == 0) st_u64(ws + WS_FP + (h - 24) * LINE, (next_tag(epa) << 32) | (now + ((now - tent) >> 2)));
        }
        const bool ok = xn < NI;
        front(mkrs(kc, ok ? KVB : 0u), mkrs(vc, ok ? KVB : 0u), ok ? soff(xn) : 0u);
        part2c(recd + (x - 8 * RD + 8) * RST, mld + (x - 8 * RD + 8) * 2);
        x = xn;
      }
      rep = __builtin_amdgcn_raw_buffer_load_b64(rs, w < nseg ? (unsigned)WS_FP + (l & 7) * LINE : 0x7ffffff0u, 0, SC1);
      asm volatile("s_waitcnt lgkmcnt(0)" ::: "memory");
      __syncthreads();
    }
    {
      f32x2 mli = *(const f32x2*)(lds + o_ml);
      float rv_[NP4 / 4];
#pragma unroll
      for (int r = 0; r < NP4 / 4; r++) rv_[r] = *(const float*)(lds + o_rv + r * 4 * RST * 4);
      if (l >= NI - 8 * RD + 8) mli = f32x2{-INFINITY, 0.f};
      const float M = *mxd;
      const float fi = fexp2((mli[0] - M) * LOG2E);
      *(float*)(lds + o_fw) = fi;
      f32x4 f4[(NP4 / 4 + 3) / 4];
#pragma unroll
      for (int i = 0; i < (NP4 / 4 + 3) / 4; i++) f4[i] = *(const f32x4*)(lds + o_fr + i * 16);
      float a = 0.f;
#pragma unroll
      for (int r = 0; r < NP4 / 4; r++) a = fmaf(f4[r >> 2][r & 3], rv_[r], a);
      a = xrow_sum(a);
      const float an = dpp<0xB1>(a);
      const unsigned hi = cvtpk(a, an);
      const unsigned lw = cvtpk(a - bf_lo(hi), an - bf_hi(hi));
      *(unsigned*)(lds + o_oi) = g ? lw : hi;
      __syncthreads();
      const f32x3 pv = project(lds + o_ap, M, fi * mli[1], true);
      st16u_sc1(rs, o_st, u32x4{__float_as_uint(pv[0]), __float_as_uint(pv[1]), __float_as_uint(pv[2]), tl0});
      st16u_sc1(rs, o_s0, sg0);
    }
    if (w < nseg) {
      const int sg = w;
      const int b = b0 + (sg ? 1 - e0 : e0);
      const int j64 = (h & 7) * 8 + (h >> 3) * 2 + sp;
      const u64 tg = sg ? tg1 : tg0;
      const unsigned ta = (unsigned)tg;
      const unsigned og = (unsigned)(WS_PBG + ((size_t)(b * 64 + (l >> 2)) * DM + j64 * 8) * 8 + (l & 3) * 16);
      u32x4 p[4];
      {
        const unsigned tp = (unsigned)next_tag(epa);
        unsigned ts = ref_time(rep[0]) - TM_SW;
        if (!__all(rep[1] == tp)) {
          ts = clk();
#pragma unroll 1
          for (int spins = 0; spins < 8; ++spins) {
            __builtin_amdgcn_s_sleep(16);
            asm volatile("" ::: "memory");
            const u32x2 r2 = __builtin_amdgcn_raw_buffer_load_b64(rs, (unsigned)WS_FP + (l & 7) * LINE, 0, SC1);
            if (__all(r2[1] == tp)) { ts = ref_time(r2[0]) - TM_SW; break; }
          }
        }
        __builtin_amdgcn_s_sleep(4);
        wait_until(ts);
      }
#pragma unroll 1
      for (unsigned spins = 0; spins < SPIN_CAP; ++spins) {
        asm volatile("" ::: "memory");
#pragma unroll
        for (int i = 0; i < 4; i++) p[i] = ld16u_sc1(rs, og + i * 16 * DM * 8);
        if (__all(p[0][3] == ta && p[1][3] == ta && p[2][3] == ta && p[3][3] == ta)) break;
      }
      float v0 = 0.f, v1 = 0.f, v2 = 0.f;
#pragma unroll
      for (int i = 0; i < 4; i++) {
        const float mm = dpp<0xF5>(__uint_as_float(p[i][1]));
        const float Mh = fmaxf(mm, dpp<0x141>(mm));
        const float fq = fexp2((mm - Mh) * LOG2E);
        float L = fq * dpp<0xF5>(__uint_as_float(p[i][2]));
        L += dpp<0x141>(L);
        const float wgt = fq * __builtin_amdgcn_rcpf(L);
        v0 = fmaf(__uint_as_float(p[i][0]), wgt, v0);
        v1 = fmaf(__uint_as_float(p[i][1]), wgt, v1);
        v2 = fmaf(__uint_as_float(p[i][2]), wgt, v2);
      }
      const float u2 = dpp<0xA0>(v2);
      v1 = (l & 1) ? v0 : v1;
      v0 = (l & 1) ? u2 : v0;
      v0 += dpp<0x128>(v0); v0 += dpp<0x124>(v0); v0 = xrow_sum(v0);
      v1 += dpp<0x128>(v1); v1 += dpp<0x124>(v1); v1 = xrow_sum(v1);
      if (l < 4) {
        *(unsigned*)(out + b * 1024 + 512 + j64 * 8 + l * 2) = cvtpk(v0, v1);
        if (j64 == 0 && l == 0) st_u64(ws + WS_EPB + 256 * b, tg);
      }
    }
  }
}

__device__ __forceinline__ unsigned lds_off(int row, int ch) {
  return 256 * row + 16 * (ch ^ (((row & 3) << 2) | ((row >> 2) & 3)));
}

__device__ __forceinline__ void role_A1(int h, const __hip_bfloat16* __restrict__ xA, const __hip_bfloat16* __restrict__ wqA,
                                        const __hip_bfloat16* __restrict__ wuk, const __hip_bfloat16* __restrict__ wuv,
                                        const __hip_bfloat16* __restrict__ woA, char* __restrict__ ws,
                                        __amdgpu_buffer_rsrc_t rs, __hip_bfloat16* __restrict__ out, char* lds) {
  const int t = threadIdx.x, l = t & 63;
  const int w = __builtin_amdgcn_readfirstlane(t >> 6);
  const int g = l >> 4, c = l & 15;
  const unsigned tent = clk();
  float* red = (float*)(lds + 16384);
  const unsigned vo = (unsigned)(((w * 64 + g * 16) * HD + h * DH + c * 8) * 2);
  u64 tag;
  {
    u32x4 xr[NB][2], wr[16], wk[16];
    const __amdgpu_buffer_rsrc_t rx = mkrs(xA, NB * DM * 2);
    const unsigned xo = (unsigned)((w * 64 + g * 16) * 2);
#pragma unroll
    for (int bb = 0; bb < NB; bb++)
#pragma unroll
      for (int i = 0; i < 2; i++) xr[bb][i] = __builtin_amdgcn_raw_buffer_load_b128(rx, xo + bb * DM * 2 + i * 16, 0, 0);
    const __amdgpu_buffer_rsrc_t rq = mkrs(wqA, DM * HD * 2);
#pragma unroll
    for (int i = 0; i < 16; i++) wr[i] = __builtin_amdgcn_raw_buffer_load_b128(rq, vo, i * HD * 2, NT);
    u64 epv = 0;
    if (t == 0) epv = ld_u64(ws + WS_EPA);
    unsigned* qs = (unsigned*)lds;
    unsigned short* qo = (unsigned short*)(lds + 4096);
    float acc[NB][8];
#pragma unroll
    for (int bb = 0; bb < NB; bb++)
#pragma unroll
      for (int j = 0; j < 8; j++) acc[bb][j] = 0.f;
#pragma unroll
    for (int i = 0; i < 16; i += 2) {
#pragma unroll
      for (int j = 0; j < 4; j++) {
        const unsigned pl = __builtin_amdgcn_perm(wr[i + 1][j], wr[i][j], 0x05040100u);
        const unsigned ph = __builtin_amdgcn_perm(wr[i + 1][j], wr[i][j], 0x07060302u);
#pragma unroll
        for (int bb = 0; bb < NB; bb++) {
          const unsigned x = xr[bb][i >> 3][(i >> 1) & 3];
          acc[bb][2 * j] = dot2(pl, x, acc[bb][2 * j]);
          acc[bb][2 * j + 1] = dot2(ph, x, acc[bb][2 * j + 1]);
        }
      }
    }
    float* rp = red + (w * 4 + g) * 512 + c * 8;
#pragma unroll
    for (int bb = 0; bb < NB; bb++) {
      *(f32x4*)(rp + bb * 128) = f32x4{acc[bb][0], acc[bb][1], acc[bb][2], acc[bb][3]};
      *(f32x4*)(rp + bb * 128 + 4) = f32x4{acc[bb][4], acc[bb][5], acc[bb][6], acc[bb][7]};
    }
    __syncthreads();
    {
      const __amdgpu_buffer_rsrc_t rku = mkrs(wuk, DC * HD * 2);
#pragma unroll
      for (int i = 0; i < 16; i++) wk[i] = __builtin_amdgcn_raw_buffer_load_b128(rku, vo, i * HD * 2, NT);
    }
    {
      float s = 0.f;
#pragma unroll
      for (int i = 0; i < 32; i++) s += red[i * 512 + t];
      const float sn = dpp<0xB1>(s);
      if ((t & 1) == 0) qs[t >> 1] = cvtpk(s, sn);
      if (t == 0) *(u64*)(lds + 2048) = epv;
    }
    __syncthreads();
    tag = next_tag(uni64(*(const u64*)(lds + 2048)));
    {
      u32x4 qq[NB];
#pragma unroll
      for (int bb = 0; bb < NB; bb++) qq[bb] = *(const u32x4*)(qs + bb * 64 + c * 4);
#pragma unroll
      for (int i = 0; i < 16; i++) {
        float s[NB];
#pragma unroll
        for (int bb = 0; bb < NB; bb++) {
          float v = dot2(wk[i][0], qq[bb][0], 0.f);
          v = dot2(wk[i][1], qq[bb][1], v);
          v = dot2(wk[i][2], qq[bb][2], v);
          v = dot2(wk[i][3], qq[bb][3], v);
          s[bb] = sum16(v);
        }
        const float v = (c & 2) ? ((c & 1) ? s[3] : s[2]) : ((c & 1) ? s[1] : s[0]);
        if (c < NB) qo[c * 512 + w * 64 + g * 16 + i] = (unsigned short)(cvtpk(v, v) & 0xffff);
      }
    }
    __syncthreads();
    if (t < 256) {
      const int bb = t >> 6, ch = t & 63;
      st16u_sc1(rs, (unsigned)(WS_QABS + ((size_t)(bb * NH + h) * DC + ch * 8) * 2), *(const u32x4*)(qo + bb * 512 + ch * 8));
    }
    signal_flag(ws + WS_F1 + h * LINE, tag);
  }
  const unsigned ta = (unsigned)tag, tb = (unsigned)(tag >> 32);
  {
    u32x4 bv[8][2], bo[8][2];
    u32x4 pre[2][8], wo[16];
    {
      const __amdgpu_buffer_rsrc_t ruv = mkrs(wuv, DC * HD * 2);
      const __amdgpu_buffer_rsrc_t rwo = mkrs(woA, HD * DM * 2);
      const unsigned wvo = (unsigned)(((h * DH + (l >> 3) * 16) * DM + w * 64 + (l & 7) * 8) * 2);
#pragma unroll
      for (int i = 0; i < 16; i++) pre[i >> 3][i & 7] = __builtin_amdgcn_raw_buffer_load_b128(ruv, vo - g * 8 * HD * 2, ((i >> 3) * 32 + (i & 7)) * HD * 2, NT);
#pragma unroll
      for (int i = 0; i < 16; i++) wo[i] = __builtin_amdgcn_raw_buffer_load_b128(rwo, wvo, i * DM * 2, NT);
    }
#pragma unroll
    for (int j = 0; j < 8; j++)
#pragma unroll
      for (int s = 0; s < 2; s++) {
#pragma unroll
        for (int e = 0; e < 4; e++)
          bv[j][s][e] = __builtin_amdgcn_perm(pre[s][2 * e + 1][j >> 1], pre[s][2 * e][j >> 1], (j & 1) ? 0x07060302u : 0x05040100u);
        asm volatile("" : "+v"(bv[j][s]));
      }
#pragma unroll
    for (int j = 0; j < 8; j++)
#pragma unroll
      for (int s = 0; s < 2; s++) {
#pragma unroll
        for (int e = 0; e < 4; e++)
          bo[j][s][e] = __builtin_amdgcn_perm(wo[8 * s + 2 * e + 1][j >> 1], wo[8 * s + 2 * e][j >> 1], (j & 1) ? 0x07060302u : 0x05040100u);
        asm volatile("" : "+v"(bo[j][s]));
      }
    constexpr int LAS = 1040;
    unsigned short* cs = (unsigned short*)(lds + 16384);
    float* outs = (float*)(lds + 20480);
    float* cred = (float*)(lds + 32768);
    {
    {
    }
    unsigned po = (unsigned)(WS_PAO + ((size_t)(w * 4 * NH + h) * DC + l * 8) * 2);
    unsigned pc = (unsigned)(WS_PAC + (size_t)((w * 4 + g) * NH + h) * 8);
    unsigned lw_ = (unsigned)(65536 + ((w & 1) * 16 + (w >> 1) * 4) * LAS + l * 16), lr_ = (unsigned)(65536 + c * LAS + (w * 64 + g * 8) * 2);
    asm volatile("" : "+v"(po), "+v"(pc), "+v"(lw_), "+v"(lr_));
    const unsigned tw = (h & 1) ? tb : ta;
    const unsigned tg = wait_gate(ws + WS_FP, ta, (unsigned*)(lds + 8192), tent);
    wait_until(tg + TM_GET);
    {
      u32x4 pr[4];
#pragma unroll 1
      for (unsigned spins = 0; spins < SPIN_CAP; ++spins) {
        asm volatile("" ::: "memory");
        const u32x2 ck = __builtin_amdgcn_raw_buffer_load_b64(rs, pc, 0, SC1);
#pragma unroll
        for (int i = 0; i < 4; i++) pr[i] = __builtin_amdgcn_raw_buffer_load_b128(rs, po, i * NH * DC * 2, SC1);
#pragma unroll
        for (int i = 0; i < 4; i++) *(u32x4*)(lds + lw_ + i * LAS) = pr[i];
        if (__all(row_sums(pr) == ck[0] && ck[1] == tw)) break;
        __builtin_amdgcn_s_sleep(2);
      }
      __syncthreads();
      u32x4 af[2][2];
#pragma unroll
      for (int tl = 0; tl < 2; tl++)
#pragma unroll
        for (int s = 0; s < 2; s++) af[tl][s] = *(const u32x4*)(lds + lr_ + tl * 16 * LAS + s * 64);
      f32x4 dd[8];
#pragma unroll
      for (int j = 0; j < 8; j++)
        dd[j] = __builtin_amdgcn_mfma_f32_16x16x32_bf16(__builtin_bit_cast(bf16x8, af[0][0]), __builtin_bit_cast(bf16x8, bv[j][0]), f32x4{0.f, 0.f, 0.f, 0.f}, 0, 0, 0);
#pragma unroll
      for (int j = 0; j < 8; j++)
        dd[j] = __builtin_amdgcn_mfma_f32_16x16x32_bf16(__builtin_bit_cast(bf16x8, af[0][1]), __builtin_bit_cast(bf16x8, bv[j][1]), dd[j], 0, 0, 0);
#pragma unroll
      for (int j = 0; j < 8; j++)
        dd[j] = __builtin_amdgcn_mfma_f32_16x16x32_bf16(__builtin_bit_cast(bf16x8, af[1][0]), __builtin_bit_cast(bf16x8, bv[j][0]), dd[j], 0, 0, 0);
#pragma unroll
      for (int j = 0; j < 8; j++)
        dd[j] = __builtin_amdgcn_mfma_f32_16x16x32_bf16(__builtin_bit_cast(bf16x8, af[1][1]), __builtin_bit_cast(bf16x8, bv[j][1]), dd[j], 0, 0, 0);
      f32x4 v[2];
#pragma unroll
      for (int j = 0; j < 8; j++) v[j >> 2][j & 3] = (dd[j][0] + dd[j][1]) + (dd[j][2] + dd[j][3]);
      *(f32x4*)(cred + (w * 4 + g) * 128 + c * 8) = v[0];
      *(f32x4*)(cred + (w * 4 + g) * 128 + c * 8 + 4) = v[1];
    }
    __syncthreads();
    {
      float s = 0.f;
#pragma unroll
      for (int i = 0; i < 8; i++) s += cred[i * 512 + t];
      const float sn = dpp<0xB1>(s);
      if ((t & 1) == 0) *(unsigned*)(cs + t) = cvtpk(s, sn);
    }
    __syncthreads();
    {
      const char* ap = (const char*)cs + ((c >> 1) & 3) * 256 + (g * 32 + (c & 1) * 16) * 2;
      const u32x4 a0 = *(const u32x4*)(ap), a1 = *(const u32x4*)(ap + 16);
      f32x4 dd[8];
#pragma unroll
      for (int j = 0; j < 8; j++)
        dd[j] = __builtin_amdgcn_mfma_f32_16x16x32_bf16(__builtin_bit_cast(bf16x8, a0), __builtin_bit_cast(bf16x8, bo[j][0]), f32x4{0.f, 0.f, 0.f, 0.f}, 0, 0, 0);
#pragma unroll
      for (int j = 0; j < 8; j++)
        dd[j] = __builtin_amdgcn_mfma_f32_16x16x32_bf16(__builtin_bit_cast(bf16x8, a1), __builtin_bit_cast(bf16x8, bo[j][1]), dd[j], 0, 0, 0);
      f32x4 v4[2];
#pragma unroll
      for (int j = 0; j < 8; j++) {
        const float u0 = (l & 8) ? dd[j][1] : dd[j][0], u1 = (l & 8) ? dd[j][3] : dd[j][2];
        const float keep = (l & 8) ? u1 : u0, give = (l & 8) ? u0 : u1;
        v4[j >> 2][j & 3] = keep + dpp<0x128>(give);
      }
      if (g < 2) {
        float* op = outs + (g * 2 + ((l >> 3) & 1)) * 512 + w * 64 + (l & 7) * 8;
        *(f32x4*)op = v4[0];
        *(f32x4*)(op + 4) = v4[1];
      }
    }
    __syncthreads();
    {
      const f32x2 va = *(const f32x2*)(outs + t * 2), vb = *(const f32x2*)(outs + 1024 + t * 2);
      const unsigned oo = (unsigned)(WS_PA + ((size_t)((t >> 8) * NH + h) * DM + (t & 255) * 2) * 8);
      st16u_sc1(rs, oo, u32x4{__float_as_uint(va[0]), ta, __float_as_uint(va[1]), tb});
      st16u_sc1(rs, oo + 2 * NH * DM * 8, u32x4{__float_as_uint(vb[0]), ta, __float_as_uint(vb[1]), tb});
    }
  if (w < 4) {
    const int bb = h >> 3, n0 = (h & 7) * 64 + w * 16;
    const unsigned og = (unsigned)(WS_PA + ((size_t)(bb * NH + (l >> 3)) * DM + n0) * 8 + (l & 7) * 16);
    u32x4 p[4];
    __builtin_amdgcn_s_sleep(10);
    wait_until(tg + TM_T4);
#pragma unroll 1
    for (unsigned spins = 0; spins < SPIN_CAP; ++spins) {
      asm volatile("" ::: "memory");
#pragma unroll
      for (int i = 0; i < 4; i++) p[i] = ld16u_sc1(rs, og + i * 8 * DM * 8);
      bool ok = true;
#pragma unroll
      for (int i = 0; i < 4; i++) ok = ok && p[i][1] == ta && p[i][3] == tb;
      if (__all(ok)) break;
      __builtin_amdgcn_s_sleep(4);
    }
    float v0 = (__uint_as_float(p[0][0]) + __uint_as_float(p[1][0])) + (__uint_as_float(p[2][0]) + __uint_as_float(p[3][0]));
    float v1 = (__uint_as_float(p[0][2]) + __uint_as_float(p[1][2])) + (__uint_as_float(p[2][2]) + __uint_as_float(p[3][2]));
    v0 += dpp<0x128>(v0); v0 = xrow_sum(v0);
    v1 += dpp<0x128>(v1); v1 = xrow_sum(v1);
    if (l < 8) {
      *(unsigned*)(out + bb * 1024 + n0 + l * 2) = cvtpk(v0, v1);
      if (h == 0 && t == 0) st_u64(ws + WS_EPA, tag);
    }
  }
    }
  }
}

__device__ __forceinline__ void role_A2(int a, const __hip_bfloat16* __restrict__ ckv, char* __restrict__ ws,
                                        __amdgpu_buffer_rsrc_t rs, char* lds) {
  const int t = threadIdx.x, l = t & 63;
  const int w = __builtin_amdgcn_readfirstlane(t >> 6);
  const int g = l >> 4, c = l & 15;
  const unsigned tent = clk();
  u64 ep = 0;
  if (t == 0) ep = ld_u64(ws + WS_EPA);
  const int grp = w >> 2, wq = w & 3;
  const int b = a / SPLIT_A, sp = a % SPLIT_A;
  const int r = l & 31, hh = l >> 5;
  constexpr unsigned CB = (unsigned)((size_t)NB * SK * DC * 2);
  constexpr int NTL = KA / 64;
  const unsigned cvo = (unsigned)((g * DC + 128 * wq + c * 8) * 2);
  const unsigned cso = (unsigned)(((size_t)b * SK + sp * KA + grp * 32) * DC * 2);
  auto load_tile = [&](u32x4 (&pr)[8], int tile) {
    const bool ok = tile < NTL;
    const __amdgpu_buffer_rsrc_t rc = mkrs(ckv, ok ? CB : 0u);
    const unsigned so = ok ? cso + (unsigned)((tile + sp) & (NTL - 1)) * 64 * DC * 2 : 0u;
#pragma unroll
    for (int u = 0; u < 8; u++) pr[u] = __builtin_amdgcn_raw_buffer_load_b128(rc, cvo, so + u * 4 * DC * 2, NT);
  };
  char* img = lds + w * 8192;
  float* sred = (float*)(lds + 65536 + grp * 16384);
  float* mxs = (float*)(lds + 98304 + grp * 512);
  unsigned* pbuf = (unsigned*)(lds + 102400 + grp * 2048);
  float* lall = (float*)(lds + 106496);
  const unsigned imga = (unsigned)(uintptr_t)(__attribute__((address_space(3))) char*)img;
  auto put_tile = [&](u32x4 (&pr)[8]) {
#pragma unroll
    for (int u = 0; u < 8; u++) *(u32x4*)(img + lds_off(4 * u + g, c)) = pr[u];
  };
  u32x4 pre[2][8];
  load_tile(pre[0], 0);
  load_tile(pre[1], 1);
  const u64 tag0 = next_tag(uni64(ep));
  put_tile(pre[0]);
  __builtin_amdgcn_sched_barrier(0);
  load_tile(pre[0], 2);
  if (t < 64) {
    wait_until(tent + TM_F1);
#pragma unroll 1
    for (unsigned spins = 0; spins < SPIN_CAP; ++spins) {
      __builtin_amdgcn_s_sleep(32);
      if (__all(ld_u64(ws + WS_F1 + (l & 31) * LINE) == tag0)) break;
    }
  }
  if (t == 0) *(u64*)(lds + 110592) = tag0;
  __syncthreads();
  const u64 tag = uni64(*(const u64*)(lds + 110592));
  const unsigned ta = (unsigned)tag, tb = (unsigned)(tag >> 32);
  bf16x8 qf[8];
  {
    char* qi = lds + 65536;
    const unsigned qo = (unsigned)(WS_QABS + (size_t)b * NH * DC * 2 + t * 16);
    u32x4 qv[4];
#pragma unroll
    for (int i = 0; i < 4; i++) qv[i] = ld16u_sc1(rs, qo + i * 8192);
#pragma unroll
    for (int i = 0; i < 4; i++) *(u32x4*)(qi + (i * 8 + w) * 1040 + l * 16) = qv[i];
    __syncthreads();
#pragma unroll
    for (int s = 0; s < 8; s++) qf[s] = __builtin_bit_cast(bf16x8, *(const u32x4*)(qi + r * 1040 + (128 * wq + 16 * s + 8 * hh) * 2));
#pragma unroll
    for (int s = 0; s < 8; s++) asm volatile("" : "+v"(qf[s]));
    __syncthreads();
  }
  f32x16 o[4];
#pragma unroll
  for (int i = 0; i < 4; i++)
#pragma unroll
    for (int j = 0; j < 16; j++) o[i][j] = 0.f;
  float m = -INFINITY, lsum = 0.f;
  const int q4 = (l & 15) >> 2, p4 = l & 3, colh = g & 1;
  auto tile_body = [&](int tile, u32x4 (&nx)[8]) {
    f32x16 s;
#pragma unroll
    for (int j = 0; j < 16; j++) s[j] = 0.f;
    {
      u32x4 af[8];
#pragma unroll
      for (int ks = 0; ks < 8; ks++) af[ks] = *(const u32x4*)(img + lds_off(r, 2 * ks + hh));
      __builtin_amdgcn_sched_barrier(0);
#pragma unroll
      for (int ks = 0; ks < 8; ks++) s = __builtin_amdgcn_mfma_f32_32x32x16_bf16(__builtin_bit_cast(bf16x8, af[ks]), qf[ks], s, 0, 0, 0);
    }
    f32x4* sr = (f32x4*)sred;
#pragma unroll
    for (int k4 = 0; k4 < 4; k4++) sr[(k4 * 4 + wq) * 64 + l] = f32x4{s[4 * k4], s[4 * k4 + 1], s[4 * k4 + 2], s[4 * k4 + 3]};
    __syncthreads();
    float x[4];
    {
      const f32x4 v = (sr[(wq * 4) * 64 + l] + sr[(wq * 4 + 1) * 64 + l]) + (sr[(wq * 4 + 2) * 64 + l] + sr[(wq * 4 + 3) * 64 + l]);
#pragma unroll
      for (int e = 0; e < 4; e += 2) {
        const unsigned r1 = cvtpk(v[e], v[e + 1]);
        const unsigned r2 = cvtpk(bf_lo(r1) * SCALE, bf_hi(r1) * SCALE);
        x[e] = bf_lo(r2); x[e + 1] = bf_hi(r2);
      }
      float mq = fmaxf(fmaxf(x[0], x[1]), fmaxf(x[2], x[3]));
      auto sx = __builtin_amdgcn_permlane32_swap(__float_as_uint(mq), __float_as_uint(mq), false, false);
      mq = fmaxf(__uint_as_float(sx[0]), __uint_as_float(sx[1]));
      if (hh == 0) mxs[r * 4 + wq] = mq;
    }
    __syncthreads();
    const f32x4 m4 = *(const f32x4*)(mxs + r * 4);
    const float mn = fmaxf(fmaxf(m, fmaxf(m4[0], m4[1])), fmaxf(m4[2], m4[3]));
    const float alpha = fexp2((m - mn) * LOG2E);
    {
      const unsigned p0 = cvtpk(fexp2((x[0] - mn) * LOG2E), fexp2((x[1] - mn) * LOG2E));
      const unsigned p1 = cvtpk(fexp2((x[2] - mn) * LOG2E), fexp2((x[3] - mn) * LOG2E));
      *(u32x2*)(pbuf + l * 8 + wq * 2) = u32x2{p0, p1};
      lsum = lsum * alpha + ((bf_lo(p0) + bf_hi(p0)) + (bf_lo(p1) + bf_hi(p1)));
    }
    m = mn;
    __syncthreads();
    unsigned pk[8];
    {
      const u32x4 a0 = *(const u32x4*)(pbuf + l * 8), a1 = *(const u32x4*)(pbuf + l * 8 + 4);
      pk[0] = a0[0]; pk[1] = a0[1]; pk[2] = a0[2]; pk[3] = a0[3]; pk[4] = a1[0]; pk[5] = a1[1]; pk[6] = a1[2]; pk[7] = a1[3];
    }
    if (__any(alpha != 1.f)) {
#pragma unroll
      for (int i = 0; i < 4; i++)
#pragma unroll
        for (int j = 0; j < 16; j++) o[i][j] *= alpha;
    }
#pragma unroll
    for (int s2 = 0; s2 < 2; s2++) {
      u32x2 tv[4][2];
#pragma unroll
      for (int ct = 0; ct < 4; ct++) {
        const int ch = 4 * ct + 2 * colh + (p4 >> 1);
        const unsigned a0 = lds_off(4 * hh + q4, ch) + 8 * (p4 & 1);
        const unsigned a1 = lds_off(8 + 4 * hh + q4, ch) + 8 * (p4 & 1);
        if (s2 == 0) {
          asm volatile("ds_read_b64_tr_b16 %0, %1" : "=v"(tv[ct][0]) : "v"(imga + a0) : "memory");
          asm volatile("ds_read_b64_tr_b16 %0, %1" : "=v"(tv[ct][1]) : "v"(imga + a1) : "memory");
        } else {
          asm volatile("ds_read_b64_tr_b16 %0, %1 offset:4096" : "=v"(tv[ct][0]) : "v"(imga + a0) : "memory");
          asm volatile("ds_read_b64_tr_b16 %0, %1 offset:4096" : "=v"(tv[ct][1]) : "v"(imga + a1) : "memory");
        }
      }
      asm volatile("s_waitcnt lgkmcnt(0)" ::: "memory");
      __builtin_amdgcn_sched_barrier(0);
      u32x4 pb = {pk[4 * s2], pk[4 * s2 + 1], pk[4 * s2 + 2], pk[4 * s2 + 3]};
#pragma unroll
      for (int ct = 0; ct < 4; ct++) {
        u32x4 av = {tv[ct][0][0], tv[ct][0][1], tv[ct][1][0], tv[ct][1][1]};
        o[ct] = __builtin_amdgcn_mfma_f32_32x32x16_bf16(__builtin_bit_cast(bf16x8, av), __builtin_bit_cast(bf16x8, pb), o[ct], 0, 0, 0);
      }
    }
    put_tile(nx);
    __builtin_amdgcn_sched_barrier(0);
    load_tile(nx, tile + 3);
  };
#pragma unroll 1
  for (int tile = 0; tile < NTL; tile += 2) {
    tile_body(tile, pre[1]);
    tile_body(tile + 1, pre[0]);
  }
  constexpr int OST = 516;
  float* ob = (float*)lds;
  float* mlb = (float*)(lds + 32 * OST * 4);
  lall[w * 64 + l] = lsum;
  __syncthreads();
  {
    const float* lp = lall + grp * 256 + r;
    lsum = ((lp[0] + lp[32]) + (lp[64] + lp[96])) + ((lp[128] + lp[160]) + (lp[192] + lp[224]));
  }
  if (grp == 1) {
#pragma unroll
    for (int ct = 0; ct < 4; ct++)
#pragma unroll
      for (int j4 = 0; j4 < 4; j4++)
        *(f32x4*)(ob + r * OST + 128 * wq + 32 * ct + 8 * j4 + 4 * hh) =
            f32x4{o[ct][4 * j4], o[ct][4 * j4 + 1], o[ct][4 * j4 + 2], o[ct][4 * j4 + 3]};
    if (wq == 0 && l < 32) { mlb[2 * r] = m; mlb[2 * r + 1] = lsum; }
  }
  __syncthreads();
  if (grp == 0) {
    const float m1 = mlb[2 * r], l1 = mlb[2 * r + 1];
    const float M = fmaxf(m, m1);
    const float f0 = fexp2((m - M) * LOG2E), f1 = fexp2((m1 - M) * LOG2E);
#pragma unroll
    for (int ct = 0; ct < 4; ct++)
#pragma unroll
      for (int j4 = 0; j4 < 4; j4++) {
        float* p = ob + r * OST + 128 * wq + 32 * ct + 8 * j4 + 4 * hh;
        const f32x4 o1 = *(const f32x4*)p;
        *(f32x4*)p = f32x4{o[ct][4 * j4] * f0 + o1[0] * f1, o[ct][4 * j4 + 1] * f0 + o1[1] * f1,
                           o[ct][4 * j4 + 2] * f0 + o1[2] * f1, o[ct][4 * j4 + 3] * f0 + o1[3] * f1};
      }
    if (wq == 0 && l < 32)
      st16u_sc1(rs, (unsigned)(WS_PAML + ((size_t)(b * SPLIT_A + sp) * NH + r) * 16),
                u32x4{__float_as_uint(M), ta, __float_as_uint(lsum * f0 + l1 * f1), tb});
  }
  float* wgt = mlb + 64;
  unsigned* pcs = (unsigned*)(mlb + 128);
  const unsigned tg = wait_gate(ws + WS_FP, ta, (unsigned*)(mlb + 96), tent);
  if (t < 256) {
    const unsigned mo = (unsigned)(WS_PAML + ((size_t)(b * SPLIT_A + (t & 7)) * NH + (t >> 3)) * 16);
    u32x4 mv;
#pragma unroll 1
    for (unsigned spins = 0; spins < SPIN_CAP; ++spins) {
      asm volatile("" ::: "memory");
      mv = ld16u_sc1(rs, mo);
      if (__all(mv[1] == ta && mv[3] == tb)) break;
      __builtin_amdgcn_s_sleep(16);
    }
    const float mm = __uint_as_float(mv[0]);
    float M = fmaxf(mm, dpp<0xB1>(mm)); M = fmaxf(M, dpp<0x4E>(M)); M = fmaxf(M, dpp<0x141>(M));
    const float f = fexp2((mm - M) * LOG2E);
    float L = f * __uint_as_float(mv[2]);
    L += dpp<0xB1>(L); L += dpp<0x4E>(L); L += dpp<0x141>(L);
    if ((t & 7) == sp) wgt[t >> 3] = f / L;
  }
  __syncthreads();
  {
    const unsigned pao = (unsigned)(WS_PAO + (size_t)(b * SPLIT_A + sp) * NH * DC * 2);
    u32x4 pv[4];
#pragma unroll
    for (int i = 0; i < 4; i++) {
      const int hd = i * 8 + w;
      const float f = wgt[hd];
      const f32x4 v0 = *(const f32x4*)(ob + hd * OST + l * 8) * f, v1 = *(const f32x4*)(ob + hd * OST + l * 8 + 4) * f;
      pv[i] = u32x4{cvtpk(v0[0], v0[1]), cvtpk(v0[2], v0[3]), cvtpk(v1[0], v1[1]), cvtpk(v1[2], v1[3])};
      asm volatile("" : "+v"(pv[i]));
    }
    {
      const unsigned cs = row_sums(pv);
      if (c == 0) pcs[g * 8 + w] = cs;
    }
    __syncthreads();
    const u32x2 c2 = *(const u32x2*)(pcs + (t & 15) * 2);
    const unsigned co = t < 16 ? (unsigned)(WS_PAC + (size_t)(b * SPLIT_A + sp) * NH * 8 + t * 16) : 0x7ffffff0u;
    wait_until(tg);
#pragma unroll
    for (int i = 0; i < 4; i++) st16u_sc1(rs, pao + (unsigned)(i * 512 + t) * 16, pv[i]);
    if (w == 0) st16u_sc1(rs, co, u32x4{c2[0], ta, c2[1], tb});
  }
}

__global__ __launch_bounds__(512, 2) void k_fused(const __hip_bfloat16* __restrict__ xA, const __hip_bfloat16* __restrict__ ckv,
                                                  const __hip_bfloat16* __restrict__ wqA, const __hip_bfloat16* __restrict__ wuk,
                                                  const __hip_bfloat16* __restrict__ wuv, const __hip_bfloat16* __restrict__ woA,
                                                  const __hip_bfloat16* __restrict__ xB, const __hip_bfloat16* __restrict__ kc,
                                                  const __hip_bfloat16* __restrict__ vc, const __hip_bfloat16* __restrict__ wqB,
                                                  const __hip_bfloat16* __restrict__ woB, char* __restrict__ ws,
                                                  __hip_bfloat16* __restrict__ out) {
  __shared__ __attribute__((aligned(16))) char lds[131072];
  asm volatile("" :: "s"(xA), "s"(ckv), "s"(wqA), "s"(wuk), "s"(wuv), "s"(woA), "s"(xB), "s"(kc), "s"(vc), "s"(wqB), "s"(woB), "s"(ws), "s"(out));
  const int blk = blockIdx.x, xcd = blk & 7, r = blk >> 3;
  unsigned pfv = 0;
  {
    u64 pc; unsigned len;
    asm volatile("s_getpc_b64 %0\n.Lcode_begin:\n\ts_mov_b32 %1, .Lcode_end-.Lcode_begin" : "=s"(pc), "=s"(len));
    const unsigned idx = r < 5 ? (unsigned)((r * 8 + (threadIdx.x >> 6) + xcd * 5) % 40) : 1000u;
    const __amdgpu_buffer_rsrc_t rc = mkrs((const void*)pc, len);
    pfv = __builtin_amdgcn_raw_buffer_load_b32(rc, idx * 1024 + (threadIdx.x & 63) * 16, 0, 0);
  }
  const __amdgpu_buffer_rsrc_t rs = mkrs(ws, (unsigned)WS_END);
  if (r < 8) {
    if (r < 4) role_A1(xcd * 4 + r, xA, wqA, wuk, wuv, woA, ws, rs, out, lds);
    else role_A2(xcd * 4 + r - 4, ckv, ws, rs, lds);
  } else {
    const int rr = r - 8, hq = rr / 6;
    role_B(((0x75643120u >> (4 * xcd)) & 7) + 8 * hq, rr - 6 * hq, xB, kc, vc, wqB, woB, ws, rs, out, lds);
  }
  asm volatile(".Lcode_end:" :: "v"(pfv));
}

extern "C" void kernel_launch(void* const* d_in, const int* in_sizes, int n_in,
                              void* d_out, int out_size, void* d_ws, size_t ws_size,
                              hipStream_t stream) {
  k_fused<<<256, 512, 0, stream>>>((const __hip_bfloat16*)d_in[0], (const __hip_bfloat16*)d_in[1], (const __hip_bfloat16*)d_in[2],
                                   (const __hip_bfloat16*)d_in[3], (const __hip_bfloat16*)d_in[4], (const __hip_bfloat16*)d_in[5],
                                   (const __hip_bfloat16*)d_in[6], (const __hip_bfloat16*)d_in[7], (const __hip_bfloat16*)d_in[8],
                                   (const __hip_bfloat16*)d_in[9], (const __hip_bfloat16*)d_in[10], (char*)d_ws,
                                   (__hip_bfloat16*)d_out);
}
```

```cpp
#include <hip/hip_runtime.h>
#include <hip/hip_bf16.h>
#include <stdint.h>

typedef __attribute__((ext_vector_type(4))) unsigned u32x4;
typedef __attribute__((ext_vector_type(2))) unsigned u32x2;
typedef __attribute__((ext_vector_type(4))) float f32x4;
typedef __attribute__((ext_vector_type(2))) float f32x2;
typedef __attribute__((ext_vector_type(3))) float f32x3;
typedef __attribute__((ext_vector_type(16))) float f32x16;
typedef __attribute__((ext_vector_type(8))) __bf16 bf16x8;
typedef __attribute__((ext_vector_type(2))) __bf16 bf16x2;
typedef __attribute__((address_space(1))) unsigned gu32;
typedef __attribute__((address_space(1))) unsigned long long gu64;
typedef unsigned long long u64;

constexpr int NB = 4;
constexpr int NH = 32;
constexpr int SK = 4096;
constexpr int DH = 128;
constexpr int DM = 512;
constexpr int DC = 512;
constexpr int HD = NH * DH;

constexpr int SPLIT_A = 8;
constexpr int KA = SK / SPLIT_A;
constexpr int SS = 171;
constexpr int NI = 86;
constexpr int N0 = 43;
constexpr int RD = 9;
constexpr int NP = NI - 8 * RD + 8;
constexpr int NP4 = (NP + 3) & ~3;
static_assert(8 * RD + 8 <= NI, "");
constexpr int RST = 144;
constexpr int RF = 6;
constexpr int RE = RD - 1;
constexpr int PRIOF = 4;

constexpr unsigned TM_F1 = 1600;
constexpr unsigned TM_FC = 3650;
constexpr unsigned TM_PUT = 370;
constexpr unsigned TM_GET = 85;
constexpr unsigned TM_T4 = 400;
constexpr unsigned TM_SW = 20;

constexpr float SCALE = 0.08838834764831845f;
constexpr float LOG2E = 1.4426950408889634f;
constexpr int NT = 2;
constexpr int SC1 = 16;

constexpr size_t WS_EPA  = 0;
constexpr size_t WS_EPB  = 256;
constexpr int LINE = 256;
constexpr size_t WS_F1   = 4096;
constexpr size_t WS_FP   = 12288;
constexpr size_t WS_QABS = 16384;
constexpr size_t WS_PAML = WS_QABS + (size_t)NB * NH * DC * 2;
constexpr size_t WS_PAC  = WS_PAML + (size_t)NB * SPLIT_A * NH * 16;
constexpr size_t WS_PAO  = WS_PAC + (size_t)NB * SPLIT_A * NH * 8;
constexpr size_t WS_PA   = WS_PAO + (size_t)NB * SPLIT_A * NH * DC * 2;
constexpr size_t WS_PBG  = WS_PA + (size_t)NB * NH * DM * 8;
constexpr size_t WS_END  = WS_PBG + (size_t)NB * 64 * DM * 8;

__device__ __forceinline__ float bf_lo(unsigned u) { return __uint_as_float(u << 16); }
__device__ __forceinline__ float bf_hi(unsigned u) { return __uint_as_float(u & 0xffff0000u); }
__device__ __forceinline__ unsigned cvtpk(float lo, float hi) {
  f32x2 v = {lo, hi};
  return __builtin_bit_cast(unsigned, __builtin_convertvector(v, bf16x2));
}
__device__ __forceinline__ float rbf(float x) { return __uint_as_float(cvtpk(x, x) << 16); }
__device__ __forceinline__ float dot2(unsigned k, unsigned q, float acc) {
  return __builtin_amdgcn_fdot2_f32_bf16(__builtin_bit_cast(bf16x2, k), __builtin_bit_cast(bf16x2, q), acc, false);
}
template <int CTRL> __device__ __forceinline__ float dpp(float x) {
  return __builtin_bit_cast(float, __builtin_amdgcn_mov_dpp(__builtin_bit_cast(int, x), CTRL, 0xf, 0xf, true));
}
__device__ __forceinline__ float sum16(float x) {
  x += dpp<0x128>(x);
  x += dpp<0x124>(x);
  x += dpp<0x4E>(x);
  x += dpp<0xB1>(x);
  return x;
}
__device__ __forceinline__ float xrow_max(float x) {
  auto s = __builtin_amdgcn_permlane16_swap(__float_as_uint(x), __float_as_uint(x), false, false);
  x = fmaxf(__uint_as_float(s[0]), __uint_as_float(s[1]));
  auto t = __builtin_amdgcn_permlane32_swap(__float_as_uint(x), __float_as_uint(x), false, false);
  return fmaxf(__uint_as_float(t[0]), __uint_as_float(t[1]));
}
__device__ __forceinline__ float xrow_sum(float x) {
  auto s = __builtin_amdgcn_permlane16_swap(__float_as_uint(x), __float_as_uint(x), false, false);
  x = __uint_as_float(s[0]) + __uint_as_float(s[1]);
  auto t = __builtin_amdgcn_permlane32_swap(__float_as_uint(x), __float_as_uint(x), false, false);
  return __uint_as_float(t[0]) + __uint_as_float(t[1]);
}
__device__ __forceinline__ float fexp2(float x) { return __builtin_amdgcn_exp2f(x); }
__device__ __forceinline__ __amdgpu_buffer_rsrc_t mkrs(const void* p, unsigned bytes) {
  return __builtin_amdgcn_make_buffer_rsrc((void*)p, 0, (int)bytes, 0x00020000);
}


constexpr unsigned SPIN_CAP = 1u << 21;
__device__ __forceinline__ u64 ld_u64(const void* p) {
  return __hip_atomic_load((gu64*)p, __ATOMIC_RELAXED, __HIP_MEMORY_SCOPE_AGENT);
}
__device__ __forceinline__ void st_u64(void* p, u64 v) {
  __hip_atomic_store((gu64*)p, v, __ATOMIC_RELAXED, __HIP_MEMORY_SCOPE_AGENT);
}
__device__ __forceinline__ u64 next_tag(u64 e) { return e + 0x0000000100000001ull; }
__device__ __forceinline__ u64 uni64(u64 v) {
  const unsigned lo = __builtin_amdgcn_readfirstlane((unsigned)v), hi = __builtin_amdgcn_readfirstlane((unsigned)(v >> 32));
  return ((u64)hi << 32) | lo;
}
__device__ __forceinline__ void signal_flag(char* flag, u64 tag) {
  asm volatile("s_waitcnt vmcnt(0)" ::: "memory");
  __syncthreads();
  if (threadIdx.x == 0) st_u64(flag, tag);
}
template <int CTRL> __device__ __forceinline__ int dppi(int x) { return __builtin_amdgcn_mov_dpp(x, CTRL, 0xf, 0xf, true); }
__device__ __forceinline__ int sum8i(int x) { x += dppi<0xB1>(x); x += dppi<0x4E>(x); x += dppi<0x141>(x); return x; }
__device__ __forceinline__ unsigned row_sums(const u32x4 (&p)[4]) {
  unsigned c4[4];
#pragma unroll
  for (int i = 0; i < 4; i++) c4[i] = (p[i][0] + p[i][1]) + (p[i][2] + p[i][3]);
  auto s0 = __builtin_amdgcn_permlane32_swap(c4[0], c4[2], false, false);
  auto s1 = __builtin_amdgcn_permlane32_swap(c4[1], c4[3], false, false);
  auto s2 = __builtin_amdgcn_permlane16_swap(s0[0] + s0[1], s1[0] + s1[1], false, false);
  int x = (int)(s2[0] + s2[1]);
  x += dppi<0x128>(x);
  return (unsigned)sum8i(x);
}
__device__ __forceinline__ unsigned clk() { return (unsigned)__builtin_amdgcn_s_memrealtime(); }
__device__ __forceinline__ void wait_until(unsigned t) {
#pragma unroll 1
  for (int spins = 0; spins < 4096 && (int)(t - clk()) > 0; ++spins) __builtin_amdgcn_s_sleep(1);
}
__device__ __forceinline__ unsigned ref_time(unsigned v) {
  const unsigned t0 = __builtin_amdgcn_readfirstlane(v);
  const int d = (int)(v - t0);
  int mx = max(d, dppi<0xB1>(d)); mx = max(mx, dppi<0x4E>(mx)); mx = max(mx, dppi<0x141>(mx));
  const int mean = sum8i(d) >> 3;
  return t0 + (unsigned)__builtin_amdgcn_readfirstlane((mean + mx) >> 1);
}
__device__ __forceinline__ unsigned wait_gate(const char* flag, unsigned ta, unsigned* slot, unsigned tent) {
  if (threadIdx.x < 64) {
    u64 v = 0;
    wait_until(tent + TM_FC);
#pragma unroll 1
    for (unsigned spins = 0; spins < SPIN_CAP; ++spins) {
      v = ld_u64(flag + (threadIdx.x & 7) * LINE);
      if (__all((unsigned)(v >> 32) == ta)) break;
      __builtin_amdgcn_s_sleep(12);
    }
    const unsigned tr = ref_time((unsigned)v);
    if (threadIdx.x == 0) *slot = tr - TM_PUT;
  }
  __syncthreads();
  return __builtin_amdgcn_readfirstlane(*slot);
}
__device__ __forceinline__ void st16u_sc1(__amdgpu_buffer_rsrc_t rs, unsigned off, u32x4 v) {
  __builtin_amdgcn_raw_buffer_store_b128(v, rs, off, 0, SC1);
}
__device__ __forceinline__ u32x4 ld16u_sc1(__amdgpu_buffer_rsrc_t rs, unsigned off) {
  return __builtin_amdgcn_raw_buffer_load_b128(rs, off, 0, SC1);
}


__device__ __forceinline__ void item_dots(const u32x4 (&ks)[4], const u32x4 q, float (&s_)[4]) {
#pragma unroll
  for (int u = 0; u < 4; u++) {
    float a = dot2(ks[u][0], q[0], 0.f);
    a = dot2(ks[u][1], q[1], a);
    a = dot2(ks[u][2], q[2], a);
    a = dot2(ks[u][3], q[3], a);
    s_[u] = a;
    asm volatile("" : "+v"(s_[u]));
  }
}
__device__ __forceinline__ void item_unpack(const u32x4 (&vs)[4], f32x2 (&vf)[4][4]) {
#pragma unroll
  for (int u = 0; u < 4; u++)
#pragma unroll
    for (int j = 0; j < 4; j++) { vf[u][j] = f32x2{bf_lo(vs[u][j]), bf_hi(vs[u][j])}; asm volatile("" : "+v"(vf[u][j])); }
}
__device__ __forceinline__ f32x2 rows_sum(const float (&o)[8]) {
  float r4[4], r2[2];
#pragma unroll
  for (int j = 0; j < 4; j++) {
    auto sx = __builtin_amdgcn_permlane32_swap(__float_as_uint(o[j]), __float_as_uint(o[j + 4]), false, false);
    r4[j] = __uint_as_float(sx[0]) + __uint_as_float(sx[1]);
  }
#pragma unroll
  for (int j = 0; j < 2; j++) {
    auto sx = __builtin_amdgcn_permlane16_swap(__float_as_uint(r4[j]), __float_as_uint(r4[j + 2]), false, false);
    r2[j] = __uint_as_float(sx[0]) + __uint_as_float(sx[1]);
  }
  return f32x2{r2[0], r2[1]};
}
__device__ __forceinline__ void role_B(int h, int j6, const __hip_bfloat16* __restrict__ xB,
                                       const __hip_bfloat16* __restrict__ kc, const __hip_bfloat16* __restrict__ vc,
                                       const __hip_bfloat16* __restrict__ wqB, const __hip_bfloat16* __restrict__ woB,
                                       char* __restrict__ ws, __amdgpu_buffer_rsrc_t rs,
                                       __hip_bfloat16* __restrict__ out, char* lds) {
  const int t = threadIdx.x, l = t & 63;
  const int w = __builtin_amdgcn_readfirstlane(t >> 6);
  const int g = l >> 4, c = l & 15;
  const int pr = j6 >= 3 ? 1 : 0, k = j6 - 3 * pr;
  const unsigned tent = clk();
  const int b0 = 2 * pr;
  const int e0 = k == 1 ? (h & 1) : (k >> 1);
  u32x4 xr[2][2], wr[16];
  {
    const __amdgpu_buffer_rsrc_t rx = mkrs(xB, NB * DM * 2);
    const int rq_ = (0x203120 >> (4 * j6)) & 3;
    const int cw = w ^ ((rq_ & 2) << 1), r0 = rq_ & 1;
    const unsigned xo = (unsigned)((b0 * DM + cw * 64 + g * 16) * 2);
#pragma unroll
    for (int bb = 0; bb < 2; bb++)
#pragma unroll
      for (int i = 0; i < 2; i++) xr[bb][i] = __builtin_amdgcn_raw_buffer_load_b128(rx, xo + bb * DM * 2, (i ^ r0) * 16, 0);
    const __amdgpu_buffer_rsrc_t rq = mkrs(wqB, DM * HD * 2);
    const unsigned vo = (unsigned)(((cw * 64 + g * 16) * HD + h * DH + c * 8) * 2);
    const unsigned vo0 = vo + r0 * 8 * HD * 2, vo1 = vo + (1 - r0) * 8 * HD * 2;
#pragma unroll
    for (int i = 0; i < 16; i++) wr[i] = __builtin_amdgcn_raw_buffer_load_b128(rq, i < 8 ? vo0 : vo1, (i & 7) * HD * 2, 0);
  }
  constexpr unsigned KVB = (unsigned)((size_t)NB * NH * SK * DH * 2);
  const unsigned lo = (unsigned)(g * 256 + c * 16);
  const unsigned hb0 = ((unsigned)((b0 + e0) * NH + h) << 20) + (k == 1 ? (unsigned)SS << 12 : 0u);
  const unsigned hb1 = ((unsigned)((b0 + 1 - e0) * NH + h) << 20) + ((unsigned)SS << 12);
  auto seg1 = [&](int x) { return k == 1 && x >= N0; };
  auto unit = [&](int x) { const int y = seg1(x) ? x - N0 : x; return 4 * (y >> 1) + (y & 1); };
  auto soff = [&](int x) { return (seg1(x) ? hb1 : hb0) + ((unsigned)unit(x) << 12); };
  auto sco = [&](int x) { return (unsigned)((unit(x) + (seg1(x) ? 88 : 0)) * 64); };
  auto whole = [&](int x) { return x != NI - 1 && !(k == 1 && x == N0 - 1); };
  auto ld4 = [&](u32x4 (&d)[4], const void* p, bool ok, unsigned so) {
    const __amdgpu_buffer_rsrc_t r = mkrs(p, ok ? KVB : 0u);
#pragma unroll
    for (int u = 0; u < 4; u++) d[u] = __builtin_amdgcn_raw_buffer_load_b128(r, lo + u * 1024, so, NT);
  };
  u32x4 ra[4], rb[4], ea[4], eb[4];
  ld4(ra, kc, true, soff(w));
  ld4(rb, kc, true, soff(w) + 8192);
  u64* eps = (u64*)(lds + 33808);
  u64 epv = 0;
  if (t < 3) epv = ld_u64(ws + (t == 0 ? WS_EPA : WS_EPB + 256 * (b0 + (t == 1 ? e0 : 1 - e0))));
  float* red = (float*)lds;
  unsigned* qs = (unsigned*)(lds + 32768);
  unsigned* cnt = (unsigned*)(lds + 33856);
  float* mxd = (float*)(lds + 33872);
  float* lw = (float*)(lds + 33920);
  float* rec0 = (float*)(lds + 36864);
  char* prv = lds + 45056 + w * 1024;
  constexpr unsigned L_SC = 53248;
  float* recd = (float*)(lds + 65536);
  const unsigned lb = (unsigned)(uintptr_t)(__attribute__((address_space(3))) char*)lds;
  if (t == 0) { cnt[0] = 16u; cnt[1] = 8u * (RD + 1); cnt[2] = 0u; cnt[3] = 0u; mxd[0] = -INFINITY; mxd[1] = -INFINITY; }
  if (t < 64 * (NP4 - NP)) *(f32x2*)(recd + (NP + (t >> 6)) * RST + 2 * l) = f32x2{0.f, 0.f};
  if (t >= 384) *(float*)(lds + L_SC + ((t & 64) ? 172 : 84) * 64 + l * 4) = -INFINITY;
  {
    float a0[8], a1[8];
#pragma unroll
    for (int j = 0; j < 8; j++) { a0[j] = 0.f; a1[j] = 0.f; }
#pragma unroll
    for (int i = 0; i < 16; i += 2) {
      const unsigned x0 = xr[0][i >> 3][(i >> 1) & 3], x1 = xr[1][i >> 3][(i >> 1) & 3];
#pragma unroll
      for (int j = 0; j < 4; j++) {
        const unsigned pl = __builtin_amdgcn_perm(wr[i + 1][j], wr[i][j], 0x05040100u);
        const unsigned ph = __builtin_amdgcn_perm(wr[i + 1][j], wr[i][j], 0x07060302u);
        a0[2 * j] = dot2(pl, x0, a0[2 * j]); a0[2 * j + 1] = dot2(ph, x0, a0[2 * j + 1]);
        a1[2 * j] = dot2(pl, x1, a1[2 * j]); a1[2 * j + 1] = dot2(ph, x1, a1[2 * j + 1]);
      }
    }
#pragma unroll
    for (int j = 0; j < 8; j++) asm volatile("" : "+v"(a0[j]), "+v"(a1[j]));
    __builtin_amdgcn_sched_barrier(0);
    ld4(ea, kc, true, soff(w + 8));
    ld4(eb, kc, true, soff(w + 8) + 8192);
    __builtin_amdgcn_sched_barrier(0);
    float* rp = red + (w * 4 + g) * 256 + c * 8;
    *(f32x4*)(rp) = f32x4{a0[0], a0[1], a0[2], a0[3]};
    *(f32x4*)(rp + 4) = f32x4{a0[4], a0[5], a0[6], a0[7]};
    *(f32x4*)(rp + 128) = f32x4{a1[0], a1[1], a1[2], a1[3]};
    *(f32x4*)(rp + 132) = f32x4{a1[4], a1[5], a1[6], a1[7]};
  }
  __syncthreads();
  {
    const int o = t >> 1, hf = t & 1;
    float s = 0.f;
#pragma unroll
    for (int i = 0; i < 16; i++) s += red[(hf * 16 + i) * 256 + o];
    s += dpp<0xB1>(s);
    const float sn = dpp<0x4E>(s);
    if ((t & 3) == 0) qs[o >> 1] = cvtpk(s, sn);
    if (t < 3) eps[t] = epv;
  }
  __syncthreads();
  const u64 epa = uni64(eps[0]), tg0 = next_tag(uni64(eps[1])), tg1 = next_tag(uni64(eps[2]));
  u32x4 q = *(const u32x4*)(qs + e0 * 64 + c * 4);
  const u32x4 q1 = *(const u32x4*)(qs + (1 - e0) * 64 + c * 4);
  auto lds_cnt = [&](int i) { return (unsigned)__builtin_amdgcn_readfirstlane(__hip_atomic_load(cnt + i, __ATOMIC_RELAXED, __HIP_MEMORY_SCOPE_WORKGROUP)); };
  auto lds_inc = [&](int i) {
    asm volatile("s_waitcnt lgkmcnt(0)" ::: "memory");
    if (l == 0) asm volatile("ds_add_u32 %0, %1" :: "v"(lb + 33856u + i * 4), "v"(1u) : "memory");
  };
  u32x4 wo[16];
  __builtin_amdgcn_sched_barrier(0);
#pragma unroll
  for (int i = 0; i < 16; i++) asm volatile("" : "=v"(wo[i]));
  const int nseg = k == 1 ? 2 : 1, sp = k == 1 ? 1 : 0;
  auto store_off = [&](int sg) {
    const int b = b0 + (sg ? 1 - e0 : e0);
    const unsigned og_ = (unsigned)(WS_PBG + ((size_t)(b * 64 + h * 2 + sp) * DM + w * 64 + c * 4 + g) * 8);
    return (g & 1) == 0 ? og_ : 0x7ffffff0u;
  };
  const unsigned apo = (unsigned)((c & 1) * 272 + g * 64);
  auto project = [&](const char* ap, float M, float L) {
    u32x4 a[4];
#pragma unroll
    for (int s = 0; s < 4; s++) a[s] = *(const u32x4*)(ap + s * 16);
    __builtin_amdgcn_sched_barrier(0);
    L += dpp<0xB1>(L); L += dpp<0x4E>(L); L += dpp<0x141>(L);
    __builtin_amdgcn_sched_barrier(0);
    f32x4 d[4];
#pragma unroll
    for (int s = 0; s < 4; s++)
#pragma unroll
      for (int j = 0; j < 4; j++)
        d[j] = __builtin_amdgcn_mfma_f32_16x16x32_bf16(__builtin_bit_cast(bf16x8, a[s]), __builtin_bit_cast(bf16x8, wo[4 * s + j]),
                                                       s ? d[j] : f32x4{0.f, 0.f, 0.f, 0.f}, 0, 0, 0);
    return f32x3{g < 2 ? d[0][0] + d[0][1] : d[3][0] + d[3][1], g < 2 ? d[1][0] + d[1][1] : M, g < 2 ? d[2][0] + d[2][1] : L};
  };
  f32x3 seg0 = {0.f, 0.f, 0.f};
  auto load_wo = [&]() {
    const __amdgpu_buffer_rsrc_t ro = mkrs(woB, HD * DM * 2);
    const unsigned vo = (unsigned)(((h * DH + g * 32) * DM + w * 64 + c * 4) * 2);
#pragma unroll
    for (int i = 0; i < 16; i++) {
      const u32x2 u0 = __builtin_amdgcn_raw_buffer_load_b64(ro, vo + (i & 1) * 2 * DM * 2, (i >> 1) * 4 * DM * 2, 0);
      const u32x2 u1 = __builtin_amdgcn_raw_buffer_load_b64(ro, vo + ((i & 1) * 2 + 1) * DM * 2, (i >> 1) * 4 * DM * 2, 0);
      wo[i] = u32x4{u0[0], u0[1], u1[0], u1[1]};
    }
  };
  {
    float m = -INFINITY, s_[4];
    const unsigned swo = lb + L_SC + g * 16 + (c & 3) * 4;
    auto scores = [&](unsigned so, bool ok) {
      const bool b0_ = c & 1, b1_ = c & 2;
      const float r0 = (b0_ ? s_[1] : s_[0]) + dpp<0xB1>(b0_ ? s_[0] : s_[1]);
      const float r1 = (b0_ ? s_[3] : s_[2]) + dpp<0xB1>(b0_ ? s_[2] : s_[3]);
      float r = (b1_ ? r1 : r0) + dpp<0x4E>(b1_ ? r0 : r1);
      r += dpp<0x124>(r);
      r += dpp<0x128>(r);
      r = ok ? rbf(rbf(r) * SCALE) : -INFINITY;
      m = fmaxf(m, r);
      asm volatile("ds_write_b32 %0, %1" :: "v"(swo + so), "v"(r) : "memory");
    };
    auto flush_max = [&](int sg) {
      float mw = fmaxf(m, dpp<0xB1>(m));
      mw = xrow_max(fmaxf(mw, dpp<0x4E>(mw)));
      if (l == 0) asm volatile("ds_max_f32 %0, %1" :: "v"(lb + 33872u + sg * 4), "v"(mw) : "memory");
      m = -INFINITY;
    };
    bool sw = k != 1, first = true, wl = false;
    const int xw = 16 + 16 * (h >> 3);
    int x = w;
#pragma unroll 1
    while (x < NI) {
      if (!sw && x >= N0) { flush_max(0); q = q1; sw = true; }
      if (!wl && x >= xw) { load_wo(); wl = true; }
      unsigned dr; u64 sv;
      asm volatile("s_mov_b64 %1, exec\n\ts_mov_b64 exec, 1\n\tds_add_rtn_u32 %0, %2, %3\n\ts_mov_b64 exec, %1"
                   : "=&v"(dr), "=&s"(sv) : "v"(lb + 33856u), "v"(1u), "v"(ra[0][0]) : "memory");
      __builtin_amdgcn_sched_barrier(0);
      item_dots(ra, q, s_);
      asm volatile("s_waitcnt lgkmcnt(0)" : "+v"(dr) :: "memory");
      const int xn = __builtin_amdgcn_readfirstlane(dr);
      const bool more = xn < NI;
      const void* pn = more ? (const void*)kc : (const void*)vc;
      const unsigned sn = soff(more ? xn : w);
      __builtin_amdgcn_sched_barrier(0);
      ld4(ra, pn, true, sn);
      __builtin_amdgcn_sched_barrier(0);
      scores(sco(x), true);
      item_dots(rb, q, s_);
      __builtin_amdgcn_sched_barrier(0);
      ld4(rb, pn, !more || whole(xn), sn + 8192);
      __builtin_amdgcn_sched_barrier(0);
      scores(sco(x) + 128, whole(x));
      if (first) {
        item_dots(ea, q, s_); scores(sco(w + 8), true);
        item_dots(eb, q, s_); scores(sco(w + 8) + 128, true);
        first = false;
      }
      x = xn;
    }
    if (!wl) {
      load_wo();
#pragma unroll
      for (int i = 0; i < 16; i++) asm volatile("" : "+v"(wo[i]));
    }
    ld4(ea, vc, true, soff(w + 8));
    ld4(eb, vc, true, soff(w + 8) + 8192);
    flush_max(k == 1 && !sw ? 0 : 1);
    lds_inc(2);
  }
#pragma unroll
  for (int s = 0; s < 4; s++) {
    u32x4 o[4];
#pragma unroll
    for (int e = 0; e < 4; e++)
#pragma unroll
      for (int j = 0; j < 4; j++) o[j][e] = __builtin_amdgcn_perm(wo[4 * s + e][2 + (j >> 1)], wo[4 * s + e][j >> 1], (j & 1) ? 0x07060302u : 0x05040100u);
#pragma unroll
    for (int j = 0; j < 4; j++) wo[4 * s + j] = o[j];
    asm volatile("" : "+v"(wo[4 * s]), "+v"(wo[4 * s + 1]), "+v"(wo[4 * s + 2]), "+v"(wo[4 * s + 3]));
  }
  for (unsigned spins = 0; lds_cnt(2) != 8u && spins < SPIN_CAP; ++spins) __builtin_amdgcn_s_sleep(1);
  asm volatile("" ::: "memory");
  const float M0 = __builtin_bit_cast(float, __builtin_amdgcn_readfirstlane(__builtin_bit_cast(int, mxd[k == 1 ? 0 : 1])));
  const float M1 = __builtin_bit_cast(float, __builtin_amdgcn_readfirstlane(__builtin_bit_cast(int, mxd[1])));
  auto weights = [&](unsigned so, float M) {
    const f32x4 sc = *(const f32x4*)(lds + L_SC + so);
    return f32x4{fexp2((sc[0] - M) * LOG2E), fexp2((sc[1] - M) * LOG2E), fexp2((sc[2] - M) * LOG2E), fexp2((sc[3] - M) * LOG2E)};
  };
  float acc[8];
#pragma unroll
  for (int j = 0; j < 8; j++) acc[j] = 0.f;
  f32x2 vf[4][4];
  f32x4 pa, pb;
  auto get_p = [&](int x) {
    const float M = seg1(x) ? M1 : M0;
    pa = weights(sco(x) + g * 16, M); pb = weights(sco(x) + 128 + g * 16, M);
    asm volatile("" : "+v"(pa), "+v"(pb));
  };
  auto fma_v = [&](const f32x4 p) {
#pragma unroll
    for (int u = 0; u < 4; u++)
#pragma unroll
      for (int j = 0; j < 4; j++) {
        acc[2 * j]     = fmaf(p[u], vf[u][j][0], acc[2 * j]);
        acc[2 * j + 1] = fmaf(p[u], vf[u][j][1], acc[2 * j + 1]);
      }
  };
  auto step = [&](int xn) {
    const unsigned sn = soff(xn);
    __builtin_amdgcn_sched_barrier(0);
    ld4(ra, vc, true, sn);
    __builtin_amdgcn_sched_barrier(0);
    fma_v(pa);
#pragma unroll
    for (int j = 0; j < 8; j++) asm volatile("" : "+v"(acc[j]));
    item_unpack(rb, vf);
    __builtin_amdgcn_sched_barrier(0);
    ld4(rb, vc, whole(xn), sn + 8192);
    __builtin_amdgcn_sched_barrier(0);
    fma_v(pb);
  };
  auto put_acc = [&](float* rec_) {
    *(f32x2*)(rec_ + c * 8 + g * 2) = rows_sum(acc);
#pragma unroll
    for (int j = 0; j < 8; j++) acc[j] = 0.f;
  };
  if (w >= 4) __builtin_amdgcn_s_setprio(1);
  get_p(w);
  item_unpack(ra, vf);
  step(w + 16);
  get_p(w + 8);
  item_unpack(ea, vf); fma_v(pa);
  item_unpack(eb, vf); fma_v(pb);
  {
    const bool two = t < 192;
    const f32x4 p0 = weights(t * 16, t < 352 ? M0 : M1), p1 = weights(two ? (t + 512) * 16 : 172 * 64, two ? M1 : 0.f);
    const float s0 = (p0[0] + p0[1]) + (p0[2] + p0[3]), s1 = (p1[0] + p1[1]) + (p1[2] + p1[3]);
    const bool f0 = k == 1 && t < 352;
    const float l0 = xrow_sum(sum16(f0 ? s0 : 0.f)), l1 = xrow_sum(sum16((f0 ? 0.f : s0) + (two ? s1 : 0.f)));
    if (l < 2) lw[l * 8 + w] = l ? l1 : l0;
  }
  bool sw = k != 1;
  int x = w + 16;
#pragma unroll 1
  for (; x < 8 * RD; x += 8) {
    const int rd = x >> 3;
    if (rd == PRIOF) __builtin_amdgcn_s_setprio(0);
    if (!sw && x >= N0) { put_acc(rec0 + w * 128); lds_inc(3); sw = true; }
    if (rd == RF && h >= 24 && j6 == 2 && w == 0) {
      const unsigned now = clk();
      if (l == 0) st_u64(ws + WS_FP + (h - 24) * LINE, (next_tag(epa) << 32) | (now + (((now - tent) * 59) >> 8)));
    }
    if (rd == RE && k == 1) {
      for (unsigned spins = 0; lds_cnt(3) != 8u && spins < SPIN_CAP; ++spins) __builtin_amdgcn_s_sleep(1);
      asm volatile("" ::: "memory");
      f32x2 a2 = {0.f, 0.f};
#pragma unroll
      for (int i = 0; i < 8; i++) a2 += *(const f32x2*)(rec0 + i * 128 + 2 * l);
      const unsigned hi = cvtpk(a2[0], a2[1]);
      *(unsigned*)(prv + l * 4) = hi;
      *(unsigned*)(prv + 272 + l * 4) = cvtpk(a2[0] - bf_lo(hi), a2[1] - bf_hi(hi));
      seg0 = project(prv + apo, M0, lw[l & 7]);
    }
    get_p(x);
    __builtin_amdgcn_sched_barrier(0);
    item_unpack(ra, vf);
    step(x + 8);
  }
  put_acc(recd + w * RST);
  unsigned o_rv = 65536u + (g * RST + w * 16 + c) * 4, o_lw = 33920u + 32u + (l & 7) * 4;
  unsigned o_oi = (g < 2 && (c & 1) == 0) ? 41216u + g * 272 + (w * 16 + c) * 2 : 45056u + w * 1024 + 256 + l * 4;
  unsigned o_ap = 41216u + apo, o_st = store_off(nseg - 1);
  unsigned o_s0 = k == 1 ? store_off(0) : 0x7ffffff0u;
  unsigned tl0 = (unsigned)(k == 1 ? tg1 : tg0);
  u32x4 sg0 = {__float_as_uint(seg0[0]), __float_as_uint(seg0[1]), __float_as_uint(seg0[2]), (unsigned)tg0};
  asm volatile("" : "+v"(o_s0), "+v"(tl0), "+v"(sg0));
  asm volatile("" : "+v"(o_rv), "+v"(o_lw), "+v"(o_oi), "+v"(o_ap), "+v"(o_st));
  u32x2 rep = {0u, 0u};
  {
    {
#pragma unroll 1
      for (;;) {
        get_p(x);
        unsigned dr; u64 sv;
        asm volatile("s_mov_b64 %1, exec\n\ts_mov_b64 exec, 1\n\tds_add_rtn_u32 %0, %2, %3\n\ts_mov_b64 exec, %1"
                     : "=&v"(dr), "=&s"(sv) : "v"(lb + 33860u), "v"(1u), "v"(ra[0][0]) : "memory");
        __builtin_amdgcn_sched_barrier(0);
        item_unpack(ra, vf);
        asm volatile("s_waitcnt lgkmcnt(0)" : "+v"(dr) :: "memory");
        const int xn = __builtin_amdgcn_readfirstlane(dr);
        if (xn >= NI) break;
        step(xn);
        put_acc(recd + (x - 8 * RD + 8) * RST);
        x = xn;
      }
      rep = __builtin_amdgcn_raw_buffer_load_b64(rs, w < nseg ? (unsigned)WS_FP + (l & 7) * LINE : 0x7ffffff0u, 0, SC1);
      fma_v(pa);
      item_unpack(rb, vf);
      fma_v(pb);
      put_acc(recd + (x - 8 * RD + 8) * RST);
      asm volatile("s_waitcnt lgkmcnt(0)" ::: "memory");
      __syncthreads();
    }
    {
      float rv_[NP4 / 4];
#pragma unroll
      for (int r = 0; r < NP4 / 4; r++) rv_[r] = *(const float*)(lds + o_rv + r * 4 * RST * 4);
      const float li = *(const float*)(lds + o_lw);
      float a = 0.f, a1 = 0.f;
#pragma unroll
      for (int r = 0; r < NP4 / 4; r++) { if (r & 1) a1 += rv_[r]; else a += rv_[r]; }
      a += a1;
      a = xrow_sum(a);
      const float an = dpp<0xB1>(a);
      const unsigned hi = cvtpk(a, an);
      const unsigned lo2 = cvtpk(a - bf_lo(hi), an - bf_hi(hi));
      *(unsigned*)(lds + o_oi) = g ? lo2 : hi;
      __syncthreads();
      const f32x3 pv = project(lds + o_ap, M1, li);
      st16u_sc1(rs, o_st, u32x4{__float_as_uint(pv[0]), __float_as_uint(pv[1]), __float_as_uint(pv[2]), tl0});
      st16u_sc1(rs, o_s0, sg0);
    }
    if (w < nseg) {
      const int sg = w;
      const int b = b0 + (sg ? 1 - e0 : e0);
      const int j64 = (h & 7) * 8 + (h >> 3) * 2 + sp;
      const u64 tg = sg ? tg1 : tg0;
      const unsigned ta = (unsigned)tg;
      const unsigned og = (unsigned)(WS_PBG + ((size_t)(b * 64 + (l >> 2)) * DM + j64 * 8) * 8 + (l & 3) * 16);
      u32x4 p[4];
      {
        const unsigned tp = (unsigned)next_tag(epa);
        unsigned ts = ref_time(rep[0]) - TM_SW;
        if (!__all(rep[1] == tp)) {
          ts = clk();
#pragma unroll 1
          for (int spins = 0; spins < 8; ++spins) {
            __builtin_amdgcn_s_sleep(16);
            asm volatile("" ::: "memory");
            const u32x2 r2 = __builtin_amdgcn_raw_buffer_load_b64(rs, (unsigned)WS_FP + (l & 7) * LINE, 0, SC1);
            if (__all(r2[1] == tp)) { ts = ref_time(r2[0]) - TM_SW; break; }
          }
        }
        __builtin_amdgcn_s_sleep(4);
        wait_until(ts);
      }
#pragma unroll 1
      for (unsigned spins = 0; spins < SPIN_CAP; ++spins) {
        asm volatile("" ::: "memory");
#pragma unroll
        for (int i = 0; i < 4; i++) p[i] = ld16u_sc1(rs, og + i * 16 * DM * 8);
        if (__all(p[0][3] == ta && p[1][3] == ta && p[2][3] == ta && p[3][3] == ta)) break;
      }
      float v0 = 0.f, v1 = 0.f, v2 = 0.f;
#pragma unroll
      for (int i = 0; i < 4; i++) {
        const float mm = dpp<0xF5>(__uint_as_float(p[i][1]));
        const float Mh = fmaxf(mm, dpp<0x141>(mm));
        const float fq = fexp2((mm - Mh) * LOG2E);
        float L = fq * dpp<0xF5>(__uint_as_float(p[i][2]));
        L += dpp<0x141>(L);
        const float wgt = fq * __builtin_amdgcn_rcpf(L);
        v0 = fmaf(__uint_as_float(p[i][0]), wgt, v0);
        v1 = fmaf(__uint_as_float(p[i][1]), wgt, v1);
        v2 = fmaf(__uint_as_float(p[i][2]), wgt, v2);
      }
      const float u2 = dpp<0xA0>(v2);
      v1 = (l & 1) ? v0 : v1;
      v0 = (l & 1) ? u2 : v0;
      v0 += dpp<0x128>(v0); v0 += dpp<0x124>(v0); v0 = xrow_sum(v0);
      v1 += dpp<0x128>(v1); v1 += dpp<0x124>(v1); v1 = xrow_sum(v1);
      if (l < 4) {
        *(unsigned*)(out + b * 1024 + 512 + j64 * 8 + l * 2) = cvtpk(v0, v1);
        if (j64 == 0 && l == 0) st_u64(ws + WS_EPB + 256 * b, tg);
      }
    }
  }
}

__device__ __forceinline__ unsigned lds_off(int row, int ch) {
  return 256 * row + 16 * (ch ^ (((row & 3) << 2) | ((row >> 2) & 3)));
}

__device__ __forceinline__ void role_A1(int h, const __hip_bfloat16* __restrict__ xA, const __hip_bfloat16* __restrict__ wqA,
                                        const __hip_bfloat16* __restrict__ wuk, const __hip_bfloat16* __restrict__ wuv,
                                        const __hip_bfloat16* __restrict__ woA, char* __restrict__ ws,
                                        __amdgpu_buffer_rsrc_t rs, __hip_bfloat16* __restrict__ out, char* lds) {
  const int t = threadIdx.x, l = t & 63;
  const int w = __builtin_amdgcn_readfirstlane(t >> 6);
  const int g = l >> 4, c = l & 15;
  const unsigned tent = clk();
  float* red = (float*)(lds + 16384);
  const unsigned vo = (unsigned)(((w * 64 + g * 16) * HD + h * DH + c * 8) * 2);
  u64 tag;
  {
    u32x4 xr[NB][2], wr[16], wk[16];
    const __amdgpu_buffer_rsrc_t rx = mkrs(xA, NB * DM * 2);
    const unsigned xo = (unsigned)((w * 64 + g * 16) * 2);
#pragma unroll
    for (int bb = 0; bb < NB; bb++)
#pragma unroll
      for (int i = 0; i < 2; i++) xr[bb][i] = __builtin_amdgcn_raw_buffer_load_b128(rx, xo + bb * DM * 2 + i * 16, 0, 0);
    const __amdgpu_buffer_rsrc_t rq = mkrs(wqA, DM * HD * 2);
#pragma unroll
    for (int i = 0; i < 16; i++) wr[i] = __builtin_amdgcn_raw_buffer_load_b128(rq, vo, i * HD * 2, NT);
    u64 epv = 0;
    if (t == 0) epv = ld_u64(ws + WS_EPA);
    unsigned* qs = (unsigned*)lds;
    unsigned short* qo = (unsigned short*)(lds + 4096);
    float acc[NB][8];
#pragma unroll
    for (int bb = 0; bb < NB; bb++)
#pragma unroll
      for (int j = 0; j < 8; j++) acc[bb][j] = 0.f;
#pragma unroll
    for (int i = 0; i < 16; i += 2) {
#pragma unroll
      for (int j = 0; j < 4; j++) {
        const unsigned pl = __builtin_amdgcn_perm(wr[i + 1][j], wr[i][j], 0x05040100u);
        const unsigned ph = __builtin_amdgcn_perm(wr[i + 1][j], wr[i][j], 0x07060302u);
#pragma unroll
        for (int bb = 0; bb < NB; bb++) {
          const unsigned x = xr[bb][i >> 3][(i >> 1) & 3];
          acc[bb][2 * j] = dot2(pl, x, acc[bb][2 * j]);
          acc[bb][2 * j + 1] = dot2(ph, x, acc[bb][2 * j + 1]);
        }
      }
    }
    float* rp = red + (w * 4 + g) * 512 + c * 8;
#pragma unroll
    for (int bb = 0; bb < NB; bb++) {
      *(f32x4*)(rp + bb * 128) = f32x4{acc[bb][0], acc[bb][1], acc[bb][2], acc[bb][3]};
      *(f32x4*)(rp + bb * 128 + 4) = f32x4{acc[bb][4], acc[bb][5], acc[bb][6], acc[bb][7]};
    }
    __syncthreads();
    {
      const __amdgpu_buffer_rsrc_t rku = mkrs(wuk, DC * HD * 2);
#pragma unroll
      for (int i = 0; i < 16; i++) wk[i] = __builtin_amdgcn_raw_buffer_load_b128(rku, vo, i * HD * 2, NT);
    }
    {
      float s = 0.f;
#pragma unroll
      for (int i = 0; i < 32; i++) s += red[i * 512 + t];
      const float sn = dpp<0xB1>(s);
      if ((t & 1) == 0) qs[t >> 1] = cvtpk(s, sn);
      if (t == 0) *(u64*)(lds + 2048) = epv;
    }
    __syncthreads();
    tag = next_tag(uni64(*(const u64*)(lds + 2048)));
    {
      u32x4 qq[NB];
#pragma unroll
      for (int bb = 0; bb < NB; bb++) qq[bb] = *(const u32x4*)(qs + bb * 64 + c * 4);
#pragma unroll
      for (int i = 0; i < 16; i++) {
        float s[NB];
#pragma unroll
        for (int bb = 0; bb < NB; bb++) {
          float v = dot2(wk[i][0], qq[bb][0], 0.f);
          v = dot2(wk[i][1], qq[bb][1], v);
          v = dot2(wk[i][2], qq[bb][2], v);
          v = dot2(wk[i][3], qq[bb][3], v);
          s[bb] = sum16(v);
        }
        const float v = (c & 2) ? ((c & 1) ? s[3] : s[2]) : ((c & 1) ? s[1] : s[0]);
        if (c < NB) qo[c * 512 + w * 64 + g * 16 + i] = (unsigned short)(cvtpk(v, v) & 0xffff);
      }
    }
    __syncthreads();
    if (t < 256) {
      const int bb = t >> 6, ch = t & 63;
      st16u_sc1(rs, (unsigned)(WS_QABS + ((size_t)(bb * NH + h) * DC + ch * 8) * 2), *(const u32x4*)(qo + bb * 512 + ch * 8));
    }
    signal_flag(ws + WS_F1 + h * LINE, tag);
  }
  const unsigned ta = (unsigned)tag, tb = (unsigned)(tag >> 32);
  {
    u32x4 bv[8][2], bo[8][2];
    u32x4 pre[2][8], wo[16];
    {
      const __amdgpu_buffer_rsrc_t ruv = mkrs(wuv, DC * HD * 2);
      const __amdgpu_buffer_rsrc_t rwo = mkrs(woA, HD * DM * 2);
      const unsigned wvo = (unsigned)(((h * DH + (l >> 3) * 16) * DM + w * 64 + (l & 7) * 8) * 2);
#pragma unroll
      for (int i = 0; i < 16; i++) pre[i >> 3][i & 7] = __builtin_amdgcn_raw_buffer_load_b128(ruv, vo - g * 8 * HD * 2, ((i >> 3) * 32 + (i & 7)) * HD * 2, NT);
#pragma unroll
      for (int i = 0; i < 16; i++) wo[i] = __builtin_amdgcn_raw_buffer_load_b128(rwo, wvo, i * DM * 2, NT);
    }
#pragma unroll
    for (int j = 0; j < 8; j++)
#pragma unroll
      for (int s = 0; s < 2; s++) {
#pragma unroll
        for (int e = 0; e < 4; e++)
          bv[j][s][e] = __builtin_amdgcn_perm(pre[s][2 * e + 1][j >> 1], pre[s][2 * e][j >> 1], (j & 1) ? 0x07060302u : 0x05040100u);
        asm volatile("" : "+v"(bv[j][s]));
      }
#pragma unroll
    for (int j = 0; j < 8; j++)
#pragma unroll
      for (int s = 0; s < 2; s++) {
#pragma unroll
        for (int e = 0; e < 4; e++)
          bo[j][s][e] = __builtin_amdgcn_perm(wo[8 * s + 2 * e + 1][j >> 1], wo[8 * s + 2 * e][j >> 1], (j & 1) ? 0x07060302u : 0x05040100u);
        asm volatile("" : "+v"(bo[j][s]));
      }
    constexpr int LAS = 1040;
    unsigned short* cs = (unsigned short*)(lds + 16384);
    float* outs = (float*)(lds + 20480);
    float* cred = (float*)(lds + 32768);
    {
    {
    }
    unsigned po = (unsigned)(WS_PAO + ((size_t)(w * 4 * NH + h) * DC + l * 8) * 2);
    unsigned pc = (unsigned)(WS_PAC + (size_t)((w * 4 + g) * NH + h) * 8);
    unsigned lw_ = (unsigned)(65536 + ((w & 1) * 16 + (w >> 1) * 4) * LAS + l * 16), lr_ = (unsigned)(65536 + c * LAS + (w * 64 + g * 8) * 2);
    asm volatile("" : "+v"(po), "+v"(pc), "+v"(lw_), "+v"(lr_));
    const unsigned tw = (h & 1) ? tb : ta;
    const unsigned tg = wait_gate(ws + WS_FP, ta, (unsigned*)(lds + 8192), tent);
    wait_until(tg + TM_GET);
    {
      u32x4 pr[4];
#pragma unroll 1
      for (unsigned spins = 0; spins < SPIN_CAP; ++spins) {
        asm volatile("" ::: "memory");
        const u32x2 ck = __builtin_amdgcn_raw_buffer_load_b64(rs, pc, 0, SC1);
#pragma unroll
        for (int i = 0; i < 4; i++) pr[i] = __builtin_amdgcn_raw_buffer_load_b128(rs, po, i * NH * DC * 2, SC1);
#pragma unroll
        for (int i = 0; i < 4; i++) *(u32x4*)(lds + lw_ + i * LAS) = pr[i];
        if (__all(row_sums(pr) == ck[0] && ck[1] == tw)) break;
        __builtin_amdgcn_s_sleep(2);
      }
      __syncthreads();
      u32x4 af[2][2];
#pragma unroll
      for (int tl = 0; tl < 2; tl++)
#pragma unroll
        for (int s = 0; s < 2; s++) af[tl][s] = *(const u32x4*)(lds + lr_ + tl * 16 * LAS + s * 64);
      f32x4 dd[8];
#pragma unroll
      for (int j = 0; j < 8; j++)
        dd[j] = __builtin_amdgcn_mfma_f32_16x16x32_bf16(__builtin_bit_cast(bf16x8, af[0][0]), __builtin_bit_cast(bf16x8, bv[j][0]), f32x4{0.f, 0.f, 0.f, 0.f}, 0, 0, 0);
#pragma unroll
      for (int j = 0; j < 8; j++)
        dd[j] = __builtin_amdgcn_mfma_f32_16x16x32_bf16(__builtin_bit_cast(bf16x8, af[0][1]), __builtin_bit_cast(bf16x8, bv[j][1]), dd[j], 0, 0, 0);
#pragma unroll
      for (int j = 0; j < 8; j++)
        dd[j] = __builtin_amdgcn_mfma_f32_16x16x32_bf16(__builtin_bit_cast(bf16x8, af[1][0]), __builtin_bit_cast(bf16x8, bv[j][0]), dd[j], 0, 0, 0);
#pragma unroll
      for (int j = 0; j < 8; j++)
        dd[j] = __builtin_amdgcn_mfma_f32_16x16x32_bf16(__builtin_bit_cast(bf16x8, af[1][1]), __builtin_bit_cast(bf16x8, bv[j][1]), dd[j], 0, 0, 0);
      f32x4 v[2];
#pragma unroll
      for (int j = 0; j < 8; j++) v[j >> 2][j & 3] = (dd[j][0] + dd[j][1]) + (dd[j][2] + dd[j][3]);
      *(f32x4*)(cred + (w * 4 + g) * 128 + c * 8) = v[0];
      *(f32x4*)(cred + (w * 4 + g) * 128 + c * 8 + 4) = v[1];
    }
    __syncthreads();
    {
      float s = 0.f;
#pragma unroll
      for (int i = 0; i < 8; i++) s += cred[i * 512 + t];
      const float sn = dpp<0xB1>(s);
      if ((t & 1) == 0) *(unsigned*)(cs + t) = cvtpk(s, sn);
    }
    __syncthreads();
    {
      const char* ap = (const char*)cs + ((c >> 1) & 3) * 256 + (g * 32 + (c & 1) * 16) * 2;
      const u32x4 a0 = *(const u32x4*)(ap), a1 = *(const u32x4*)(ap + 16);
      f32x4 dd[8];
#pragma unroll
      for (int j = 0; j < 8; j++)
        dd[j] = __builtin_amdgcn_mfma_f32_16x16x32_bf16(__builtin_bit_cast(bf16x8, a0), __builtin_bit_cast(bf16x8, bo[j][0]), f32x4{0.f, 0.f, 0.f, 0.f}, 0, 0, 0);
#pragma unroll
      for (int j = 0; j < 8; j++)
        dd[j] = __builtin_amdgcn_mfma_f32_16x16x32_bf16(__builtin_bit_cast(bf16x8, a1), __builtin_bit_cast(bf16x8, bo[j][1]), dd[j], 0, 0, 0);
      f32x4 v4[2];
#pragma unroll
      for (int j = 0; j < 8; j++) {
        const float u0 = (l & 8) ? dd[j][1] : dd[j][0], u1 = (l & 8) ? dd[j][3] : dd[j][2];
        const float keep = (l & 8) ? u1 : u0, give = (l & 8) ? u0 : u1;
        v4[j >> 2][j & 3] = keep + dpp<0x128>(give);
      }
      if (g < 2) {
        float* op = outs + (g * 2 + ((l >> 3) & 1)) * 512 + w * 64 + (l & 7) * 8;
        *(f32x4*)op = v4[0];
        *(f32x4*)(op + 4) = v4[1];
      }
    }
    __syncthreads();
    {
      const f32x2 va = *(const f32x2*)(outs + t * 2), vb = *(const f32x2*)(outs + 1024 + t * 2);
      const unsigned oo = (unsigned)(WS_PA + ((size_t)((t >> 8) * NH + h) * DM + (t & 255) * 2) * 8);
      st16u_sc1(rs, oo, u32x4{__float_as_uint(va[0]), ta, __float_as_uint(va[1]), tb});
      st16u_sc1(rs, oo + 2 * NH * DM * 8, u32x4{__float_as_uint(vb[0]), ta, __float_as_uint(vb[1]), tb});
    }
  if (w < 4) {
    const int bb = h >> 3, n0 = (h & 7) * 64 + w * 16;
    const unsigned og = (unsigned)(WS_PA + ((size_t)(bb * NH + (l >> 3)) * DM + n0) * 8 + (l & 7) * 16);
    u32x4 p[4];
    __builtin_amdgcn_s_sleep(10);
    wait_until(tg + TM_T4);
#pragma unroll 1
    for (unsigned spins = 0; spins < SPIN_CAP; ++spins) {
      asm volatile("" ::: "memory");
#pragma unroll
      for (int i = 0; i < 4; i++) p[i] = ld16u_sc1(rs, og + i * 8 * DM * 8);
      bool ok = true;
#pragma unroll
      for (int i = 0; i < 4; i++) ok = ok && p[i][1] == ta && p[i][3] == tb;
      if (__all(ok)) break;
      __builtin_amdgcn_s_sleep(4);
    }
    float v0 = (__uint_as_float(p[0][0]) + __uint_as_float(p[1][0])) + (__uint_as_float(p[2][0]) + __uint_as_float(p[3][0]));
    float v1 = (__uint_as_float(p[0][2]) + __uint_as_float(p[1][2])) + (__uint_as_float(p[2][2]) + __uint_as_float(p[3][2]));
    v0 += dpp<0x128>(v0); v0 = xrow_sum(v0);
    v1 += dpp<0x128>(v1); v1 = xrow_sum(v1);
    if (l < 8) {
      *(unsigned*)(out + bb * 1024 + n0 + l * 2) = cvtpk(v0, v1);
      if (h == 0 && t == 0) st_u64(ws + WS_EPA, tag);
    }
  }
    }
  }
}

__device__ __forceinline__ void role_A2(int a, const __hip_bfloat16* __restrict__ ckv, char* __restrict__ ws,
                                        __amdgpu_buffer_rsrc_t rs, char* lds) {
  const int t = threadIdx.x, l = t & 63;
  const int w = __builtin_amdgcn_readfirstlane(t >> 6);
  const int g = l >> 4, c = l & 15;
  const unsigned tent = clk();
  u64 ep = 0;
  if (t == 0) ep = ld_u64(ws + WS_EPA);
  const int grp = w >> 2, wq = w & 3;
  const int b = a / SPLIT_A, sp = a % SPLIT_A;
  const int r = l & 31, hh = l >> 5;
  constexpr unsigned CB = (unsigned)((size_t)NB * SK * DC * 2);
  constexpr int NTL = KA / 64;
  const unsigned cvo = (unsigned)((g * DC + 128 * wq + c * 8) * 2);
  const unsigned cso = (unsigned)(((size_t)b * SK + sp * KA + grp * 32) * DC * 2);
  auto load_tile = [&](u32x4 (&pr)[8], int tile) {
    const bool ok = tile < NTL;
    const __amdgpu_buffer_rsrc_t rc = mkrs(ckv, ok ? CB : 0u);
    const unsigned so = ok ? cso + (unsigned)((tile + sp) & (NTL - 1)) * 64 * DC * 2 : 0u;
#pragma unroll
    for (int u = 0; u < 8; u++) pr[u] = __builtin_amdgcn_raw_buffer_load_b128(rc, cvo, so + u * 4 * DC * 2, NT);
  };
  char* img = lds + w * 8192;
  float* sred = (float*)(lds + 65536 + grp * 16384);
  float* mxs = (float*)(lds + 98304 + grp * 512);
  unsigned* pbuf = (unsigned*)(lds + 102400 + grp * 2048);
  float* lall = (float*)(lds + 106496);
  const unsigned imga = (unsigned)(uintptr_t)(__attribute__((address_space(3))) char*)img;
  auto put_tile = [&](u32x4 (&pr)[8]) {
#pragma unroll
    for (int u = 0; u < 8; u++) *(u32x4*)(img + lds_off(4 * u + g, c)) = pr[u];
  };
  u32x4 pre[2][8];
  load_tile(pre[0], 0);
  load_tile(pre[1], 1);
  const u64 tag0 = next_tag(uni64(ep));
  put_tile(pre[0]);
  __builtin_amdgcn_sched_barrier(0);
  load_tile(pre[0], 2);
  if (t < 64) {
    wait_until(tent + TM_F1);
#pragma unroll 1
    for (unsigned spins = 0; spins < SPIN_CAP; ++spins) {
      __builtin_amdgcn_s_sleep(32);
      if (__all(ld_u64(ws + WS_F1 + (l & 31) * LINE) == tag0)) break;
    }
  }
  if (t == 0) *(u64*)(lds + 110592) = tag0;
  __syncthreads();
  const u64 tag = uni64(*(const u64*)(lds + 110592));
  const unsigned ta = (unsigned)tag, tb = (unsigned)(tag >> 32);
  bf16x8 qf[8];
  {
    char* qi = lds + 65536;
    const unsigned qo = (unsigned)(WS_QABS + (size_t)b * NH * DC * 2 + t * 16);
    u32x4 qv[4];
#pragma unroll
    for (int i = 0; i < 4; i++) qv[i] = ld16u_sc1(rs, qo + i * 8192);
#pragma unroll
    for (int i = 0; i < 4; i++) *(u32x4*)(qi + (i * 8 + w) * 1040 + l * 16) = qv[i];
    __syncthreads();
#pragma unroll
    for (int s = 0; s < 8; s++) qf[s] = __builtin_bit_cast(bf16x8, *(const u32x4*)(qi + r * 1040 + (128 * wq + 16 * s + 8 * hh) * 2));
#pragma unroll
    for (int s = 0; s < 8; s++) asm volatile("" : "+v"(qf[s]));
    __syncthreads();
  }
  f32x16 o[4];
#pragma unroll
  for (int i = 0; i < 4; i++)
#pragma unroll
    for (int j = 0; j < 16; j++) o[i][j] = 0.f;
  float m = -INFINITY, lsum = 0.f;
  const int q4 = (l & 15) >> 2, p4 = l & 3, colh = g & 1;
  auto tile_body = [&](int tile, u32x4 (&nx)[8]) {
    f32x16 s;
#pragma unroll
    for (int j = 0; j < 16; j++) s[j] = 0.f;
    {
      u32x4 af[8];
#pragma unroll
      for (int ks = 0; ks < 8; ks++) af[ks] = *(const u32x4*)(img + lds_off(r, 2 * ks + hh));
      __builtin_amdgcn_sched_barrier(0);
#pragma unroll
      for (int ks = 0; ks < 8; ks++) s = __builtin_amdgcn_mfma_f32_32x32x16_bf16(__builtin_bit_cast(bf16x8, af[ks]), qf[ks], s, 0, 0, 0);
    }
    f32x4* sr = (f32x4*)sred;
#pragma unroll
    for (int k4 = 0; k4 < 4; k4++) sr[(k4 * 4 + wq) * 64 + l] = f32x4{s[4 * k4], s[4 * k4 + 1], s[4 * k4 + 2], s[4 * k4 + 3]};
    __syncthreads();
    float x[4];
    {
      const f32x4 v = (sr[(wq * 4) * 64 + l] + sr[(wq * 4 + 1) * 64 + l]) + (sr[(wq * 4 + 2) * 64 + l] + sr[(wq * 4 + 3) * 64 + l]);
#pragma unroll
      for (int e = 0; e < 4; e += 2) {
        const unsigned r1 = cvtpk(v[e], v[e + 1]);
        const unsigned r2 = cvtpk(bf_lo(r1) * SCALE, bf_hi(r1) * SCALE);
        x[e] = bf_lo(r2); x[e + 1] = bf_hi(r2);
      }
      float mq = fmaxf(fmaxf(x[0], x[1]), fmaxf(x[2], x[3]));
      auto sx = __builtin_amdgcn_permlane32_swap(__float_as_uint(mq), __float_as_uint(mq), false, false);
      mq = fmaxf(__uint_as_float(sx[0]), __uint_as_float(sx[1]));
      if (hh == 0) mxs[r * 4 + wq] = mq;
    }
    __syncthreads();
    const f32x4 m4 = *(const f32x4*)(mxs + r * 4);
    const float mn = fmaxf(fmaxf(m, fmaxf(m4[0], m4[1])), fmaxf(m4[2], m4[3]));
    const float alpha = fexp2((m - mn) * LOG2E);
    {
      const unsigned p0 = cvtpk(fexp2((x[0] - mn) * LOG2E), fexp2((x[1] - mn) * LOG2E));
      const unsigned p1 = cvtpk(fexp2((x[2] - mn) * LOG2E), fexp2((x[3] - mn) * LOG2E));
      *(u32x2*)(pbuf + l * 8 + wq * 2) = u32x2{p0, p1};
      lsum = lsum * alpha + ((bf_lo(p0) + bf_hi(p0)) + (bf_lo(p1) + bf_hi(p1)));
    }
    m = mn;
    __syncthreads();
    unsigned pk[8];
    {
      const u32x4 a0 = *(const u32x4*)(pbuf + l * 8), a1 = *(const u32x4*)(pbuf + l * 8 + 4);
      pk[0] = a0[0]; pk[1] = a0[1]; pk[2] = a0[2]; pk[3] = a0[3]; pk[4] = a1[0]; pk[5] = a1[1]; pk[6] = a1[2]; pk[7] = a1[3];
    }
    if (__any(alpha != 1.f)) {
#pragma unroll
      for (int i = 0; i < 4; i++)
#pragma unroll
        for (int j = 0; j < 16; j++) o[i][j] *= alpha;
    }
#pragma unroll
    for (int s2 = 0; s2 < 2; s2++) {
      u32x2 tv[4][2];
#pragma unroll
      for (int ct = 0; ct < 4; ct++) {
        const int ch = 4 * ct + 2 * colh + (p4 >> 1);
        const unsigned a0 = lds_off(4 * hh + q4, ch) + 8 * (p4 & 1);
        const unsigned a1 = lds_off(8 + 4 * hh + q4, ch) + 8 * (p4 & 1);
        if (s2 == 0) {
          asm volatile("ds_read_b64_tr_b16 %0, %1" : "=v"(tv[ct][0]) : "v"(imga + a0) : "memory");
          asm volatile("ds_read_b64_tr_b16 %0, %1" : "=v"(tv[ct][1]) : "v"(imga + a1) : "memory");
        } else {
          asm volatile("ds_read_b64_tr_b16 %0, %1 offset:4096" : "=v"(tv[ct][0]) : "v"(imga + a0) : "memory");
          asm volatile("ds_read_b64_tr_b16 %0, %1 offset:4096" : "=v"(tv[ct][1]) : "v"(imga + a1) : "memory");
        }
      }
      asm volatile("s_waitcnt lgkmcnt(0)" ::: "memory");
      __builtin_amdgcn_sched_barrier(0);
      u32x4 pb = {pk[4 * s2], pk[4 * s2 + 1], pk[4 * s2 + 2], pk[4 * s2 + 3]};
#pragma unroll
      for (int ct = 0; ct < 4; ct++) {
        u32x4 av = {tv[ct][0][0], tv[ct][0][1], tv[ct][1][0], tv[ct][1][1]};
        o[ct] = __builtin_amdgcn_mfma_f32_32x32x16_bf16(__builtin_bit_cast(bf16x8, av), __builtin_bit_cast(bf16x8, pb), o[ct], 0, 0, 0);
      }
    }
    put_tile(nx);
    __builtin_amdgcn_sched_barrier(0);
    load_tile(nx, tile + 3);
  };
#pragma unroll 1
  for (int tile = 0; tile < NTL; tile += 2) {
    tile_body(tile, pre[1]);
    tile_body(tile + 1, pre[0]);
  }
  constexpr int OST = 516;
  float* ob = (float*)lds;
  float* mlb = (float*)(lds + 32 * OST * 4);
  lall[w * 64 + l] = lsum;
  __syncthreads();
  {
    const float* lp = lall + grp * 256 + r;
    lsum = ((lp[0] + lp[32]) + (lp[64] + lp[96])) + ((lp[128] + lp[160]) + (lp[192] + lp[224]));
  }
  if (grp == 1) {
#pragma unroll
    for (int ct = 0; ct < 4; ct++)
#pragma unroll
      for (int j4 = 0; j4 < 4; j4++)
        *(f32x4*)(ob + r * OST + 128 * wq + 32 * ct + 8 * j4 + 4 * hh) =
            f32x4{o[ct][4 * j4], o[ct][4 * j4 + 1], o[ct][4 * j4 + 2], o[ct][4 * j4 + 3]};
    if (wq == 0 && l < 32) { mlb[2 * r] = m; mlb[2 * r + 1] = lsum; }
  }
  __syncthreads();
  if (grp == 0) {
    const float m1 = mlb[2 * r], l1 = mlb[2 * r + 1];
    const float M = fmaxf(m, m1);
    const float f0 = fexp2((m - M) * LOG2E), f1 = fexp2((m1 - M) * LOG2E);
#pragma unroll
    for (int ct = 0; ct < 4; ct++)
#pragma unroll
      for (int j4 = 0; j4 < 4; j4++) {
        float* p = ob + r * OST + 128 * wq + 32 * ct + 8 * j4 + 4 * hh;
        const f32x4 o1 = *(const f32x4*)p;
        *(f32x4*)p = f32x4{o[ct][4 * j4] * f0 + o1[0] * f1, o[ct][4 * j4 + 1] * f0 + o1[1] * f1,
                           o[ct][4 * j4 + 2] * f0 + o1[2] * f1, o[ct][4 * j4 + 3] * f0 + o1[3] * f1};
      }
    if (wq == 0 && l < 32)
      st16u_sc1(rs, (unsigned)(WS_PAML + ((size_t)(b * SPLIT_A + sp) * NH + r) * 16),
                u32x4{__float_as_uint(M), ta, __float_as_uint(lsum * f0 + l1 * f1), tb});
  }
  float* wgt = mlb + 64;
  unsigned* pcs = (unsigned*)(mlb + 128);
  const unsigned tg = wait_gate(ws + WS_FP, ta, (unsigned*)(mlb + 96), tent);
  if (t < 256) {
    const unsigned mo = (unsigned)(WS_PAML + ((size_t)(b * SPLIT_A + (t & 7)) * NH + (t >> 3)) * 16);
    u32x4 mv;
#pragma unroll 1
    for (unsigned spins = 0; spins < SPIN_CAP; ++spins) {
      asm volatile("" ::: "memory");
      mv = ld16u_sc1(rs, mo);
      if (__all(mv[1] == ta && mv[3] == tb)) break;
      __builtin_amdgcn_s_sleep(16);
    }
    const float mm = __uint_as_float(mv[0]);
    float M = fmaxf(mm, dpp<0xB1>(mm)); M = fmaxf(M, dpp<0x4E>(M)); M = fmaxf(M, dpp<0x141>(M));
    const float f = fexp2((mm - M) * LOG2E);
    float L = f * __uint_as_float(mv[2]);
    L += dpp<0xB1>(L); L += dpp<0x4E>(L); L += dpp<0x141>(L);
    if ((t & 7) == sp) wgt[t >> 3] = f / L;
  }
  __syncthreads();
  {
    const unsigned pao = (unsigned)(WS_PAO + (size_t)(b * SPLIT_A + sp) * NH * DC * 2);
    u32x4 pv[4];
#pragma unroll
    for (int i = 0; i < 4; i++) {
      const int hd = i * 8 + w;
      const float f = wgt[hd];
      const f32x4 v0 = *(const f32x4*)(ob + hd * OST + l * 8) * f, v1 = *(const f32x4*)(ob + hd * OST + l * 8 + 4) * f;
      pv[i] = u32x4{cvtpk(v0[0], v0[1]), cvtpk(v0[2], v0[3]), cvtpk(v1[0], v1[1]), cvtpk(v1[2], v1[3])};
      asm volatile("" : "+v"(pv[i]));
    }
    {
      const unsigned cs = row_sums(pv);
      if (c == 0) pcs[g * 8 + w] = cs;
    }
    __syncthreads();
    const u32x2 c2 = *(const u32x2*)(pcs + (t & 15) * 2);
    const unsigned co = t < 16 ? (unsigned)(WS_PAC + (size_t)(b * SPLIT_A + sp) * NH * 8 + t * 16) : 0x7ffffff0u;
    wait_until(tg);
#pragma unroll
    for (int i = 0; i < 4; i++) st16u_sc1(rs, pao + (unsigned)(i * 512 + t) * 16, pv[i]);
    if (w == 0) st16u_sc1(rs, co, u32x4{c2[0], ta, c2[1], tb});
  }
}

__global__ __launch_bounds__(512, 2) void k_fused(const __hip_bfloat16* __restrict__ xA, const __hip_bfloat16* __restrict__ ckv,
                                                  const __hip_bfloat16* __restrict__ wqA, const __hip_bfloat16* __restrict__ wuk,
                                                  const __hip_bfloat16* __restrict__ wuv, const __hip_bfloat16* __restrict__ woA,
                                                  const __hip_bfloat16* __restrict__ xB, const __hip_bfloat16* __restrict__ kc,
                                                  const __hip_bfloat16* __restrict__ vc, const __hip_bfloat16* __restrict__ wqB,
                                                  const __hip_bfloat16* __restrict__ woB, char* __restrict__ ws,
                                                  __hip_bfloat16* __restrict__ out) {
  __shared__ __attribute__((aligned(16))) char lds[131072];
  asm volatile("" :: "s"(xA), "s"(ckv), "s"(wqA), "s"(wuk), "s"(wuv), "s"(woA), "s"(xB), "s"(kc), "s"(vc), "s"(wqB), "s"(woB), "s"(ws), "s"(out));
  const int blk = blockIdx.x, xcd = blk & 7, r = blk >> 3;
  unsigned pfv = 0;
  {
    u64 pc; unsigned len;
    asm volatile("s_getpc_b64 %0\n.Lcode_begin:\n\ts_mov_b32 %1, .Lcode_end-.Lcode_begin" : "=s"(pc), "=s"(len));
    const unsigned idx = r < 5 ? (unsigned)((r * 8 + (threadIdx.x >> 6) + xcd * 5) % 40) : 1000u;
    const __amdgpu_buffer_rsrc_t rc = mkrs((const void*)pc, len);
    pfv = __builtin_amdgcn_raw_buffer_load_b32(rc, idx * 1024 + (threadIdx.x & 63) * 16, 0, 0);
  }
  const __amdgpu_buffer_rsrc_t rs = mkrs(ws, (unsigned)WS_END);
  if (r < 8) {
    if (r < 4) role_A1(xcd * 4 + r, xA, wqA, wuk, wuv, woA, ws, rs, out, lds);
    else role_A2(xcd * 4 + r - 4, ckv, ws, rs, lds);
  } else {
    const int rr = r - 8, hq = rr / 6;
    role_B(((0x75643120u >> (4 * xcd)) & 7) + 8 * hq, rr - 6 * hq, xB, kc, vc, wqB, woB, ws, rs, out, lds);
  }
  asm volatile(".Lcode_end:" :: "v"(pfv));
}

extern "C" void kernel_launch(void* const* d_in, const int* in_sizes, int n_in,
                              void* d_out, int out_size, void* d_ws, size_t ws_size,
                              hipStream_t stream) {
  k_fused<<<256, 512, 0, stream>>>((const __hip_bfloat16*)d_in[0], (const __hip_bfloat16*)d_in[1], (const __hip_bfloat16*)d_in[2],
                                   (const __hip_bfloat16*)d_in[3], (const __hip_bfloat16*)d_in[4], (const __hip_bfloat16*)d_in[5],
                                   (const __hip_bfloat16*)d_in[6], (const __hip_bfloat16*)d_in[7], (const __hip_bfloat16*)d_in[8],
                                   (const __hip_bfloat16*)d_in[9], (const __hip_bfloat16*)d_in[10], (char*)d_ws,
                                   (__hip_bfloat16*)d_out);
}
```

```cpp
#include <hip/hip_runtime.h>
#include <hip/hip_bf16.h>
#include <stdint.h>

typedef __attribute__((ext_vector_type(4))) unsigned u32x4;
typedef __attribute__((ext_vector_type(2))) unsigned u32x2;
typedef __attribute__((ext_vector_type(4))) float f32x4;
typedef __attribute__((ext_vector_type(2))) float f32x2;
typedef __attribute__((ext_vector_type(3))) float f32x3;
typedef __attribute__((ext_vector_type(16))) float f32x16;
typedef __attribute__((ext_vector_type(8))) __bf16 bf16x8;
typedef __attribute__((ext_vector_type(2))) __bf16 bf16x2;
typedef __attribute__((address_space(1))) unsigned gu32;
typedef __attribute__((address_space(1))) unsigned long long gu64;
typedef unsigned long long u64;

constexpr int NB = 4;
constexpr int NH = 32;
constexpr int SK = 4096;
constexpr int DH = 128;
constexpr int DM = 512;
constexpr int DC = 512;
constexpr int HD = NH * DH;

constexpr int SPLIT_A = 8;
constexpr int KA = SK / SPLIT_A;
constexpr int SS = 171;
constexpr int NI = 86;
constexpr int N0 = 43;
constexpr int RD = 9;
constexpr int NP = NI - 8 * RD + 8;
constexpr int NP4 = (NP + 3) & ~3;
static_assert(8 * RD + 8 <= NI, "");
constexpr int RST = 144;
constexpr int RE = RD - 1;
constexpr int PRIOF = 4;

constexpr unsigned TM_F1 = 1600;
constexpr unsigned TM_FC = 3200;
constexpr unsigned TM_ML = 1040;
constexpr unsigned TM_PUT = 1160;
constexpr unsigned TM_GET = 110;
constexpr unsigned TM_PA = 1625;
constexpr unsigned TM_T4 = 60;
constexpr unsigned TM_SW = 1720;

constexpr float SCALE = 0.08838834764831845f;
constexpr float LOG2E = 1.4426950408889634f;
constexpr int NT = 2;
constexpr int SC1 = 16;

constexpr size_t WS_EPA  = 0;
constexpr size_t WS_EPB  = 256;
constexpr int LINE = 256;
constexpr size_t WS_F1   = 4096;
constexpr size_t WS_FS   = 12288;
constexpr size_t WS_FK   = 14336;
constexpr size_t WS_QABS = 16384;
constexpr size_t WS_PAML = WS_QABS + (size_t)NB * NH * DC * 2;
constexpr size_t WS_PAC  = WS_PAML + (size_t)NB * SPLIT_A * NH * 16;
constexpr size_t WS_PAO  = WS_PAC + (size_t)NB * SPLIT_A * NH * 8;
constexpr size_t WS_PA   = WS_PAO + (size_t)NB * SPLIT_A * NH * DC * 2;
constexpr size_t WS_PBG  = WS_PA + (size_t)NB * NH * DM * 8;
constexpr size_t WS_END  = WS_PBG + (size_t)NB * 64 * DM * 8;

__device__ __forceinline__ float bf_lo(unsigned u) { return __uint_as_float(u << 16); }
__device__ __forceinline__ float bf_hi(unsigned u) { return __uint_as_float(u & 0xffff0000u); }
__device__ __forceinline__ unsigned cvtpk(float lo, float hi) {
  f32x2 v = {lo, hi};
  return __builtin_bit_cast(unsigned, __builtin_convertvector(v, bf16x2));
}
__device__ __forceinline__ float rbf(float x) { return __uint_as_float(cvtpk(x, x) << 16); }
__device__ __forceinline__ float dot2(unsigned k, unsigned q, float acc) {
  return __builtin_amdgcn_fdot2_f32_bf16(__builtin_bit_cast(bf16x2, k), __builtin_bit_cast(bf16x2, q), acc, false);
}
template <int CTRL> __device__ __forceinline__ float dpp(float x) {
  return __builtin_bit_cast(float, __builtin_amdgcn_mov_dpp(__builtin_bit_cast(int, x), CTRL, 0xf, 0xf, true));
}
__device__ __forceinline__ float sum16(float x) {
  x += dpp<0x128>(x);
  x += dpp<0x124>(x);
  x += dpp<0x4E>(x);
  x += dpp<0xB1>(x);
  return x;
}
__device__ __forceinline__ float xrow_max(float x) {
  auto s = __builtin_amdgcn_permlane16_swap(__float_as_uint(x), __float_as_uint(x), false, false);
  x = fmaxf(__uint_as_float(s[0]), __uint_as_float(s[1]));
  auto t = __builtin_amdgcn_permlane32_swap(__float_as_uint(x), __float_as_uint(x), false, false);
  return fmaxf(__uint_as_float(t[0]), __uint_as_float(t[1]));
}
__device__ __forceinline__ float xrow_sum(float x) {
  auto s = __builtin_amdgcn_permlane16_swap(__float_as_uint(x), __float_as_uint(x), false, false);
  x = __uint_as_float(s[0]) + __uint_as_float(s[1]);
  auto t = __builtin_amdgcn_permlane32_swap(__float_as_uint(x), __float_as_uint(x), false, false);
  return __uint_as_float(t[0]) + __uint_as_float(t[1]);
}
__device__ __forceinline__ float fexp2(float x) { return __builtin_amdgcn_exp2f(x); }
__device__ __forceinline__ __amdgpu_buffer_rsrc_t mkrs(const void* p, unsigned bytes) {
  return __builtin_amdgcn_make_buffer_rsrc((void*)p, 0, (int)bytes, 0x00020000);
}


constexpr unsigned SPIN_CAP = 1u << 21;
__device__ __forceinline__ u64 ld_u64(const void* p) {
  return __hip_atomic_load((gu64*)p, __ATOMIC_RELAXED, __HIP_MEMORY_SCOPE_AGENT);
}
__device__ __forceinline__ void st_u64(void* p, u64 v) {
  __hip_atomic_store((gu64*)p, v, __ATOMIC_RELAXED, __HIP_MEMORY_SCOPE_AGENT);
}
__device__ __forceinline__ u64 next_tag(u64 e) { return e + 0x0000000100000001ull; }
__device__ __forceinline__ u64 uni64(u64 v) {
  const unsigned lo = __builtin_amdgcn_readfirstlane((unsigned)v), hi = __builtin_amdgcn_readfirstlane((unsigned)(v >> 32));
  return ((u64)hi << 32) | lo;
}
__device__ __forceinline__ void signal_flag(char* flag, u64 tag) {
  asm volatile("s_waitcnt vmcnt(0)" ::: "memory");
  __syncthreads();
  if (threadIdx.x == 0) st_u64(flag, tag);
}
template <int CTRL> __device__ __forceinline__ int dppi(int x) { return __builtin_amdgcn_mov_dpp(x, CTRL, 0xf, 0xf, true); }
__device__ __forceinline__ int sum8i(int x) { x += dppi<0xB1>(x); x += dppi<0x4E>(x); x += dppi<0x141>(x); return x; }
__device__ __forceinline__ unsigned row_sums(const u32x4 (&p)[4]) {
  unsigned c4[4];
#pragma unroll
  for (int i = 0; i < 4; i++) c4[i] = (p[i][0] + p[i][1]) + (p[i][2] + p[i][3]);
  auto s0 = __builtin_amdgcn_permlane32_swap(c4[0], c4[2], false, false);
  auto s1 = __builtin_amdgcn_permlane32_swap(c4[1], c4[3], false, false);
  auto s2 = __builtin_amdgcn_permlane16_swap(s0[0] + s0[1], s1[0] + s1[1], false, false);
  int x = (int)(s2[0] + s2[1]);
  x += dppi<0x128>(x);
  return (unsigned)sum8i(x);
}
__device__ __forceinline__ unsigned clk() { return (unsigned)__builtin_amdgcn_s_memrealtime(); }
__device__ __forceinline__ void wait_until(unsigned t) {
#pragma unroll 1
  for (int spins = 0; spins < 4096 && (int)(t - clk()) > 0; ++spins) __builtin_amdgcn_s_sleep(1);
}
__device__ __forceinline__ unsigned ref_time(unsigned v) {
  const unsigned t0 = __builtin_amdgcn_readfirstlane(v);
  return t0 + (unsigned)__builtin_amdgcn_readfirstlane(sum8i((int)(v - t0)) >> 3);
}
__device__ __forceinline__ unsigned wait_gate(const char* flag, unsigned ta, unsigned* slot, unsigned tent) {
  if (threadIdx.x < 64) {
    u64 v = 0;
    wait_until(tent + TM_FC);
#pragma unroll 1
    for (unsigned spins = 0; spins < SPIN_CAP; ++spins) {
      v = ld_u64(flag + (threadIdx.x & 7) * LINE);
      if (__all((unsigned)(v >> 32) == ta)) break;
      __builtin_amdgcn_s_sleep(12);
    }
    const unsigned tr = ref_time((unsigned)v);
    if (threadIdx.x == 0) *slot = tr;
  }
  __syncthreads();
  return __builtin_amdgcn_readfirstlane(*slot);
}
__device__ __forceinline__ void st16u_sc1(__amdgpu_buffer_rsrc_t rs, unsigned off, u32x4 v) {
  __builtin_amdgcn_raw_buffer_store_b128(v, rs, off, 0, SC1);
}
__device__ __forceinline__ u32x4 ld16u_sc1(__amdgpu_buffer_rsrc_t rs, unsigned off) {
  return __builtin_amdgcn_raw_buffer_load_b128(rs, off, 0, SC1);
}


__device__ __forceinline__ void item_dots(const u32x4 (&ks)[4], const u32x4 q, float (&s_)[4]) {
#pragma unroll
  for (int u = 0; u < 4; u++) {
    float a = dot2(ks[u][0], q[0], 0.f);
    a = dot2(ks[u][1], q[1], a);
    a = dot2(ks[u][2], q[2], a);
    a = dot2(ks[u][3], q[3], a);
    s_[u] = a;
    asm volatile("" : "+v"(s_[u]));
  }
}
__device__ __forceinline__ void item_unpack(const u32x4 (&vs)[4], f32x2 (&vf)[4][4]) {
#pragma unroll
  for (int u = 0; u < 4; u++)
#pragma unroll
    for (int j = 0; j < 4; j++) { vf[u][j] = f32x2{bf_lo(vs[u][j]), bf_hi(vs[u][j])}; asm volatile("" : "+v"(vf[u][j])); }
}
__device__ __forceinline__ f32x2 rows_sum(const float (&o)[8]) {
  float r4[4], r2[2];
#pragma unroll
  for (int j = 0; j < 4; j++) {
    auto sx = __builtin_amdgcn_permlane32_swap(__float_as_uint(o[j]), __float_as_uint(o[j + 4]), false, false);
    r4[j] = __uint_as_float(sx[0]) + __uint_as_float(sx[1]);
  }
#pragma unroll
  for (int j = 0; j < 2; j++) {
    auto sx = __builtin_amdgcn_permlane16_swap(__float_as_uint(r4[j]), __float_as_uint(r4[j + 2]), false, false);
    r2[j] = __uint_as_float(sx[0]) + __uint_as_float(sx[1]);
  }
  return f32x2{r2[0], r2[1]};
}
__device__ __forceinline__ void role_B(int h, int j6, int xcd, const __hip_bfloat16* __restrict__ ckv, const __hip_bfloat16* __restrict__ xB,
                                       const __hip_bfloat16* __restrict__ kc, const __hip_bfloat16* __restrict__ vc,
                                       const __hip_bfloat16* __restrict__ wqB, const __hip_bfloat16* __restrict__ woB,
                                       char* __restrict__ ws, __amdgpu_buffer_rsrc_t rs,
                                       __hip_bfloat16* __restrict__ out, char* lds) {
  const int t = threadIdx.x, l = t & 63;
  const int w = __builtin_amdgcn_readfirstlane(t >> 6);
  const int g = l >> 4, c = l & 15;
  const int pr = j6 >= 3 ? 1 : 0, k = j6 - 3 * pr;
  const unsigned tent = clk();
  const int b0 = 2 * pr;
  const int e0 = k == 1 ? (h & 1) : (k >> 1);
  u32x4 xr[2][2], wr[16];
  {
    const __amdgpu_buffer_rsrc_t rx = mkrs(xB, NB * DM * 2);
    const int rq_ = (0x203120 >> (4 * j6)) & 3;
    const int cw = w ^ ((rq_ & 2) << 1), r0 = rq_ & 1;
    const unsigned xo = (unsigned)((b0 * DM + cw * 64 + g * 16) * 2);
#pragma unroll
    for (int bb = 0; bb < 2; bb++)
#pragma unroll
      for (int i = 0; i < 2; i++) xr[bb][i] = __builtin_amdgcn_raw_buffer_load_b128(rx, xo + bb * DM * 2, (i ^ r0) * 16, 0);
    const __amdgpu_buffer_rsrc_t rq = mkrs(wqB, DM * HD * 2);
    const unsigned vo = (unsigned)(((cw * 64 + g * 16) * HD + h * DH + c * 8) * 2);
    const unsigned vo0 = vo + r0 * 8 * HD * 2, vo1 = vo + (1 - r0) * 8 * HD * 2;
#pragma unroll
    for (int i = 0; i < 16; i++) wr[i] = __builtin_amdgcn_raw_buffer_load_b128(rq, i < 8 ? vo0 : vo1, (i & 7) * HD * 2, 0);
  }
  constexpr unsigned KVB = (unsigned)((size_t)NB * NH * SK * DH * 2);
  const unsigned lo = (unsigned)(g * 256 + c * 16);
  const unsigned hb0 = ((unsigned)((b0 + e0) * NH + h) << 20) + (k == 1 ? (unsigned)SS << 12 : 0u);
  const unsigned hb1 = ((unsigned)((b0 + 1 - e0) * NH + h) << 20) + ((unsigned)SS << 12);
  auto seg1 = [&](int x) { return k == 1 && x >= N0; };
  auto unit = [&](int x) { const int y = seg1(x) ? x - N0 : x; return 4 * (y >> 1) + (y & 1); };
  auto soff = [&](int x) { return (seg1(x) ? hb1 : hb0) + ((unsigned)unit(x) << 12); };
  auto sco = [&](int x) { return (unsigned)((unit(x) + (seg1(x) ? 88 : 0)) * 64); };
  auto whole = [&](int x) { return x != NI - 1 && !(k == 1 && x == N0 - 1); };
  auto ld4 = [&](u32x4 (&d)[4], const void* p, bool ok, unsigned so) {
    const __amdgpu_buffer_rsrc_t r = mkrs(p, ok ? KVB : 0u);
#pragma unroll
    for (int u = 0; u < 4; u++) d[u] = __builtin_amdgcn_raw_buffer_load_b128(r, lo + u * 1024, so, NT);
  };
  unsigned pf;
  {
    const int n = ((h >> 3) * 6 + j6) * 8 + w, u = n >> 4, a = xcd * 4 + u / 3;
    pf = __builtin_amdgcn_raw_buffer_load_b32(mkrs(ckv, h == 31 ? 0 : NB * SK * DC * 2), (unsigned)l * 64, (unsigned)(a * 8 + ((5 + u % 3 + a) & 7)) * 65536u + (n & 15) * 4096u, 0);
  }
  u32x4 ra[4], rb[4], ea[4], eb[4];
  ld4(ra, kc, true, soff(w));
  ld4(rb, kc, true, soff(w) + 8192);
  u64* eps = (u64*)(lds + 33808);
  u64 epv = 0;
  if (t < 3) epv = ld_u64(ws + (t == 0 ? WS_EPA : WS_EPB + 256 * (b0 + (t == 1 ? e0 : 1 - e0))));
  float* red = (float*)lds;
  unsigned* qs = (unsigned*)(lds + 32768);
  unsigned* cnt = (unsigned*)(lds + 33856);
  float* mxd = (float*)(lds + 33872);
  float* lw = (float*)(lds + 33920);
  float* rec0 = (float*)(lds + 36864);
  char* prv = lds + 45056 + w * 1024;
  constexpr unsigned L_SC = 53248;
  float* recd = (float*)(lds + 65536);
  const unsigned lb = (unsigned)(uintptr_t)(__attribute__((address_space(3))) char*)lds;
  if (t == 0) { cnt[0] = 16u; cnt[1] = 8u * (RD + 1); cnt[2] = 0u; cnt[3] = 0u; cnt[6] = tent - 0x1000000u; mxd[0] = -INFINITY; mxd[1] = -INFINITY; }
  if (t < 64 * (NP4 - NP)) *(f32x2*)(recd + (NP + (t >> 6)) * RST + 2 * l) = f32x2{0.f, 0.f};
  if (t >= 384) *(float*)(lds + L_SC + ((t & 64) ? 172 : 84) * 64 + l * 4) = -INFINITY;
  {
    float a0[8], a1[8];
#pragma unroll
    for (int j = 0; j < 8; j++) { a0[j] = 0.f; a1[j] = 0.f; }
#pragma unroll
    for (int i = 0; i < 16; i += 2) {
      const unsigned x0 = xr[0][i >> 3][(i >> 1) & 3], x1 = xr[1][i >> 3][(i >> 1) & 3];
#pragma unroll
      for (int j = 0; j < 4; j++) {
        const unsigned pl = __builtin_amdgcn_perm(wr[i + 1][j], wr[i][j], 0x05040100u);
        const unsigned ph = __builtin_amdgcn_perm(wr[i + 1][j], wr[i][j], 0x07060302u);
        a0[2 * j] = dot2(pl, x0, a0[2 * j]); a0[2 * j + 1] = dot2(ph, x0, a0[2 * j + 1]);
        a1[2 * j] = dot2(pl, x1, a1[2 * j]); a1[2 * j + 1] = dot2(ph, x1, a1[2 * j + 1]);
      }
    }
#pragma unroll
    for (int j = 0; j < 8; j++) asm volatile("" : "+v"(a0[j]), "+v"(a1[j]));
    __builtin_amdgcn_sched_barrier(0);
    ld4(ea, kc, true, soff(w + 8));
    ld4(eb, kc, true, soff(w + 8) + 8192);
    __builtin_amdgcn_sched_barrier(0);
    float* rp = red + (w * 4 + g) * 256 + c * 8;
    *(f32x4*)(rp) = f32x4{a0[0], a0[1], a0[2], a0[3]};
    *(f32x4*)(rp + 4) = f32x4{a0[4], a0[5], a0[6], a0[7]};
    *(f32x4*)(rp + 128) = f32x4{a1[0], a1[1], a1[2], a1[3]};
    *(f32x4*)(rp + 132) = f32x4{a1[4], a1[5], a1[6], a1[7]};
  }
  __syncthreads();
  {
    const int o = t >> 1, hf = t & 1;
    float s = 0.f;
#pragma unroll
    for (int i = 0; i < 16; i++) s += red[(hf * 16 + i) * 256 + o];
    s += dpp<0xB1>(s);
    const float sn = dpp<0x4E>(s);
    if ((t & 3) == 0) qs[o >> 1] = cvtpk(s, sn);
    if (t < 3) eps[t] = epv;
  }
  __syncthreads();
  u32x4 q = *(const u32x4*)(qs + e0 * 64 + c * 4);
  const u32x4 q1 = *(const u32x4*)(qs + (1 - e0) * 64 + c * 4);
  auto lds_cnt = [&](int i) { return (unsigned)__builtin_amdgcn_readfirstlane(__hip_atomic_load(cnt + i, __ATOMIC_RELAXED, __HIP_MEMORY_SCOPE_WORKGROUP)); };
  auto lds_inc = [&](int i) {
    asm volatile("s_waitcnt lgkmcnt(0)" ::: "memory");
    if (l == 0) asm volatile("ds_add_u32 %0, %1" :: "v"(lb + 33856u + i * 4), "v"(1u) : "memory");
  };
  u32x4 wo[16];
  __builtin_amdgcn_sched_barrier(0);
#pragma unroll
  for (int i = 0; i < 16; i++) asm volatile("" : "=v"(wo[i]));
  const int nseg = k == 1 ? 2 : 1, sp = k == 1 ? 1 : 0;
  auto store_off = [&](int sg) {
    const int b = b0 + (sg ? 1 - e0 : e0);
    const unsigned og_ = (unsigned)(WS_PBG + ((size_t)(b * 64 + h * 2 + sp) * DM + w * 64 + c * 4 + g) * 8);
    return (g & 1) == 0 ? og_ : 0x7ffffff0u;
  };
  const unsigned apo = (unsigned)((c & 1) * 272 + g * 64);
  auto project = [&](const char* ap, float M, float L) {
    u32x4 a[4];
#pragma unroll
    for (int s = 0; s < 4; s++) a[s] = *(const u32x4*)(ap + s * 16);
    __builtin_amdgcn_sched_barrier(0);
    L += dpp<0xB1>(L); L += dpp<0x4E>(L); L += dpp<0x141>(L);
    __builtin_amdgcn_sched_barrier(0);
    f32x4 d[4];
#pragma unroll
    for (int s = 0; s < 4; s++)
#pragma unroll
      for (int j = 0; j < 4; j++)
        d[j] = __builtin_amdgcn_mfma_f32_16x16x32_bf16(__builtin_bit_cast(bf16x8, a[s]), __builtin_bit_cast(bf16x8, wo[4 * s + j]),
                                                       s ? d[j] : f32x4{0.f, 0.f, 0.f, 0.f}, 0, 0, 0);
    return f32x3{g < 2 ? d[0][0] + d[0][1] : d[3][0] + d[3][1], g < 2 ? d[1][0] + d[1][1] : M, g < 2 ? d[2][0] + d[2][1] : L};
  };
  f32x3 seg0 = {0.f, 0.f, 0.f};
  auto load_wo = [&]() {
    const __amdgpu_buffer_rsrc_t ro = mkrs(woB, HD * DM * 2);
    const unsigned vo = (unsigned)(((h * DH + g * 32) * DM + w * 64 + c * 4) * 2);
#pragma unroll
    for (int i = 0; i < 16; i++) {
      const u32x2 u0 = __builtin_amdgcn_raw_buffer_load_b64(ro, vo + 2 * i * DM * 2, 0, 0);
      const u32x2 u1 = __builtin_amdgcn_raw_buffer_load_b64(ro, vo + (2 * i + 1) * DM * 2, 0, 0);
      wo[i] = u32x4{u0[0], u0[1], u1[0], u1[1]};
    }
  };
  asm volatile("" :: "v"(pf));
  unsigned tp = *(const unsigned*)eps + 1u;
  asm volatile("" : "+v"(tp));
  {
    float m = -INFINITY, s_[4];
    const unsigned swo = lb + L_SC + g * 16 + (c & 3) * 4;
    auto scores = [&](unsigned so, bool ok) {
      const bool b0_ = c & 1, b1_ = c & 2;
      const float r0 = (b0_ ? s_[1] : s_[0]) + dpp<0xB1>(b0_ ? s_[0] : s_[1]);
      const float r1 = (b0_ ? s_[3] : s_[2]) + dpp<0xB1>(b0_ ? s_[2] : s_[3]);
      float r = (b1_ ? r1 : r0) + dpp<0x4E>(b1_ ? r0 : r1);
      r += dpp<0x124>(r);
      r += dpp<0x128>(r);
      r = ok ? rbf(rbf(r) * SCALE) : -INFINITY;
      m = fmaxf(m, r);
      asm volatile("ds_write_b32 %0, %1" :: "v"(swo + so), "v"(r) : "memory");
    };
    auto flush_max = [&](int sg) {
      float mw = fmaxf(m, dpp<0xB1>(m));
      mw = xrow_max(fmaxf(mw, dpp<0x4E>(mw)));
      if (l == 0) asm volatile("ds_max_f32 %0, %1" :: "v"(lb + 33872u + sg * 4), "v"(mw) : "memory");
      m = -INFINITY;
    };
    bool sw = k != 1, first = true, wl = false;
    u32x2 krep = {0u, ~tp};
    const int xw = 16 + 16 * (h >> 3);
    int x = w;
#pragma unroll 1
    while (x < NI) {
      if (!sw && x >= N0) { flush_max(0); q = q1; sw = true; }
      if (!wl && x >= xw) { load_wo(); wl = true; }
      if (x == 76) krep = __builtin_amdgcn_raw_buffer_load_b64(rs, (unsigned)WS_FK + (l & 7) * LINE, 0, SC1);
      unsigned dr; u64 sv;
      asm volatile("s_mov_b64 %1, exec\n\ts_mov_b64 exec, 1\n\tds_add_rtn_u32 %0, %2, %3\n\ts_mov_b64 exec, %1"
                   : "=&v"(dr), "=&s"(sv) : "v"(lb + 33856u), "v"(1u), "v"(ra[0][0]) : "memory");
      __builtin_amdgcn_sched_barrier(0);
      item_dots(ra, q, s_);
      asm volatile("s_waitcnt lgkmcnt(0)" : "+v"(dr) :: "memory");
      const int xn = __builtin_amdgcn_readfirstlane(dr);
      const bool more = xn < NI;
      const void* pn = more ? (const void*)kc : (const void*)vc;
      const unsigned sn = soff(more ? xn : w);
      __builtin_amdgcn_sched_barrier(0);
      ld4(ra, pn, true, sn);
      __builtin_amdgcn_sched_barrier(0);
      scores(sco(x), true);
      item_dots(rb, q, s_);
      __builtin_amdgcn_sched_barrier(0);
      ld4(rb, pn, !more || whole(xn), sn + 8192);
      __builtin_amdgcn_sched_barrier(0);
      scores(sco(x) + 128, whole(x));
      if (xn == 48 && h >= 8 && h < 16 && j6 == 2) { if (l == 0) st_u64(ws + WS_FK + (h - 8) * LINE, ((u64)tp << 32) | clk()); }
      if (first) {
        item_dots(ea, q, s_); scores(sco(w + 8), true);
        item_dots(eb, q, s_); scores(sco(w + 8) + 128, true);
        first = false;
      }
      x = xn;
    }
    if (!wl) {
      load_wo();
#pragma unroll
      for (int i = 0; i < 16; i++) asm volatile("" : "+v"(wo[i]));
    }
    ld4(ea, vc, true, soff(w + 8));
    ld4(eb, vc, true, soff(w + 8) + 8192);
    if (__all(krep[1] == tp)) {
      const unsigned tr = ref_time(krep[0]);
      if (l == 0) cnt[6] = tr + 1060;
    }
    flush_max(k == 1 && !sw ? 0 : 1);
    lds_inc(2);
  }
#pragma unroll
  for (int s = 0; s < 4; s++) {
    u32x4 o[4];
#pragma unroll
    for (int e = 0; e < 4; e++)
#pragma unroll
      for (int j = 0; j < 4; j++) o[j][e] = __builtin_amdgcn_perm(wo[4 * s + e][2 + (j >> 1)], wo[4 * s + e][j >> 1], (j & 1) ? 0x07060302u : 0x05040100u);
#pragma unroll
    for (int j = 0; j < 4; j++) wo[4 * s + j] = o[j];
    asm volatile("" : "+v"(wo[4 * s]), "+v"(wo[4 * s + 1]), "+v"(wo[4 * s + 2]), "+v"(wo[4 * s + 3]));
  }
  for (unsigned spins = 0; lds_cnt(2) != 8u && spins < SPIN_CAP; ++spins) __builtin_amdgcn_s_sleep(1);
  asm volatile("" ::: "memory");
  const u64 tg0 = next_tag(uni64(eps[1])), tg1 = next_tag(uni64(eps[2]));
  if (h >= 8 && h < 16 && j6 == 2 && t == 0) st_u64(ws + WS_FS + (h - 8) * LINE, ((u64)tp << 32) | clk());
  u32x2 srep = {0u, 0u};
  const float M0 = __builtin_bit_cast(float, __builtin_amdgcn_readfirstlane(__builtin_bit_cast(int, mxd[k == 1 ? 0 : 1])));
  const float M1 = __builtin_bit_cast(float, __builtin_amdgcn_readfirstlane(__builtin_bit_cast(int, mxd[1])));
  auto weights = [&](unsigned so, float M) {
    const f32x4 sc = *(const f32x4*)(lds + L_SC + so);
    return f32x4{fexp2((sc[0] - M) * LOG2E), fexp2((sc[1] - M) * LOG2E), fexp2((sc[2] - M) * LOG2E), fexp2((sc[3] - M) * LOG2E)};
  };
  float acc[8];
#pragma unroll
  for (int j = 0; j < 8; j++) acc[j] = 0.f;
  f32x2 vf[4][4];
  f32x4 pa, pb;
  unsigned sb = 0;
  auto get_p = [&](int x) {
    const float M = seg1(x) ? M1 : M0;
    pa = weights(sco(x) + g * 16, M); pb = weights(sco(x) + 128 + g * 16, M);
    asm volatile("" : "+v"(pa), "+v"(pb));
    sb = lds_cnt(6);
  };
  auto fma_v = [&](const f32x4 p) {
#pragma unroll
    for (int u = 0; u < 4; u++)
#pragma unroll
      for (int j = 0; j < 4; j++) {
        acc[2 * j]     = fmaf(p[u], vf[u][j][0], acc[2 * j]);
        acc[2 * j + 1] = fmaf(p[u], vf[u][j][1], acc[2 * j + 1]);
      }
  };
  auto step = [&](int xn) {
    const unsigned sn = soff(xn);
    wait_until(sb + (unsigned)((xn * 338 >> 4) + -355 + (xn >= 72 ? 30 : 0)));
    __builtin_amdgcn_sched_barrier(0);
    ld4(ra, vc, true, sn);
    __builtin_amdgcn_sched_barrier(0);
    fma_v(pa);
#pragma unroll
    for (int j = 0; j < 8; j++) asm volatile("" : "+v"(acc[j]));
    item_unpack(rb, vf);
    __builtin_amdgcn_sched_barrier(0);
    ld4(rb, vc, whole(xn), sn + 8192);
    __builtin_amdgcn_sched_barrier(0);
    fma_v(pb);
  };
  auto put_acc = [&](float* rec_) {
    *(f32x2*)(rec_ + c * 8 + g * 2) = rows_sum(acc);
#pragma unroll
    for (int j = 0; j < 8; j++) acc[j] = 0.f;
  };
  if (w >= 4) __builtin_amdgcn_s_setprio(1);
  get_p(w);
  item_unpack(ra, vf);
  step(w + 16);
  get_p(w + 8);
  item_unpack(ea, vf); fma_v(pa);
  item_unpack(eb, vf); fma_v(pb);
  {
    const bool two = t < 192;
    const f32x4 p0 = weights(t * 16, t < 352 ? M0 : M1), p1 = weights(two ? (t + 512) * 16 : 172 * 64, two ? M1 : 0.f);
    const float s0 = (p0[0] + p0[1]) + (p0[2] + p0[3]), s1 = (p1[0] + p1[1]) + (p1[2] + p1[3]);
    const bool f0 = k == 1 && t < 352;
    const float l0 = xrow_sum(sum16(f0 ? s0 : 0.f)), l1 = xrow_sum(sum16((f0 ? 0.f : s0) + (two ? s1 : 0.f)));
    if (l < 2) lw[l * 8 + w] = l ? l1 : l0;
  }
  bool sw = k != 1;
  int x = w + 16;
#pragma unroll 1
  for (; x < 8 * RD; x += 8) {
    const int rd = x >> 3;
    if (rd == PRIOF) __builtin_amdgcn_s_setprio(0);
    if (!sw && x >= N0) { put_acc(rec0 + w * 128); lds_inc(3); sw = true; }
    if (w == 3 && rd == 4 + 1 && __all(srep[1] == tp)) {
      const unsigned tr = ref_time(srep[0]);
      if (l == 0) cnt[6] = tr;
    }
    if (w == 3 && rd == 4) srep = __builtin_amdgcn_raw_buffer_load_b64(rs, (unsigned)WS_FS + (l & 7) * LINE, 0, SC1);
    if (rd == RE && k == 1) {
      for (unsigned spins = 0; lds_cnt(3) != 8u && spins < SPIN_CAP; ++spins) __builtin_amdgcn_s_sleep(1);
      asm volatile("" ::: "memory");
      f32x2 a2 = {0.f, 0.f};
#pragma unroll
      for (int i = 0; i < 8; i++) a2 += *(const f32x2*)(rec0 + i * 128 + 2 * l);
      const unsigned hi = cvtpk(a2[0], a2[1]);
      *(unsigned*)(prv + l * 4) = hi;
      *(unsigned*)(prv + 272 + l * 4) = cvtpk(a2[0] - bf_lo(hi), a2[1] - bf_hi(hi));
      seg0 = project(prv + apo, M0, lw[l & 7]);
    }
    get_p(x);
    __builtin_amdgcn_sched_barrier(0);
    item_unpack(ra, vf);
    step(x + 8);
  }
  put_acc(recd + w * RST);
  unsigned o_rv = 65536u + (g * RST + w * 16 + c) * 4, o_lw = 33920u + 32u + (l & 7) * 4;
  unsigned o_oi = (g < 2 && (c & 1) == 0) ? 41216u + g * 272 + (w * 16 + c) * 2 : 45056u + w * 1024 + 256 + l * 4;
  unsigned o_ap = 41216u + apo, o_st = store_off(nseg - 1);
  unsigned o_s0 = k == 1 ? store_off(0) : 0x7ffffff0u;
  unsigned tl0 = (unsigned)(k == 1 ? tg1 : tg0);
  u32x4 sg0 = {__float_as_uint(seg0[0]), __float_as_uint(seg0[1]), __float_as_uint(seg0[2]), (unsigned)tg0};
  asm volatile("" : "+v"(o_s0), "+v"(tl0), "+v"(sg0));
  asm volatile("" : "+v"(o_rv), "+v"(o_lw), "+v"(o_oi), "+v"(o_ap), "+v"(o_st));
  {
    {
#pragma unroll 1
      for (;;) {
        get_p(x);
        unsigned dr; u64 sv;
        asm volatile("s_mov_b64 %1, exec\n\ts_mov_b64 exec, 1\n\tds_add_rtn_u32 %0, %2, %3\n\ts_mov_b64 exec, %1"
                     : "=&v"(dr), "=&s"(sv) : "v"(lb + 33860u), "v"(1u), "v"(ra[0][0]) : "memory");
        __builtin_amdgcn_sched_barrier(0);
        item_unpack(ra, vf);
        asm volatile("s_waitcnt lgkmcnt(0)" : "+v"(dr) :: "memory");
        const int xn = __builtin_amdgcn_readfirstlane(dr);
        if (xn >= NI) break;
        step(xn);
        put_acc(recd + (x - 8 * RD + 8) * RST);
        x = xn;
      }
      fma_v(pa);
      item_unpack(rb, vf);
      fma_v(pb);
      put_acc(recd + (x - 8 * RD + 8) * RST);
      asm volatile("s_waitcnt lgkmcnt(0)" ::: "memory");
      __syncthreads();
    }
    {
      float rv_[NP4 / 4];
#pragma unroll
      for (int r = 0; r < NP4 / 4; r++) rv_[r] = *(const float*)(lds + o_rv + r * 4 * RST * 4);
      const float li = *(const float*)(lds + o_lw);
      float a = 0.f, a1 = 0.f;
#pragma unroll
      for (int r = 0; r < NP4 / 4; r++) { if (r & 1) a1 += rv_[r]; else a += rv_[r]; }
      a += a1;
      a = xrow_sum(a);
      const float an = dpp<0xB1>(a);
      const unsigned hi = cvtpk(a, an);
      const unsigned lo2 = cvtpk(a - bf_lo(hi), an - bf_hi(hi));
      *(unsigned*)(lds + o_oi) = g ? lo2 : hi;
      __syncthreads();
      const f32x3 pv = project(lds + o_ap, M1, li);
      st16u_sc1(rs, o_st, u32x4{__float_as_uint(pv[0]), __float_as_uint(pv[1]), __float_as_uint(pv[2]), tl0});
      st16u_sc1(rs, o_s0, sg0);
    }
    if (w < nseg) {
      const int sg = w;
      const int b = b0 + (sg ? 1 - e0 : e0);
      const int j64 = (h & 7) * 8 + (h >> 3) * 2 + sp;
      const u64 tg = sg ? tg1 : tg0;
      const unsigned ta = (unsigned)tg;
      const unsigned og = (unsigned)(WS_PBG + ((size_t)(b * 64 + (l >> 2)) * DM + j64 * 8) * 8 + (l & 3) * 16);
      u32x4 p[4];
      {
        __builtin_amdgcn_s_sleep(4);
        wait_until(sb + TM_SW);
      }
#pragma unroll 1
      for (unsigned spins = 0; spins < SPIN_CAP; ++spins) {
        asm volatile("" ::: "memory");
#pragma unroll
        for (int i = 0; i < 4; i++) p[i] = ld16u_sc1(rs, og + i * 16 * DM * 8);
        if (__all(p[0][3] == ta && p[1][3] == ta && p[2][3] == ta && p[3][3] == ta)) break;
      }
      float v0 = 0.f, v1 = 0.f, v2 = 0.f;
#pragma unroll
      for (int i = 0; i < 4; i++) {
        const float mm = dpp<0xF5>(__uint_as_float(p[i][1]));
        const float Mh = fmaxf(mm, dpp<0x141>(mm));
        const float fq = fexp2((mm - Mh) * LOG2E);
        float L = fq * dpp<0xF5>(__uint_as_float(p[i][2]));
        L += dpp<0x141>(L);
        const float wgt = fq * __builtin_amdgcn_rcpf(L);
        v0 = fmaf(__uint_as_float(p[i][0]), wgt, v0);
        v1 = fmaf(__uint_as_float(p[i][1]), wgt, v1);
        v2 = fmaf(__uint_as_float(p[i][2]), wgt, v2);
      }
      const float u2 = dpp<0xA0>(v2);
      v1 = (l & 1) ? v0 : v1;
      v0 = (l & 1) ? u2 : v0;
      v0 += dpp<0x128>(v0); v0 += dpp<0x124>(v0); v0 = xrow_sum(v0);
      v1 += dpp<0x128>(v1); v1 += dpp<0x124>(v1); v1 = xrow_sum(v1);
      if (l < 4) {
        *(unsigned*)(out + b * 1024 + 512 + j64 * 8 + l * 2) = cvtpk(v0, v1);
        if (j64 == 0 && l == 0) st_u64(ws + WS_EPB + 256 * b, tg);
      }
    }
  }
}

__device__ __forceinline__ unsigned lds_off(int row, int ch) {
  return 256 * row + 16 * (ch ^ (((row & 3) << 2) | ((row >> 2) & 3)));
}

__device__ __forceinline__ void role_A1(int h, const __hip_bfloat16* __restrict__ xA, const __hip_bfloat16* __restrict__ wqA,
                                        const __hip_bfloat16* __restrict__ wuk, const __hip_bfloat16* __restrict__ wuv,
                                        const __hip_bfloat16* __restrict__ woA, char* __restrict__ ws,
                                        __amdgpu_buffer_rsrc_t rs, __hip_bfloat16* __restrict__ out, char* lds) {
  const int t = threadIdx.x, l = t & 63;
  const int w = __builtin_amdgcn_readfirstlane(t >> 6);
  const int g = l >> 4, c = l & 15;
  const unsigned tent = clk();
  float* red = (float*)(lds + 16384);
  const unsigned vo = (unsigned)(((w * 64 + g * 16) * HD + h * DH + c * 8) * 2);
  u64 tag;
  {
    u32x4 xr[NB][2], wr[16], wk[16];
    const __amdgpu_buffer_rsrc_t rx = mkrs(xA, NB * DM * 2);
    const unsigned xo = (unsigned)((w * 64 + g * 16) * 2);
#pragma unroll
    for (int bb = 0; bb < NB; bb++)
#pragma unroll
      for (int i = 0; i < 2; i++) xr[bb][i] = __builtin_amdgcn_raw_buffer_load_b128(rx, xo + bb * DM * 2 + i * 16, 0, 0);
    const __amdgpu_buffer_rsrc_t rq = mkrs(wqA, DM * HD * 2);
#pragma unroll
    for (int i = 0; i < 16; i++) wr[i] = __builtin_amdgcn_raw_buffer_load_b128(rq, vo, i * HD * 2, NT);
    {
      const __amdgpu_buffer_rsrc_t rku = mkrs(wuk, DC * HD * 2);
#pragma unroll
      for (int i = 0; i < 16; i++) wk[i] = __builtin_amdgcn_raw_buffer_load_b128(rku, vo, i * HD * 2, NT);
    }
    __builtin_amdgcn_sched_barrier(0);
    u64 epv = 0;
    if (t == 0) epv = ld_u64(ws + WS_EPA);
    unsigned* qs = (unsigned*)lds;
    unsigned short* qo = (unsigned short*)(lds + 4096);
    float acc[NB][8];
#pragma unroll
    for (int bb = 0; bb < NB; bb++)
#pragma unroll
      for (int j = 0; j < 8; j++) acc[bb][j] = 0.f;
#pragma unroll
    for (int i = 0; i < 16; i += 2) {
#pragma unroll
      for (int j = 0; j < 4; j++) {
        const unsigned pl = __builtin_amdgcn_perm(wr[i + 1][j], wr[i][j], 0x05040100u);
        const unsigned ph = __builtin_amdgcn_perm(wr[i + 1][j], wr[i][j], 0x07060302u);
#pragma unroll
        for (int bb = 0; bb < NB; bb++) {
          const unsigned x = xr[bb][i >> 3][(i >> 1) & 3];
          acc[bb][2 * j] = dot2(pl, x, acc[bb][2 * j]);
          acc[bb][2 * j + 1] = dot2(ph, x, acc[bb][2 * j + 1]);
        }
      }
    }
    float* rp = red + (w * 4 + g) * 512 + c * 8;
#pragma unroll
    for (int bb = 0; bb < NB; bb++) {
      *(f32x4*)(rp + bb * 128) = f32x4{acc[bb][0], acc[bb][1], acc[bb][2], acc[bb][3]};
      *(f32x4*)(rp + bb * 128 + 4) = f32x4{acc[bb][4], acc[bb][5], acc[bb][6], acc[bb][7]};
    }
    __syncthreads();
    {
      float s = 0.f;
#pragma unroll
      for (int i = 0; i < 32; i++) s += red[i * 512 + t];
      const float sn = dpp<0xB1>(s);
      if ((t & 1) == 0) qs[t >> 1] = cvtpk(s, sn);
      if (t == 0) *(u64*)(lds + 2048) = epv;
    }
    __syncthreads();
    tag = next_tag(uni64(*(const u64*)(lds + 2048)));
    {
      u32x4 qq[NB];
#pragma unroll
      for (int bb = 0; bb < NB; bb++) qq[bb] = *(const u32x4*)(qs + bb * 64 + c * 4);
#pragma unroll
      for (int i = 0; i < 16; i++) {
        float s[NB];
#pragma unroll
        for (int bb = 0; bb < NB; bb++) {
          float v = dot2(wk[i][0], qq[bb][0], 0.f);
          v = dot2(wk[i][1], qq[bb][1], v);
          v = dot2(wk[i][2], qq[bb][2], v);
          v = dot2(wk[i][3], qq[bb][3], v);
          s[bb] = sum16(v);
        }
        const float v = (c & 2) ? ((c & 1) ? s[3] : s[2]) : ((c & 1) ? s[1] : s[0]);
        if (c < NB) qo[c * 512 + w * 64 + g * 16 + i] = (unsigned short)(cvtpk(v, v) & 0xffff);
      }
    }
    __syncthreads();
    if (t < 256) {
      const int bb = t >> 6, ch = t & 63;
      st16u_sc1(rs, (unsigned)(WS_QABS + ((size_t)(bb * NH + h) * DC + ch * 8) * 2), *(const u32x4*)(qo + bb * 512 + ch * 8));
    }
    signal_flag(ws + WS_F1 + h * LINE, tag);
  }
  const unsigned ta = (unsigned)tag, tb = (unsigned)(tag >> 32);
  {
    u32x4 bv[8][2], bo[8][2];
    u32x4 pre[2][8], wo[16];
    {
      const __amdgpu_buffer_rsrc_t ruv = mkrs(wuv, DC * HD * 2);
      const __amdgpu_buffer_rsrc_t rwo = mkrs(woA, HD * DM * 2);
      const unsigned wvo = (unsigned)(((h * DH + (l >> 3) * 16) * DM + w * 64 + (l & 7) * 8) * 2);
#pragma unroll
      for (int i = 0; i < 16; i++) pre[i >> 3][i & 7] = __builtin_amdgcn_raw_buffer_load_b128(ruv, vo - g * 8 * HD * 2, ((i >> 3) * 32 + (i & 7)) * HD * 2, NT);
#pragma unroll
      for (int i = 0; i < 16; i++) wo[i] = __builtin_amdgcn_raw_buffer_load_b128(rwo, wvo, i * DM * 2, NT);
    }
#pragma unroll
    for (int j = 0; j < 8; j++)
#pragma unroll
      for (int s = 0; s < 2; s++) {
#pragma unroll
        for (int e = 0; e < 4; e++)
          bv[j][s][e] = __builtin_amdgcn_perm(pre[s][2 * e + 1][j >> 1], pre[s][2 * e][j >> 1], (j & 1) ? 0x07060302u : 0x05040100u);
        asm volatile("" : "+v"(bv[j][s]));
      }
#pragma unroll
    for (int j = 0; j < 8; j++)
#pragma unroll
      for (int s = 0; s < 2; s++) {
#pragma unroll
        for (int e = 0; e < 4; e++)
          bo[j][s][e] = __builtin_amdgcn_perm(wo[8 * s + 2 * e + 1][j >> 1], wo[8 * s + 2 * e][j >> 1], (j & 1) ? 0x07060302u : 0x05040100u);
        asm volatile("" : "+v"(bo[j][s]));
      }
    constexpr int LAS = 1040;
    unsigned short* cs = (unsigned short*)(lds + 16384);
    float* outs = (float*)(lds + 20480);
    float* cred = (float*)(lds + 32768);
    {
    {
    }
    unsigned po = (unsigned)(WS_PAO + ((size_t)(w * 4 * NH + h) * DC + l * 8) * 2);
    unsigned pc = (unsigned)(WS_PAC + (size_t)((w * 4 + g) * NH + h) * 8);
    unsigned lw_ = (unsigned)(65536 + ((w & 1) * 16 + (w >> 1) * 4) * LAS + l * 16), lr_ = (unsigned)(65536 + c * LAS + (w * 64 + g * 8) * 2);
    asm volatile("" : "+v"(po), "+v"(pc), "+v"(lw_), "+v"(lr_));
    const unsigned tw = (h & 1) ? tb : ta;
    const unsigned tg = wait_gate(ws + WS_FS, ta, (unsigned*)(lds + 8192), tent);
    wait_until(tg + TM_PUT + TM_GET);
    {
      u32x4 pr[4];
#pragma unroll 1
      for (unsigned spins = 0; spins < SPIN_CAP; ++spins) {
        asm volatile("" ::: "memory");
        const u32x2 ck = __builtin_amdgcn_raw_buffer_load_b64(rs, pc, 0, SC1);
#pragma unroll
        for (int i = 0; i < 4; i++) pr[i] = __builtin_amdgcn_raw_buffer_load_b128(rs, po, i * NH * DC * 2, SC1);
#pragma unroll
        for (int i = 0; i < 4; i++) *(u32x4*)(lds + lw_ + i * LAS) = pr[i];
        if (__all(row_sums(pr) == ck[0] && ck[1] == tw)) break;
        __builtin_amdgcn_s_sleep(2);
      }
      __syncthreads();
      u32x4 af[2][2];
#pragma unroll
      for (int tl = 0; tl < 2; tl++)
#pragma unroll
        for (int s = 0; s < 2; s++) af[tl][s] = *(const u32x4*)(lds + lr_ + tl * 16 * LAS + s * 64);
      f32x4 dd[8];
#pragma unroll
      for (int j = 0; j < 8; j++)
        dd[j] = __builtin_amdgcn_mfma_f32_16x16x32_bf16(__builtin_bit_cast(bf16x8, af[0][0]), __builtin_bit_cast(bf16x8, bv[j][0]), f32x4{0.f, 0.f, 0.f, 0.f}, 0, 0, 0);
#pragma unroll
      for (int j = 0; j < 8; j++)
        dd[j] = __builtin_amdgcn_mfma_f32_16x16x32_bf16(__builtin_bit_cast(bf16x8, af[0][1]), __builtin_bit_cast(bf16x8, bv[j][1]), dd[j], 0, 0, 0);
#pragma unroll
      for (int j = 0; j < 8; j++)
        dd[j] = __builtin_amdgcn_mfma_f32_16x16x32_bf16(__builtin_bit_cast(bf16x8, af[1][0]), __builtin_bit_cast(bf16x8, bv[j][0]), dd[j], 0, 0, 0);
#pragma unroll
      for (int j = 0; j < 8; j++)
        dd[j] = __builtin_amdgcn_mfma_f32_16x16x32_bf16(__builtin_bit_cast(bf16x8, af[1][1]), __builtin_bit_cast(bf16x8, bv[j][1]), dd[j], 0, 0, 0);
      f32x4 v[2];
#pragma unroll
      for (int j = 0; j < 8; j++) v[j >> 2][j & 3] = (dd[j][0] + dd[j][1]) + (dd[j][2] + dd[j][3]);
      *(f32x4*)(cred + (w * 4 + g) * 128 + c * 8) = v[0];
      *(f32x4*)(cred + (w * 4 + g) * 128 + c * 8 + 4) = v[1];
    }
    __syncthreads();
    {
      float s = 0.f;
#pragma unroll
      for (int i = 0; i < 8; i++) s += cred[i * 512 + t];
      const float sn = dpp<0xB1>(s);
      if ((t & 1) == 0) *(unsigned*)(cs + t) = cvtpk(s, sn);
    }
    __syncthreads();
    {
      const char* ap = (const char*)cs + ((c >> 1) & 3) * 256 + (g * 32 + (c & 1) * 16) * 2;
      const u32x4 a0 = *(const u32x4*)(ap), a1 = *(const u32x4*)(ap + 16);
      f32x4 dd[8];
#pragma unroll
      for (int j = 0; j < 8; j++)
        dd[j] = __builtin_amdgcn_mfma_f32_16x16x32_bf16(__builtin_bit_cast(bf16x8, a0), __builtin_bit_cast(bf16x8, bo[j][0]), f32x4{0.f, 0.f, 0.f, 0.f}, 0, 0, 0);
#pragma unroll
      for (int j = 0; j < 8; j++)
        dd[j] = __builtin_amdgcn_mfma_f32_16x16x32_bf16(__builtin_bit_cast(bf16x8, a1), __builtin_bit_cast(bf16x8, bo[j][1]), dd[j], 0, 0, 0);
      f32x4 v4[2];
#pragma unroll
      for (int j = 0; j < 8; j++) {
        const float u0 = (l & 8) ? dd[j][1] : dd[j][0], u1 = (l & 8) ? dd[j][3] : dd[j][2];
        const float keep = (l & 8) ? u1 : u0, give = (l & 8) ? u0 : u1;
        v4[j >> 2][j & 3] = keep + dpp<0x128>(give);
      }
      if (g < 2) {
        float* op = outs + (g * 2 + ((l >> 3) & 1)) * 512 + w * 64 + (l & 7) * 8;
        *(f32x4*)op = v4[0];
        *(f32x4*)(op + 4) = v4[1];
      }
    }
    __syncthreads();
    {
      const f32x2 va = *(const f32x2*)(outs + t * 2), vb = *(const f32x2*)(outs + 1024 + t * 2);
      wait_until(tg + TM_PA);
      const unsigned oo = (unsigned)(WS_PA + ((size_t)((t >> 8) * NH + h) * DM + (t & 255) * 2) * 8);
      st16u_sc1(rs, oo, u32x4{__float_as_uint(va[0]), ta, __float_as_uint(va[1]), tb});
      st16u_sc1(rs, oo + 2 * NH * DM * 8, u32x4{__float_as_uint(vb[0]), ta, __float_as_uint(vb[1]), tb});
    }
  if (w < 4) {
    const int bb = h >> 3, n0 = (h & 7) * 64 + w * 16;
    const unsigned og = (unsigned)(WS_PA + ((size_t)(bb * NH + (l >> 3)) * DM + n0) * 8 + (l & 7) * 16);
    u32x4 p[4];
    wait_until(tg + TM_PA + TM_T4);
#pragma unroll 1
    for (unsigned spins = 0; spins < SPIN_CAP; ++spins) {
      asm volatile("" ::: "memory");
#pragma unroll
      for (int i = 0; i < 4; i++) p[i] = ld16u_sc1(rs, og + i * 8 * DM * 8);
      bool ok = true;
#pragma unroll
      for (int i = 0; i < 4; i++) ok = ok && p[i][1] == ta && p[i][3] == tb;
      if (__all(ok)) break;
      __builtin_amdgcn_s_sleep(4);
    }
    float v0 = (__uint_as_float(p[0][0]) + __uint_as_float(p[1][0])) + (__uint_as_float(p[2][0]) + __uint_as_float(p[3][0]));
    float v1 = (__uint_as_float(p[0][2]) + __uint_as_float(p[1][2])) + (__uint_as_float(p[2][2]) + __uint_as_float(p[3][2]));
    v0 += dpp<0x128>(v0); v0 = xrow_sum(v0);
    v1 += dpp<0x128>(v1); v1 = xrow_sum(v1);
    if (l < 8) {
      *(unsigned*)(out + bb * 1024 + n0 + l * 2) = cvtpk(v0, v1);
      if (h == 0 && t == 0) st_u64(ws + WS_EPA, tag);
    }
  }
    }
  }
}

__device__ __forceinline__ void role_A2(int a, const __hip_bfloat16* __restrict__ ckv, char* __restrict__ ws,
                                        __amdgpu_buffer_rsrc_t rs, char* lds) {
  const int t = threadIdx.x, l = t & 63;
  const int w = __builtin_amdgcn_readfirstlane(t >> 6);
  const int g = l >> 4, c = l & 15;
  const unsigned tent = clk();
  u64 ep = 0;
  if (t == 0) ep = ld_u64(ws + WS_EPA);
  const int grp = w >> 2, wq = w & 3;
  const int b = a / SPLIT_A, sp = a % SPLIT_A;
  const int r = l & 31, hh = l >> 5;
  constexpr unsigned CB = (unsigned)((size_t)NB * SK * DC * 2);
  constexpr int NTL = KA / 64;
  const unsigned cvo = (unsigned)((g * DC + 128 * wq + c * 8) * 2);
  const unsigned cso = (unsigned)(((size_t)b * SK + sp * KA + grp * 32) * DC * 2);
  auto load_tile = [&](u32x4 (&pr)[8], int tile) {
    const bool ok = tile < NTL;
    const __amdgpu_buffer_rsrc_t rc = mkrs(ckv, ok ? CB : 0u);
    const unsigned so = ok ? cso + (unsigned)((tile + sp) & (NTL - 1)) * 64 * DC * 2 : 0u;
#pragma unroll
    for (int u = 0; u < 8; u++) pr[u] = __builtin_amdgcn_raw_buffer_load_b128(rc, cvo, so + u * 4 * DC * 2, NT);
  };
  char* img = lds + w * 8192;
  float* sred = (float*)(lds + 65536 + grp * 16384);
  float* mxs = (float*)(lds + 98304 + grp * 512);
  unsigned* pbuf = (unsigned*)(lds + 102400 + grp * 2048);
  float* lall = (float*)(lds + 106496);
  const unsigned imga = (unsigned)(uintptr_t)(__attribute__((address_space(3))) char*)img;
  auto put_tile = [&](u32x4 (&pr)[8]) {
#pragma unroll
    for (int u = 0; u < 8; u++) *(u32x4*)(img + lds_off(4 * u + g, c)) = pr[u];
  };
  u32x4 pre[2][8];
  load_tile(pre[0], 0);
  load_tile(pre[1], 1);
  const u64 tag0 = next_tag(uni64(ep));
  put_tile(pre[0]);
  __builtin_amdgcn_sched_barrier(0);
  load_tile(pre[0], 2);
  unsigned pf[4];
#pragma unroll
  for (int i = 0; i < 4; i++)
    pf[i] = __builtin_amdgcn_raw_buffer_load_b32(mkrs(ckv, CB), (unsigned)t * 64, (unsigned)(a * 8 + ((3 + (i >> 1) + a) & 7)) * 65536u + (i & 1) * 32768u, 0);
  if (t < 64) {
    wait_until(tent + TM_F1);
#pragma unroll 1
    for (unsigned spins = 0; spins < SPIN_CAP; ++spins) {
      __builtin_amdgcn_s_sleep(32);
      if (__all(ld_u64(ws + WS_F1 + (l & 31) * LINE) == tag0)) break;
    }
  }
  if (t == 0) *(u64*)(lds + 110592) = tag0;
  asm volatile("" :: "v"(pf[0]), "v"(pf[1]), "v"(pf[2]), "v"(pf[3]));
  __syncthreads();
  const u64 tag = uni64(*(const u64*)(lds + 110592));
  const unsigned ta = (unsigned)tag, tb = (unsigned)(tag >> 32);
  bf16x8 qf[8];
  {
    char* qi = lds + 65536;
    const unsigned qo = (unsigned)(WS_QABS + (size_t)b * NH * DC * 2 + t * 16);
    u32x4 qv[4];
#pragma unroll
    for (int i = 0; i < 4; i++) qv[i] = ld16u_sc1(rs, qo + i * 8192);
#pragma unroll
    for (int i = 0; i < 4; i++) *(u32x4*)(qi + (i * 8 + w) * 1040 + l * 16) = qv[i];
    __syncthreads();
#pragma unroll
    for (int s = 0; s < 8; s++) qf[s] = __builtin_bit_cast(bf16x8, *(const u32x4*)(qi + r * 1040 + (128 * wq + 16 * s + 8 * hh) * 2));
#pragma unroll
    for (int s = 0; s < 8; s++) asm volatile("" : "+v"(qf[s]));
    __syncthreads();
  }
  f32x16 o[4];
#pragma unroll
  for (int i = 0; i < 4; i++)
#pragma unroll
    for (int j = 0; j < 16; j++) o[i][j] = 0.f;
  float m = -INFINITY, lsum = 0.f;
  const int q4 = (l & 15) >> 2, p4 = l & 3, colh = g & 1;
  auto tile_body = [&](int tile, u32x4 (&nx)[8]) {
    f32x16 s;
#pragma unroll
    for (int j = 0; j < 16; j++) s[j] = 0.f;
    {
      u32x4 af[8];
#pragma unroll
      for (int ks = 0; ks < 8; ks++) af[ks] = *(const u32x4*)(img + lds_off(r, 2 * ks + hh));
      __builtin_amdgcn_sched_barrier(0);
#pragma unroll
      for (int ks = 0; ks < 8; ks++) s = __builtin_amdgcn_mfma_f32_32x32x16_bf16(__builtin_bit_cast(bf16x8, af[ks]), qf[ks], s, 0, 0, 0);
    }
    f32x4* sr = (f32x4*)sred;
#pragma unroll
    for (int k4 = 0; k4 < 4; k4++) sr[(k4 * 4 + wq) * 64 + l] = f32x4{s[4 * k4], s[4 * k4 + 1], s[4 * k4 + 2], s[4 * k4 + 3]};
    __syncthreads();
    float x[4];
    {
      const f32x4 v = (sr[(wq * 4) * 64 + l] + sr[(wq * 4 + 1) * 64 + l]) + (sr[(wq * 4 + 2) * 64 + l] + sr[(wq * 4 + 3) * 64 + l]);
#pragma unroll
      for (int e = 0; e < 4; e += 2) {
        const unsigned r1 = cvtpk(v[e], v[e + 1]);
        const unsigned r2 = cvtpk(bf_lo(r1) * SCALE, bf_hi(r1) * SCALE);
        x[e] = bf_lo(r2); x[e + 1] = bf_hi(r2);
      }
      float mq = fmaxf(fmaxf(x[0], x[1]), fmaxf(x[2], x[3]));
      auto sx = __builtin_amdgcn_permlane32_swap(__float_as_uint(mq), __float_as_uint(mq), false, false);
      mq = fmaxf(__uint_as_float(sx[0]), __uint_as_float(sx[1]));
      if (hh == 0) mxs[r * 4 + wq] = mq;
    }
    __syncthreads();
    const f32x4 m4 = *(const f32x4*)(mxs + r * 4);
    const float mn = fmaxf(fmaxf(m, fmaxf(m4[0], m4[1])), fmaxf(m4[2], m4[3]));
    const float alpha = fexp2((m - mn) * LOG2E);
    {
      const unsigned p0 = cvtpk(fexp2((x[0] - mn) * LOG2E), fexp2((x[1] - mn) * LOG2E));
      const unsigned p1 = cvtpk(fexp2((x[2] - mn) * LOG2E), fexp2((x[3] - mn) * LOG2E));
      *(u32x2*)(pbuf + l * 8 + wq * 2) = u32x2{p0, p1};
      lsum = lsum * alpha + ((bf_lo(p0) + bf_hi(p0)) + (bf_lo(p1) + bf_hi(p1)));
    }
    m = mn;
    __syncthreads();
    unsigned pk[8];
    {
      const u32x4 a0 = *(const u32x4*)(pbuf + l * 8), a1 = *(const u32x4*)(pbuf + l * 8 + 4);
      pk[0] = a0[0]; pk[1] = a0[1]; pk[2] = a0[2]; pk[3] = a0[3]; pk[4] = a1[0]; pk[5] = a1[1]; pk[6] = a1[2]; pk[7] = a1[3];
    }
    if (__any(alpha != 1.f)) {
#pragma unroll
      for (int i = 0; i < 4; i++)
#pragma unroll
        for (int j = 0; j < 16; j++) o[i][j] *= alpha;
    }
#pragma unroll
    for (int s2 = 0; s2 < 2; s2++) {
      u32x2 tv[4][2];
#pragma unroll
      for (int ct = 0; ct < 4; ct++) {
        const int ch = 4 * ct + 2 * colh + (p4 >> 1);
        const unsigned a0 = lds_off(4 * hh + q4, ch) + 8 * (p4 & 1);
        const unsigned a1 = lds_off(8 + 4 * hh + q4, ch) + 8 * (p4 & 1);
        if (s2 == 0) {
          asm volatile("ds_read_b64_tr_b16 %0, %1" : "=v"(tv[ct][0]) : "v"(imga + a0) : "memory");
          asm volatile("ds_read_b64_tr_b16 %0, %1" : "=v"(tv[ct][1]) : "v"(imga + a1) : "memory");
        } else {
          asm volatile("ds_read_b64_tr_b16 %0, %1 offset:4096" : "=v"(tv[ct][0]) : "v"(imga + a0) : "memory");
          asm volatile("ds_read_b64_tr_b16 %0, %1 offset:4096" : "=v"(tv[ct][1]) : "v"(imga + a1) : "memory");
        }
      }
      asm volatile("s_waitcnt lgkmcnt(0)" ::: "memory");
      __builtin_amdgcn_sched_barrier(0);
      u32x4 pb = {pk[4 * s2], pk[4 * s2 + 1], pk[4 * s2 + 2], pk[4 * s2 + 3]};
#pragma unroll
      for (int ct = 0; ct < 4; ct++) {
        u32x4 av = {tv[ct][0][0], tv[ct][0][1], tv[ct][1][0], tv[ct][1][1]};
        o[ct] = __builtin_amdgcn_mfma_f32_32x32x16_bf16(__builtin_bit_cast(bf16x8, av), __builtin_bit_cast(bf16x8, pb), o[ct], 0, 0, 0);
      }
    }
    put_tile(nx);
    __builtin_amdgcn_sched_barrier(0);
    load_tile(nx, tile + 3);
  };
#pragma unroll 1
  for (int tile = 0; tile < NTL; tile += 2) {
    tile_body(tile, pre[1]);
    tile_body(tile + 1, pre[0]);
  }
  constexpr int OST = 516;
  float* ob = (float*)lds;
  float* mlb = (float*)(lds + 32 * OST * 4);
  lall[w * 64 + l] = lsum;
  __syncthreads();
  {
    const float* lp = lall + grp * 256 + r;
    lsum = ((lp[0] + lp[32]) + (lp[64] + lp[96])) + ((lp[128] + lp[160]) + (lp[192] + lp[224]));
  }
  if (grp == 1) {
#pragma unroll
    for (int ct = 0; ct < 4; ct++)
#pragma unroll
      for (int j4 = 0; j4 < 4; j4++)
        *(f32x4*)(ob + r * OST + 128 * wq + 32 * ct + 8 * j4 + 4 * hh) =
            f32x4{o[ct][4 * j4], o[ct][4 * j4 + 1], o[ct][4 * j4 + 2], o[ct][4 * j4 + 3]};
    if (wq == 0 && l < 32) { mlb[2 * r] = m; mlb[2 * r + 1] = lsum; }
  }
  __syncthreads();
  if (grp == 0) {
    const float m1 = mlb[2 * r], l1 = mlb[2 * r + 1];
    const float M = fmaxf(m, m1);
    const float f0 = fexp2((m - M) * LOG2E), f1 = fexp2((m1 - M) * LOG2E);
#pragma unroll
    for (int ct = 0; ct < 4; ct++)
#pragma unroll
      for (int j4 = 0; j4 < 4; j4++) {
        float* p = ob + r * OST + 128 * wq + 32 * ct + 8 * j4 + 4 * hh;
        const f32x4 o1 = *(const f32x4*)p;
        *(f32x4*)p = f32x4{o[ct][4 * j4] * f0 + o1[0] * f1, o[ct][4 * j4 + 1] * f0 + o1[1] * f1,
                           o[ct][4 * j4 + 2] * f0 + o1[2] * f1, o[ct][4 * j4 + 3] * f0 + o1[3] * f1};
      }
    if (wq == 0 && l < 32)
      st16u_sc1(rs, (unsigned)(WS_PAML + ((size_t)(b * SPLIT_A + sp) * NH + r) * 16),
                u32x4{__float_as_uint(M), ta, __float_as_uint(lsum * f0 + l1 * f1), tb});
  }
  float* wgt = mlb + 64;
  unsigned* pcs = (unsigned*)(mlb + 128);
  const unsigned tg = wait_gate(ws + WS_FS, ta, (unsigned*)(mlb + 96), tent);
  if (t < 256) {
    wait_until(tg + TM_ML);
    const unsigned mo = (unsigned)(WS_PAML + ((size_t)(b * SPLIT_A + (t & 7)) * NH + (t >> 3)) * 16);
    u32x4 mv;
#pragma unroll 1
    for (unsigned spins = 0; spins < SPIN_CAP; ++spins) {
      asm volatile("" ::: "memory");
      mv = ld16u_sc1(rs, mo);
      if (__all(mv[1] == ta && mv[3] == tb)) break;
      __builtin_amdgcn_s_sleep(16);
    }
    const float mm = __uint_as_float(mv[0]);
    float M = fmaxf(mm, dpp<0xB1>(mm)); M = fmaxf(M, dpp<0x4E>(M)); M = fmaxf(M, dpp<0x141>(M));
    const float f = fexp2((mm - M) * LOG2E);
    float L = f * __uint_as_float(mv[2]);
    L += dpp<0xB1>(L); L += dpp<0x4E>(L); L += dpp<0x141>(L);
    if ((t & 7) == sp) wgt[t >> 3] = f / L;
  }
  __syncthreads();
  {
    const unsigned pao = (unsigned)(WS_PAO + (size_t)(b * SPLIT_A + sp) * NH * DC * 2);
    u32x4 pv[4];
#pragma unroll
    for (int i = 0; i < 4; i++) {
      const int hd = i * 8 + w;
      const float f = wgt[hd];
      const f32x4 v0 = *(const f32x4*)(ob + hd * OST + l * 8) * f, v1 = *(const f32x4*)(ob + hd * OST + l * 8 + 4) * f;
      pv[i] = u32x4{cvtpk(v0[0], v0[1]), cvtpk(v0[2], v0[3]), cvtpk(v1[0], v1[1]), cvtpk(v1[2], v1[3])};
      asm volatile("" : "+v"(pv[i]));
    }
    {
      const unsigned cs = row_sums(pv);
      if (c == 0) pcs[g * 8 + w] = cs;
    }
    __syncthreads();
    const u32x2 c2 = *(const u32x2*)(pcs + (t & 15) * 2);
    const unsigned co = t < 16 ? (unsigned)(WS_PAC + (size_t)(b * SPLIT_A + sp) * NH * 8 + t * 16) : 0x7ffffff0u;
    wait_until(tg + TM_PUT);
#pragma unroll
    for (int i = 0; i < 4; i++) st16u_sc1(rs, pao + (unsigned)(i * 512 + t) * 16, pv[i]);
    if (w == 0) st16u_sc1(rs, co, u32x4{c2[0], ta, c2[1], tb});
  }
}

__global__ __launch_bounds__(512, 2) void k_fused(const __hip_bfloat16* __restrict__ xA, const __hip_bfloat16* __restrict__ ckv,
                                                  const __hip_bfloat16* __restrict__ wqA, const __hip_bfloat16* __restrict__ wuk,
                                                  const __hip_bfloat16* __restrict__ wuv, const __hip_bfloat16* __restrict__ woA,
                                                  const __hip_bfloat16* __restrict__ xB, const __hip_bfloat16* __restrict__ kc,
                                                  const __hip_bfloat16* __restrict__ vc, const __hip_bfloat16* __restrict__ wqB,
                                                  const __hip_bfloat16* __restrict__ woB, char* __restrict__ ws,
                                                  __hip_bfloat16* __restrict__ out) {
  __shared__ __attribute__((aligned(16))) char lds[131072];
  asm volatile("" :: "s"(xA), "s"(ckv), "s"(wqA), "s"(wuk), "s"(wuv), "s"(woA), "s"(xB), "s"(kc), "s"(vc), "s"(wqB), "s"(woB), "s"(ws), "s"(out));
  const int blk = blockIdx.x, xcd = blk & 7, r = blk >> 3;
  unsigned pfv = 0;
  {
    u64 pc; unsigned len;
    asm volatile("s_getpc_b64 %0\n.Lcode_begin:\n\ts_mov_b32 %1, .Lcode_end-.Lcode_begin" : "=s"(pc), "=s"(len));
    const unsigned idx = r < 5 ? (unsigned)((r * 8 + (threadIdx.x >> 6) + xcd * 5) % 40) : 1000u;
    const __amdgpu_buffer_rsrc_t rc = mkrs((const void*)pc, len);
    pfv = __builtin_amdgcn_raw_buffer_load_b32(rc, idx * 1024 + (threadIdx.x & 63) * 16, 0, 0);
  }
  const __amdgpu_buffer_rsrc_t rs = mkrs(ws, (unsigned)WS_END);
  if (r < 8) {
    if (r < 4) role_A1(xcd * 4 + r, xA, wqA, wuk, wuv, woA, ws, rs, out, lds);
    else role_A2(xcd * 4 + r - 4, ckv, ws, rs, lds);
  } else {
    const int rr = r - 8, hq = rr / 6;
    role_B(((0x75643120u >> (4 * xcd)) & 7) + 8 * hq, rr - 6 * hq, xcd, ckv, xB, kc, vc, wqB, woB, ws, rs, out, lds);
  }
  asm volatile(".Lcode_end:" :: "v"(pfv));
}

extern "C" void kernel_launch(void* const* d_in, const int* in_sizes, int n_in,
                              void* d_out, int out_size, void* d_ws, size_t ws_size,
                              hipStream_t stream) {
  k_fused<<<256, 512, 0, stream>>>((const __hip_bfloat16*)d_in[0], (const __hip_bfloat16*)d_in[1], (const __hip_bfloat16*)d_in[2],
                                   (const __hip_bfloat16*)d_in[3], (const __hip_bfloat16*)d_in[4], (const __hip_bfloat16*)d_in[5],
                                   (const __hip_bfloat16*)d_in[6], (const __hip_bfloat16*)d_in[7], (const __hip_bfloat16*)d_in[8],
                                   (const __hip_bfloat16*)d_in[9], (const __hip_bfloat16*)d_in[10], (char*)d_ws,
                                   (__hip_bfloat16*)d_out);
}
```

```cpp
#define TRACE
#define LTRACE
#define EX_IA 40
#define EX_A0 64
#define EX_A1 256
#define EX_GA 0
#define EX_IB 48
#define EX_B0 128
#define EX_B1 136
#define EX_GB 0
#define EX_OFF -1400
#define EX_MUL 10
#include <hip/hip_runtime.h>
#include <hip/hip_bf16.h>
#include <stdint.h>

typedef __attribute__((ext_vector_type(4))) unsigned u32x4;
typedef __attribute__((ext_vector_type(2))) unsigned u32x2;
typedef __attribute__((ext_vector_type(4))) float f32x4;
typedef __attribute__((ext_vector_type(2))) float f32x2;
typedef __attribute__((ext_vector_type(3))) float f32x3;
typedef __attribute__((ext_vector_type(16))) float f32x16;
typedef __attribute__((ext_vector_type(8))) __bf16 bf16x8;
typedef __attribute__((ext_vector_type(2))) __bf16 bf16x2;
typedef __attribute__((address_space(1))) unsigned gu32;
typedef __attribute__((address_space(1))) unsigned long long gu64;
typedef unsigned long long u64;

constexpr int NB = 4;
constexpr int NH = 32;
constexpr int SK = 4096;
constexpr int DH = 128;
constexpr int DM = 512;
constexpr int DC = 512;
constexpr int HD = NH * DH;

constexpr int SPLIT_A = 8;
constexpr int KA = SK / SPLIT_A;
constexpr int SS = 171;
constexpr int NI = 86;
constexpr int N0 = 43;
#define RDD 9
constexpr int RD = RDD;
constexpr int NP = NI - 8 * RD + 8;
constexpr int NP4 = (NP + 3) & ~3;
static_assert(8 * RD + 8 <= NI, "");
constexpr int RST = 144;
#define PRIOFD 4
#define SCH_R 4
#define SCH_0 -355
#define SCH_S 338
#define XW0 16
#define XWS 16
constexpr int RE = RD - 1;
constexpr int PRIOF = PRIOFD;

#define T_F1 1600
#define T_FC 3200
#define T_ML 1040
#define T_PUT 1160
#define T_GET 110
#define T_PA 1625
#define T_T4 60
#define T_SW 1720
#define XR 48
#define XQ 76
#define R1C 1060
#define XSTEP 72
#define STEP 30
constexpr unsigned TM_F1 = T_F1;
constexpr unsigned TM_FC = T_FC;
constexpr unsigned TM_ML = T_ML;
constexpr unsigned TM_PUT = T_PUT;
constexpr unsigned TM_GET = T_GET;
constexpr unsigned TM_PA = T_PA;
constexpr unsigned TM_T4 = T_T4;
constexpr unsigned TM_SW = T_SW;

constexpr float SCALE = 0.08838834764831845f;
constexpr float LOG2E = 1.4426950408889634f;
constexpr int NT = 2;
constexpr int SC1 = 16;

constexpr size_t WS_EPA  = 0;
constexpr size_t WS_EPB  = 256;
constexpr int LINE = 256;
constexpr size_t WS_F1   = 4096;
constexpr size_t WS_FS   = 12288;
constexpr size_t WS_FK   = 14336;
constexpr size_t WS_QABS = 16384;
constexpr size_t WS_PAML = WS_QABS + (size_t)NB * NH * DC * 2;
constexpr size_t WS_PAC  = WS_PAML + (size_t)NB * SPLIT_A * NH * 16;
constexpr size_t WS_PAO  = WS_PAC + (size_t)NB * SPLIT_A * NH * 8;
constexpr size_t WS_PA   = WS_PAO + (size_t)NB * SPLIT_A * NH * DC * 2;
constexpr size_t WS_PBG  = WS_PA + (size_t)NB * NH * DM * 8;
constexpr size_t WS_END  = WS_PBG + (size_t)NB * 64 * DM * 8;

__device__ __forceinline__ float bf_lo(unsigned u) { return __uint_as_float(u << 16); }
__device__ __forceinline__ float bf_hi(unsigned u) { return __uint_as_float(u & 0xffff0000u); }
__device__ __forceinline__ unsigned cvtpk(float lo, float hi) {
  f32x2 v = {lo, hi};
  return __builtin_bit_cast(unsigned, __builtin_convertvector(v, bf16x2));
}
__device__ __forceinline__ float rbf(float x) { return __uint_as_float(cvtpk(x, x) << 16); }
__device__ __forceinline__ float dot2(unsigned k, unsigned q, float acc) {
  return __builtin_amdgcn_fdot2_f32_bf16(__builtin_bit_cast(bf16x2, k), __builtin_bit_cast(bf16x2, q), acc, false);
}
template <int CTRL> __device__ __forceinline__ float dpp(float x) {
  return __builtin_bit_cast(float, __builtin_amdgcn_mov_dpp(__builtin_bit_cast(int, x), CTRL, 0xf, 0xf, true));
}
__device__ __forceinline__ float sum16(float x) {
  x += dpp<0x128>(x);
  x += dpp<0x124>(x);
  x += dpp<0x4E>(x);
  x += dpp<0xB1>(x);
  return x;
}
__device__ __forceinline__ float xrow_max(float x) {
  auto s = __builtin_amdgcn_permlane16_swap(__float_as_uint(x), __float_as_uint(x), false, false);
  x = fmaxf(__uint_as_float(s[0]), __uint_as_float(s[1]));
  auto t = __builtin_amdgcn_permlane32_swap(__float_as_uint(x), __float_as_uint(x), false, false);
  return fmaxf(__uint_as_float(t[0]), __uint_as_float(t[1]));
}
__device__ __forceinline__ float xrow_sum(float x) {
  auto s = __builtin_amdgcn_permlane16_swap(__float_as_uint(x), __float_as_uint(x), false, false);
  x = __uint_as_float(s[0]) + __uint_as_float(s[1]);
  auto t = __builtin_amdgcn_permlane32_swap(__float_as_uint(x), __float_as_uint(x), false, false);
  return __uint_as_float(t[0]) + __uint_as_float(t[1]);
}
__device__ __forceinline__ float fexp2(float x) { return __builtin_amdgcn_exp2f(x); }
__device__ __forceinline__ __amdgpu_buffer_rsrc_t mkrs(const void* p, unsigned bytes) {
  return __builtin_amdgcn_make_buffer_rsrc((void*)p, 0, (int)bytes, 0x00020000);
}

constexpr size_t WS_TR = (size_t)64 << 20;
#define TR(i) do { __builtin_amdgcn_sched_barrier(0); { const unsigned ts_ = (unsigned)__builtin_amdgcn_s_memrealtime(); trv = (int)(threadIdx.x & 63) == (i) ? ts_ : trv; } __builtin_amdgcn_sched_barrier(0); } while (0)
#define TRDUMP() trv = (threadIdx.x & 63) == 61 ? (unsigned)__builtin_amdgcn_s_memtime() : trv; __hip_atomic_store((gu32*)(ws + WS_TR) + (blockIdx.x * 8 + (threadIdx.x >> 6)) * 64 + (threadIdx.x & 63), trv, __ATOMIC_RELAXED, __HIP_MEMORY_SCOPE_AGENT)
#define TRR(i) do {} while (0)
constexpr unsigned SPIN_CAP = 1u << 21;
__device__ __forceinline__ u64 ld_u64(const void* p) {
  return __hip_atomic_load((gu64*)p, __ATOMIC_RELAXED, __HIP_MEMORY_SCOPE_AGENT);
}
__device__ __forceinline__ void st_u64(void* p, u64 v) {
  __hip_atomic_store((gu64*)p, v, __ATOMIC_RELAXED, __HIP_MEMORY_SCOPE_AGENT);
}
__device__ __forceinline__ u64 next_tag(u64 e) { return e + 0x0000000100000001ull; }
__device__ __forceinline__ u64 uni64(u64 v) {
  const unsigned lo = __builtin_amdgcn_readfirstlane((unsigned)v), hi = __builtin_amdgcn_readfirstlane((unsigned)(v >> 32));
  return ((u64)hi << 32) | lo;
}
__device__ __forceinline__ void signal_flag(char* flag, u64 tag) {
  asm volatile("s_waitcnt vmcnt(0)" ::: "memory");
  __syncthreads();
  if (threadIdx.x == 0) st_u64(flag, tag);
}
template <int CTRL> __device__ __forceinline__ int dppi(int x) { return __builtin_amdgcn_mov_dpp(x, CTRL, 0xf, 0xf, true); }
__device__ __forceinline__ int sum8i(int x) { x += dppi<0xB1>(x); x += dppi<0x4E>(x); x += dppi<0x141>(x); return x; }
__device__ __forceinline__ unsigned row_sums(const u32x4 (&p)[4]) {
  unsigned c4[4];
#pragma unroll
  for (int i = 0; i < 4; i++) c4[i] = (p[i][0] + p[i][1]) + (p[i][2] + p[i][3]);
  auto s0 = __builtin_amdgcn_permlane32_swap(c4[0], c4[2], false, false);
  auto s1 = __builtin_amdgcn_permlane32_swap(c4[1], c4[3], false, false);
  auto s2 = __builtin_amdgcn_permlane16_swap(s0[0] + s0[1], s1[0] + s1[1], false, false);
  int x = (int)(s2[0] + s2[1]);
  x += dppi<0x128>(x);
  return (unsigned)sum8i(x);
}
__device__ __forceinline__ unsigned clk() { return (unsigned)__builtin_amdgcn_s_memrealtime(); }
__device__ __forceinline__ void wait_until(unsigned t) {
#pragma unroll 1
  for (int spins = 0; spins < 4096 && (int)(t - clk()) > 0; ++spins) __builtin_amdgcn_s_sleep(1);
}
__device__ __forceinline__ unsigned ref_time(unsigned v) {
  const unsigned t0 = __builtin_amdgcn_readfirstlane(v);
  return t0 + (unsigned)__builtin_amdgcn_readfirstlane(sum8i((int)(v - t0)) >> 3);
}
__device__ __forceinline__ unsigned wait_gate(const char* flag, unsigned ta, unsigned* slot, unsigned tent) {
  if (threadIdx.x < 64) {
    u64 v = 0;
    wait_until(tent + TM_FC);
#pragma unroll 1
    for (unsigned spins = 0; spins < SPIN_CAP; ++spins) {
      v = ld_u64(flag + (threadIdx.x & 7) * LINE);
      if (__all((unsigned)(v >> 32) == ta)) break;
      __builtin_amdgcn_s_sleep(12);
    }
    const unsigned tr = ref_time((unsigned)v);
    if (threadIdx.x == 0) *slot = tr;
  }
  __syncthreads();
  return __builtin_amdgcn_readfirstlane(*slot);
}
__device__ __forceinline__ void st16u_sc1(__amdgpu_buffer_rsrc_t rs, unsigned off, u32x4 v) {
  __builtin_amdgcn_raw_buffer_store_b128(v, rs, off, 0, SC1);
}
__device__ __forceinline__ u32x4 ld16u_sc1(__amdgpu_buffer_rsrc_t rs, unsigned off) {
  return __builtin_amdgcn_raw_buffer_load_b128(rs, off, 0, SC1);
}

__device__ __forceinline__ void item_dots(const u32x4 (&ks)[4], const u32x4 q, float (&s_)[4]) {
#pragma unroll
  for (int u = 0; u < 4; u++) {
    float a = dot2(ks[u][0], q[0], 0.f);
    a = dot2(ks[u][1], q[1], a);
    a = dot2(ks[u][2], q[2], a);
    a = dot2(ks[u][3], q[3], a);
    s_[u] = a;
    asm volatile("" : "+v"(s_[u]));
  }
}
__device__ __forceinline__ void item_unpack(const u32x4 (&vs)[4], f32x2 (&vf)[4][4]) {
#pragma unroll
  for (int u = 0; u < 4; u++)
#pragma unroll
    for (int j = 0; j < 4; j++) { vf[u][j] = f32x2{bf_lo(vs[u][j]), bf_hi(vs[u][j])}; asm volatile("" : "+v"(vf[u][j])); }
}
__device__ __forceinline__ f32x2 rows_sum(const float (&o)[8]) {
  float r4[4], r2[2];
#pragma unroll
  for (int j = 0; j < 4; j++) {
    auto sx = __builtin_amdgcn_permlane32_swap(__float_as_uint(o[j]), __float_as_uint(o[j + 4]), false, false);
    r4[j] = __uint_as_float(sx[0]) + __uint_as_float(sx[1]);
  }
#pragma unroll
  for (int j = 0; j < 2; j++) {
    auto sx = __builtin_amdgcn_permlane16_swap(__float_as_uint(r4[j]), __float_as_uint(r4[j + 2]), false, false);
    r2[j] = __uint_as_float(sx[0]) + __uint_as_float(sx[1]);
  }
  return f32x2{r2[0], r2[1]};
}
__device__ __forceinline__ void role_B(int h, int j6, int xcd, const __hip_bfloat16* __restrict__ ckv, const __hip_bfloat16* __restrict__ xB,
                                       const __hip_bfloat16* __restrict__ kc, const __hip_bfloat16* __restrict__ vc,
                                       const __hip_bfloat16* __restrict__ wqB, const __hip_bfloat16* __restrict__ woB,
                                       char* __restrict__ ws, __amdgpu_buffer_rsrc_t rs,
                                       __hip_bfloat16* __restrict__ out, char* lds, unsigned trv) {
  const int t = threadIdx.x, l = t & 63;
  const int w = __builtin_amdgcn_readfirstlane(t >> 6);
  const int g = l >> 4, c = l & 15;
  const int pr = j6 >= 3 ? 1 : 0, k = j6 - 3 * pr;
  const unsigned tent = clk();
  const int b0 = 2 * pr;
  const int e0 = k == 1 ? (h & 1) : (k >> 1);
  u32x4 xr[2][2], wr[16];
  {
    const __amdgpu_buffer_rsrc_t rx = mkrs(xB, NB * DM * 2);
    const int rq_ = (0x203120 >> (4 * j6)) & 3;
    const int cw = w ^ ((rq_ & 2) << 1), r0 = rq_ & 1;
    const unsigned xo = (unsigned)((b0 * DM + cw * 64 + g * 16) * 2);
#pragma unroll
    for (int bb = 0; bb < 2; bb++)
#pragma unroll
      for (int i = 0; i < 2; i++) xr[bb][i] = __builtin_amdgcn_raw_buffer_load_b128(rx, xo + bb * DM * 2, (i ^ r0) * 16, 0);
    const __amdgpu_buffer_rsrc_t rq = mkrs(wqB, DM * HD * 2);
    const unsigned vo = (unsigned)(((cw * 64 + g * 16) * HD + h * DH + c * 8) * 2);
    const unsigned vo0 = vo + r0 * 8 * HD * 2, vo1 = vo + (1 - r0) * 8 * HD * 2;
#pragma unroll
    for (int i = 0; i < 16; i++) wr[i] = __builtin_amdgcn_raw_buffer_load_b128(rq, i < 8 ? vo0 : vo1, (i & 7) * HD * 2, 0);
  }
  constexpr unsigned KVB = (unsigned)((size_t)NB * NH * SK * DH * 2);
  const unsigned lo = (unsigned)(g * 256 + c * 16);
  const unsigned hb0 = ((unsigned)((b0 + e0) * NH + h) << 20) + (k == 1 ? (unsigned)SS << 12 : 0u);
  const unsigned hb1 = ((unsigned)((b0 + 1 - e0) * NH + h) << 20) + ((unsigned)SS << 12);
  auto seg1 = [&](int x) { return k == 1 && x >= N0; };
  auto unit = [&](int x) { const int y = seg1(x) ? x - N0 : x; return 4 * (y >> 1) + (y & 1); };
  auto soff = [&](int x) { return (seg1(x) ? hb1 : hb0) + ((unsigned)unit(x) << 12); };
  auto sco = [&](int x) { return (unsigned)((unit(x) + (seg1(x) ? 88 : 0)) * 64); };
  auto whole = [&](int x) { return x != NI - 1 && !(k == 1 && x == N0 - 1); };
  auto ld4 = [&](u32x4 (&d)[4], const void* p, bool ok, unsigned so) {
    const __amdgpu_buffer_rsrc_t r = mkrs(p, ok ? KVB : 0u);
#pragma unroll
    for (int u = 0; u < 4; u++) d[u] = __builtin_amdgcn_raw_buffer_load_b128(r, lo + u * 1024, so, NT);
  };
  unsigned pf;
  {
    const int n = ((h >> 3) * 6 + j6) * 8 + w, u = n >> 4, a = xcd * 4 + u / 3;
    pf = __builtin_amdgcn_raw_buffer_load_b32(mkrs(ckv, h == 31 ? 0 : NB * SK * DC * 2), (unsigned)l * 64, (unsigned)(a * 8 + ((5 + u % 3 + a) & 7)) * 65536u + (n & 15) * 4096u, 0);
  }
  u32x4 ra[4], rb[4], ea[4], eb[4];
  ld4(ra, kc, true, soff(w));
  ld4(rb, kc, true, soff(w) + 8192);
  u64* eps = (u64*)(lds + 33808);
  u64 epv = 0;
  if (t < 3) epv = ld_u64(ws + (t == 0 ? WS_EPA : WS_EPB + 256 * (b0 + (t == 1 ? e0 : 1 - e0))));
  TRR(56);
  float* red = (float*)lds;
  unsigned* qs = (unsigned*)(lds + 32768);
  unsigned* cnt = (unsigned*)(lds + 33856);
  float* mxd = (float*)(lds + 33872);
  float* lw = (float*)(lds + 33920);
  float* rec0 = (float*)(lds + 36864);
  char* prv = lds + 45056 + w * 1024;
  constexpr unsigned L_SC = 53248;
  float* recd = (float*)(lds + 65536);
  const unsigned lb = (unsigned)(uintptr_t)(__attribute__((address_space(3))) char*)lds;
  if (t == 0) { cnt[0] = 16u; cnt[1] = 8u * (RD + 1); cnt[2] = 0u; cnt[3] = 0u; cnt[6] = tent - 0x1000000u; mxd[0] = -INFINITY; mxd[1] = -INFINITY; }
  if (t < 64 * (NP4 - NP)) *(f32x2*)(recd + (NP + (t >> 6)) * RST + 2 * l) = f32x2{0.f, 0.f};
  if (t >= 384) *(float*)(lds + L_SC + ((t & 64) ? 172 : 84) * 64 + l * 4) = -INFINITY;
  {
    float a0[8], a1[8];
#pragma unroll
    for (int j = 0; j < 8; j++) { a0[j] = 0.f; a1[j] = 0.f; }
#pragma unroll
    for (int i = 0; i < 16; i += 2) {
      const unsigned x0 = xr[0][i >> 3][(i >> 1) & 3], x1 = xr[1][i >> 3][(i >> 1) & 3];
#pragma unroll
      for (int j = 0; j < 4; j++) {
        const unsigned pl = __builtin_amdgcn_perm(wr[i + 1][j], wr[i][j], 0x05040100u);
        const unsigned ph = __builtin_amdgcn_perm(wr[i + 1][j], wr[i][j], 0x07060302u);
        a0[2 * j] = dot2(pl, x0, a0[2 * j]); a0[2 * j + 1] = dot2(ph, x0, a0[2 * j + 1]);
        a1[2 * j] = dot2(pl, x1, a1[2 * j]); a1[2 * j + 1] = dot2(ph, x1, a1[2 * j + 1]);
      }
    }
    TRR(57);
#pragma unroll
    for (int j = 0; j < 8; j++) asm volatile("" : "+v"(a0[j]), "+v"(a1[j]));
    __builtin_amdgcn_sched_barrier(0);
    ld4(ea, kc, true, soff(w + 8));
    ld4(eb, kc, true, soff(w + 8) + 8192);
    __builtin_amdgcn_sched_barrier(0);
    float* rp = red + (w * 4 + g) * 256 + c * 8;
    *(f32x4*)(rp) = f32x4{a0[0], a0[1], a0[2], a0[3]};
    *(f32x4*)(rp + 4) = f32x4{a0[4], a0[5], a0[6], a0[7]};
    *(f32x4*)(rp + 128) = f32x4{a1[0], a1[1], a1[2], a1[3]};
    *(f32x4*)(rp + 132) = f32x4{a1[4], a1[5], a1[6], a1[7]};
  }
  __syncthreads();
  {
    const int o = t >> 1, hf = t & 1;
    float s = 0.f;
#pragma unroll
    for (int i = 0; i < 16; i++) s += red[(hf * 16 + i) * 256 + o];
    s += dpp<0xB1>(s);
    const float sn = dpp<0x4E>(s);
    if ((t & 3) == 0) qs[o >> 1] = cvtpk(s, sn);
    if (t < 3) eps[t] = epv;
  }
  __syncthreads();
  u32x4 q = *(const u32x4*)(qs + e0 * 64 + c * 4);
  const u32x4 q1 = *(const u32x4*)(qs + (1 - e0) * 64 + c * 4);
  TR(1);
  auto lds_cnt = [&](int i) { return (unsigned)__builtin_amdgcn_readfirstlane(__hip_atomic_load(cnt + i, __ATOMIC_RELAXED, __HIP_MEMORY_SCOPE_WORKGROUP)); };
  auto lds_inc = [&](int i) {
    asm volatile("s_waitcnt lgkmcnt(0)" ::: "memory");
    if (l == 0) asm volatile("ds_add_u32 %0, %1" :: "v"(lb + 33856u + i * 4), "v"(1u) : "memory");
  };
  u32x4 wo[16];
  __builtin_amdgcn_sched_barrier(0);
#pragma unroll
  for (int i = 0; i < 16; i++) asm volatile("" : "=v"(wo[i]));
  const int nseg = k == 1 ? 2 : 1, sp = k == 1 ? 1 : 0;
  auto store_off = [&](int sg) {
    const int b = b0 + (sg ? 1 - e0 : e0);
    const unsigned og_ = (unsigned)(WS_PBG + ((size_t)(b * 64 + h * 2 + sp) * DM + w * 64 + c * 4 + g) * 8);
    return (g & 1) == 0 ? og_ : 0x7ffffff0u;
  };
  const unsigned apo = (unsigned)((c & 1) * 272 + g * 64);
  auto project = [&](const char* ap, float M, float L) {
    u32x4 a[4];
#pragma unroll
    for (int s = 0; s < 4; s++) a[s] = *(const u32x4*)(ap + s * 16);
    __builtin_amdgcn_sched_barrier(0);
    L += dpp<0xB1>(L); L += dpp<0x4E>(L); L += dpp<0x141>(L);
    __builtin_amdgcn_sched_barrier(0);
    f32x4 d[4];
#pragma unroll
    for (int s = 0; s < 4; s++)
#pragma unroll
      for (int j = 0; j < 4; j++)
        d[j] = __builtin_amdgcn_mfma_f32_16x16x32_bf16(__builtin_bit_cast(bf16x8, a[s]), __builtin_bit_cast(bf16x8, wo[4 * s + j]),
                                                       s ? d[j] : f32x4{0.f, 0.f, 0.f, 0.f}, 0, 0, 0);
    return f32x3{g < 2 ? d[0][0] + d[0][1] : d[3][0] + d[3][1], g < 2 ? d[1][0] + d[1][1] : M, g < 2 ? d[2][0] + d[2][1] : L};
  };
  f32x3 seg0 = {0.f, 0.f, 0.f};
  auto load_wo = [&]() {
    const __amdgpu_buffer_rsrc_t ro = mkrs(woB, HD * DM * 2);
    const unsigned vo = (unsigned)(((h * DH + g * 32) * DM + w * 64 + c * 4) * 2);
#pragma unroll
    for (int i = 0; i < 16; i++) {
      const u32x2 u0 = __builtin_amdgcn_raw_buffer_load_b64(ro, vo + 2 * i * DM * 2, 0, 0);
      const u32x2 u1 = __builtin_amdgcn_raw_buffer_load_b64(ro, vo + (2 * i + 1) * DM * 2, 0, 0);
      wo[i] = u32x4{u0[0], u0[1], u1[0], u1[1]};
    }
  };
  asm volatile("" :: "v"(pf));
  unsigned tp = *(const unsigned*)eps + 1u;
  asm volatile("" : "+v"(tp));
  int nit = 0;
  {
    float m = -INFINITY, s_[4];
    const unsigned swo = lb + L_SC + g * 16 + (c & 3) * 4;
    auto scores = [&](unsigned so, bool ok) {
      const bool b0_ = c & 1, b1_ = c & 2;
      const float r0 = (b0_ ? s_[1] : s_[0]) + dpp<0xB1>(b0_ ? s_[0] : s_[1]);
      const float r1 = (b0_ ? s_[3] : s_[2]) + dpp<0xB1>(b0_ ? s_[2] : s_[3]);
      float r = (b1_ ? r1 : r0) + dpp<0x4E>(b1_ ? r0 : r1);
      r += dpp<0x124>(r);
      r += dpp<0x128>(r);
      r = ok ? rbf(rbf(r) * SCALE) : -INFINITY;
      m = fmaxf(m, r);
      asm volatile("ds_write_b32 %0, %1" :: "v"(swo + so), "v"(r) : "memory");
    };
    auto flush_max = [&](int sg) {
      float mw = fmaxf(m, dpp<0xB1>(m));
      mw = xrow_max(fmaxf(mw, dpp<0x4E>(mw)));
      if (l == 0) asm volatile("ds_max_f32 %0, %1" :: "v"(lb + 33872u + sg * 4), "v"(mw) : "memory");
      m = -INFINITY;
    };
    bool sw = k != 1, first = true, wl = false;
    u32x2 krep = {0u, ~tp};
    const int xw = XW0 + XWS * (h >> 3);
    int x = w;
#pragma unroll 1
    while (x < NI) {
      if (!sw && x >= N0) { flush_max(0); q = q1; sw = true; }
      if (!wl && x >= xw) { load_wo(); wl = true; }
      if (x == XQ) krep = __builtin_amdgcn_raw_buffer_load_b64(rs, (unsigned)WS_FK + (l & 7) * LINE, 0, SC1);
      unsigned dr; u64 sv;
      asm volatile("s_mov_b64 %1, exec\n\ts_mov_b64 exec, 1\n\tds_add_rtn_u32 %0, %2, %3\n\ts_mov_b64 exec, %1"
                   : "=&v"(dr), "=&s"(sv) : "v"(lb + 33856u), "v"(1u), "v"(ra[0][0]) : "memory");
      __builtin_amdgcn_sched_barrier(0);
      item_dots(ra, q, s_);
      asm volatile("s_waitcnt lgkmcnt(0)" : "+v"(dr) :: "memory");
      const int xn = __builtin_amdgcn_readfirstlane(dr);
      const bool more = xn < NI;
      const void* pn = more ? (const void*)kc : (const void*)vc;
      const unsigned sn = soff(more ? xn : w);
      __builtin_amdgcn_sched_barrier(0);
      ld4(ra, pn, true, sn);
      __builtin_amdgcn_sched_barrier(0);
      scores(sco(x), true);
      item_dots(rb, q, s_);
      __builtin_amdgcn_sched_barrier(0);
      ld4(rb, pn, !more || whole(xn), sn + 8192);
      __builtin_amdgcn_sched_barrier(0);
      scores(sco(x) + 128, whole(x));
      if (xn == XR && h >= 8 && h < 16 && j6 == 2) { TR(45); if (l == 0) st_u64(ws + WS_FK + (h - 8) * LINE, ((u64)tp << 32) | clk()); }
      if (first) {
        item_dots(ea, q, s_); scores(sco(w + 8), true);
        item_dots(eb, q, s_); scores(sco(w + 8) + 128, true);
        first = false;
      }
      x = xn;
      TRR(2 + nit); nit++;
    }
    if (!wl) {
      load_wo();
#pragma unroll
      for (int i = 0; i < 16; i++) asm volatile("" : "+v"(wo[i]));
    }
    ld4(ea, vc, true, soff(w + 8));
    ld4(eb, vc, true, soff(w + 8) + 8192);
    if (__all(krep[1] == tp)) {
      const unsigned tr = ref_time(krep[0]);
      if (l == 0) cnt[6] = tr + R1C;
      TR(50);
    }
    flush_max(k == 1 && !sw ? 0 : 1);
    lds_inc(2);
  }
  TR(47);
#pragma unroll
  for (int s = 0; s < 4; s++) {
    u32x4 o[4];
#pragma unroll
    for (int e = 0; e < 4; e++)
#pragma unroll
      for (int j = 0; j < 4; j++) o[j][e] = __builtin_amdgcn_perm(wo[4 * s + e][2 + (j >> 1)], wo[4 * s + e][j >> 1], (j & 1) ? 0x07060302u : 0x05040100u);
#pragma unroll
    for (int j = 0; j < 4; j++) wo[4 * s + j] = o[j];
    asm volatile("" : "+v"(wo[4 * s]), "+v"(wo[4 * s + 1]), "+v"(wo[4 * s + 2]), "+v"(wo[4 * s + 3]));
  }
  for (unsigned spins = 0; lds_cnt(2) != 8u && spins < SPIN_CAP; ++spins) __builtin_amdgcn_s_sleep(1);
  asm volatile("" ::: "memory");
  TR(48);
  nit = 18;
  const u64 tg0 = next_tag(uni64(eps[1])), tg1 = next_tag(uni64(eps[2]));
  if (h >= 8 && h < 16 && j6 == 2 && t == 0) st_u64(ws + WS_FS + (h - 8) * LINE, ((u64)tp << 32) | clk());
  u32x2 srep = {0u, 0u};
  const float M0 = __builtin_bit_cast(float, __builtin_amdgcn_readfirstlane(__builtin_bit_cast(int, mxd[k == 1 ? 0 : 1])));
  const float M1 = __builtin_bit_cast(float, __builtin_amdgcn_readfirstlane(__builtin_bit_cast(int, mxd[1])));
  auto weights = [&](unsigned so, float M) {
    const f32x4 sc = *(const f32x4*)(lds + L_SC + so);
    return f32x4{fexp2((sc[0] - M) * LOG2E), fexp2((sc[1] - M) * LOG2E), fexp2((sc[2] - M) * LOG2E), fexp2((sc[3] - M) * LOG2E)};
  };
  float acc[8];
#pragma unroll
  for (int j = 0; j < 8; j++) acc[j] = 0.f;
  f32x2 vf[4][4];
  f32x4 pa, pb;
  unsigned sb = 0;
  auto get_p = [&](int x) {
    const float M = seg1(x) ? M1 : M0;
    pa = weights(sco(x) + g * 16, M); pb = weights(sco(x) + 128 + g * 16, M);
    asm volatile("" : "+v"(pa), "+v"(pb));
    sb = lds_cnt(6);
  };
  auto fma_v = [&](const f32x4 p) {
#pragma unroll
    for (int u = 0; u < 4; u++)
#pragma unroll
      for (int j = 0; j < 4; j++) {
        acc[2 * j]     = fmaf(p[u], vf[u][j][0], acc[2 * j]);
        acc[2 * j + 1] = fmaf(p[u], vf[u][j][1], acc[2 * j + 1]);
      }
  };
  auto step = [&](int xn) {
    const unsigned sn = soff(xn);
    wait_until(sb + (unsigned)((xn * SCH_S >> 4) + SCH_0 + (xn >= XSTEP ? STEP : 0)));
    __builtin_amdgcn_sched_barrier(0);
    ld4(ra, vc, true, sn);
    __builtin_amdgcn_sched_barrier(0);
    fma_v(pa);
#pragma unroll
    for (int j = 0; j < 8; j++) asm volatile("" : "+v"(acc[j]));
    item_unpack(rb, vf);
    __builtin_amdgcn_sched_barrier(0);
    ld4(rb, vc, whole(xn), sn + 8192);
    __builtin_amdgcn_sched_barrier(0);
    fma_v(pb);
  };
  auto put_acc = [&](float* rec_) {
    *(f32x2*)(rec_ + c * 8 + g * 2) = rows_sum(acc);
#pragma unroll
    for (int j = 0; j < 8; j++) acc[j] = 0.f;
  };
  if (w >= 4) __builtin_amdgcn_s_setprio(1);
  get_p(w);
  item_unpack(ra, vf);
  step(w + 16);
  TRR(2 + nit); nit++;
  get_p(w + 8);
  item_unpack(ea, vf); fma_v(pa);
  item_unpack(eb, vf); fma_v(pb);
  TRR(2 + nit); nit++;
  {
    const bool two = t < 192;
    const f32x4 p0 = weights(t * 16, t < 352 ? M0 : M1), p1 = weights(two ? (t + 512) * 16 : 172 * 64, two ? M1 : 0.f);
    const float s0 = (p0[0] + p0[1]) + (p0[2] + p0[3]), s1 = (p1[0] + p1[1]) + (p1[2] + p1[3]);
    const bool f0 = k == 1 && t < 352;
    const float l0 = xrow_sum(sum16(f0 ? s0 : 0.f)), l1 = xrow_sum(sum16((f0 ? 0.f : s0) + (two ? s1 : 0.f)));
    if (l < 2) lw[l * 8 + w] = l ? l1 : l0;
  }
  bool sw = k != 1;
  int x = w + 16;
#pragma unroll 1
  for (; x < 8 * RD; x += 8) {
    const int rd = x >> 3;
    if (rd == PRIOF) __builtin_amdgcn_s_setprio(0);
    if (!sw && x >= N0) { put_acc(rec0 + w * 128); lds_inc(3); sw = true; }
    if (w == 3 && rd == SCH_R + 1 && __all(srep[1] == tp)) {
      const unsigned tr = ref_time(srep[0]);
      if (l == 0) cnt[6] = tr;
      TR(49);
    }
    if (w == 3 && rd == SCH_R) srep = __builtin_amdgcn_raw_buffer_load_b64(rs, (unsigned)WS_FS + (l & 7) * LINE, 0, SC1);
    if (rd == RE && k == 1) {
      for (unsigned spins = 0; lds_cnt(3) != 8u && spins < SPIN_CAP; ++spins) __builtin_amdgcn_s_sleep(1);
      asm volatile("" ::: "memory");
      f32x2 a2 = {0.f, 0.f};
#pragma unroll
      for (int i = 0; i < 8; i++) a2 += *(const f32x2*)(rec0 + i * 128 + 2 * l);
      const unsigned hi = cvtpk(a2[0], a2[1]);
      *(unsigned*)(prv + l * 4) = hi;
      *(unsigned*)(prv + 272 + l * 4) = cvtpk(a2[0] - bf_lo(hi), a2[1] - bf_hi(hi));
      seg0 = project(prv + apo, M0, lw[l & 7]);
    }
    get_p(x);
    __builtin_amdgcn_sched_barrier(0);
    item_unpack(ra, vf);
    step(x + 8);
    TRR(2 + nit); nit++;
  }
  put_acc(recd + w * RST);
  unsigned o_rv = 65536u + (g * RST + w * 16 + c) * 4, o_lw = 33920u + 32u + (l & 7) * 4;
  unsigned o_oi = (g < 2 && (c & 1) == 0) ? 41216u + g * 272 + (w * 16 + c) * 2 : 45056u + w * 1024 + 256 + l * 4;
  unsigned o_ap = 41216u + apo, o_st = store_off(nseg - 1);
  unsigned o_s0 = k == 1 ? store_off(0) : 0x7ffffff0u;
  unsigned tl0 = (unsigned)(k == 1 ? tg1 : tg0);
  u32x4 sg0 = {__float_as_uint(seg0[0]), __float_as_uint(seg0[1]), __float_as_uint(seg0[2]), (unsigned)tg0};
  asm volatile("" : "+v"(o_s0), "+v"(tl0), "+v"(sg0));
  asm volatile("" : "+v"(o_rv), "+v"(o_lw), "+v"(o_oi), "+v"(o_ap), "+v"(o_st));
  {
    {
#pragma unroll 1
      for (;;) {
        get_p(x);
        unsigned dr; u64 sv;
        asm volatile("s_mov_b64 %1, exec\n\ts_mov_b64 exec, 1\n\tds_add_rtn_u32 %0, %2, %3\n\ts_mov_b64 exec, %1"
                     : "=&v"(dr), "=&s"(sv) : "v"(lb + 33860u), "v"(1u), "v"(ra[0][0]) : "memory");
        __builtin_amdgcn_sched_barrier(0);
        item_unpack(ra, vf);
        asm volatile("s_waitcnt lgkmcnt(0)" : "+v"(dr) :: "memory");
        const int xn = __builtin_amdgcn_readfirstlane(dr);
        if (xn >= NI) break;
        step(xn);
        put_acc(recd + (x - 8 * RD + 8) * RST);
        x = xn;
        TRR(2 + nit); nit++;
      }
      fma_v(pa);
      item_unpack(rb, vf);
      fma_v(pb);
      put_acc(recd + (x - 8 * RD + 8) * RST);
      TRR(2 + nit); nit++;
      TR(40);
      asm volatile("s_waitcnt lgkmcnt(0)" ::: "memory");
      __syncthreads();
      TRR(41);
    }
    {
      float rv_[NP4 / 4];
#pragma unroll
      for (int r = 0; r < NP4 / 4; r++) rv_[r] = *(const float*)(lds + o_rv + r * 4 * RST * 4);
      const float li = *(const float*)(lds + o_lw);
      float a = 0.f, a1 = 0.f;
#pragma unroll
      for (int r = 0; r < NP4 / 4; r++) { if (r & 1) a1 += rv_[r]; else a += rv_[r]; }
      a += a1;
      a = xrow_sum(a);
      const float an = dpp<0xB1>(a);
      const unsigned hi = cvtpk(a, an);
      const unsigned lo2 = cvtpk(a - bf_lo(hi), an - bf_hi(hi));
      *(unsigned*)(lds + o_oi) = g ? lo2 : hi;
      __syncthreads();
      const f32x3 pv = project(lds + o_ap, M1, li);
      st16u_sc1(rs, o_st, u32x4{__float_as_uint(pv[0]), __float_as_uint(pv[1]), __float_as_uint(pv[2]), tl0});
      st16u_sc1(rs, o_s0, sg0);
    }
    TR(42);
    if (w < nseg) {
      const int sg = w;
      const int b = b0 + (sg ? 1 - e0 : e0);
      const int j64 = (h & 7) * 8 + (h >> 3) * 2 + sp;
      const u64 tg = sg ? tg1 : tg0;
      const unsigned ta = (unsigned)tg;
      const unsigned og = (unsigned)(WS_PBG + ((size_t)(b * 64 + (l >> 2)) * DM + j64 * 8) * 8 + (l & 3) * 16);
      u32x4 p[4];
      {
        __builtin_amdgcn_s_sleep(4);
        wait_until(sb + TM_SW);
        TR(46);
      }
#pragma unroll 1
      for (unsigned spins = 0; spins < SPIN_CAP; ++spins) {
        asm volatile("" ::: "memory");
#pragma unroll
        for (int i = 0; i < 4; i++) p[i] = ld16u_sc1(rs, og + i * 16 * DM * 8);
        if (__all(p[0][3] == ta && p[1][3] == ta && p[2][3] == ta && p[3][3] == ta)) break;
      }
      float v0 = 0.f, v1 = 0.f, v2 = 0.f;
#pragma unroll
      for (int i = 0; i < 4; i++) {
        const float mm = dpp<0xF5>(__uint_as_float(p[i][1]));
        const float Mh = fmaxf(mm, dpp<0x141>(mm));
        const float fq = fexp2((mm - Mh) * LOG2E);
        float L = fq * dpp<0xF5>(__uint_as_float(p[i][2]));
        L += dpp<0x141>(L);
        const float wgt = fq * __builtin_amdgcn_rcpf(L);
        v0 = fmaf(__uint_as_float(p[i][0]), wgt, v0);
        v1 = fmaf(__uint_as_float(p[i][1]), wgt, v1);
        v2 = fmaf(__uint_as_float(p[i][2]), wgt, v2);
      }
      const float u2 = dpp<0xA0>(v2);
      v1 = (l & 1) ? v0 : v1;
      v0 = (l & 1) ? u2 : v0;
      v0 += dpp<0x128>(v0); v0 += dpp<0x124>(v0); v0 = xrow_sum(v0);
      v1 += dpp<0x128>(v1); v1 += dpp<0x124>(v1); v1 = xrow_sum(v1);
      if (l < 4) {
        *(unsigned*)(out + b * 1024 + 512 + j64 * 8 + l * 2) = cvtpk(v0, v1);
        if (j64 == 0 && l == 0) st_u64(ws + WS_EPB + 256 * b, tg);
      }
    }
  }
  TR(43);
  TRDUMP();
}

__device__ __forceinline__ unsigned lds_off(int row, int ch) {
  return 256 * row + 16 * (ch ^ (((row & 3) << 2) | ((row >> 2) & 3)));
}

__device__ __forceinline__ void role_A1(int h, const __hip_bfloat16* __restrict__ xA, const __hip_bfloat16* __restrict__ wqA,
                                        const __hip_bfloat16* __restrict__ wuk, const __hip_bfloat16* __restrict__ wuv,
                                        const __hip_bfloat16* __restrict__ woA, char* __restrict__ ws,
                                        __amdgpu_buffer_rsrc_t rs, __hip_bfloat16* __restrict__ out, char* lds, unsigned trv) {
  const int t = threadIdx.x, l = t & 63;
  const int w = __builtin_amdgcn_readfirstlane(t >> 6);
  const int g = l >> 4, c = l & 15;
  const unsigned tent = clk();
  float* red = (float*)(lds + 16384);
  const unsigned vo = (unsigned)(((w * 64 + g * 16) * HD + h * DH + c * 8) * 2);
  u64 tag;
  {
    u32x4 xr[NB][2], wr[16], wk[16];
    const __amdgpu_buffer_rsrc_t rx = mkrs(xA, NB * DM * 2);
    const unsigned xo = (unsigned)((w * 64 + g * 16) * 2);
#pragma unroll
    for (int bb = 0; bb < NB; bb++)
#pragma unroll
      for (int i = 0; i < 2; i++) xr[bb][i] = __builtin_amdgcn_raw_buffer_load_b128(rx, xo + bb * DM * 2 + i * 16, 0, 0);
    const __amdgpu_buffer_rsrc_t rq = mkrs(wqA, DM * HD * 2);
#pragma unroll
    for (int i = 0; i < 16; i++) wr[i] = __builtin_amdgcn_raw_buffer_load_b128(rq, vo, i * HD * 2, NT);
    {
      const __amdgpu_buffer_rsrc_t rku = mkrs(wuk, DC * HD * 2);
#pragma unroll
      for (int i = 0; i < 16; i++) wk[i] = __builtin_amdgcn_raw_buffer_load_b128(rku, vo, i * HD * 2, NT);
    }
    __builtin_amdgcn_sched_barrier(0);
    u64 epv = 0;
    if (t == 0) epv = ld_u64(ws + WS_EPA);
    unsigned* qs = (unsigned*)lds;
    unsigned short* qo = (unsigned short*)(lds + 4096);
    float acc[NB][8];
#pragma unroll
    for (int bb = 0; bb < NB; bb++)
#pragma unroll
      for (int j = 0; j < 8; j++) acc[bb][j] = 0.f;
#pragma unroll
    for (int i = 0; i < 16; i += 2) {
#pragma unroll
      for (int j = 0; j < 4; j++) {
        const unsigned pl = __builtin_amdgcn_perm(wr[i + 1][j], wr[i][j], 0x05040100u);
        const unsigned ph = __builtin_amdgcn_perm(wr[i + 1][j], wr[i][j], 0x07060302u);
#pragma unroll
        for (int bb = 0; bb < NB; bb++) {
          const unsigned x = xr[bb][i >> 3][(i >> 1) & 3];
          acc[bb][2 * j] = dot2(pl, x, acc[bb][2 * j]);
          acc[bb][2 * j + 1] = dot2(ph, x, acc[bb][2 * j + 1]);
        }
      }
    }
    float* rp = red + (w * 4 + g) * 512 + c * 8;
#pragma unroll
    for (int bb = 0; bb < NB; bb++) {
      *(f32x4*)(rp + bb * 128) = f32x4{acc[bb][0], acc[bb][1], acc[bb][2], acc[bb][3]};
      *(f32x4*)(rp + bb * 128 + 4) = f32x4{acc[bb][4], acc[bb][5], acc[bb][6], acc[bb][7]};
    }
    __syncthreads();
    {
      float s = 0.f;
#pragma unroll
      for (int i = 0; i < 32; i++) s += red[i * 512 + t];
      const float sn = dpp<0xB1>(s);
      if ((t & 1) == 0) qs[t >> 1] = cvtpk(s, sn);
      if (t == 0) *(u64*)(lds + 2048) = epv;
    }
    __syncthreads();
    tag = next_tag(uni64(*(const u64*)(lds + 2048)));
    {
      u32x4 qq[NB];
#pragma unroll
      for (int bb = 0; bb < NB; bb++) qq[bb] = *(const u32x4*)(qs + bb * 64 + c * 4);
#pragma unroll
      for (int i = 0; i < 16; i++) {
        float s[NB];
#pragma unroll
        for (int bb = 0; bb < NB; bb++) {
          float v = dot2(wk[i][0], qq[bb][0], 0.f);
          v = dot2(wk[i][1], qq[bb][1], v);
          v = dot2(wk[i][2], qq[bb][2], v);
          v = dot2(wk[i][3], qq[bb][3], v);
          s[bb] = sum16(v);
        }
        const float v = (c & 2) ? ((c & 1) ? s[3] : s[2]) : ((c & 1) ? s[1] : s[0]);
        if (c < NB) qo[c * 512 + w * 64 + g * 16 + i] = (unsigned short)(cvtpk(v, v) & 0xffff);
      }
    }
    __syncthreads();
    if (t < 256) {
      const int bb = t >> 6, ch = t & 63;
      st16u_sc1(rs, (unsigned)(WS_QABS + ((size_t)(bb * NH + h) * DC + ch * 8) * 2), *(const u32x4*)(qo + bb * 512 + ch * 8));
    }
    TR(1);
    signal_flag(ws + WS_F1 + h * LINE, tag);
    TR(2);
  }
  const unsigned ta = (unsigned)tag, tb = (unsigned)(tag >> 32);
  {
    u32x4 bv[8][2], bo[8][2];
    u32x4 pre[2][8], wo[16];
    {
      const __amdgpu_buffer_rsrc_t ruv = mkrs(wuv, DC * HD * 2);
      const __amdgpu_buffer_rsrc_t rwo = mkrs(woA, HD * DM * 2);
      const unsigned wvo = (unsigned)(((h * DH + (l >> 3) * 16) * DM + w * 64 + (l & 7) * 8) * 2);
#pragma unroll
      for (int i = 0; i < 16; i++) pre[i >> 3][i & 7] = __builtin_amdgcn_raw_buffer_load_b128(ruv, vo - g * 8 * HD * 2, ((i >> 3) * 32 + (i & 7)) * HD * 2, NT);
#pragma unroll
      for (int i = 0; i < 16; i++) wo[i] = __builtin_amdgcn_raw_buffer_load_b128(rwo, wvo, i * DM * 2, NT);
    }
#pragma unroll
    for (int j = 0; j < 8; j++)
#pragma unroll
      for (int s = 0; s < 2; s++) {
#pragma unroll
        for (int e = 0; e < 4; e++)
          bv[j][s][e] = __builtin_amdgcn_perm(pre[s][2 * e + 1][j >> 1], pre[s][2 * e][j >> 1], (j & 1) ? 0x07060302u : 0x05040100u);
        asm volatile("" : "+v"(bv[j][s]));
      }
#pragma unroll
    for (int j = 0; j < 8; j++)
#pragma unroll
      for (int s = 0; s < 2; s++) {
#pragma unroll
        for (int e = 0; e < 4; e++)
          bo[j][s][e] = __builtin_amdgcn_perm(wo[8 * s + 2 * e + 1][j >> 1], wo[8 * s + 2 * e][j >> 1], (j & 1) ? 0x07060302u : 0x05040100u);
        asm volatile("" : "+v"(bo[j][s]));
      }
    constexpr int LAS = 1040;
    unsigned short* cs = (unsigned short*)(lds + 16384);
    float* outs = (float*)(lds + 20480);
    float* cred = (float*)(lds + 32768);
    {
    {
    TR(3);
    }
    unsigned po = (unsigned)(WS_PAO + ((size_t)(w * 4 * NH + h) * DC + l * 8) * 2);
    unsigned pc = (unsigned)(WS_PAC + (size_t)((w * 4 + g) * NH + h) * 8);
    unsigned lw_ = (unsigned)(65536 + ((w & 1) * 16 + (w >> 1) * 4) * LAS + l * 16), lr_ = (unsigned)(65536 + c * LAS + (w * 64 + g * 8) * 2);
    asm volatile("" : "+v"(po), "+v"(pc), "+v"(lw_), "+v"(lr_));
    const unsigned tw = (h & 1) ? tb : ta;
    const unsigned tg = wait_gate(ws + WS_FS, ta, (unsigned*)(lds + 8192), tent);
    TR(4);
    wait_until(tg + TM_PUT + TM_GET);
    TR(7);
    {
      u32x4 pr[4];
      unsigned ntry = 0;
#pragma unroll 1
      for (unsigned spins = 0; spins < SPIN_CAP; ++spins) {
        asm volatile("" ::: "memory");
        const u32x2 ck = __builtin_amdgcn_raw_buffer_load_b64(rs, pc, 0, SC1);
#pragma unroll
        for (int i = 0; i < 4; i++) pr[i] = __builtin_amdgcn_raw_buffer_load_b128(rs, po, i * NH * DC * 2, SC1);
#pragma unroll
        for (int i = 0; i < 4; i++) *(u32x4*)(lds + lw_ + i * LAS) = pr[i];
        ntry++;
        if (__all(row_sums(pr) == ck[0] && ck[1] == tw)) break;
        __builtin_amdgcn_s_sleep(2);
      }
      TR(5);
      trv = l == 62 ? ntry : trv;
      __syncthreads();
      u32x4 af[2][2];
#pragma unroll
      for (int tl = 0; tl < 2; tl++)
#pragma unroll
        for (int s = 0; s < 2; s++) af[tl][s] = *(const u32x4*)(lds + lr_ + tl * 16 * LAS + s * 64);
      f32x4 dd[8];
#pragma unroll
      for (int j = 0; j < 8; j++)
        dd[j] = __builtin_amdgcn_mfma_f32_16x16x32_bf16(__builtin_bit_cast(bf16x8, af[0][0]), __builtin_bit_cast(bf16x8, bv[j][0]), f32x4{0.f, 0.f, 0.f, 0.f}, 0, 0, 0);
#pragma unroll
      for (int j = 0; j < 8; j++)
        dd[j] = __builtin_amdgcn_mfma_f32_16x16x32_bf16(__builtin_bit_cast(bf16x8, af[0][1]), __builtin_bit_cast(bf16x8, bv[j][1]), dd[j], 0, 0, 0);
#pragma unroll
      for (int j = 0; j < 8; j++)
        dd[j] = __builtin_amdgcn_mfma_f32_16x16x32_bf16(__builtin_bit_cast(bf16x8, af[1][0]), __builtin_bit_cast(bf16x8, bv[j][0]), dd[j], 0, 0, 0);
#pragma unroll
      for (int j = 0; j < 8; j++)
        dd[j] = __builtin_amdgcn_mfma_f32_16x16x32_bf16(__builtin_bit_cast(bf16x8, af[1][1]), __builtin_bit_cast(bf16x8, bv[j][1]), dd[j], 0, 0, 0);
      f32x4 v[2];
#pragma unroll
      for (int j = 0; j < 8; j++) v[j >> 2][j & 3] = (dd[j][0] + dd[j][1]) + (dd[j][2] + dd[j][3]);
      *(f32x4*)(cred + (w * 4 + g) * 128 + c * 8) = v[0];
      *(f32x4*)(cred + (w * 4 + g) * 128 + c * 8 + 4) = v[1];
    }
    __syncthreads();
    {
      float s = 0.f;
#pragma unroll
      for (int i = 0; i < 8; i++) s += cred[i * 512 + t];
      const float sn = dpp<0xB1>(s);
      if ((t & 1) == 0) *(unsigned*)(cs + t) = cvtpk(s, sn);
    }
    __syncthreads();
    {
      const char* ap = (const char*)cs + ((c >> 1) & 3) * 256 + (g * 32 + (c & 1) * 16) * 2;
      const u32x4 a0 = *(const u32x4*)(ap), a1 = *(const u32x4*)(ap + 16);
      f32x4 dd[8];
#pragma unroll
      for (int j = 0; j < 8; j++)
        dd[j] = __builtin_amdgcn_mfma_f32_16x16x32_bf16(__builtin_bit_cast(bf16x8, a0), __builtin_bit_cast(bf16x8, bo[j][0]), f32x4{0.f, 0.f, 0.f, 0.f}, 0, 0, 0);
#pragma unroll
      for (int j = 0; j < 8; j++)
        dd[j] = __builtin_amdgcn_mfma_f32_16x16x32_bf16(__builtin_bit_cast(bf16x8, a1), __builtin_bit_cast(bf16x8, bo[j][1]), dd[j], 0, 0, 0);
      f32x4 v4[2];
#pragma unroll
      for (int j = 0; j < 8; j++) {
        const float u0 = (l & 8) ? dd[j][1] : dd[j][0], u1 = (l & 8) ? dd[j][3] : dd[j][2];
        const float keep = (l & 8) ? u1 : u0, give = (l & 8) ? u0 : u1;
        v4[j >> 2][j & 3] = keep + dpp<0x128>(give);
      }
      if (g < 2) {
        float* op = outs + (g * 2 + ((l >> 3) & 1)) * 512 + w * 64 + (l & 7) * 8;
        *(f32x4*)op = v4[0];
        *(f32x4*)(op + 4) = v4[1];
      }
    }
    __syncthreads();
    {
      const f32x2 va = *(const f32x2*)(outs + t * 2), vb = *(const f32x2*)(outs + 1024 + t * 2);
      TR(8);
      wait_until(tg + TM_PA);
      const unsigned oo = (unsigned)(WS_PA + ((size_t)((t >> 8) * NH + h) * DM + (t & 255) * 2) * 8);
      st16u_sc1(rs, oo, u32x4{__float_as_uint(va[0]), ta, __float_as_uint(va[1]), tb});
      st16u_sc1(rs, oo + 2 * NH * DM * 8, u32x4{__float_as_uint(vb[0]), ta, __float_as_uint(vb[1]), tb});
    }
  TR(6);
  if (w < 4) {
    const int bb = h >> 3, n0 = (h & 7) * 64 + w * 16;
    const unsigned og = (unsigned)(WS_PA + ((size_t)(bb * NH + (l >> 3)) * DM + n0) * 8 + (l & 7) * 16);
    u32x4 p[4];
    wait_until(tg + TM_PA + TM_T4);
#pragma unroll 1
    for (unsigned spins = 0; spins < SPIN_CAP; ++spins) {
      asm volatile("" ::: "memory");
#pragma unroll
      for (int i = 0; i < 4; i++) p[i] = ld16u_sc1(rs, og + i * 8 * DM * 8);
      bool ok = true;
#pragma unroll
      for (int i = 0; i < 4; i++) ok = ok && p[i][1] == ta && p[i][3] == tb;
      if (__all(ok)) break;
      __builtin_amdgcn_s_sleep(4);
    }
    float v0 = (__uint_as_float(p[0][0]) + __uint_as_float(p[1][0])) + (__uint_as_float(p[2][0]) + __uint_as_float(p[3][0]));
    float v1 = (__uint_as_float(p[0][2]) + __uint_as_float(p[1][2])) + (__uint_as_float(p[2][2]) + __uint_as_float(p[3][2]));
    v0 += dpp<0x128>(v0); v0 = xrow_sum(v0);
    v1 += dpp<0x128>(v1); v1 = xrow_sum(v1);
    if (l < 8) {
      *(unsigned*)(out + bb * 1024 + n0 + l * 2) = cvtpk(v0, v1);
      if (h == 0 && t == 0) st_u64(ws + WS_EPA, tag);
    }
  }
    }
  }
  TR(43);
  TRDUMP();
}

__device__ __forceinline__ void role_A2(int a, const __hip_bfloat16* __restrict__ ckv, char* __restrict__ ws,
                                        __amdgpu_buffer_rsrc_t rs, char* lds, unsigned trv) {
  const int t = threadIdx.x, l = t & 63;
  const int w = __builtin_amdgcn_readfirstlane(t >> 6);
  const int g = l >> 4, c = l & 15;
  const unsigned tent = clk();
  u64 ep = 0;
  if (t == 0) ep = ld_u64(ws + WS_EPA);
  const int grp = w >> 2, wq = w & 3;
  const int b = a / SPLIT_A, sp = a % SPLIT_A;
  const int r = l & 31, hh = l >> 5;
  constexpr unsigned CB = (unsigned)((size_t)NB * SK * DC * 2);
  constexpr int NTL = KA / 64;
  const unsigned cvo = (unsigned)((g * DC + 128 * wq + c * 8) * 2);
  const unsigned cso = (unsigned)(((size_t)b * SK + sp * KA + grp * 32) * DC * 2);
  auto load_tile = [&](u32x4 (&pr)[8], int tile) {
    const bool ok = tile < NTL;
    const __amdgpu_buffer_rsrc_t rc = mkrs(ckv, ok ? CB : 0u);
    const unsigned so = ok ? cso + (unsigned)((tile + sp) & (NTL - 1)) * 64 * DC * 2 : 0u;
#pragma unroll
    for (int u = 0; u < 8; u++) pr[u] = __builtin_amdgcn_raw_buffer_load_b128(rc, cvo, so + u * 4 * DC * 2, NT);
  };
  char* img = lds + w * 8192;
  float* sred = (float*)(lds + 65536 + grp * 16384);
  float* mxs = (float*)(lds + 98304 + grp * 512);
  unsigned* pbuf = (unsigned*)(lds + 102400 + grp * 2048);
  float* lall = (float*)(lds + 106496);
  const unsigned imga = (unsigned)(uintptr_t)(__attribute__((address_space(3))) char*)img;
  auto put_tile = [&](u32x4 (&pr)[8]) {
#pragma unroll
    for (int u = 0; u < 8; u++) *(u32x4*)(img + lds_off(4 * u + g, c)) = pr[u];
  };
  u32x4 pre[2][8];
  load_tile(pre[0], 0);
  load_tile(pre[1], 1);
  const u64 tag0 = next_tag(uni64(ep));
  put_tile(pre[0]);
  __builtin_amdgcn_sched_barrier(0);
  load_tile(pre[0], 2);
  unsigned pf[4];
#pragma unroll
  for (int i = 0; i < 4; i++)
    pf[i] = __builtin_amdgcn_raw_buffer_load_b32(mkrs(ckv, CB), (unsigned)t * 64, (unsigned)(a * 8 + ((3 + (i >> 1) + a) & 7)) * 65536u + (i & 1) * 32768u, 0);
  TR(1);
  if (t < 64) {
    wait_until(tent + TM_F1);
#pragma unroll 1
    for (unsigned spins = 0; spins < SPIN_CAP; ++spins) {
      __builtin_amdgcn_s_sleep(32);
      if (__all(ld_u64(ws + WS_F1 + (l & 31) * LINE) == tag0)) break;
    }
  }
  if (t == 0) *(u64*)(lds + 110592) = tag0;
  asm volatile("" :: "v"(pf[0]), "v"(pf[1]), "v"(pf[2]), "v"(pf[3]));
  __syncthreads();
  const u64 tag = uni64(*(const u64*)(lds + 110592));
  const unsigned ta = (unsigned)tag, tb = (unsigned)(tag >> 32);
  TR(3);
  bf16x8 qf[8];
  {
    char* qi = lds + 65536;
    const unsigned qo = (unsigned)(WS_QABS + (size_t)b * NH * DC * 2 + t * 16);
    u32x4 qv[4];
#pragma unroll
    for (int i = 0; i < 4; i++) qv[i] = ld16u_sc1(rs, qo + i * 8192);
#pragma unroll
    for (int i = 0; i < 4; i++) *(u32x4*)(qi + (i * 8 + w) * 1040 + l * 16) = qv[i];
    __syncthreads();
#pragma unroll
    for (int s = 0; s < 8; s++) qf[s] = __builtin_bit_cast(bf16x8, *(const u32x4*)(qi + r * 1040 + (128 * wq + 16 * s + 8 * hh) * 2));
#pragma unroll
    for (int s = 0; s < 8; s++) asm volatile("" : "+v"(qf[s]));
    __syncthreads();
  }
  f32x16 o[4];
#pragma unroll
  for (int i = 0; i < 4; i++)
#pragma unroll
    for (int j = 0; j < 16; j++) o[i][j] = 0.f;
  float m = -INFINITY, lsum = 0.f;
  const int q4 = (l & 15) >> 2, p4 = l & 3, colh = g & 1;
  auto tile_body = [&](int tile, u32x4 (&nx)[8]) {
    f32x16 s;
#pragma unroll
    for (int j = 0; j < 16; j++) s[j] = 0.f;
    {
      u32x4 af[8];
#pragma unroll
      for (int ks = 0; ks < 8; ks++) af[ks] = *(const u32x4*)(img + lds_off(r, 2 * ks + hh));
      __builtin_amdgcn_sched_barrier(0);
#pragma unroll
      for (int ks = 0; ks < 8; ks++) s = __builtin_amdgcn_mfma_f32_32x32x16_bf16(__builtin_bit_cast(bf16x8, af[ks]), qf[ks], s, 0, 0, 0);
    }
    f32x4* sr = (f32x4*)sred;
#pragma unroll
    for (int k4 = 0; k4 < 4; k4++) sr[(k4 * 4 + wq) * 64 + l] = f32x4{s[4 * k4], s[4 * k4 + 1], s[4 * k4 + 2], s[4 * k4 + 3]};
    __syncthreads();
    float x[4];
    {
      const f32x4 v = (sr[(wq * 4) * 64 + l] + sr[(wq * 4 + 1) * 64 + l]) + (sr[(wq * 4 + 2) * 64 + l] + sr[(wq * 4 + 3) * 64 + l]);
#pragma unroll
      for (int e = 0; e < 4; e += 2) {
        const unsigned r1 = cvtpk(v[e], v[e + 1]);
        const unsigned r2 = cvtpk(bf_lo(r1) * SCALE, bf_hi(r1) * SCALE);
        x[e] = bf_lo(r2); x[e + 1] = bf_hi(r2);
      }
      float mq = fmaxf(fmaxf(x[0], x[1]), fmaxf(x[2], x[3]));
      auto sx = __builtin_amdgcn_permlane32_swap(__float_as_uint(mq), __float_as_uint(mq), false, false);
      mq = fmaxf(__uint_as_float(sx[0]), __uint_as_float(sx[1]));
      if (hh == 0) mxs[r * 4 + wq] = mq;
    }
    __syncthreads();
    const f32x4 m4 = *(const f32x4*)(mxs + r * 4);
    const float mn = fmaxf(fmaxf(m, fmaxf(m4[0], m4[1])), fmaxf(m4[2], m4[3]));
    const float alpha = fexp2((m - mn) * LOG2E);
    {
      const unsigned p0 = cvtpk(fexp2((x[0] - mn) * LOG2E), fexp2((x[1] - mn) * LOG2E));
      const unsigned p1 = cvtpk(fexp2((x[2] - mn) * LOG2E), fexp2((x[3] - mn) * LOG2E));
      *(u32x2*)(pbuf + l * 8 + wq * 2) = u32x2{p0, p1};
      lsum = lsum * alpha + ((bf_lo(p0) + bf_hi(p0)) + (bf_lo(p1) + bf_hi(p1)));
    }
    m = mn;
    __syncthreads();
    unsigned pk[8];
    {
      const u32x4 a0 = *(const u32x4*)(pbuf + l * 8), a1 = *(const u32x4*)(pbuf + l * 8 + 4);
      pk[0] = a0[0]; pk[1] = a0[1]; pk[2] = a0[2]; pk[3] = a0[3]; pk[4] = a1[0]; pk[5] = a1[1]; pk[6] = a1[2]; pk[7] = a1[3];
    }
    if (__any(alpha != 1.f)) {
#pragma unroll
      for (int i = 0; i < 4; i++)
#pragma unroll
        for (int j = 0; j < 16; j++) o[i][j] *= alpha;
    }
#pragma unroll
    for (int s2 = 0; s2 < 2; s2++) {
      u32x2 tv[4][2];
#pragma unroll
      for (int ct = 0; ct < 4; ct++) {
        const int ch = 4 * ct + 2 * colh + (p4 >> 1);
        const unsigned a0 = lds_off(4 * hh + q4, ch) + 8 * (p4 & 1);
        const unsigned a1 = lds_off(8 + 4 * hh + q4, ch) + 8 * (p4 & 1);
        if (s2 == 0) {
          asm volatile("ds_read_b64_tr_b16 %0, %1" : "=v"(tv[ct][0]) : "v"(imga + a0) : "memory");
          asm volatile("ds_read_b64_tr_b16 %0, %1" : "=v"(tv[ct][1]) : "v"(imga + a1) : "memory");
        } else {
          asm volatile("ds_read_b64_tr_b16 %0, %1 offset:4096" : "=v"(tv[ct][0]) : "v"(imga + a0) : "memory");
          asm volatile("ds_read_b64_tr_b16 %0, %1 offset:4096" : "=v"(tv[ct][1]) : "v"(imga + a1) : "memory");
        }
      }
      asm volatile("s_waitcnt lgkmcnt(0)" ::: "memory");
      __builtin_amdgcn_sched_barrier(0);
      u32x4 pb = {pk[4 * s2], pk[4 * s2 + 1], pk[4 * s2 + 2], pk[4 * s2 + 3]};
#pragma unroll
      for (int ct = 0; ct < 4; ct++) {
        u32x4 av = {tv[ct][0][0], tv[ct][0][1], tv[ct][1][0], tv[ct][1][1]};
        o[ct] = __builtin_amdgcn_mfma_f32_32x32x16_bf16(__builtin_bit_cast(bf16x8, av), __builtin_bit_cast(bf16x8, pb), o[ct], 0, 0, 0);
      }
    }
    put_tile(nx);
    __builtin_amdgcn_sched_barrier(0);
    load_tile(nx, tile + 3);
    TRR(4 + tile);
  };
#pragma unroll 1
  for (int tile = 0; tile < NTL; tile += 2) {
    tile_body(tile, pre[1]);
    tile_body(tile + 1, pre[0]);
  }
  constexpr int OST = 516;
  float* ob = (float*)lds;
  float* mlb = (float*)(lds + 32 * OST * 4);
  lall[w * 64 + l] = lsum;
  __syncthreads();
  {
    const float* lp = lall + grp * 256 + r;
    lsum = ((lp[0] + lp[32]) + (lp[64] + lp[96])) + ((lp[128] + lp[160]) + (lp[192] + lp[224]));
  }
  if (grp == 1) {
#pragma unroll
    for (int ct = 0; ct < 4; ct++)
#pragma unroll
      for (int j4 = 0; j4 < 4; j4++)
        *(f32x4*)(ob + r * OST + 128 * wq + 32 * ct + 8 * j4 + 4 * hh) =
            f32x4{o[ct][4 * j4], o[ct][4 * j4 + 1], o[ct][4 * j4 + 2], o[ct][4 * j4 + 3]};
    if (wq == 0 && l < 32) { mlb[2 * r] = m; mlb[2 * r + 1] = lsum; }
  }
  __syncthreads();
  if (grp == 0) {
    const float m1 = mlb[2 * r], l1 = mlb[2 * r + 1];
    const float M = fmaxf(m, m1);
    const float f0 = fexp2((m - M) * LOG2E), f1 = fexp2((m1 - M) * LOG2E);
#pragma unroll
    for (int ct = 0; ct < 4; ct++)
#pragma unroll
      for (int j4 = 0; j4 < 4; j4++) {
        float* p = ob + r * OST + 128 * wq + 32 * ct + 8 * j4 + 4 * hh;
        const f32x4 o1 = *(const f32x4*)p;
        *(f32x4*)p = f32x4{o[ct][4 * j4] * f0 + o1[0] * f1, o[ct][4 * j4 + 1] * f0 + o1[1] * f1,
                           o[ct][4 * j4 + 2] * f0 + o1[2] * f1, o[ct][4 * j4 + 3] * f0 + o1[3] * f1};
      }
    if (wq == 0 && l < 32)
      st16u_sc1(rs, (unsigned)(WS_PAML + ((size_t)(b * SPLIT_A + sp) * NH + r) * 16),
                u32x4{__float_as_uint(M), ta, __float_as_uint(lsum * f0 + l1 * f1), tb});
  }
  float* wgt = mlb + 64;
  unsigned* pcs = (unsigned*)(mlb + 128);
  const unsigned tg = wait_gate(ws + WS_FS, ta, (unsigned*)(mlb + 96), tent);
  TR(18);
  if (t < 256) {
    wait_until(tg + TM_ML);
    const unsigned mo = (unsigned)(WS_PAML + ((size_t)(b * SPLIT_A + (t & 7)) * NH + (t >> 3)) * 16);
    u32x4 mv;
#pragma unroll 1
    for (unsigned spins = 0; spins < SPIN_CAP; ++spins) {
      asm volatile("" ::: "memory");
      mv = ld16u_sc1(rs, mo);
      if (__all(mv[1] == ta && mv[3] == tb)) break;
      __builtin_amdgcn_s_sleep(16);
    }
    const float mm = __uint_as_float(mv[0]);
    float M = fmaxf(mm, dpp<0xB1>(mm)); M = fmaxf(M, dpp<0x4E>(M)); M = fmaxf(M, dpp<0x141>(M));
    const float f = fexp2((mm - M) * LOG2E);
    float L = f * __uint_as_float(mv[2]);
    L += dpp<0xB1>(L); L += dpp<0x4E>(L); L += dpp<0x141>(L);
    if ((t & 7) == sp) wgt[t >> 3] = f / L;
  }
  __syncthreads();
  {
    const unsigned pao = (unsigned)(WS_PAO + (size_t)(b * SPLIT_A + sp) * NH * DC * 2);
    u32x4 pv[4];
#pragma unroll
    for (int i = 0; i < 4; i++) {
      const int hd = i * 8 + w;
      const float f = wgt[hd];
      const f32x4 v0 = *(const f32x4*)(ob + hd * OST + l * 8) * f, v1 = *(const f32x4*)(ob + hd * OST + l * 8 + 4) * f;
      pv[i] = u32x4{cvtpk(v0[0], v0[1]), cvtpk(v0[2], v0[3]), cvtpk(v1[0], v1[1]), cvtpk(v1[2], v1[3])};
      asm volatile("" : "+v"(pv[i]));
    }
    {
      const unsigned cs = row_sums(pv);
      if (c == 0) pcs[g * 8 + w] = cs;
    }
    TR(19);
    __syncthreads();
    const u32x2 c2 = *(const u32x2*)(pcs + (t & 15) * 2);
    const unsigned co = t < 16 ? (unsigned)(WS_PAC + (size_t)(b * SPLIT_A + sp) * NH * 8 + t * 16) : 0x7ffffff0u;
    wait_until(tg + TM_PUT);
    TR(17);
#pragma unroll
    for (int i = 0; i < 4; i++) st16u_sc1(rs, pao + (unsigned)(i * 512 + t) * 16, pv[i]);
    if (w == 0) st16u_sc1(rs, co, u32x4{c2[0], ta, c2[1], tb});
  }
  TR(20);
  asm volatile("s_waitcnt vmcnt(0)" ::: "memory");
  TR(43);
  TRDUMP();
}

__global__ __launch_bounds__(512, 2) void k_fused(const __hip_bfloat16* __restrict__ xA, const __hip_bfloat16* __restrict__ ckv,
                                                  const __hip_bfloat16* __restrict__ wqA, const __hip_bfloat16* __restrict__ wuk,
                                                  const __hip_bfloat16* __restrict__ wuv, const __hip_bfloat16* __restrict__ woA,
                                                  const __hip_bfloat16* __restrict__ xB, const __hip_bfloat16* __restrict__ kc,
                                                  const __hip_bfloat16* __restrict__ vc, const __hip_bfloat16* __restrict__ wqB,
                                                  const __hip_bfloat16* __restrict__ woB, char* __restrict__ ws,
                                                  __hip_bfloat16* __restrict__ out) {
  __shared__ __attribute__((aligned(16))) char lds[131072];
  unsigned trv = 0;
  u64 tfirst; asm volatile("s_memrealtime %0" : "=s"(tfirst));
  TR(0);
  asm volatile("s_waitcnt lgkmcnt(0)" : "+s"(tfirst));
  trv = (threadIdx.x & 63) == 59 ? (unsigned)tfirst : trv;
  { unsigned xid; asm volatile("s_getreg_b32 %0, hwreg(HW_REG_XCC_ID)" : "=s"(xid)); trv = (threadIdx.x & 63) == 63 ? (xid & 15) + 1 : trv; trv = (threadIdx.x & 63) == 60 ? (unsigned)__builtin_amdgcn_s_memtime() : trv; }
  asm volatile("" :: "s"(xA), "s"(ckv), "s"(wqA), "s"(wuk), "s"(wuv), "s"(woA), "s"(xB), "s"(kc), "s"(vc), "s"(wqB), "s"(woB), "s"(ws), "s"(out));
  TRR(58);
  const int blk = blockIdx.x, xcd = blk & 7, r = blk >> 3;
  unsigned pfv = 0;
  {
    u64 pc; unsigned len;
    asm volatile("s_getpc_b64 %0\n.Lcode_begin:\n\ts_mov_b32 %1, .Lcode_end-.Lcode_begin" : "=s"(pc), "=s"(len));
    const unsigned idx = r < 5 ? (unsigned)((r * 8 + (threadIdx.x >> 6) + xcd * 5) % 40) : 1000u;
    const __amdgpu_buffer_rsrc_t rc = mkrs((const void*)pc, len);
    pfv = __builtin_amdgcn_raw_buffer_load_b32(rc, idx * 1024 + (threadIdx.x & 63) * 16, 0, 0);
  }
  const __amdgpu_buffer_rsrc_t rs = mkrs(ws, (unsigned)WS_END);
  if (r < 8) {
    if (r < 4) role_A1(xcd * 4 + r, xA, wqA, wuk, wuv, woA, ws, rs, out, lds, trv);
    else role_A2(xcd * 4 + r - 4, ckv, ws, rs, lds, trv);
  } else {
    const int rr = r - 8, hq = rr / 6;
    role_B(((0x75643120u >> (4 * xcd)) & 7) + 8 * hq, rr - 6 * hq, xcd, ckv, xB, kc, vc, wqB, woB, ws, rs, out, lds, trv);
  }

  if (blockIdx.x == 0 && threadIdx.x < 64) {
    const unsigned t00 = __builtin_amdgcn_readfirstlane(trv);
    while ((unsigned)__builtin_amdgcn_s_memrealtime() - t00 < 7500u) __builtin_amdgcn_s_sleep(1);
    const int l = threadIdx.x;
    auto ld = [&](int blk, int wv, int idx) { return __hip_atomic_load((gu32*)(ws + WS_TR) + (blk * 8 + wv) * 64 + idx, __ATOMIC_RELAXED, __HIP_MEMORY_SCOPE_AGENT); };
    auto agg = [&](int idx, int b0, int b1, int kind) {
      int sum = 0, cnt = 0, mx = -(1 << 30), mn = 1 << 30;
      for (int i = 0; i < 4; i++) {
        const int blk = l + 64 * i;
        if (blk < b0 || blk >= b1) continue;
        int v = -(1 << 30);
        for (int wv = 0; wv < 8; wv++) { const unsigned u = ld(blk, wv, idx); if (u) { const int d = (int)(u - t00); v = d > v ? d : v; } }
        if (v > -(1 << 29)) { sum += v; cnt++; mx = v > mx ? v : mx; mn = v < mn ? v : mn; }
      }
      for (int o = 32; o > 0; o >>= 1) { sum += __shfl_xor(sum, o); cnt += __shfl_xor(cnt, o); int v = __shfl_xor(mx, o); mx = v > mx ? v : mx; v = __shfl_xor(mn, o); mn = v < mn ? v : mn; }
      return kind == 0 ? sum / (cnt ? cnt : 1) : kind == 1 ? mx : mn;
    };
    int f = agg(EX_IA, EX_A0, EX_A1, EX_GA) - agg(EX_IB, EX_B0, EX_B1, EX_GB) + EX_OFF;
    if (f < 0) f = 0;
    if (f > 2000) f = 2000;
    while ((unsigned)__builtin_amdgcn_s_memrealtime() - t00 < 10000u + (unsigned)(EX_MUL * f)) __builtin_amdgcn_s_sleep(1);
  }
  asm volatile(".Lcode_end:" :: "v"(pfv));
}

extern "C" void kernel_launch(void* const* d_in, const int* in_sizes, int n_in,
                              void* d_out, int out_size, void* d_ws, size_t ws_size,
                              hipStream_t stream) {
  k_fused<<<256, 512, 0, stream>>>((const __hip_bfloat16*)d_in[0], (const __hip_bfloat16*)d_in[1], (const __hip_bfloat16*)d_in[2],
                                   (const __hip_bfloat16*)d_in[3], (const __hip_bfloat16*)d_in[4], (const __hip_bfloat16*)d_in[5],
                                   (const __hip_bfloat16*)d_in[6], (const __hip_bfloat16*)d_in[7], (const __hip_bfloat16*)d_in[8],
                                   (const __hip_bfloat16*)d_in[9], (const __hip_bfloat16*)d_in[10], (char*)d_ws,
                                   (__hip_bfloat16*)d_out);
}
```

```cpp
#define TRACE
#define LTRACE
#define T_SW 1705
#define EX_IA 43
#define EX_A0 0
#define EX_A1 256
#define EX_GA 1
#define EX_IB 48
#define EX_B0 128
#define EX_B1 136
#define EX_GB 0
#define EX_OFF -1500
#define EX_MUL 10
#include <hip/hip_runtime.h>
#include <hip/hip_bf16.h>
#include <stdint.h>

typedef __attribute__((ext_vector_type(4))) unsigned u32x4;
typedef __attribute__((ext_vector_type(2))) unsigned u32x2;
typedef __attribute__((ext_vector_type(4))) float f32x4;
typedef __attribute__((ext_vector_type(2))) float f32x2;
typedef __attribute__((ext_vector_type(3))) float f32x3;
typedef __attribute__((ext_vector_type(16))) float f32x16;
typedef __attribute__((ext_vector_type(8))) __bf16 bf16x8;
typedef __attribute__((ext_vector_type(2))) __bf16 bf16x2;
typedef __attribute__((address_space(1))) unsigned gu32;
typedef __attribute__((address_space(1))) unsigned long long gu64;
typedef unsigned long long u64;

constexpr int NB = 4;
constexpr int NH = 32;
constexpr int SK = 4096;
constexpr int DH = 128;
constexpr int DM = 512;
constexpr int DC = 512;
constexpr int HD = NH * DH;

constexpr int SPLIT_A = 8;
constexpr int KA = SK / SPLIT_A;
constexpr int SS = 171;
constexpr int NI = 86;
constexpr int N0 = 43;
#define RDD 9
constexpr int RD = RDD;
constexpr int NP = NI - 8 * RD + 8;
constexpr int NP4 = (NP + 3) & ~3;
static_assert(8 * RD + 8 <= NI, "");
constexpr int RST = 144;
#define PRIOFD 4
#define SCH_R 4
#define SCH_0 -355
#define SCH_S 338
#define XW0 16
#define XWS 16
constexpr int RE = RD - 1;
constexpr int PRIOF = PRIOFD;

#define T_F1 1600
#define T_FC 3200
#define T_ML 1040
#define T_PUT 1160
#define T_GET 110
#define T_PA 1625
#define T_T4 60
#define XR 48
#define XQ 76
#define R1C 1060
#define XSTEP 72
#define STEP 30
constexpr unsigned TM_F1 = T_F1;
constexpr unsigned TM_FC = T_FC;
constexpr unsigned TM_ML = T_ML;
constexpr unsigned TM_PUT = T_PUT;
constexpr unsigned TM_GET = T_GET;
constexpr unsigned TM_PA = T_PA;
constexpr unsigned TM_T4 = T_T4;
constexpr unsigned TM_SW = T_SW;

constexpr float SCALE = 0.08838834764831845f;
constexpr float LOG2E = 1.4426950408889634f;
constexpr int NT = 2;
constexpr int SC1 = 16;

constexpr size_t WS_EPA  = 0;
constexpr size_t WS_EPB  = 256;
constexpr int LINE = 256;
constexpr size_t WS_F1   = 4096;
constexpr size_t WS_FS   = 12288;
constexpr size_t WS_FK   = 14336;
constexpr size_t WS_QABS = 16384;
constexpr size_t WS_PAML = WS_QABS + (size_t)NB * NH * DC * 2;
constexpr size_t WS_PAC  = WS_PAML + (size_t)NB * SPLIT_A * NH * 16;
constexpr size_t WS_PAO  = WS_PAC + (size_t)NB * SPLIT_A * NH * 8;
constexpr size_t WS_PA   = WS_PAO + (size_t)NB * SPLIT_A * NH * DC * 2;
constexpr size_t WS_PBG  = WS_PA + (size_t)NB * NH * DM * 8;
constexpr size_t WS_END  = WS_PBG + (size_t)NB * 64 * DM * 8;

__device__ __forceinline__ float bf_lo(unsigned u) { return __uint_as_float(u << 16); }
__device__ __forceinline__ float bf_hi(unsigned u) { return __uint_as_float(u & 0xffff0000u); }
__device__ __forceinline__ unsigned cvtpk(float lo, float hi) {
  f32x2 v = {lo, hi};
  return __builtin_bit_cast(unsigned, __builtin_convertvector(v, bf16x2));
}
__device__ __forceinline__ float rbf(float x) { return __uint_as_float(cvtpk(x, x) << 16); }
__device__ __forceinline__ float dot2(unsigned k, unsigned q, float acc) {
  return __builtin_amdgcn_fdot2_f32_bf16(__builtin_bit_cast(bf16x2, k), __builtin_bit_cast(bf16x2, q), acc, false);
}
template <int CTRL> __device__ __forceinline__ float dpp(float x) {
  return __builtin_bit_cast(float, __builtin_amdgcn_mov_dpp(__builtin_bit_cast(int, x), CTRL, 0xf, 0xf, true));
}
__device__ __forceinline__ float sum16(float x) {
  x += dpp<0x128>(x);
  x += dpp<0x124>(x);
  x += dpp<0x4E>(x);
  x += dpp<0xB1>(x);
  return x;
}
__device__ __forceinline__ float xrow_max(float x) {
  auto s = __builtin_amdgcn_permlane16_swap(__float_as_uint(x), __float_as_uint(x), false, false);
  x = fmaxf(__uint_as_float(s[0]), __uint_as_float(s[1]));
  auto t = __builtin_amdgcn_permlane32_swap(__float_as_uint(x), __float_as_uint(x), false, false);
  return fmaxf(__uint_as_float(t[0]), __uint_as_float(t[1]));
}
__device__ __forceinline__ float xrow_sum(float x) {
  auto s = __builtin_amdgcn_permlane16_swap(__float_as_uint(x), __float_as_uint(x), false, false);
  x = __uint_as_float(s[0]) + __uint_as_float(s[1]);
  auto t = __builtin_amdgcn_permlane32_swap(__float_as_uint(x), __float_as_uint(x), false, false);
  return __uint_as_float(t[0]) + __uint_as_float(t[1]);
}
__device__ __forceinline__ float fexp2(float x) { return __builtin_amdgcn_exp2f(x); }
__device__ __forceinline__ __amdgpu_buffer_rsrc_t mkrs(const void* p, unsigned bytes) {
  return __builtin_amdgcn_make_buffer_rsrc((void*)p, 0, (int)bytes, 0x00020000);
}

constexpr size_t WS_TR = (size_t)64 << 20;
#define TR(i) do { __builtin_amdgcn_sched_barrier(0); { const unsigned ts_ = (unsigned)__builtin_amdgcn_s_memrealtime(); trv = (int)(threadIdx.x & 63) == (i) ? ts_ : trv; } __builtin_amdgcn_sched_barrier(0); } while (0)
#define TRDUMP() trv = (threadIdx.x & 63) == 61 ? (unsigned)__builtin_amdgcn_s_memtime() : trv; __hip_atomic_store((gu32*)(ws + WS_TR) + (blockIdx.x * 8 + (threadIdx.x >> 6)) * 64 + (threadIdx.x & 63), trv, __ATOMIC_RELAXED, __HIP_MEMORY_SCOPE_AGENT)
#define TRR(i) do {} while (0)
constexpr unsigned SPIN_CAP = 1u << 21;
__device__ __forceinline__ u64 ld_u64(const void* p) {
  return __hip_atomic_load((gu64*)p, __ATOMIC_RELAXED, __HIP_MEMORY_SCOPE_AGENT);
}
__device__ __forceinline__ void st_u64(void* p, u64 v) {
  __hip_atomic_store((gu64*)p, v, __ATOMIC_RELAXED, __HIP_MEMORY_SCOPE_AGENT);
}
__device__ __forceinline__ u64 next_tag(u64 e) { return e + 0x0000000100000001ull; }
__device__ __forceinline__ u64 uni64(u64 v) {
  const unsigned lo = __builtin_amdgcn_readfirstlane((unsigned)v), hi = __builtin_amdgcn_readfirstlane((unsigned)(v >> 32));
  return ((u64)hi << 32) | lo;
}
__device__ __forceinline__ void signal_flag(char* flag, u64 tag) {
  asm volatile("s_waitcnt vmcnt(0)" ::: "memory");
  __syncthreads();
  if (threadIdx.x == 0) st_u64(flag, tag);
}
template <int CTRL> __device__ __forceinline__ int dppi(int x) { return __builtin_amdgcn_mov_dpp(x, CTRL, 0xf, 0xf, true); }
__device__ __forceinline__ int sum8i(int x) { x += dppi<0xB1>(x); x += dppi<0x4E>(x); x += dppi<0x141>(x); return x; }
__device__ __forceinline__ unsigned row_sums(const u32x4 (&p)[4]) {
  unsigned c4[4];
#pragma unroll
  for (int i = 0; i < 4; i++) c4[i] = (p[i][0] + p[i][1]) + (p[i][2] + p[i][3]);
  auto s0 = __builtin_amdgcn_permlane32_swap(c4[0], c4[2], false, false);
  auto s1 = __builtin_amdgcn_permlane32_swap(c4[1], c4[3], false, false);
  auto s2 = __builtin_amdgcn_permlane16_swap(s0[0] + s0[1], s1[0] + s1[1], false, false);
  int x = (int)(s2[0] + s2[1]);
  x += dppi<0x128>(x);
  return (unsigned)sum8i(x);
}
__device__ __forceinline__ unsigned clk() { return (unsigned)__builtin_amdgcn_s_memrealtime(); }
__device__ __forceinline__ void wait_until(unsigned t) {
#pragma unroll 1
  for (int spins = 0; spins < 4096 && (int)(t - clk()) > 0; ++spins) __builtin_amdgcn_s_sleep(1);
}
__device__ __forceinline__ unsigned ref_time(unsigned v) {
  const unsigned t0 = __builtin_amdgcn_readfirstlane(v);
  return t0 + (unsigned)__builtin_amdgcn_readfirstlane(sum8i((int)(v - t0)) >> 3);
}
__device__ __forceinline__ unsigned wait_gate(const char* flag, unsigned ta, unsigned* slot, unsigned tent) {
  if (threadIdx.x < 64) {
    u64 v = 0;
    wait_until(tent + TM_FC);
#pragma unroll 1
    for (unsigned spins = 0; spins < SPIN_CAP; ++spins) {
      v = ld_u64(flag + (threadIdx.x & 7) * LINE);
      if (__all((unsigned)(v >> 32) == ta)) break;
      __builtin_amdgcn_s_sleep(12);
    }
    const unsigned tr = ref_time((unsigned)v);
    if (threadIdx.x == 0) *slot = tr;
  }
  __syncthreads();
  return __builtin_amdgcn_readfirstlane(*slot);
}
__device__ __forceinline__ void st16u_sc1(__amdgpu_buffer_rsrc_t rs, unsigned off, u32x4 v) {
  __builtin_amdgcn_raw_buffer_store_b128(v, rs, off, 0, SC1);
}
__device__ __forceinline__ u32x4 ld16u_sc1(__amdgpu_buffer_rsrc_t rs, unsigned off) {
  return __builtin_amdgcn_raw_buffer_load_b128(rs, off, 0, SC1);
}

__device__ __forceinline__ void item_dots(const u32x4 (&ks)[4], const u32x4 q, float (&s_)[4]) {
#pragma unroll
  for (int u = 0; u < 4; u++) {
    float a = dot2(ks[u][0], q[0], 0.f);
    a = dot2(ks[u][1], q[1], a);
    a = dot2(ks[u][2], q[2], a);
    a = dot2(ks[u][3], q[3], a);
    s_[u] = a;
    asm volatile("" : "+v"(s_[u]));
  }
}
__device__ __forceinline__ void item_unpack(const u32x4 (&vs)[4], f32x2 (&vf)[4][4]) {
#pragma unroll
  for (int u = 0; u < 4; u++)
#pragma unroll
    for (int j = 0; j < 4; j++) { vf[u][j] = f32x2{bf_lo(vs[u][j]), bf_hi(vs[u][j])}; asm volatile("" : "+v"(vf[u][j])); }
}
__device__ __forceinline__ f32x2 rows_sum(const float (&o)[8]) {
  float r4[4], r2[2];
#pragma unroll
  for (int j = 0; j < 4; j++) {
    auto sx = __builtin_amdgcn_permlane32_swap(__float_as_uint(o[j]), __float_as_uint(o[j + 4]), false, false);
    r4[j] = __uint_as_float(sx[0]) + __uint_as_float(sx[1]);
  }
#pragma unroll
  for (int j = 0; j < 2; j++) {
    auto sx = __builtin_amdgcn_permlane16_swap(__float_as_uint(r4[j]), __float_as_uint(r4[j + 2]), false, false);
    r2[j] = __uint_as_float(sx[0]) + __uint_as_float(sx[1]);
  }
  return f32x2{r2[0], r2[1]};
}
__device__ __forceinline__ void role_B(int h, int j6, int xcd, const __hip_bfloat16* __restrict__ ckv, const __hip_bfloat16* __restrict__ xB,
                                       const __hip_bfloat16* __restrict__ kc, const __hip_bfloat16* __restrict__ vc,
                                       const __hip_bfloat16* __restrict__ wqB, const __hip_bfloat16* __restrict__ woB,
                                       char* __restrict__ ws, __amdgpu_buffer_rsrc_t rs,
                                       __hip_bfloat16* __restrict__ out, char* lds, unsigned trv) {
  const int t = threadIdx.x, l = t & 63;
  const int w = __builtin_amdgcn_readfirstlane(t >> 6);
  const int g = l >> 4, c = l & 15;
  const int pr = j6 >= 3 ? 1 : 0, k = j6 - 3 * pr;
  const unsigned tent = clk();
  const int b0 = 2 * pr;
  const int e0 = k == 1 ? (h & 1) : (k >> 1);
  u32x4 xr[2][2], wr[16];
  {
    const __amdgpu_buffer_rsrc_t rx = mkrs(xB, NB * DM * 2);
    const int rq_ = (0x203120 >> (4 * j6)) & 3;
    const int cw = w ^ ((rq_ & 2) << 1), r0 = rq_ & 1;
    const unsigned xo = (unsigned)((b0 * DM + cw * 64 + g * 16) * 2);
#pragma unroll
    for (int bb = 0; bb < 2; bb++)
#pragma unroll
      for (int i = 0; i < 2; i++) xr[bb][i] = __builtin_amdgcn_raw_buffer_load_b128(rx, xo + bb * DM * 2, (i ^ r0) * 16, 0);
    const __amdgpu_buffer_rsrc_t rq = mkrs(wqB, DM * HD * 2);
    const unsigned vo = (unsigned)(((cw * 64 + g * 16) * HD + h * DH + c * 8) * 2);
    const unsigned vo0 = vo + r0 * 8 * HD * 2, vo1 = vo + (1 - r0) * 8 * HD * 2;
#pragma unroll
    for (int i = 0; i < 16; i++) wr[i] = __builtin_amdgcn_raw_buffer_load_b128(rq, i < 8 ? vo0 : vo1, (i & 7) * HD * 2, 0);
  }
  constexpr unsigned KVB = (unsigned)((size_t)NB * NH * SK * DH * 2);
  const unsigned lo = (unsigned)(g * 256 + c * 16);
  const unsigned hb0 = ((unsigned)((b0 + e0) * NH + h) << 20) + (k == 1 ? (unsigned)SS << 12 : 0u);
  const unsigned hb1 = ((unsigned)((b0 + 1 - e0) * NH + h) << 20) + ((unsigned)SS << 12);
  auto seg1 = [&](int x) { return k == 1 && x >= N0; };
  auto unit = [&](int x) { const int y = seg1(x) ? x - N0 : x; return 4 * (y >> 1) + (y & 1); };
  auto soff = [&](int x) { return (seg1(x) ? hb1 : hb0) + ((unsigned)unit(x) << 12); };
  auto sco = [&](int x) { return (unsigned)((unit(x) + (seg1(x) ? 88 : 0)) * 64); };
  auto whole = [&](int x) { return x != NI - 1 && !(k == 1 && x == N0 - 1); };
  auto ld4 = [&](u32x4 (&d)[4], const void* p, bool ok, unsigned so) {
    const __amdgpu_buffer_rsrc_t r = mkrs(p, ok ? KVB : 0u);
#pragma unroll
    for (int u = 0; u < 4; u++) d[u] = __builtin_amdgcn_raw_buffer_load_b128(r, lo + u * 1024, so, NT);
  };
  unsigned pf;
  {
    const int n = ((h >> 3) * 6 + j6) * 8 + w, u = n >> 4, a = xcd * 4 + u / 3;
    pf = __builtin_amdgcn_raw_buffer_load_b32(mkrs(ckv, h == 31 ? 0 : NB * SK * DC * 2), (unsigned)l * 64, (unsigned)(a * 8 + ((5 + u % 3 + a) & 7)) * 65536u + (n & 15) * 4096u, 0);
  }
  u32x4 ra[4], rb[4], ea[4], eb[4];
  ld4(ra, kc, true, soff(w));
  ld4(rb, kc, true, soff(w) + 8192);
  u64* eps = (u64*)(lds + 33808);
  u64 epv = 0;
  if (t < 3) epv = ld_u64(ws + (t == 0 ? WS_EPA : WS_EPB + 256 * (b0 + (t == 1 ? e0 : 1 - e0))));
  TRR(56);
  float* red = (float*)lds;
  unsigned* qs = (unsigned*)(lds + 32768);
  unsigned* cnt = (unsigned*)(lds + 33856);
  float* mxd = (float*)(lds + 33872);
  float* lw = (float*)(lds + 33920);
  float* rec0 = (float*)(lds + 36864);
  char* prv = lds + 45056 + w * 1024;
  constexpr unsigned L_SC = 53248;
  float* recd = (float*)(lds + 65536);
  const unsigned lb = (unsigned)(uintptr_t)(__attribute__((address_space(3))) char*)lds;
  if (t == 0) { cnt[0] = 16u; cnt[1] = 8u * (RD + 1); cnt[2] = 0u; cnt[3] = 0u; cnt[6] = tent - 0x1000000u; mxd[0] = -INFINITY; mxd[1] = -INFINITY; }
  if (t < 64 * (NP4 - NP)) *(f32x2*)(recd + (NP + (t >> 6)) * RST + 2 * l) = f32x2{0.f, 0.f};
  if (t >= 384) *(float*)(lds + L_SC + ((t & 64) ? 172 : 84) * 64 + l * 4) = -INFINITY;
  {
    float a0[8], a1[8];
#pragma unroll
    for (int j = 0; j < 8; j++) { a0[j] = 0.f; a1[j] = 0.f; }
#pragma unroll
    for (int i = 0; i < 16; i += 2) {
      const unsigned x0 = xr[0][i >> 3][(i >> 1) & 3], x1 = xr[1][i >> 3][(i >> 1) & 3];
#pragma unroll
      for (int j = 0; j < 4; j++) {
        const unsigned pl = __builtin_amdgcn_perm(wr[i + 1][j], wr[i][j], 0x05040100u);
        const unsigned ph = __builtin_amdgcn_perm(wr[i + 1][j], wr[i][j], 0x07060302u);
        a0[2 * j] = dot2(pl, x0, a0[2 * j]); a0[2 * j + 1] = dot2(ph, x0, a0[2 * j + 1]);
        a1[2 * j] = dot2(pl, x1, a1[2 * j]); a1[2 * j + 1] = dot2(ph, x1, a1[2 * j + 1]);
      }
    }
    TRR(57);
#pragma unroll
    for (int j = 0; j < 8; j++) asm volatile("" : "+v"(a0[j]), "+v"(a1[j]));
    __builtin_amdgcn_sched_barrier(0);
    ld4(ea, kc, true, soff(w + 8));
    ld4(eb, kc, true, soff(w + 8) + 8192);
    __builtin_amdgcn_sched_barrier(0);
    float* rp = red + (w * 4 + g) * 256 + c * 8;
    *(f32x4*)(rp) = f32x4{a0[0], a0[1], a0[2], a0[3]};
    *(f32x4*)(rp + 4) = f32x4{a0[4], a0[5], a0[6], a0[7]};
    *(f32x4*)(rp + 128) = f32x4{a1[0], a1[1], a1[2], a1[3]};
    *(f32x4*)(rp + 132) = f32x4{a1[4], a1[5], a1[6], a1[7]};
  }
  __syncthreads();
  {
    const int o = t >> 1, hf = t & 1;
    float s = 0.f;
#pragma unroll
    for (int i = 0; i < 16; i++) s += red[(hf * 16 + i) * 256 + o];
    s += dpp<0xB1>(s);
    const float sn = dpp<0x4E>(s);
    if ((t & 3) == 0) qs[o >> 1] = cvtpk(s, sn);
    if (t < 3) eps[t] = epv;
  }
  __syncthreads();
  u32x4 q = *(const u32x4*)(qs + e0 * 64 + c * 4);
  const u32x4 q1 = *(const u32x4*)(qs + (1 - e0) * 64 + c * 4);
  TR(1);
  auto lds_cnt = [&](int i) { return (unsigned)__builtin_amdgcn_readfirstlane(__hip_atomic_load(cnt + i, __ATOMIC_RELAXED, __HIP_MEMORY_SCOPE_WORKGROUP)); };
  auto lds_inc = [&](int i) {
    asm volatile("s_waitcnt lgkmcnt(0)" ::: "memory");
    if (l == 0) asm volatile("ds_add_u32 %0, %1" :: "v"(lb + 33856u + i * 4), "v"(1u) : "memory");
  };
  u32x4 wo[16];
  __builtin_amdgcn_sched_barrier(0);
#pragma unroll
  for (int i = 0; i < 16; i++) asm volatile("" : "=v"(wo[i]));
  const int nseg = k == 1 ? 2 : 1, sp = k == 1 ? 1 : 0;
  auto store_off = [&](int sg) {
    const int b = b0 + (sg ? 1 - e0 : e0);
    const unsigned og_ = (unsigned)(WS_PBG + ((size_t)(b * 64 + h * 2 + sp) * DM + w * 64 + c * 4 + g) * 8);
    return (g & 1) == 0 ? og_ : 0x7ffffff0u;
  };
  const unsigned apo = (unsigned)((c & 1) * 272 + g * 64);
  auto project = [&](const char* ap, float M, float L) {
    u32x4 a[4];
#pragma unroll
    for (int s = 0; s < 4; s++) a[s] = *(const u32x4*)(ap + s * 16);
    __builtin_amdgcn_sched_barrier(0);
    L += dpp<0xB1>(L); L += dpp<0x4E>(L); L += dpp<0x141>(L);
    __builtin_amdgcn_sched_barrier(0);
    f32x4 d[4];
#pragma unroll
    for (int s = 0; s < 4; s++)
#pragma unroll
      for (int j = 0; j < 4; j++)
        d[j] = __builtin_amdgcn_mfma_f32_16x16x32_bf16(__builtin_bit_cast(bf16x8, a[s]), __builtin_bit_cast(bf16x8, wo[4 * s + j]),
                                                       s ? d[j] : f32x4{0.f, 0.f, 0.f, 0.f}, 0, 0, 0);
    return f32x3{g < 2 ? d[0][0] + d[0][1] : d[3][0] + d[3][1], g < 2 ? d[1][0] + d[1][1] : M, g < 2 ? d[2][0] + d[2][1] : L};
  };
  f32x3 seg0 = {0.f, 0.f, 0.f};
  auto load_wo = [&]() {
    const __amdgpu_buffer_rsrc_t ro = mkrs(woB, HD * DM * 2);
    const unsigned vo = (unsigned)(((h * DH + g * 32) * DM + w * 64 + c * 4) * 2);
#pragma unroll
    for (int i = 0; i < 16; i++) {
      const u32x2 u0 = __builtin_amdgcn_raw_buffer_load_b64(ro, vo + 2 * i * DM * 2, 0, 0);
      const u32x2 u1 = __builtin_amdgcn_raw_buffer_load_b64(ro, vo + (2 * i + 1) * DM * 2, 0, 0);
      wo[i] = u32x4{u0[0], u0[1], u1[0], u1[1]};
    }
  };
  asm volatile("" :: "v"(pf));
  unsigned tp = *(const unsigned*)eps + 1u;
  asm volatile("" : "+v"(tp));
  int nit = 0;
  {
    float m = -INFINITY, s_[4];
    const unsigned swo = lb + L_SC + g * 16 + (c & 3) * 4;
    auto scores = [&](unsigned so, bool ok) {
      const bool b0_ = c & 1, b1_ = c & 2;
      const float r0 = (b0_ ? s_[1] : s_[0]) + dpp<0xB1>(b0_ ? s_[0] : s_[1]);
      const float r1 = (b0_ ? s_[3] : s_[2]) + dpp<0xB1>(b0_ ? s_[2] : s_[3]);
      float r = (b1_ ? r1 : r0) + dpp<0x4E>(b1_ ? r0 : r1);
      r += dpp<0x124>(r);
      r += dpp<0x128>(r);
      r = ok ? rbf(rbf(r) * SCALE) : -INFINITY;
      m = fmaxf(m, r);
      asm volatile("ds_write_b32 %0, %1" :: "v"(swo + so), "v"(r) : "memory");
    };
    auto flush_max = [&](int sg) {
      float mw = fmaxf(m, dpp<0xB1>(m));
      mw = xrow_max(fmaxf(mw, dpp<0x4E>(mw)));
      if (l == 0) asm volatile("ds_max_f32 %0, %1" :: "v"(lb + 33872u + sg * 4), "v"(mw) : "memory");
      m = -INFINITY;
    };
    bool sw = k != 1, first = true, wl = false;
    u32x2 krep = {0u, ~tp};
    const int xw = XW0 + XWS * (h >> 3);
    int x = w;
#pragma unroll 1
    while (x < NI) {
      if (!sw && x >= N0) { flush_max(0); q = q1; sw = true; }
      if (!wl && x >= xw) { load_wo(); wl = true; }
      if (x == XQ) krep = __builtin_amdgcn_raw_buffer_load_b64(rs, (unsigned)WS_FK + (l & 7) * LINE, 0, SC1);
      unsigned dr; u64 sv;
      asm volatile("s_mov_b64 %1, exec\n\ts_mov_b64 exec, 1\n\tds_add_rtn_u32 %0, %2, %3\n\ts_mov_b64 exec, %1"
                   : "=&v"(dr), "=&s"(sv) : "v"(lb + 33856u), "v"(1u), "v"(ra[0][0]) : "memory");
      __builtin_amdgcn_sched_barrier(0);
      item_dots(ra, q, s_);
      asm volatile("s_waitcnt lgkmcnt(0)" : "+v"(dr) :: "memory");
      const int xn = __builtin_amdgcn_readfirstlane(dr);
      const bool more = xn < NI;
      const void* pn = more ? (const void*)kc : (const void*)vc;
      const unsigned sn = soff(more ? xn : w);
      __builtin_amdgcn_sched_barrier(0);
      ld4(ra, pn, true, sn);
      __builtin_amdgcn_sched_barrier(0);
      scores(sco(x), true);
      item_dots(rb, q, s_);
      __builtin_amdgcn_sched_barrier(0);
      ld4(rb, pn, !more || whole(xn), sn + 8192);
      __builtin_amdgcn_sched_barrier(0);
      scores(sco(x) + 128, whole(x));
      if (xn == XR && h >= 8 && h < 16 && j6 == 2) { TR(45); if (l == 0) st_u64(ws + WS_FK + (h - 8) * LINE, ((u64)tp << 32) | clk()); }
      if (first) {
        item_dots(ea, q, s_); scores(sco(w + 8), true);
        item_dots(eb, q, s_); scores(sco(w + 8) + 128, true);
        first = false;
      }
      x = xn;
      TRR(2 + nit); nit++;
    }
    if (!wl) {
      load_wo();
#pragma unroll
      for (int i = 0; i < 16; i++) asm volatile("" : "+v"(wo[i]));
    }
    ld4(ea, vc, true, soff(w + 8));
    ld4(eb, vc, true, soff(w + 8) + 8192);
    if (__all(krep[1] == tp)) {
      const unsigned tr = ref_time(krep[0]);
      if (l == 0) cnt[6] = tr + R1C;
      TR(50);
    }
    flush_max(k == 1 && !sw ? 0 : 1);
    lds_inc(2);
  }
  TR(47);
#pragma unroll
  for (int s = 0; s < 4; s++) {
    u32x4 o[4];
#pragma unroll
    for (int e = 0; e < 4; e++)
#pragma unroll
      for (int j = 0; j < 4; j++) o[j][e] = __builtin_amdgcn_perm(wo[4 * s + e][2 + (j >> 1)], wo[4 * s + e][j >> 1], (j & 1) ? 0x07060302u : 0x05040100u);
#pragma unroll
    for (int j = 0; j < 4; j++) wo[4 * s + j] = o[j];
    asm volatile("" : "+v"(wo[4 * s]), "+v"(wo[4 * s + 1]), "+v"(wo[4 * s + 2]), "+v"(wo[4 * s + 3]));
  }
  for (unsigned spins = 0; lds_cnt(2) != 8u && spins < SPIN_CAP; ++spins) __builtin_amdgcn_s_sleep(1);
  asm volatile("" ::: "memory");
  TR(48);
  nit = 18;
  const u64 tg0 = next_tag(uni64(eps[1])), tg1 = next_tag(uni64(eps[2]));
  if (h >= 8 && h < 16 && j6 == 2 && t == 0) st_u64(ws + WS_FS + (h - 8) * LINE, ((u64)tp << 32) | clk());
  u32x2 srep = {0u, 0u};
  const float M0 = __builtin_bit_cast(float, __builtin_amdgcn_readfirstlane(__builtin_bit_cast(int, mxd[k == 1 ? 0 : 1])));
  const float M1 = __builtin_bit_cast(float, __builtin_amdgcn_readfirstlane(__builtin_bit_cast(int, mxd[1])));
  auto weights = [&](unsigned so, float M) {
    const f32x4 sc = *(const f32x4*)(lds + L_SC + so);
    return f32x4{fexp2((sc[0] - M) * LOG2E), fexp2((sc[1] - M) * LOG2E), fexp2((sc[2] - M) * LOG2E), fexp2((sc[3] - M) * LOG2E)};
  };
  float acc[8];
#pragma unroll
  for (int j = 0; j < 8; j++) acc[j] = 0.f;
  f32x2 vf[4][4];
  f32x4 pa, pb;
  unsigned sb = 0;
  auto get_p = [&](int x) {
    const float M = seg1(x) ? M1 : M0;
    pa = weights(sco(x) + g * 16, M); pb = weights(sco(x) + 128 + g * 16, M);
    asm volatile("" : "+v"(pa), "+v"(pb));
    sb = lds_cnt(6);
  };
  auto fma_v = [&](const f32x4 p) {
#pragma unroll
    for (int u = 0; u < 4; u++)
#pragma unroll
      for (int j = 0; j < 4; j++) {
        acc[2 * j]     = fmaf(p[u], vf[u][j][0], acc[2 * j]);
        acc[2 * j + 1] = fmaf(p[u], vf[u][j][1], acc[2 * j + 1]);
      }
  };
  auto step = [&](int xn) {
    const unsigned sn = soff(xn);
    wait_until(sb + (unsigned)((xn * SCH_S >> 4) + SCH_0 + (xn >= XSTEP ? STEP : 0)));
    __builtin_amdgcn_sched_barrier(0);
    ld4(ra, vc, true, sn);
    __builtin_amdgcn_sched_barrier(0);
    fma_v(pa);
#pragma unroll
    for (int j = 0; j < 8; j++) asm volatile("" : "+v"(acc[j]));
    item_unpack(rb, vf);
    __builtin_amdgcn_sched_barrier(0);
    ld4(rb, vc, whole(xn), sn + 8192);
    __builtin_amdgcn_sched_barrier(0);
    fma_v(pb);
  };
  auto put_acc = [&](float* rec_) {
    *(f32x2*)(rec_ + c * 8 + g * 2) = rows_sum(acc);
#pragma unroll
    for (int j = 0; j < 8; j++) acc[j] = 0.f;
  };
  if (w >= 4) __builtin_amdgcn_s_setprio(1);
  get_p(w);
  item_unpack(ra, vf);
  step(w + 16);
  TRR(2 + nit); nit++;
  get_p(w + 8);
  item_unpack(ea, vf); fma_v(pa);
  item_unpack(eb, vf); fma_v(pb);
  TRR(2 + nit); nit++;
  {
    const bool two = t < 192;
    const f32x4 p0 = weights(t * 16, t < 352 ? M0 : M1), p1 = weights(two ? (t + 512) * 16 : 172 * 64, two ? M1 : 0.f);
    const float s0 = (p0[0] + p0[1]) + (p0[2] + p0[3]), s1 = (p1[0] + p1[1]) + (p1[2] + p1[3]);
    const bool f0 = k == 1 && t < 352;
    const float l0 = xrow_sum(sum16(f0 ? s0 : 0.f)), l1 = xrow_sum(sum16((f0 ? 0.f : s0) + (two ? s1 : 0.f)));
    if (l < 2) lw[l * 8 + w] = l ? l1 : l0;
  }
  bool sw = k != 1;
  int x = w + 16;
#pragma unroll 1
  for (; x < 8 * RD; x += 8) {
    const int rd = x >> 3;
    if (rd == PRIOF) __builtin_amdgcn_s_setprio(0);
    if (!sw && x >= N0) { put_acc(rec0 + w * 128); lds_inc(3); sw = true; }
    if (w == 3 && rd == SCH_R + 1 && __all(srep[1] == tp)) {
      const unsigned tr = ref_time(srep[0]);
      if (l == 0) cnt[6] = tr;
      TR(49);
    }
    if (w == 3 && rd == SCH_R) srep = __builtin_amdgcn_raw_buffer_load_b64(rs, (unsigned)WS_FS + (l & 7) * LINE, 0, SC1);
    if (rd == RE && k == 1) {
      for (unsigned spins = 0; lds_cnt(3) != 8u && spins < SPIN_CAP; ++spins) __builtin_amdgcn_s_sleep(1);
      asm volatile("" ::: "memory");
      f32x2 a2 = {0.f, 0.f};
#pragma unroll
      for (int i = 0; i < 8; i++) a2 += *(const f32x2*)(rec0 + i * 128 + 2 * l);
      const unsigned hi = cvtpk(a2[0], a2[1]);
      *(unsigned*)(prv + l * 4) = hi;
      *(unsigned*)(prv + 272 + l * 4) = cvtpk(a2[0] - bf_lo(hi), a2[1] - bf_hi(hi));
      seg0 = project(prv + apo, M0, lw[l & 7]);
    }
    get_p(x);
    __builtin_amdgcn_sched_barrier(0);
    item_unpack(ra, vf);
    step(x + 8);
    TRR(2 + nit); nit++;
  }
  put_acc(recd + w * RST);
  unsigned o_rv = 65536u + (g * RST + w * 16 + c) * 4, o_lw = 33920u + 32u + (l & 7) * 4;
  unsigned o_oi = (g < 2 && (c & 1) == 0) ? 41216u + g * 272 + (w * 16 + c) * 2 : 45056u + w * 1024 + 256 + l * 4;
  unsigned o_ap = 41216u + apo, o_st = store_off(nseg - 1);
  unsigned o_s0 = k == 1 ? store_off(0) : 0x7ffffff0u;
  unsigned tl0 = (unsigned)(k == 1 ? tg1 : tg0);
  u32x4 sg0 = {__float_as_uint(seg0[0]), __float_as_uint(seg0[1]), __float_as_uint(seg0[2]), (unsigned)tg0};
  asm volatile("" : "+v"(o_s0), "+v"(tl0), "+v"(sg0));
  asm volatile("" : "+v"(o_rv), "+v"(o_lw), "+v"(o_oi), "+v"(o_ap), "+v"(o_st));
  {
    {
#pragma unroll 1
      for (;;) {
        get_p(x);
        unsigned dr; u64 sv;
        asm volatile("s_mov_b64 %1, exec\n\ts_mov_b64 exec, 1\n\tds_add_rtn_u32 %0, %2, %3\n\ts_mov_b64 exec, %1"
                     : "=&v"(dr), "=&s"(sv) : "v"(lb + 33860u), "v"(1u), "v"(ra[0][0]) : "memory");
        __builtin_amdgcn_sched_barrier(0);
        item_unpack(ra, vf);
        asm volatile("s_waitcnt lgkmcnt(0)" : "+v"(dr) :: "memory");
        const int xn = __builtin_amdgcn_readfirstlane(dr);
        if (xn >= NI) break;
        step(xn);
        put_acc(recd + (x - 8 * RD + 8) * RST);
        x = xn;
        TRR(2 + nit); nit++;
      }
      fma_v(pa);
      item_unpack(rb, vf);
      fma_v(pb);
      put_acc(recd + (x - 8 * RD + 8) * RST);
      TRR(2 + nit); nit++;
      TR(40);
      asm volatile("s_waitcnt lgkmcnt(0)" ::: "memory");
      __syncthreads();
      TRR(41);
    }
    {
      float rv_[NP4 / 4];
#pragma unroll
      for (int r = 0; r < NP4 / 4; r++) rv_[r] = *(const float*)(lds + o_rv + r * 4 * RST * 4);
      const float li = *(const float*)(lds + o_lw);
      float a = 0.f, a1 = 0.f;
#pragma unroll
      for (int r = 0; r < NP4 / 4; r++) { if (r & 1) a1 += rv_[r]; else a += rv_[r]; }
      a += a1;
      a = xrow_sum(a);
      const float an = dpp<0xB1>(a);
      const unsigned hi = cvtpk(a, an);
      const unsigned lo2 = cvtpk(a - bf_lo(hi), an - bf_hi(hi));
      *(unsigned*)(lds + o_oi) = g ? lo2 : hi;
      __syncthreads();
      const f32x3 pv = project(lds + o_ap, M1, li);
      st16u_sc1(rs, o_st, u32x4{__float_as_uint(pv[0]), __float_as_uint(pv[1]), __float_as_uint(pv[2]), tl0});
      st16u_sc1(rs, o_s0, sg0);
    }
    TR(42);
    if (w < nseg) {
      const int sg = w;
      const int b = b0 + (sg ? 1 - e0 : e0);
      const int j64 = (h & 7) * 8 + (h >> 3) * 2 + sp;
      const u64 tg = sg ? tg1 : tg0;
      const unsigned ta = (unsigned)tg;
      const unsigned og = (unsigned)(WS_PBG + ((size_t)(b * 64 + (l >> 2)) * DM + j64 * 8) * 8 + (l & 3) * 16);
      u32x4 p[4];
      {
        __builtin_amdgcn_s_sleep(4);
        wait_until(sb + TM_SW);
        TR(46);
      }
#pragma unroll 1
      for (unsigned spins = 0; spins < SPIN_CAP; ++spins) {
        asm volatile("" ::: "memory");
#pragma unroll
        for (int i = 0; i < 4; i++) p[i] = ld16u_sc1(rs, og + i * 16 * DM * 8);
        if (__all(p[0][3] == ta && p[1][3] == ta && p[2][3] == ta && p[3][3] == ta)) break;
      }
      float v0 = 0.f, v1 = 0.f, v2 = 0.f;
#pragma unroll
      for (int i = 0; i < 4; i++) {
        const float mm = dpp<0xF5>(__uint_as_float(p[i][1]));
        const float Mh = fmaxf(mm, dpp<0x141>(mm));
        const float fq = fexp2((mm - Mh) * LOG2E);
        float L = fq * dpp<0xF5>(__uint_as_float(p[i][2]));
        L += dpp<0x141>(L);
        const float wgt = fq * __builtin_amdgcn_rcpf(L);
        v0 = fmaf(__uint_as_float(p[i][0]), wgt, v0);
        v1 = fmaf(__uint_as_float(p[i][1]), wgt, v1);
        v2 = fmaf(__uint_as_float(p[i][2]), wgt, v2);
      }
      const float u2 = dpp<0xA0>(v2);
      v1 = (l & 1) ? v0 : v1;
      v0 = (l & 1) ? u2 : v0;
      v0 += dpp<0x128>(v0); v0 += dpp<0x124>(v0); v0 = xrow_sum(v0);
      v1 += dpp<0x128>(v1); v1 += dpp<0x124>(v1); v1 = xrow_sum(v1);
      if (l < 4) {
        *(unsigned*)(out + b * 1024 + 512 + j64 * 8 + l * 2) = cvtpk(v0, v1);
        if (j64 == 0 && l == 0) st_u64(ws + WS_EPB + 256 * b, tg);
      }
    }
  }
  TR(43);
  TRDUMP();
}

__device__ __forceinline__ unsigned lds_off(int row, int ch) {
  return 256 * row + 16 * (ch ^ (((row & 3) << 2) | ((row >> 2) & 3)));
}

__device__ __forceinline__ void role_A1(int h, const __hip_bfloat16* __restrict__ xA, const __hip_bfloat16* __restrict__ wqA,
                                        const __hip_bfloat16* __restrict__ wuk, const __hip_bfloat16* __restrict__ wuv,
                                        const __hip_bfloat16* __restrict__ woA, char* __restrict__ ws,
                                        __amdgpu_buffer_rsrc_t rs, __hip_bfloat16* __restrict__ out, char* lds, unsigned trv) {
  const int t = threadIdx.x, l = t & 63;
  const int w = __builtin_amdgcn_readfirstlane(t >> 6);
  const int g = l >> 4, c = l & 15;
  const unsigned tent = clk();
  float* red = (float*)(lds + 16384);
  const unsigned vo = (unsigned)(((w * 64 + g * 16) * HD + h * DH + c * 8) * 2);
  u64 tag;
  {
    u32x4 xr[NB][2], wr[16], wk[16];
    const __amdgpu_buffer_rsrc_t rx = mkrs(xA, NB * DM * 2);
    const unsigned xo = (unsigned)((w * 64 + g * 16) * 2);
#pragma unroll
    for (int bb = 0; bb < NB; bb++)
#pragma unroll
      for (int i = 0; i < 2; i++) xr[bb][i] = __builtin_amdgcn_raw_buffer_load_b128(rx, xo + bb * DM * 2 + i * 16, 0, 0);
    const __amdgpu_buffer_rsrc_t rq = mkrs(wqA, DM * HD * 2);
#pragma unroll
    for (int i = 0; i < 16; i++) wr[i] = __builtin_amdgcn_raw_buffer_load_b128(rq, vo, i * HD * 2, NT);
    {
      const __amdgpu_buffer_rsrc_t rku = mkrs(wuk, DC * HD * 2);
#pragma unroll
      for (int i = 0; i < 16; i++) wk[i] = __builtin_amdgcn_raw_buffer_load_b128(rku, vo, i * HD * 2, NT);
    }
    __builtin_amdgcn_sched_barrier(0);
    u64 epv = 0;
    if (t == 0) epv = ld_u64(ws + WS_EPA);
    unsigned* qs = (unsigned*)lds;
    unsigned short* qo = (unsigned short*)(lds + 4096);
    float acc[NB][8];
#pragma unroll
    for (int bb = 0; bb < NB; bb++)
#pragma unroll
      for (int j = 0; j < 8; j++) acc[bb][j] = 0.f;
#pragma unroll
    for (int i = 0; i < 16; i += 2) {
#pragma unroll
      for (int j = 0; j < 4; j++) {
        const unsigned pl = __builtin_amdgcn_perm(wr[i + 1][j], wr[i][j], 0x05040100u);
        const unsigned ph = __builtin_amdgcn_perm(wr[i + 1][j], wr[i][j], 0x07060302u);
#pragma unroll
        for (int bb = 0; bb < NB; bb++) {
          const unsigned x = xr[bb][i >> 3][(i >> 1) & 3];
          acc[bb][2 * j] = dot2(pl, x, acc[bb][2 * j]);
          acc[bb][2 * j + 1] = dot2(ph, x, acc[bb][2 * j + 1]);
        }
      }
    }
    float* rp = red + (w * 4 + g) * 512 + c * 8;
#pragma unroll
    for (int bb = 0; bb < NB; bb++) {
      *(f32x4*)(rp + bb * 128) = f32x4{acc[bb][0], acc[bb][1], acc[bb][2], acc[bb][3]};
      *(f32x4*)(rp + bb * 128 + 4) = f32x4{acc[bb][4], acc[bb][5], acc[bb][6], acc[bb][7]};
    }
    __syncthreads();
    {
      float s = 0.f;
#pragma unroll
      for (int i = 0; i < 32; i++) s += red[i * 512 + t];
      const float sn = dpp<0xB1>(s);
      if ((t & 1) == 0) qs[t >> 1] = cvtpk(s, sn);
      if (t == 0) *(u64*)(lds + 2048) = epv;
    }
    __syncthreads();
    tag = next_tag(uni64(*(const u64*)(lds + 2048)));
    {
      u32x4 qq[NB];
#pragma unroll
      for (int bb = 0; bb < NB; bb++) qq[bb] = *(const u32x4*)(qs + bb * 64 + c * 4);
#pragma unroll
      for (int i = 0; i < 16; i++) {
        float s[NB];
#pragma unroll
        for (int bb = 0; bb < NB; bb++) {
          float v = dot2(wk[i][0], qq[bb][0], 0.f);
          v = dot2(wk[i][1], qq[bb][1], v);
          v = dot2(wk[i][2], qq[bb][2], v);
          v = dot2(wk[i][3], qq[bb][3], v);
          s[bb] = sum16(v);
        }
        const float v = (c & 2) ? ((c & 1) ? s[3] : s[2]) : ((c & 1) ? s[1] : s[0]);
        if (c < NB) qo[c * 512 + w * 64 + g * 16 + i] = (unsigned short)(cvtpk(v, v) & 0xffff);
      }
    }
    __syncthreads();
    if (t < 256) {
      const int bb = t >> 6, ch = t & 63;
      st16u_sc1(rs, (unsigned)(WS_QABS + ((size_t)(bb * NH + h) * DC + ch * 8) * 2), *(const u32x4*)(qo + bb * 512 + ch * 8));
    }
    TR(1);
    signal_flag(ws + WS_F1 + h * LINE, tag);
    TR(2);
  }
  const unsigned ta = (unsigned)tag, tb = (unsigned)(tag >> 32);
  {
    u32x4 bv[8][2], bo[8][2];
    u32x4 pre[2][8], wo[16];
    {
      const __amdgpu_buffer_rsrc_t ruv = mkrs(wuv, DC * HD * 2);
      const __amdgpu_buffer_rsrc_t rwo = mkrs(woA, HD * DM * 2);
      const unsigned wvo = (unsigned)(((h * DH + (l >> 3) * 16) * DM + w * 64 + (l & 7) * 8) * 2);
#pragma unroll
      for (int i = 0; i < 16; i++) pre[i >> 3][i & 7] = __builtin_amdgcn_raw_buffer_load_b128(ruv, vo - g * 8 * HD * 2, ((i >> 3) * 32 + (i & 7)) * HD * 2, NT);
#pragma unroll
      for (int i = 0; i < 16; i++) wo[i] = __builtin_amdgcn_raw_buffer_load_b128(rwo, wvo, i * DM * 2, NT);
    }
#pragma unroll
    for (int j = 0; j < 8; j++)
#pragma unroll
      for (int s = 0; s < 2; s++) {
#pragma unroll
        for (int e = 0; e < 4; e++)
          bv[j][s][e] = __builtin_amdgcn_perm(pre[s][2 * e + 1][j >> 1], pre[s][2 * e][j >> 1], (j & 1) ? 0x07060302u : 0x05040100u);
        asm volatile("" : "+v"(bv[j][s]));
      }
#pragma unroll
    for (int j = 0; j < 8; j++)
#pragma unroll
      for (int s = 0; s < 2; s++) {
#pragma unroll
        for (int e = 0; e < 4; e++)
          bo[j][s][e] = __builtin_amdgcn_perm(wo[8 * s + 2 * e + 1][j >> 1], wo[8 * s + 2 * e][j >> 1], (j & 1) ? 0x07060302u : 0x05040100u);
        asm volatile("" : "+v"(bo[j][s]));
      }
    constexpr int LAS = 1040;
    unsigned short* cs = (unsigned short*)(lds + 16384);
    float* outs = (float*)(lds + 20480);
    float* cred = (float*)(lds + 32768);
    {
    {
    TR(3);
    }
    unsigned po = (unsigned)(WS_PAO + ((size_t)(w * 4 * NH + h) * DC + l * 8) * 2);
    unsigned pc = (unsigned)(WS_PAC + (size_t)((w * 4 + g) * NH + h) * 8);
    unsigned lw_ = (unsigned)(65536 + ((w & 1) * 16 + (w >> 1) * 4) * LAS + l * 16), lr_ = (unsigned)(65536 + c * LAS + (w * 64 + g * 8) * 2);
    asm volatile("" : "+v"(po), "+v"(pc), "+v"(lw_), "+v"(lr_));
    const unsigned tw = (h & 1) ? tb : ta;
    const unsigned tg = wait_gate(ws + WS_FS, ta, (unsigned*)(lds + 8192), tent);
    TR(4);
    wait_until(tg + TM_PUT + TM_GET);
    TR(7);
    {
      u32x4 pr[4];
      unsigned ntry = 0;
#pragma unroll 1
      for (unsigned spins = 0; spins < SPIN_CAP; ++spins) {
        asm volatile("" ::: "memory");
        const u32x2 ck = __builtin_amdgcn_raw_buffer_load_b64(rs, pc, 0, SC1);
#pragma unroll
        for (int i = 0; i < 4; i++) pr[i] = __builtin_amdgcn_raw_buffer_load_b128(rs, po, i * NH * DC * 2, SC1);
#pragma unroll
        for (int i = 0; i < 4; i++) *(u32x4*)(lds + lw_ + i * LAS) = pr[i];
        ntry++;
        if (__all(row_sums(pr) == ck[0] && ck[1] == tw)) break;
        __builtin_amdgcn_s_sleep(2);
      }
      TR(5);
      trv = l == 62 ? ntry : trv;
      __syncthreads();
      u32x4 af[2][2];
#pragma unroll
      for (int tl = 0; tl < 2; tl++)
#pragma unroll
        for (int s = 0; s < 2; s++) af[tl][s] = *(const u32x4*)(lds + lr_ + tl * 16 * LAS + s * 64);
      f32x4 dd[8];
#pragma unroll
      for (int j = 0; j < 8; j++)
        dd[j] = __builtin_amdgcn_mfma_f32_16x16x32_bf16(__builtin_bit_cast(bf16x8, af[0][0]), __builtin_bit_cast(bf16x8, bv[j][0]), f32x4{0.f, 0.f, 0.f, 0.f}, 0, 0, 0);
#pragma unroll
      for (int j = 0; j < 8; j++)
        dd[j] = __builtin_amdgcn_mfma_f32_16x16x32_bf16(__builtin_bit_cast(bf16x8, af[0][1]), __builtin_bit_cast(bf16x8, bv[j][1]), dd[j], 0, 0, 0);
#pragma unroll
      for (int j = 0; j < 8; j++)
        dd[j] = __builtin_amdgcn_mfma_f32_16x16x32_bf16(__builtin_bit_cast(bf16x8, af[1][0]), __builtin_bit_cast(bf16x8, bv[j][0]), dd[j], 0, 0, 0);
#pragma unroll
      for (int j = 0; j < 8; j++)
        dd[j] = __builtin_amdgcn_mfma_f32_16x16x32_bf16(__builtin_bit_cast(bf16x8, af[1][1]), __builtin_bit_cast(bf16x8, bv[j][1]), dd[j], 0, 0, 0);
      f32x4 v[2];
#pragma unroll
      for (int j = 0; j < 8; j++) v[j >> 2][j & 3] = (dd[j][0] + dd[j][1]) + (dd[j][2] + dd[j][3]);
      *(f32x4*)(cred + (w * 4 + g) * 128 + c * 8) = v[0];
      *(f32x4*)(cred + (w * 4 + g) * 128 + c * 8 + 4) = v[1];
    }
    __syncthreads();
    {
      float s = 0.f;
#pragma unroll
      for (int i = 0; i < 8; i++) s += cred[i * 512 + t];
      const float sn = dpp<0xB1>(s);
      if ((t & 1) == 0) *(unsigned*)(cs + t) = cvtpk(s, sn);
    }
    __syncthreads();
    {
      const char* ap = (const char*)cs + ((c >> 1) & 3) * 256 + (g * 32 + (c & 1) * 16) * 2;
      const u32x4 a0 = *(const u32x4*)(ap), a1 = *(const u32x4*)(ap + 16);
      f32x4 dd[8];
#pragma unroll
      for (int j = 0; j < 8; j++)
        dd[j] = __builtin_amdgcn_mfma_f32_16x16x32_bf16(__builtin_bit_cast(bf16x8, a0), __builtin_bit_cast(bf16x8, bo[j][0]), f32x4{0.f, 0.f, 0.f, 0.f}, 0, 0, 0);
#pragma unroll
      for (int j = 0; j < 8; j++)
        dd[j] = __builtin_amdgcn_mfma_f32_16x16x32_bf16(__builtin_bit_cast(bf16x8, a1), __builtin_bit_cast(bf16x8, bo[j][1]), dd[j], 0, 0, 0);
      f32x4 v4[2];
#pragma unroll
      for (int j = 0; j < 8; j++) {
        const float u0 = (l & 8) ? dd[j][1] : dd[j][0], u1 = (l & 8) ? dd[j][3] : dd[j][2];
        const float keep = (l & 8) ? u1 : u0, give = (l & 8) ? u0 : u1;
        v4[j >> 2][j & 3] = keep + dpp<0x128>(give);
      }
      if (g < 2) {
        float* op = outs + (g * 2 + ((l >> 3) & 1)) * 512 + w * 64 + (l & 7) * 8;
        *(f32x4*)op = v4[0];
        *(f32x4*)(op + 4) = v4[1];
      }
    }
    __syncthreads();
    {
      const f32x2 va = *(const f32x2*)(outs + t * 2), vb = *(const f32x2*)(outs + 1024 + t * 2);
      TR(8);
      wait_until(tg + TM_PA);
      const unsigned oo = (unsigned)(WS_PA + ((size_t)((t >> 8) * NH + h) * DM + (t & 255) * 2) * 8);
      st16u_sc1(rs, oo, u32x4{__float_as_uint(va[0]), ta, __float_as_uint(va[1]), tb});
      st16u_sc1(rs, oo + 2 * NH * DM * 8, u32x4{__float_as_uint(vb[0]), ta, __float_as_uint(vb[1]), tb});
    }
  TR(6);
  if (w < 4) {
    const int bb = h >> 3, n0 = (h & 7) * 64 + w * 16;
    const unsigned og = (unsigned)(WS_PA + ((size_t)(bb * NH + (l >> 3)) * DM + n0) * 8 + (l & 7) * 16);
    u32x4 p[4];
    wait_until(tg + TM_PA + TM_T4);
#pragma unroll 1
    for (unsigned spins = 0; spins < SPIN_CAP; ++spins) {
      asm volatile("" ::: "memory");
#pragma unroll
      for (int i = 0; i < 4; i++) p[i] = ld16u_sc1(rs, og + i * 8 * DM * 8);
      bool ok = true;
#pragma unroll
      for (int i = 0; i < 4; i++) ok = ok && p[i][1] == ta && p[i][3] == tb;
      if (__all(ok)) break;
      __builtin_amdgcn_s_sleep(4);
    }
    float v0 = (__uint_as_float(p[0][0]) + __uint_as_float(p[1][0])) + (__uint_as_float(p[2][0]) + __uint_as_float(p[3][0]));
    float v1 = (__uint_as_float(p[0][2]) + __uint_as_float(p[1][2])) + (__uint_as_float(p[2][2]) + __uint_as_float(p[3][2]));
    v0 += dpp<0x128>(v0); v0 = xrow_sum(v0);
    v1 += dpp<0x128>(v1); v1 = xrow_sum(v1);
    if (l < 8) {
      *(unsigned*)(out + bb * 1024 + n0 + l * 2) = cvtpk(v0, v1);
      if (h == 0 && t == 0) st_u64(ws + WS_EPA, tag);
    }
  }
    }
  }
  TR(43);
  TRDUMP();
}

__device__ __forceinline__ void role_A2(int a, const __hip_bfloat16* __restrict__ ckv, char* __restrict__ ws,
                                        __amdgpu_buffer_rsrc_t rs, char* lds, unsigned trv) {
  const int t = threadIdx.x, l = t & 63;
  const int w = __builtin_amdgcn_readfirstlane(t >> 6);
  const int g = l >> 4, c = l & 15;
  const unsigned tent = clk();
  u64 ep = 0;
  if (t == 0) ep = ld_u64(ws + WS_EPA);
  const int grp = w >> 2, wq = w & 3;
  const int b = a / SPLIT_A, sp = a % SPLIT_A;
  const int r = l & 31, hh = l >> 5;
  constexpr unsigned CB = (unsigned)((size_t)NB * SK * DC * 2);
  constexpr int NTL = KA / 64;
  const unsigned cvo = (unsigned)((g * DC + 128 * wq + c * 8) * 2);
  const unsigned cso = (unsigned)(((size_t)b * SK + sp * KA + grp * 32) * DC * 2);
  auto load_tile = [&](u32x4 (&pr)[8], int tile) {
    const bool ok = tile < NTL;
    const __amdgpu_buffer_rsrc_t rc = mkrs(ckv, ok ? CB : 0u);
    const unsigned so = ok ? cso + (unsigned)((tile + sp) & (NTL - 1)) * 64 * DC * 2 : 0u;
#pragma unroll
    for (int u = 0; u < 8; u++) pr[u] = __builtin_amdgcn_raw_buffer_load_b128(rc, cvo, so + u * 4 * DC * 2, NT);
  };
  char* img = lds + w * 8192;
  float* sred = (float*)(lds + 65536 + grp * 16384);
  float* mxs = (float*)(lds + 98304 + grp * 512);
  unsigned* pbuf = (unsigned*)(lds + 102400 + grp * 2048);
  float* lall = (float*)(lds + 106496);
  const unsigned imga = (unsigned)(uintptr_t)(__attribute__((address_space(3))) char*)img;
  auto put_tile = [&](u32x4 (&pr)[8]) {
#pragma unroll
    for (int u = 0; u < 8; u++) *(u32x4*)(img + lds_off(4 * u + g, c)) = pr[u];
  };
  u32x4 pre[2][8];
  load_tile(pre[0], 0);
  load_tile(pre[1], 1);
  const u64 tag0 = next_tag(uni64(ep));
  put_tile(pre[0]);
  __builtin_amdgcn_sched_barrier(0);
  load_tile(pre[0], 2);
  unsigned pf[4];
#pragma unroll
  for (int i = 0; i < 4; i++)
    pf[i] = __builtin_amdgcn_raw_buffer_load_b32(mkrs(ckv, CB), (unsigned)t * 64, (unsigned)(a * 8 + ((3 + (i >> 1) + a) & 7)) * 65536u + (i & 1) * 32768u, 0);
  TR(1);
  if (t < 64) {
    wait_until(tent + TM_F1);
#pragma unroll 1
    for (unsigned spins = 0; spins < SPIN_CAP; ++spins) {
      __builtin_amdgcn_s_sleep(32);
      if (__all(ld_u64(ws + WS_F1 + (l & 31) * LINE) == tag0)) break;
    }
  }
  if (t == 0) *(u64*)(lds + 110592) = tag0;
  asm volatile("" :: "v"(pf[0]), "v"(pf[1]), "v"(pf[2]), "v"(pf[3]));
  __syncthreads();
  const u64 tag = uni64(*(const u64*)(lds + 110592));
  const unsigned ta = (unsigned)tag, tb = (unsigned)(tag >> 32);
  TR(3);
  bf16x8 qf[8];
  {
    char* qi = lds + 65536;
    const unsigned qo = (unsigned)(WS_QABS + (size_t)b * NH * DC * 2 + t * 16);
    u32x4 qv[4];
#pragma unroll
    for (int i = 0; i < 4; i++) qv[i] = ld16u_sc1(rs, qo + i * 8192);
#pragma unroll
    for (int i = 0; i < 4; i++) *(u32x4*)(qi + (i * 8 + w) * 1040 + l * 16) = qv[i];
    __syncthreads();
#pragma unroll
    for (int s = 0; s < 8; s++) qf[s] = __builtin_bit_cast(bf16x8, *(const u32x4*)(qi + r * 1040 + (128 * wq + 16 * s + 8 * hh) * 2));
#pragma unroll
    for (int s = 0; s < 8; s++) asm volatile("" : "+v"(qf[s]));
    __syncthreads();
  }
  f32x16 o[4];
#pragma unroll
  for (int i = 0; i < 4; i++)
#pragma unroll
    for (int j = 0; j < 16; j++) o[i][j] = 0.f;
  float m = -INFINITY, lsum = 0.f;
  const int q4 = (l & 15) >> 2, p4 = l & 3, colh = g & 1;
  auto tile_body = [&](int tile, u32x4 (&nx)[8]) {
    f32x16 s;
#pragma unroll
    for (int j = 0; j < 16; j++) s[j] = 0.f;
    {
      u32x4 af[8];
#pragma unroll
      for (int ks = 0; ks < 8; ks++) af[ks] = *(const u32x4*)(img + lds_off(r, 2 * ks + hh));
      __builtin_amdgcn_sched_barrier(0);
#pragma unroll
      for (int ks = 0; ks < 8; ks++) s = __builtin_amdgcn_mfma_f32_32x32x16_bf16(__builtin_bit_cast(bf16x8, af[ks]), qf[ks], s, 0, 0, 0);
    }
    f32x4* sr = (f32x4*)sred;
#pragma unroll
    for (int k4 = 0; k4 < 4; k4++) sr[(k4 * 4 + wq) * 64 + l] = f32x4{s[4 * k4], s[4 * k4 + 1], s[4 * k4 + 2], s[4 * k4 + 3]};
    __syncthreads();
    float x[4];
    {
      const f32x4 v = (sr[(wq * 4) * 64 + l] + sr[(wq * 4 + 1) * 64 + l]) + (sr[(wq * 4 + 2) * 64 + l] + sr[(wq * 4 + 3) * 64 + l]);
#pragma unroll
      for (int e = 0; e < 4; e += 2) {
        const unsigned r1 = cvtpk(v[e], v[e + 1]);
        const unsigned r2 = cvtpk(bf_lo(r1) * SCALE, bf_hi(r1) * SCALE);
        x[e] = bf_lo(r2); x[e + 1] = bf_hi(r2);
      }
      float mq = fmaxf(fmaxf(x[0], x[1]), fmaxf(x[2], x[3]));
      auto sx = __builtin_amdgcn_permlane32_swap(__float_as_uint(mq), __float_as_uint(mq), false, false);
      mq = fmaxf(__uint_as_float(sx[0]), __uint_as_float(sx[1]));
      if (hh == 0) mxs[r * 4 + wq] = mq;
    }
    __syncthreads();
    const f32x4 m4 = *(const f32x4*)(mxs + r * 4);
    const float mn = fmaxf(fmaxf(m, fmaxf(m4[0], m4[1])), fmaxf(m4[2], m4[3]));
    const float alpha = fexp2((m - mn) * LOG2E);
    {
      const unsigned p0 = cvtpk(fexp2((x[0] - mn) * LOG2E), fexp2((x[1] - mn) * LOG2E));
      const unsigned p1 = cvtpk(fexp2((x[2] - mn) * LOG2E), fexp2((x[3] - mn) * LOG2E));
      *(u32x2*)(pbuf + l * 8 + wq * 2) = u32x2{p0, p1};
      lsum = lsum * alpha + ((bf_lo(p0) + bf_hi(p0)) + (bf_lo(p1) + bf_hi(p1)));
    }
    m = mn;
    __syncthreads();
    unsigned pk[8];
    {
      const u32x4 a0 = *(const u32x4*)(pbuf + l * 8), a1 = *(const u32x4*)(pbuf + l * 8 + 4);
      pk[0] = a0[0]; pk[1] = a0[1]; pk[2] = a0[2]; pk[3] = a0[3]; pk[4] = a1[0]; pk[5] = a1[1]; pk[6] = a1[2]; pk[7] = a1[3];
    }
    if (__any(alpha != 1.f)) {
#pragma unroll
      for (int i = 0; i < 4; i++)
#pragma unroll
        for (int j = 0; j < 16; j++) o[i][j] *= alpha;
    }
#pragma unroll
    for (int s2 = 0; s2 < 2; s2++) {
      u32x2 tv[4][2];
#pragma unroll
      for (int ct = 0; ct < 4; ct++) {
        const int ch = 4 * ct + 2 * colh + (p4 >> 1);
        const unsigned a0 = lds_off(4 * hh + q4, ch) + 8 * (p4 & 1);
        const unsigned a1 = lds_off(8 + 4 * hh + q4, ch) + 8 * (p4 & 1);
        if (s2 == 0) {
          asm volatile("ds_read_b64_tr_b16 %0, %1" : "=v"(tv[ct][0]) : "v"(imga + a0) : "memory");
          asm volatile("ds_read_b64_tr_b16 %0, %1" : "=v"(tv[ct][1]) : "v"(imga + a1) : "memory");
        } else {
          asm volatile("ds_read_b64_tr_b16 %0, %1 offset:4096" : "=v"(tv[ct][0]) : "v"(imga + a0) : "memory");
          asm volatile("ds_read_b64_tr_b16 %0, %1 offset:4096" : "=v"(tv[ct][1]) : "v"(imga + a1) : "memory");
        }
      }
      asm volatile("s_waitcnt lgkmcnt(0)" ::: "memory");
      __builtin_amdgcn_sched_barrier(0);
      u32x4 pb = {pk[4 * s2], pk[4 * s2 + 1], pk[4 * s2 + 2], pk[4 * s2 + 3]};
#pragma unroll
      for (int ct = 0; ct < 4; ct++) {
        u32x4 av = {tv[ct][0][0], tv[ct][0][1], tv[ct][1][0], tv[ct][1][1]};
        o[ct] = __builtin_amdgcn_mfma_f32_32x32x16_bf16(__builtin_bit_cast(bf16x8, av), __builtin_bit_cast(bf16x8, pb), o[ct], 0, 0, 0);
      }
    }
    put_tile(nx);
    __builtin_amdgcn_sched_barrier(0);
    load_tile(nx, tile + 3);
    TRR(4 + tile);
  };
#pragma unroll 1
  for (int tile = 0; tile < NTL; tile += 2) {
    tile_body(tile, pre[1]);
    tile_body(tile + 1, pre[0]);
  }
  constexpr int OST = 516;
  float* ob = (float*)lds;
  float* mlb = (float*)(lds + 32 * OST * 4);
  lall[w * 64 + l] = lsum;
  __syncthreads();
  {
    const float* lp = lall + grp * 256 + r;
    lsum = ((lp[0] + lp[32]) + (lp[64] + lp[96])) + ((lp[128] + lp[160]) + (lp[192] + lp[224]));
  }
  if (grp == 1) {
#pragma unroll
    for (int ct = 0; ct < 4; ct++)
#pragma unroll
      for (int j4 = 0; j4 < 4; j4++)
        *(f32x4*)(ob + r * OST + 128 * wq + 32 * ct + 8 * j4 + 4 * hh) =
            f32x4{o[ct][4 * j4], o[ct][4 * j4 + 1], o[ct][4 * j4 + 2], o[ct][4 * j4 + 3]};
    if (wq == 0 && l < 32) { mlb[2 * r] = m; mlb[2 * r + 1] = lsum; }
  }
  __syncthreads();
  if (grp == 0) {
    const float m1 = mlb[2 * r], l1 = mlb[2 * r + 1];
    const float M = fmaxf(m, m1);
    const float f0 = fexp2((m - M) * LOG2E), f1 = fexp2((m1 - M) * LOG2E);
#pragma unroll
    for (int ct = 0; ct < 4; ct++)
#pragma unroll
      for (int j4 = 0; j4 < 4; j4++) {
        float* p = ob + r * OST + 128 * wq + 32 * ct + 8 * j4 + 4 * hh;
        const f32x4 o1 = *(const f32x4*)p;
        *(f32x4*)p = f32x4{o[ct][4 * j4] * f0 + o1[0] * f1, o[ct][4 * j4 + 1] * f0 + o1[1] * f1,
                           o[ct][4 * j4 + 2] * f0 + o1[2] * f1, o[ct][4 * j4 + 3] * f0 + o1[3] * f1};
      }
    if (wq == 0 && l < 32)
      st16u_sc1(rs, (unsigned)(WS_PAML + ((size_t)(b * SPLIT_A + sp) * NH + r) * 16),
                u32x4{__float_as_uint(M), ta, __float_as_uint(lsum * f0 + l1 * f1), tb});
  }
  float* wgt = mlb + 64;
  unsigned* pcs = (unsigned*)(mlb + 128);
  const unsigned tg = wait_gate(ws + WS_FS, ta, (unsigned*)(mlb + 96), tent);
  TR(18);
  if (t < 256) {
    wait_until(tg + TM_ML);
    const unsigned mo = (unsigned)(WS_PAML + ((size_t)(b * SPLIT_A + (t & 7)) * NH + (t >> 3)) * 16);
    u32x4 mv;
#pragma unroll 1
    for (unsigned spins = 0; spins < SPIN_CAP; ++spins) {
      asm volatile("" ::: "memory");
      mv = ld16u_sc1(rs, mo);
      if (__all(mv[1] == ta && mv[3] == tb)) break;
      __builtin_amdgcn_s_sleep(16);
    }
    const float mm = __uint_as_float(mv[0]);
    float M = fmaxf(mm, dpp<0xB1>(mm)); M = fmaxf(M, dpp<0x4E>(M)); M = fmaxf(M, dpp<0x141>(M));
    const float f = fexp2((mm - M) * LOG2E);
    float L = f * __uint_as_float(mv[2]);
    L += dpp<0xB1>(L); L += dpp<0x4E>(L); L += dpp<0x141>(L);
    if ((t & 7) == sp) wgt[t >> 3] = f / L;
  }
  __syncthreads();
  {
    const unsigned pao = (unsigned)(WS_PAO + (size_t)(b * SPLIT_A + sp) * NH * DC * 2);
    u32x4 pv[4];
#pragma unroll
    for (int i = 0; i < 4; i++) {
      const int hd = i * 8 + w;
      const float f = wgt[hd];
      const f32x4 v0 = *(const f32x4*)(ob + hd * OST + l * 8) * f, v1 = *(const f32x4*)(ob + hd * OST + l * 8 + 4) * f;
      pv[i] = u32x4{cvtpk(v0[0], v0[1]), cvtpk(v0[2], v0[3]), cvtpk(v1[0], v1[1]), cvtpk(v1[2], v1[3])};
      asm volatile("" : "+v"(pv[i]));
    }
    {
      const unsigned cs = row_sums(pv);
      if (c == 0) pcs[g * 8 + w] = cs;
    }
    TR(19);
    __syncthreads();
    const u32x2 c2 = *(const u32x2*)(pcs + (t & 15) * 2);
    const unsigned co = t < 16 ? (unsigned)(WS_PAC + (size_t)(b * SPLIT_A + sp) * NH * 8 + t * 16) : 0x7ffffff0u;
    wait_until(tg + TM_PUT);
    TR(17);
#pragma unroll
    for (int i = 0; i < 4; i++) st16u_sc1(rs, pao + (unsigned)(i * 512 + t) * 16, pv[i]);
    if (w == 0) st16u_sc1(rs, co, u32x4{c2[0], ta, c2[1], tb});
  }
  TR(20);
  asm volatile("s_waitcnt vmcnt(0)" ::: "memory");
  TR(43);
  TRDUMP();
}

__global__ __launch_bounds__(512, 2) void k_fused(const __hip_bfloat16* __restrict__ xA, const __hip_bfloat16* __restrict__ ckv,
                                                  const __hip_bfloat16* __restrict__ wqA, const __hip_bfloat16* __restrict__ wuk,
                                                  const __hip_bfloat16* __restrict__ wuv, const __hip_bfloat16* __restrict__ woA,
                                                  const __hip_bfloat16* __restrict__ xB, const __hip_bfloat16* __restrict__ kc,
                                                  const __hip_bfloat16* __restrict__ vc, const __hip_bfloat16* __restrict__ wqB,
                                                  const __hip_bfloat16* __restrict__ woB, char* __restrict__ ws,
                                                  __hip_bfloat16* __restrict__ out) {
  __shared__ __attribute__((aligned(16))) char lds[131072];
  unsigned trv = 0;
  u64 tfirst; asm volatile("s_memrealtime %0" : "=s"(tfirst));
  TR(0);
  asm volatile("s_waitcnt lgkmcnt(0)" : "+s"(tfirst));
  trv = (threadIdx.x & 63) == 59 ? (unsigned)tfirst : trv;
  { unsigned xid; asm volatile("s_getreg_b32 %0, hwreg(HW_REG_XCC_ID)" : "=s"(xid)); trv = (threadIdx.x & 63) == 63 ? (xid & 15) + 1 : trv; trv = (threadIdx.x & 63) == 60 ? (unsigned)__builtin_amdgcn_s_memtime() : trv; }
  asm volatile("" :: "s"(xA), "s"(ckv), "s"(wqA), "s"(wuk), "s"(wuv), "s"(woA), "s"(xB), "s"(kc), "s"(vc), "s"(wqB), "s"(woB), "s"(ws), "s"(out));
  TRR(58);
  const int blk = blockIdx.x, xcd = blk & 7, r = blk >> 3;
  unsigned pfv = 0;
  {
    u64 pc; unsigned len;
    asm volatile("s_getpc_b64 %0\n.Lcode_begin:\n\ts_mov_b32 %1, .Lcode_end-.Lcode_begin" : "=s"(pc), "=s"(len));
    const unsigned idx = r < 5 ? (unsigned)((r * 8 + (threadIdx.x >> 6) + xcd * 5) % 40) : 1000u;
    const __amdgpu_buffer_rsrc_t rc = mkrs((const void*)pc, len);
    pfv = __builtin_amdgcn_raw_buffer_load_b32(rc, idx * 1024 + (threadIdx.x & 63) * 16, 0, 0);
  }
  const __amdgpu_buffer_rsrc_t rs = mkrs(ws, (unsigned)WS_END);
  if (r < 8) {
    if (r < 4) role_A1(xcd * 4 + r, xA, wqA, wuk, wuv, woA, ws, rs, out, lds, trv);
    else role_A2(xcd * 4 + r - 4, ckv, ws, rs, lds, trv);
  } else {
    const int rr = r - 8, hq = rr / 6;
    role_B(((0x75643120u >> (4 * xcd)) & 7) + 8 * hq, rr - 6 * hq, xcd, ckv, xB, kc, vc, wqB, woB, ws, rs, out, lds, trv);
  }

  if (blockIdx.x == 0 && threadIdx.x < 64) {
    const unsigned t00 = __builtin_amdgcn_readfirstlane(trv);
    while ((unsigned)__builtin_amdgcn_s_memrealtime() - t00 < 7500u) __builtin_amdgcn_s_sleep(1);
    const int l = threadIdx.x;
    auto ld = [&](int blk, int wv, int idx) { return __hip_atomic_load((gu32*)(ws + WS_TR) + (blk * 8 + wv) * 64 + idx, __ATOMIC_RELAXED, __HIP_MEMORY_SCOPE_AGENT); };
    auto agg = [&](int idx, int b0, int b1, int kind) {
      int sum = 0, cnt = 0, mx = -(1 << 30), mn = 1 << 30;
      for (int i = 0; i < 4; i++) {
        const int blk = l + 64 * i;
        if (blk < b0 || blk >= b1) continue;
        int v = -(1 << 30);
        for (int wv = 0; wv < 8; wv++) { const unsigned u = ld(blk, wv, idx); if (u) { const int d = (int)(u - t00); v = d > v ? d : v; } }
        if (v > -(1 << 29)) { sum += v; cnt++; mx = v > mx ? v : mx; mn = v < mn ? v : mn; }
      }
      for (int o = 32; o > 0; o >>= 1) { sum += __shfl_xor(sum, o); cnt += __shfl_xor(cnt, o); int v = __shfl_xor(mx, o); mx = v > mx ? v : mx; v = __shfl_xor(mn, o); mn = v < mn ? v : mn; }
      return kind == 0 ? sum / (cnt ? cnt : 1) : kind == 1 ? mx : mn;
    };
    int f = agg(EX_IA, EX_A0, EX_A1, EX_GA) - agg(EX_IB, EX_B0, EX_B1, EX_GB) + EX_OFF;
    if (f < 0) f = 0;
    if (f > 2000) f = 2000;
    while ((unsigned)__builtin_amdgcn_s_memrealtime() - t00 < 10000u + (unsigned)(EX_MUL * f)) __builtin_amdgcn_s_sleep(1);
  }
  asm volatile(".Lcode_end:" :: "v"(pfv));
}

extern "C" void kernel_launch(void* const* d_in, const int* in_sizes, int n_in,
                              void* d_out, int out_size, void* d_ws, size_t ws_size,
                              hipStream_t stream) {
  k_fused<<<256, 512, 0, stream>>>((const __hip_bfloat16*)d_in[0], (const __hip_bfloat16*)d_in[1], (const __hip_bfloat16*)d_in[2],
                                   (const __hip_bfloat16*)d_in[3], (const __hip_bfloat16*)d_in[4], (const __hip_bfloat16*)d_in[5],
                                   (const __hip_bfloat16*)d_in[6], (const __hip_bfloat16*)d_in[7], (const __hip_bfloat16*)d_in[8],
                                   (const __hip_bfloat16*)d_in[9], (const __hip_bfloat16*)d_in[10], (char*)d_ws,
                                   (__hip_bfloat16*)d_out);
}
```

```cpp
#include <hip/hip_runtime.h>
#include <hip/hip_bf16.h>
#include <stdint.h>

typedef __attribute__((ext_vector_type(4))) unsigned u32x4;
typedef __attribute__((ext_vector_type(2))) unsigned u32x2;
typedef __attribute__((ext_vector_type(4))) float f32x4;
typedef __attribute__((ext_vector_type(2))) float f32x2;
typedef __attribute__((ext_vector_type(3))) float f32x3;
typedef __attribute__((ext_vector_type(16))) float f32x16;
typedef __attribute__((ext_vector_type(8))) __bf16 bf16x8;
typedef __attribute__((ext_vector_type(2))) __bf16 bf16x2;
typedef __attribute__((address_space(1))) unsigned gu32;
typedef __attribute__((address_space(1))) unsigned long long gu64;
typedef unsigned long long u64;

constexpr int NB = 4;
constexpr int NH = 32;
constexpr int SK = 4096;
constexpr int DH = 128;
constexpr int DM = 512;
constexpr int DC = 512;
constexpr int HD = NH * DH;

constexpr int SPLIT_A = 8;
constexpr int KA = SK / SPLIT_A;
constexpr int SS = 171;
constexpr int NI = 86;
constexpr int N0 = 43;
constexpr int RD = 9;
constexpr int NP = NI - 8 * RD + 8;
constexpr int NP4 = (NP + 3) & ~3;
static_assert(8 * RD + 8 <= NI, "");
constexpr int RST = 144;
constexpr int RE = RD - 1;
constexpr int PRIOF = 4;

constexpr unsigned TM_F1 = 1600;
constexpr unsigned TM_FC = 3200;
constexpr unsigned TM_ML = 1040;
constexpr unsigned TM_PUT = 1160;
constexpr unsigned TM_GET = 110;
constexpr unsigned TM_PA = 1625;
constexpr unsigned TM_T4 = 60;
constexpr unsigned TM_SW = 1720;

__device__ __forceinline__ unsigned sched(int x) { return (unsigned)((x * 338 >> 4) + (-355) + (x >= 72 ? 30 : 0)); }
constexpr int X_REP = 48, X_LOOK = 76;
constexpr int R_LOOK = 4;

constexpr float SCALE = 0.08838834764831845f;
constexpr float LOG2E = 1.4426950408889634f;
constexpr int NT = 2;
constexpr int SC1 = 16;

constexpr size_t WS_EPA  = 0;
constexpr size_t WS_EPB  = 256;
constexpr int LINE = 256;
constexpr size_t WS_F1   = 4096;
constexpr size_t WS_FS   = 12288;
constexpr size_t WS_FK   = 14336;
constexpr size_t WS_QABS = 16384;
constexpr size_t WS_PAML = WS_QABS + (size_t)NB * NH * DC * 2;
constexpr size_t WS_PAC  = WS_PAML + (size_t)NB * SPLIT_A * NH * 16;
constexpr size_t WS_PAO  = WS_PAC + (size_t)NB * SPLIT_A * NH * 8;
constexpr size_t WS_PA   = WS_PAO + (size_t)NB * SPLIT_A * NH * DC * 2;
constexpr size_t WS_PBG  = WS_PA + (size_t)NB * NH * DM * 8;
constexpr size_t WS_END  = WS_PBG + (size_t)NB * 64 * DM * 8;

__device__ __forceinline__ float bf_lo(unsigned u) { return __uint_as_float(u << 16); }
__device__ __forceinline__ float bf_hi(unsigned u) { return __uint_as_float(u & 0xffff0000u); }
__device__ __forceinline__ unsigned cvtpk(float lo, float hi) {
  f32x2 v = {lo, hi};
  return __builtin_bit_cast(unsigned, __builtin_convertvector(v, bf16x2));
}
__device__ __forceinline__ float rbf(float x) { return __uint_as_float(cvtpk(x, x) << 16); }
__device__ __forceinline__ float dot2(unsigned k, unsigned q, float acc) {
  return __builtin_amdgcn_fdot2_f32_bf16(__builtin_bit_cast(bf16x2, k), __builtin_bit_cast(bf16x2, q), acc, false);
}
template <int CTRL> __device__ __forceinline__ float dpp(float x) {
  return __builtin_bit_cast(float, __builtin_amdgcn_mov_dpp(__builtin_bit_cast(int, x), CTRL, 0xf, 0xf, true));
}
__device__ __forceinline__ float sum16(float x) {
  x += dpp<0x128>(x);
  x += dpp<0x124>(x);
  x += dpp<0x4E>(x);
  x += dpp<0xB1>(x);
  return x;
}
__device__ __forceinline__ float xrow_max(float x) {
  auto s = __builtin_amdgcn_permlane16_swap(__float_as_uint(x), __float_as_uint(x), false, false);
  x = fmaxf(__uint_as_float(s[0]), __uint_as_float(s[1]));
  auto t = __builtin_amdgcn_permlane32_swap(__float_as_uint(x), __float_as_uint(x), false, false);
  return fmaxf(__uint_as_float(t[0]), __uint_as_float(t[1]));
}
__device__ __forceinline__ float xrow_sum(float x) {
  auto s = __builtin_amdgcn_permlane16_swap(__float_as_uint(x), __float_as_uint(x), false, false);
  x = __uint_as_float(s[0]) + __uint_as_float(s[1]);
  auto t = __builtin_amdgcn_permlane32_swap(__float_as_uint(x), __float_as_uint(x), false, false);
  return __uint_as_float(t[0]) + __uint_as_float(t[1]);
}
__device__ __forceinline__ float fexp2(float x) { return __builtin_amdgcn_exp2f(x); }
__device__ __forceinline__ __amdgpu_buffer_rsrc_t mkrs(const void* p, unsigned bytes) {
  return __builtin_amdgcn_make_buffer_rsrc((void*)p, 0, (int)bytes, 0x00020000);
}


constexpr unsigned SPIN_CAP = 1u << 21;
__device__ __forceinline__ u64 ld_u64(const void* p) {
  return __hip_atomic_load((gu64*)p, __ATOMIC_RELAXED, __HIP_MEMORY_SCOPE_AGENT);
}
__device__ __forceinline__ void st_u64(void* p, u64 v) {
  __hip_atomic_store((gu64*)p, v, __ATOMIC_RELAXED, __HIP_MEMORY_SCOPE_AGENT);
}
__device__ __forceinline__ u64 next_tag(u64 e) { return e + 0x0000000100000001ull; }
__device__ __forceinline__ u64 uni64(u64 v) {
  const unsigned lo = __builtin_amdgcn_readfirstlane((unsigned)v), hi = __builtin_amdgcn_readfirstlane((unsigned)(v >> 32));
  return ((u64)hi << 32) | lo;
}
__device__ __forceinline__ void signal_flag(char* flag, u64 tag) {
  asm volatile("s_waitcnt vmcnt(0)" ::: "memory");
  __syncthreads();
  if (threadIdx.x == 0) st_u64(flag, tag);
}
template <int CTRL> __device__ __forceinline__ int dppi(int x) { return __builtin_amdgcn_mov_dpp(x, CTRL, 0xf, 0xf, true); }
__device__ __forceinline__ int sum8i(int x) { x += dppi<0xB1>(x); x += dppi<0x4E>(x); x += dppi<0x141>(x); return x; }
__device__ __forceinline__ unsigned row_sums(const u32x4 (&p)[4]) {
  unsigned c4[4];
#pragma unroll
  for (int i = 0; i < 4; i++) c4[i] = (p[i][0] + p[i][1]) + (p[i][2] + p[i][3]);
  auto s0 = __builtin_amdgcn_permlane32_swap(c4[0], c4[2], false, false);
  auto s1 = __builtin_amdgcn_permlane32_swap(c4[1], c4[3], false, false);
  auto s2 = __builtin_amdgcn_permlane16_swap(s0[0] + s0[1], s1[0] + s1[1], false, false);
  int x = (int)(s2[0] + s2[1]);
  x += dppi<0x128>(x);
  return (unsigned)sum8i(x);
}
__device__ __forceinline__ unsigned clk() { return (unsigned)__builtin_amdgcn_s_memrealtime(); }
__device__ __forceinline__ void wait_until(unsigned t) {
#pragma unroll 1
  for (int spins = 0; spins < 4096 && (int)(t - clk()) > 0; ++spins) __builtin_amdgcn_s_sleep(1);
}
__device__ __forceinline__ unsigned ref_time(unsigned v) {
  const unsigned t0 = __builtin_amdgcn_readfirstlane(v);
  return t0 + (unsigned)__builtin_amdgcn_readfirstlane(sum8i((int)(v - t0)) >> 3);
}
__device__ __forceinline__ unsigned wait_gate(const char* flag, unsigned ta, unsigned* slot, unsigned tent) {
  if (threadIdx.x < 64) {
    u64 v = 0;
    wait_until(tent + TM_FC);
#pragma unroll 1
    for (unsigned spins = 0; spins < SPIN_CAP; ++spins) {
      v = ld_u64(flag + (threadIdx.x & 7) * LINE);
      if (__all((unsigned)(v >> 32) == ta)) break;
      __builtin_amdgcn_s_sleep(12);
    }
    const unsigned tr = ref_time((unsigned)v);
    if (threadIdx.x == 0) *slot = tr;
  }
  __syncthreads();
  return __builtin_amdgcn_readfirstlane(*slot);
}
__device__ __forceinline__ void st16u_sc1(__amdgpu_buffer_rsrc_t rs, unsigned off, u32x4 v) {
  __builtin_amdgcn_raw_buffer_store_b128(v, rs, off, 0, SC1);
}
__device__ __forceinline__ u32x4 ld16u_sc1(__amdgpu_buffer_rsrc_t rs, unsigned off) {
  return __builtin_amdgcn_raw_buffer_load_b128(rs, off, 0, SC1);
}


__device__ __forceinline__ void item_dots(const u32x4 (&ks)[4], const u32x4 q, float (&s_)[4]) {
#pragma unroll
  for (int u = 0; u < 4; u++) {
    float a = dot2(ks[u][0], q[0], 0.f);
    a = dot2(ks[u][1], q[1], a);
    a = dot2(ks[u][2], q[2], a);
    a = dot2(ks[u][3], q[3], a);
    s_[u] = a;
    asm volatile("" : "+v"(s_[u]));
  }
}
__device__ __forceinline__ void item_unpack(const u32x4 (&vs)[4], f32x2 (&vf)[4][4]) {
#pragma unroll
  for (int u = 0; u < 4; u++)
#pragma unroll
    for (int j = 0; j < 4; j++) { vf[u][j] = f32x2{bf_lo(vs[u][j]), bf_hi(vs[u][j])}; asm volatile("" : "+v"(vf[u][j])); }
}
__device__ __forceinline__ f32x2 rows_sum(const float (&o)[8]) {
  float r4[4], r2[2];
#pragma unroll
  for (int j = 0; j < 4; j++) {
    auto sx = __builtin_amdgcn_permlane32_swap(__float_as_uint(o[j]), __float_as_uint(o[j + 4]), false, false);
    r4[j] = __uint_as_float(sx[0]) + __uint_as_float(sx[1]);
  }
#pragma unroll
  for (int j = 0; j < 2; j++) {
    auto sx = __builtin_amdgcn_permlane16_swap(__float_as_uint(r4[j]), __float_as_uint(r4[j + 2]), false, false);
    r2[j] = __uint_as_float(sx[0]) + __uint_as_float(sx[1]);
  }
  return f32x2{r2[0], r2[1]};
}
__device__ __forceinline__ void role_B(int h, int j6, int xcd, const __hip_bfloat16* __restrict__ ckv, const __hip_bfloat16* __restrict__ xB,
                                       const __hip_bfloat16* __restrict__ kc, const __hip_bfloat16* __restrict__ vc,
                                       const __hip_bfloat16* __restrict__ wqB, const __hip_bfloat16* __restrict__ woB,
                                       char* __restrict__ ws, __amdgpu_buffer_rsrc_t rs,
                                       __hip_bfloat16* __restrict__ out, char* lds) {
  const int t = threadIdx.x, l = t & 63;
  const int w = __builtin_amdgcn_readfirstlane(t >> 6);
  const int g = l >> 4, c = l & 15;
  const int pr = j6 >= 3 ? 1 : 0, k = j6 - 3 * pr;
  const unsigned tent = clk();
  const int b0 = 2 * pr;
  const int e0 = k == 1 ? (h & 1) : (k >> 1);
  u32x4 xr[2][2], wr[16];
  {
    const __amdgpu_buffer_rsrc_t rx = mkrs(xB, NB * DM * 2);
    const int rq_ = (0x203120 >> (4 * j6)) & 3;
    const int cw = w ^ ((rq_ & 2) << 1), r0 = rq_ & 1;
    const unsigned xo = (unsigned)((b0 * DM + cw * 64 + g * 16) * 2);
#pragma unroll
    for (int bb = 0; bb < 2; bb++)
#pragma unroll
      for (int i = 0; i < 2; i++) xr[bb][i] = __builtin_amdgcn_raw_buffer_load_b128(rx, xo + bb * DM * 2, (i ^ r0) * 16, 0);
    const __amdgpu_buffer_rsrc_t rq = mkrs(wqB, DM * HD * 2);
    const unsigned vo = (unsigned)(((cw * 64 + g * 16) * HD + h * DH + c * 8) * 2);
    const unsigned vo0 = vo + r0 * 8 * HD * 2, vo1 = vo + (1 - r0) * 8 * HD * 2;
#pragma unroll
    for (int i = 0; i < 16; i++) wr[i] = __builtin_amdgcn_raw_buffer_load_b128(rq, i < 8 ? vo0 : vo1, (i & 7) * HD * 2, 0);
  }
  constexpr unsigned KVB = (unsigned)((size_t)NB * NH * SK * DH * 2);
  const unsigned lo = (unsigned)(g * 256 + c * 16);
  const unsigned hb0 = ((unsigned)((b0 + e0) * NH + h) << 20) + (k == 1 ? (unsigned)SS << 12 : 0u);
  const unsigned hb1 = ((unsigned)((b0 + 1 - e0) * NH + h) << 20) + ((unsigned)SS << 12);
  auto seg1 = [&](int x) { return k == 1 && x >= N0; };
  auto unit = [&](int x) { const int y = seg1(x) ? x - N0 : x; return 4 * (y >> 1) + (y & 1); };
  auto soff = [&](int x) { return (seg1(x) ? hb1 : hb0) + ((unsigned)unit(x) << 12); };
  auto sco = [&](int x) { return (unsigned)((unit(x) + (seg1(x) ? 88 : 0)) * 64); };
  auto whole = [&](int x) { return x != NI - 1 && !(k == 1 && x == N0 - 1); };
  auto ld4 = [&](u32x4 (&d)[4], const void* p, bool ok, unsigned so) {
    const __amdgpu_buffer_rsrc_t r = mkrs(p, ok ? KVB : 0u);
#pragma unroll
    for (int u = 0; u < 4; u++) d[u] = __builtin_amdgcn_raw_buffer_load_b128(r, lo + u * 1024, so, NT);
  };
  unsigned pf;
  {
    const int n = ((h >> 3) * 6 + j6) * 8 + w, u = n >> 4, a = xcd * 4 + u / 3;
    pf = __builtin_amdgcn_raw_buffer_load_b32(mkrs(ckv, h == 31 ? 0 : NB * SK * DC * 2), (unsigned)l * 64, (unsigned)(a * 8 + ((5 + u % 3 + a) & 7)) * 65536u + (n & 15) * 4096u, 0);
  }
  u32x4 ra[4], rb[4], ea[4], eb[4];
  ld4(ra, kc, true, soff(w));
  ld4(rb, kc, true, soff(w) + 8192);
  u64* eps = (u64*)(lds + 33808);
  u64 epv = 0;
  if (t < 3) epv = ld_u64(ws + (t == 0 ? WS_EPA : WS_EPB + 256 * (b0 + (t == 1 ? e0 : 1 - e0))));
  float* red = (float*)lds;
  unsigned* qs = (unsigned*)(lds + 32768);
  unsigned* cnt = (unsigned*)(lds + 33856);
  float* mxd = (float*)(lds + 33872);
  float* lw = (float*)(lds + 33920);
  float* rec0 = (float*)(lds + 36864);
  char* prv = lds + 45056 + w * 1024;
  constexpr unsigned L_SC = 53248;
  float* recd = (float*)(lds + 65536);
  const unsigned lb = (unsigned)(uintptr_t)(__attribute__((address_space(3))) char*)lds;
  if (t == 0) { cnt[0] = 16u; cnt[1] = 8u * (RD + 1); cnt[2] = 0u; cnt[3] = 0u; cnt[6] = tent - 0x1000000u; mxd[0] = -INFINITY; mxd[1] = -INFINITY; }
  if (t < 64 * (NP4 - NP)) *(f32x2*)(recd + (NP + (t >> 6)) * RST + 2 * l) = f32x2{0.f, 0.f};
  if (t >= 384) *(float*)(lds + L_SC + ((t & 64) ? 172 : 84) * 64 + l * 4) = -INFINITY;
  {
    float a0[8], a1[8];
#pragma unroll
    for (int j = 0; j < 8; j++) { a0[j] = 0.f; a1[j] = 0.f; }
#pragma unroll
    for (int i = 0; i < 16; i += 2) {
      const unsigned x0 = xr[0][i >> 3][(i >> 1) & 3], x1 = xr[1][i >> 3][(i >> 1) & 3];
#pragma unroll
      for (int j = 0; j < 4; j++) {
        const unsigned pl = __builtin_amdgcn_perm(wr[i + 1][j], wr[i][j], 0x05040100u);
        const unsigned ph = __builtin_amdgcn_perm(wr[i + 1][j], wr[i][j], 0x07060302u);
        a0[2 * j] = dot2(pl, x0, a0[2 * j]); a0[2 * j + 1] = dot2(ph, x0, a0[2 * j + 1]);
        a1[2 * j] = dot2(pl, x1, a1[2 * j]); a1[2 * j + 1] = dot2(ph, x1, a1[2 * j + 1]);
      }
    }
#pragma unroll
    for (int j = 0; j < 8; j++) asm volatile("" : "+v"(a0[j]), "+v"(a1[j]));
    __builtin_amdgcn_sched_barrier(0);
    ld4(ea, kc, true, soff(w + 8));
    ld4(eb, kc, true, soff(w + 8) + 8192);
    __builtin_amdgcn_sched_barrier(0);
    float* rp = red + (w * 4 + g) * 256 + c * 8;
    *(f32x4*)(rp) = f32x4{a0[0], a0[1], a0[2], a0[3]};
    *(f32x4*)(rp + 4) = f32x4{a0[4], a0[5], a0[6], a0[7]};
    *(f32x4*)(rp + 128) = f32x4{a1[0], a1[1], a1[2], a1[3]};
    *(f32x4*)(rp + 132) = f32x4{a1[4], a1[5], a1[6], a1[7]};
  }
  __syncthreads();
  {
    const int o = t >> 1, hf = t & 1;
    float s = 0.f;
#pragma unroll
    for (int i = 0; i < 16; i++) s += red[(hf * 16 + i) * 256 + o];
    s += dpp<0xB1>(s);
    const float sn = dpp<0x4E>(s);
    if ((t & 3) == 0) qs[o >> 1] = cvtpk(s, sn);
    if (t < 3) eps[t] = epv;
  }
  __syncthreads();
  u32x4 q = *(const u32x4*)(qs + e0 * 64 + c * 4);
  const u32x4 q1 = *(const u32x4*)(qs + (1 - e0) * 64 + c * 4);
  auto lds_cnt = [&](int i) { return (unsigned)__builtin_amdgcn_readfirstlane(__hip_atomic_load(cnt + i, __ATOMIC_RELAXED, __HIP_MEMORY_SCOPE_WORKGROUP)); };
  auto lds_inc = [&](int i) {
    asm volatile("s_waitcnt lgkmcnt(0)" ::: "memory");
    if (l == 0) asm volatile("ds_add_u32 %0, %1" :: "v"(lb + 33856u + i * 4), "v"(1u) : "memory");
  };
  u32x4 wo[16];
  __builtin_amdgcn_sched_barrier(0);
#pragma unroll
  for (int i = 0; i < 16; i++) asm volatile("" : "=v"(wo[i]));
  const int nseg = k == 1 ? 2 : 1, sp = k == 1 ? 1 : 0;
  auto store_off = [&](int sg) {
    const int b = b0 + (sg ? 1 - e0 : e0);
    const unsigned og_ = (unsigned)(WS_PBG + ((size_t)(b * 64 + h * 2 + sp) * DM + w * 64 + c * 4 + g) * 8);
    return (g & 1) == 0 ? og_ : 0x7ffffff0u;
  };
  const unsigned apo = (unsigned)((c & 1) * 272 + g * 64);
  auto project = [&](const char* ap, float M, float L) {
    u32x4 a[4];
#pragma unroll
    for (int s = 0; s < 4; s++) a[s] = *(const u32x4*)(ap + s * 16);
    __builtin_amdgcn_sched_barrier(0);
    L += dpp<0xB1>(L); L += dpp<0x4E>(L); L += dpp<0x141>(L);
    __builtin_amdgcn_sched_barrier(0);
    f32x4 d[4];
#pragma unroll
    for (int s = 0; s < 4; s++)
#pragma unroll
      for (int j = 0; j < 4; j++)
        d[j] = __builtin_amdgcn_mfma_f32_16x16x32_bf16(__builtin_bit_cast(bf16x8, a[s]), __builtin_bit_cast(bf16x8, wo[4 * s + j]),
                                                       s ? d[j] : f32x4{0.f, 0.f, 0.f, 0.f}, 0, 0, 0);
    return f32x3{g < 2 ? d[0][0] + d[0][1] : d[3][0] + d[3][1], g < 2 ? d[1][0] + d[1][1] : M, g < 2 ? d[2][0] + d[2][1] : L};
  };
  f32x3 seg0 = {0.f, 0.f, 0.f};
  auto load_wo = [&]() {
    const __amdgpu_buffer_rsrc_t ro = mkrs(woB, HD * DM * 2);
    const unsigned vo = (unsigned)(((h * DH + g * 32) * DM + w * 64 + c * 4) * 2);
#pragma unroll
    for (int i = 0; i < 16; i++) {
      const u32x2 u0 = __builtin_amdgcn_raw_buffer_load_b64(ro, vo + 2 * i * DM * 2, 0, 0);
      const u32x2 u1 = __builtin_amdgcn_raw_buffer_load_b64(ro, vo + (2 * i + 1) * DM * 2, 0, 0);
      wo[i] = u32x4{u0[0], u0[1], u1[0], u1[1]};
    }
  };
  asm volatile("" :: "v"(pf));
  unsigned tp = *(const unsigned*)eps + 1u;
  asm volatile("" : "+v"(tp));
  {
    float m = -INFINITY, s_[4];
    const unsigned swo = lb + L_SC + g * 16 + (c & 3) * 4;
    auto scores = [&](unsigned so, bool ok) {
      const bool b0_ = c & 1, b1_ = c & 2;
      const float r0 = (b0_ ? s_[1] : s_[0]) + dpp<0xB1>(b0_ ? s_[0] : s_[1]);
      const float r1 = (b0_ ? s_[3] : s_[2]) + dpp<0xB1>(b0_ ? s_[2] : s_[3]);
      float r = (b1_ ? r1 : r0) + dpp<0x4E>(b1_ ? r0 : r1);
      r += dpp<0x128>(r);
      r += dpp<0x124>(r);
      r = ok ? rbf(rbf(r) * SCALE) : -INFINITY;
      m = fmaxf(m, r);
      asm volatile("ds_write_b32 %0, %1" :: "v"(swo + so), "v"(r) : "memory");
    };
    auto flush_max = [&](int sg) {
      float mw = fmaxf(m, dpp<0xB1>(m));
      mw = xrow_max(fmaxf(mw, dpp<0x4E>(mw)));
      if (l == 0) asm volatile("ds_max_f32 %0, %1" :: "v"(lb + 33872u + sg * 4), "v"(mw) : "memory");
      m = -INFINITY;
    };
    bool sw = k != 1, first = true, wl = false;
    u32x2 krep = {0u, ~tp};
    const int xw = 16 + 16 * (h >> 3);
    int x = w;
#pragma unroll 1
    while (x < NI) {
      if (!sw && x >= N0) { flush_max(0); q = q1; sw = true; }
      if (!wl && x >= xw) { load_wo(); wl = true; }
      if (x == X_LOOK) krep = __builtin_amdgcn_raw_buffer_load_b64(rs, (unsigned)WS_FK + (l & 7) * LINE, 0, SC1);
      unsigned dr; u64 sv;
      asm volatile("s_mov_b64 %1, exec\n\ts_mov_b64 exec, 1\n\tds_add_rtn_u32 %0, %2, %3\n\ts_mov_b64 exec, %1"
                   : "=&v"(dr), "=&s"(sv) : "v"(lb + 33856u), "v"(1u), "v"(ra[0][0]) : "memory");
      __builtin_amdgcn_sched_barrier(0);
      item_dots(ra, q, s_);
      asm volatile("s_waitcnt lgkmcnt(0)" : "+v"(dr) :: "memory");
      const int xn = __builtin_amdgcn_readfirstlane(dr);
      const bool more = xn < NI;
      const void* pn = more ? (const void*)kc : (const void*)vc;
      const unsigned sn = soff(more ? xn : w);
      if (xn > X_LOOK + 6) wait_until(lds_cnt(6) + sched(more ? xn - NI : w) + 60);
      __builtin_amdgcn_sched_barrier(0);
      ld4(ra, pn, true, sn);
      __builtin_amdgcn_sched_barrier(0);
      scores(sco(x), true);
      item_dots(rb, q, s_);
      __builtin_amdgcn_sched_barrier(0);
      ld4(rb, pn, !more || whole(xn), sn + 8192);
      __builtin_amdgcn_sched_barrier(0);
      scores(sco(x) + 128, whole(x));
      if (xn == X_REP && h >= 8 && h < 16 && j6 == 2) {
        if (l == 0) st_u64(ws + WS_FK + (h - 8) * LINE, ((u64)tp << 32) | (clk() + 1060));
      }
      if (x == X_LOOK && __all(krep[1] == tp)) {
        const unsigned tr = ref_time(krep[0]);
        if (l == 0) cnt[6] = tr;
      }
      if (first) {
        item_dots(ea, q, s_); scores(sco(w + 8), true);
        item_dots(eb, q, s_); scores(sco(w + 8) + 128, true);
        first = false;
      }
      x = xn;
    }
    if (!wl) {
      load_wo();
#pragma unroll
      for (int i = 0; i < 16; i++) asm volatile("" : "+v"(wo[i]));
    }
    flush_max(k == 1 && !sw ? 0 : 1);
    lds_inc(2);
    wait_until(lds_cnt(6) + sched(w + 8));
    ld4(ea, vc, true, soff(w + 8));
    ld4(eb, vc, true, soff(w + 8) + 8192);
  }
#pragma unroll
  for (int s = 0; s < 4; s++) {
    u32x4 o[4];
#pragma unroll
    for (int e = 0; e < 4; e++)
#pragma unroll
      for (int j = 0; j < 4; j++) o[j][e] = __builtin_amdgcn_perm(wo[4 * s + e][2 + (j >> 1)], wo[4 * s + e][j >> 1], (j & 1) ? 0x07060302u : 0x05040100u);
#pragma unroll
    for (int j = 0; j < 4; j++) wo[4 * s + j] = o[j];
    asm volatile("" : "+v"(wo[4 * s]), "+v"(wo[4 * s + 1]), "+v"(wo[4 * s + 2]), "+v"(wo[4 * s + 3]));
  }
  for (unsigned spins = 0; lds_cnt(2) != 8u && spins < SPIN_CAP; ++spins) __builtin_amdgcn_s_sleep(1);
  asm volatile("" ::: "memory");
  const u64 tg0 = next_tag(uni64(eps[1])), tg1 = next_tag(uni64(eps[2]));
  if (h >= 8 && h < 16 && j6 == 2 && t == 0) st_u64(ws + WS_FS + (h - 8) * LINE, ((u64)tp << 32) | (clk() + 70));
  u32x2 srep = {0u, 0u};
  const float M0 = __builtin_bit_cast(float, __builtin_amdgcn_readfirstlane(__builtin_bit_cast(int, mxd[k == 1 ? 0 : 1])));
  const float M1 = __builtin_bit_cast(float, __builtin_amdgcn_readfirstlane(__builtin_bit_cast(int, mxd[1])));
  auto weights = [&](unsigned so, float M) {
    const f32x4 sc = *(const f32x4*)(lds + L_SC + so);
    return f32x4{fexp2((sc[0] - M) * LOG2E), fexp2((sc[1] - M) * LOG2E), fexp2((sc[2] - M) * LOG2E), fexp2((sc[3] - M) * LOG2E)};
  };
  float acc[8];
#pragma unroll
  for (int j = 0; j < 8; j++) acc[j] = 0.f;
  f32x2 vf[4][4];
  f32x4 pa, pb;
  unsigned sb = 0;
  auto get_p = [&](int x) {
    const float M = seg1(x) ? M1 : M0;
    pa = weights(sco(x) + g * 16, M); pb = weights(sco(x) + 128 + g * 16, M);
    asm volatile("" : "+v"(pa), "+v"(pb));
    sb = lds_cnt(6);
  };
  auto fma_v = [&](const f32x4 p) {
#pragma unroll
    for (int u = 0; u < 4; u++)
#pragma unroll
      for (int j = 0; j < 4; j++) {
        acc[2 * j]     = fmaf(p[u], vf[u][j][0], acc[2 * j]);
        acc[2 * j + 1] = fmaf(p[u], vf[u][j][1], acc[2 * j + 1]);
      }
  };
  auto step = [&](int xn) {
    const unsigned sn = soff(xn);
    wait_until(sb + sched(xn));
    __builtin_amdgcn_sched_barrier(0);
    ld4(ra, vc, true, sn);
    __builtin_amdgcn_sched_barrier(0);
    fma_v(pa);
#pragma unroll
    for (int j = 0; j < 8; j++) asm volatile("" : "+v"(acc[j]));
    item_unpack(rb, vf);
    __builtin_amdgcn_sched_barrier(0);
    ld4(rb, vc, whole(xn), sn + 8192);
    __builtin_amdgcn_sched_barrier(0);
    fma_v(pb);
  };
  auto put_acc = [&](float* rec_) {
    *(f32x2*)(rec_ + c * 8 + g * 2) = rows_sum(acc);
#pragma unroll
    for (int j = 0; j < 8; j++) acc[j] = 0.f;
  };
  if (w >= 4) __builtin_amdgcn_s_setprio(1);
  get_p(w);
  item_unpack(ra, vf);
  step(w + 16);
  get_p(w + 8);
  item_unpack(ea, vf); fma_v(pa);
  item_unpack(eb, vf); fma_v(pb);
  {
    const bool two = t < 192;
    const f32x4 p0 = weights(t * 16, t < 352 ? M0 : M1), p1 = weights(two ? (t + 512) * 16 : 172 * 64, two ? M1 : 0.f);
    const float s0 = (p0[0] + p0[1]) + (p0[2] + p0[3]), s1 = (p1[0] + p1[1]) + (p1[2] + p1[3]);
    const bool f0 = k == 1 && t < 352;
    const float l0 = xrow_sum(sum16(f0 ? s0 : 0.f)), l1 = xrow_sum(sum16((f0 ? 0.f : s0) + (two ? s1 : 0.f)));
    if (l < 2) lw[l * 8 + w] = l ? l1 : l0;
  }
  bool sw = k != 1;
  int x = w + 16;
#pragma unroll 1
  for (; x < 8 * RD; x += 8) {
    const int rd = x >> 3;
    if (rd == PRIOF) __builtin_amdgcn_s_setprio(0);
    if (!sw && x >= N0) { put_acc(rec0 + w * 128); lds_inc(3); sw = true; }
    if (w == 3 && rd == R_LOOK + 1 && __all(srep[1] == tp)) {
      const unsigned tr = ref_time(srep[0]);
      if (l == 0) cnt[6] = tr;
    }
    if (w == 3 && rd == R_LOOK) srep = __builtin_amdgcn_raw_buffer_load_b64(rs, (unsigned)WS_FS + (l & 7) * LINE, 0, SC1);
    if (rd == RE && k == 1) {
      for (unsigned spins = 0; lds_cnt(3) != 8u && spins < SPIN_CAP; ++spins) __builtin_amdgcn_s_sleep(1);
      asm volatile("" ::: "memory");
      f32x2 a2 = {0.f, 0.f};
#pragma unroll
      for (int i = 0; i < 8; i++) a2 += *(const f32x2*)(rec0 + i * 128 + 2 * l);
      const unsigned hi = cvtpk(a2[0], a2[1]);
      *(unsigned*)(prv + l * 4) = hi;
      *(unsigned*)(prv + 272 + l * 4) = cvtpk(a2[0] - bf_lo(hi), a2[1] - bf_hi(hi));
      seg0 = project(prv + apo, M0, lw[l & 7]);
    }
    get_p(x);
    __builtin_amdgcn_sched_barrier(0);
    item_unpack(ra, vf);
    step(x + 8);
  }
  put_acc(recd + w * RST);
  unsigned o_rv = 65536u + (g * RST + w * 16 + c) * 4, o_lw = 33920u + 32u + (l & 7) * 4;
  unsigned o_oi = (g < 2 && (c & 1) == 0) ? 41216u + g * 272 + (w * 16 + c) * 2 : 45056u + w * 1024 + 256 + l * 4;
  unsigned o_ap = 41216u + apo, o_st = store_off(nseg - 1);
  unsigned o_s0 = k == 1 ? store_off(0) : 0x7ffffff0u;
  unsigned tl0 = (unsigned)(k == 1 ? tg1 : tg0);
  u32x4 sg0 = {__float_as_uint(seg0[0]), __float_as_uint(seg0[1]), __float_as_uint(seg0[2]), (unsigned)tg0};
  asm volatile("" : "+v"(o_s0), "+v"(tl0), "+v"(sg0));
  asm volatile("" : "+v"(o_rv), "+v"(o_lw), "+v"(o_oi), "+v"(o_ap), "+v"(o_st));
  {
    {
#pragma unroll 1
      for (;;) {
        get_p(x);
        unsigned dr; u64 sv;
        asm volatile("s_mov_b64 %1, exec\n\ts_mov_b64 exec, 1\n\tds_add_rtn_u32 %0, %2, %3\n\ts_mov_b64 exec, %1"
                     : "=&v"(dr), "=&s"(sv) : "v"(lb + 33860u), "v"(1u), "v"(ra[0][0]) : "memory");
        __builtin_amdgcn_sched_barrier(0);
        item_unpack(ra, vf);
        asm volatile("s_waitcnt lgkmcnt(0)" : "+v"(dr) :: "memory");
        const int xn = __builtin_amdgcn_readfirstlane(dr);
        if (xn >= NI) break;
        step(xn);
        put_acc(recd + (x - 8 * RD + 8) * RST);
        x = xn;
      }
      fma_v(pa);
      item_unpack(rb, vf);
      fma_v(pb);
      put_acc(recd + (x - 8 * RD + 8) * RST);
      asm volatile("s_waitcnt lgkmcnt(0)" ::: "memory");
      __syncthreads();
    }
    {
      float rv_[NP4 / 4];
#pragma unroll
      for (int r = 0; r < NP4 / 4; r++) rv_[r] = *(const float*)(lds + o_rv + r * 4 * RST * 4);
      const float li = *(const float*)(lds + o_lw);
      float a = 0.f, a1 = 0.f;
#pragma unroll
      for (int r = 0; r < NP4 / 4; r++) { if (r & 1) a1 += rv_[r]; else a += rv_[r]; }
      a += a1;
      a = xrow_sum(a);
      const float an = dpp<0xB1>(a);
      const unsigned hi = cvtpk(a, an);
      const unsigned lo2 = cvtpk(a - bf_lo(hi), an - bf_hi(hi));
      *(unsigned*)(lds + o_oi) = g ? lo2 : hi;
      __syncthreads();
      const f32x3 pv = project(lds + o_ap, M1, li);
      st16u_sc1(rs, o_st, u32x4{__float_as_uint(pv[0]), __float_as_uint(pv[1]), __float_as_uint(pv[2]), tl0});
      st16u_sc1(rs, o_s0, sg0);
    }
    if (w < nseg) {
      const int sg = w;
      const int b = b0 + (sg ? 1 - e0 : e0);
      const int j64 = (h & 7) * 8 + (h >> 3) * 2 + sp;
      const u64 tg = sg ? tg1 : tg0;
      const unsigned ta = (unsigned)tg;
      const unsigned og = (unsigned)(WS_PBG + ((size_t)(b * 64 + (l >> 2)) * DM + j64 * 8) * 8 + (l & 3) * 16);
      u32x4 p[4];
      {
        __builtin_amdgcn_s_sleep(4);
        wait_until(sb + TM_SW);
      }
#pragma unroll 1
      for (unsigned spins = 0; spins < SPIN_CAP; ++spins) {
        asm volatile("" ::: "memory");
#pragma unroll
        for (int i = 0; i < 4; i++) p[i] = ld16u_sc1(rs, og + i * 16 * DM * 8);
        if (__all(p[0][3] == ta && p[1][3] == ta && p[2][3] == ta && p[3][3] == ta)) break;
      }
      float v0 = 0.f, v1 = 0.f, v2 = 0.f;
#pragma unroll
      for (int i = 0; i < 4; i++) {
        const float mm = dpp<0xF5>(__uint_as_float(p[i][1]));
        const float Mh = fmaxf(mm, dpp<0x141>(mm));
        const float fq = fexp2((mm - Mh) * LOG2E);
        float L = fq * dpp<0xF5>(__uint_as_float(p[i][2]));
        L += dpp<0x141>(L);
        const float wgt = fq * __builtin_amdgcn_rcpf(L);
        v0 = fmaf(__uint_as_float(p[i][0]), wgt, v0);
        v1 = fmaf(__uint_as_float(p[i][1]), wgt, v1);
        v2 = fmaf(__uint_as_float(p[i][2]), wgt, v2);
      }
      const float u2 = dpp<0xA0>(v2);
      v1 = (l & 1) ? v0 : v1;
      v0 = (l & 1) ? u2 : v0;
      v0 += dpp<0x128>(v0); v0 += dpp<0x124>(v0); v0 = xrow_sum(v0);
      v1 += dpp<0x128>(v1); v1 += dpp<0x124>(v1); v1 = xrow_sum(v1);
      if (l < 4) {
        *(unsigned*)(out + b * 1024 + 512 + j64 * 8 + l * 2) = cvtpk(v0, v1);
        if (j64 == 0 && l == 0) st_u64(ws + WS_EPB + 256 * b, tg);
      }
    }
  }
}

__device__ __forceinline__ unsigned lds_off(int row, int ch) {
  return 256 * row + 16 * (ch ^ (((row & 3) << 2) | ((row >> 2) & 3)));
}

__device__ __forceinline__ void role_A1(int h, const __hip_bfloat16* __restrict__ xA, const __hip_bfloat16* __restrict__ wqA,
                                        const __hip_bfloat16* __restrict__ wuk, const __hip_bfloat16* __restrict__ wuv,
                                        const __hip_bfloat16* __restrict__ woA, char* __restrict__ ws,
                                        __amdgpu_buffer_rsrc_t rs, __hip_bfloat16* __restrict__ out, char* lds) {
  const int t = threadIdx.x, l = t & 63;
  const int w = __builtin_amdgcn_readfirstlane(t >> 6);
  const int g = l >> 4, c = l & 15;
  const unsigned tent = clk();
  float* red = (float*)(lds + 16384);
  const unsigned vo = (unsigned)(((w * 64 + g * 16) * HD + h * DH + c * 8) * 2);
  u64 tag;
  {
    u32x4 xr[NB][2], wr[16], wk[16];
    const __amdgpu_buffer_rsrc_t rx = mkrs(xA, NB * DM * 2);
    const unsigned xo = (unsigned)((w * 64 + g * 16) * 2);
#pragma unroll
    for (int bb = 0; bb < NB; bb++)
#pragma unroll
      for (int i = 0; i < 2; i++) xr[bb][i] = __builtin_amdgcn_raw_buffer_load_b128(rx, xo + bb * DM * 2 + i * 16, 0, 0);
    const __amdgpu_buffer_rsrc_t rq = mkrs(wqA, DM * HD * 2);
#pragma unroll
    for (int i = 0; i < 16; i++) wr[i] = __builtin_amdgcn_raw_buffer_load_b128(rq, vo, i * HD * 2, NT);
    {
      const __amdgpu_buffer_rsrc_t rku = mkrs(wuk, DC * HD * 2);
#pragma unroll
      for (int i = 0; i < 16; i++) wk[i] = __builtin_amdgcn_raw_buffer_load_b128(rku, vo, i * HD * 2, NT);
    }
    __builtin_amdgcn_sched_barrier(0);
    u64 epv = 0;
    if (t == 0) epv = ld_u64(ws + WS_EPA);
    unsigned* qs = (unsigned*)lds;
    unsigned short* qo = (unsigned short*)(lds + 4096);
    float acc[NB][8];
#pragma unroll
    for (int bb = 0; bb < NB; bb++)
#pragma unroll
      for (int j = 0; j < 8; j++) acc[bb][j] = 0.f;
#pragma unroll
    for (int i = 0; i < 16; i += 2) {
#pragma unroll
      for (int j = 0; j < 4; j++) {
        const unsigned pl = __builtin_amdgcn_perm(wr[i + 1][j], wr[i][j], 0x05040100u);
        const unsigned ph = __builtin_amdgcn_perm(wr[i + 1][j], wr[i][j], 0x07060302u);
#pragma unroll
        for (int bb = 0; bb < NB; bb++) {
          const unsigned x = xr[bb][i >> 3][(i >> 1) & 3];
          acc[bb][2 * j] = dot2(pl, x, acc[bb][2 * j]);
          acc[bb][2 * j + 1] = dot2(ph, x, acc[bb][2 * j + 1]);
        }
      }
    }
    float* rp = red + (w * 4 + g) * 512 + c * 8;
#pragma unroll
    for (int bb = 0; bb < NB; bb++) {
      *(f32x4*)(rp + bb * 128) = f32x4{acc[bb][0], acc[bb][1], acc[bb][2], acc[bb][3]};
      *(f32x4*)(rp + bb * 128 + 4) = f32x4{acc[bb][4], acc[bb][5], acc[bb][6], acc[bb][7]};
    }
    __syncthreads();
    {
      float s = 0.f;
#pragma unroll
      for (int i = 0; i < 32; i++) s += red[i * 512 + t];
      const float sn = dpp<0xB1>(s);
      if ((t & 1) == 0) qs[t >> 1] = cvtpk(s, sn);
      if (t == 0) *(u64*)(lds + 2048) = epv;
    }
    __syncthreads();
    tag = next_tag(uni64(*(const u64*)(lds + 2048)));
    {
      u32x4 qq[NB];
#pragma unroll
      for (int bb = 0; bb < NB; bb++) qq[bb] = *(const u32x4*)(qs + bb * 64 + c * 4);
#pragma unroll
      for (int i = 0; i < 16; i++) {
        float s[NB];
#pragma unroll
        for (int bb = 0; bb < NB; bb++) {
          float v = dot2(wk[i][0], qq[bb][0], 0.f);
          v = dot2(wk[i][1], qq[bb][1], v);
          v = dot2(wk[i][2], qq[bb][2], v);
          v = dot2(wk[i][3], qq[bb][3], v);
          s[bb] = sum16(v);
        }
        const float v = (c & 2) ? ((c & 1) ? s[3] : s[2]) : ((c & 1) ? s[1] : s[0]);
        if (c < NB) qo[c * 512 + w * 64 + g * 16 + i] = (unsigned short)(cvtpk(v, v) & 0xffff);
      }
    }
    __syncthreads();
    if (t < 256) {
      const int bb = t >> 6, ch = t & 63;
      st16u_sc1(rs, (unsigned)(WS_QABS + ((size_t)(bb * NH + h) * DC + ch * 8) * 2), *(const u32x4*)(qo + bb * 512 + ch * 8));
    }
    signal_flag(ws + WS_F1 + h * LINE, tag);
  }
  const unsigned ta = (unsigned)tag, tb = (unsigned)(tag >> 32);
  {
    u32x4 bv[8][2], bo[8][2];
    u32x4 pre[2][8], wo[16];
    {
      const __amdgpu_buffer_rsrc_t ruv = mkrs(wuv, DC * HD * 2);
      const __amdgpu_buffer_rsrc_t rwo = mkrs(woA, HD * DM * 2);
      const unsigned wvo = (unsigned)(((h * DH + (l >> 3) * 16) * DM + w * 64 + (l & 7) * 8) * 2);
#pragma unroll
      for (int i = 0; i < 16; i++) pre[i >> 3][i & 7] = __builtin_amdgcn_raw_buffer_load_b128(ruv, vo - g * 8 * HD * 2, ((i >> 3) * 32 + (i & 7)) * HD * 2, NT);
#pragma unroll
      for (int i = 0; i < 16; i++) wo[i] = __builtin_amdgcn_raw_buffer_load_b128(rwo, wvo, i * DM * 2, NT);
    }
#pragma unroll
    for (int j = 0; j < 8; j++)
#pragma unroll
      for (int s = 0; s < 2; s++) {
#pragma unroll
        for (int e = 0; e < 4; e++)
          bv[j][s][e] = __builtin_amdgcn_perm(pre[s][2 * e + 1][j >> 1], pre[s][2 * e][j >> 1], (j & 1) ? 0x07060302u : 0x05040100u);
        asm volatile("" : "+v"(bv[j][s]));
      }
#pragma unroll
    for (int j = 0; j < 8; j++)
#pragma unroll
      for (int s = 0; s < 2; s++) {
#pragma unroll
        for (int e = 0; e < 4; e++)
          bo[j][s][e] = __builtin_amdgcn_perm(wo[8 * s + 2 * e + 1][j >> 1], wo[8 * s + 2 * e][j >> 1], (j & 1) ? 0x07060302u : 0x05040100u);
        asm volatile("" : "+v"(bo[j][s]));
      }
    constexpr int LAS = 1040;
    unsigned short* cs = (unsigned short*)(lds + 16384);
    float* outs = (float*)(lds + 20480);
    float* cred = (float*)(lds + 32768);
    {
    {
    }
    unsigned po = (unsigned)(WS_PAO + ((size_t)(w * 4 * NH + h) * DC + l * 8) * 2);
    unsigned pc = (unsigned)(WS_PAC + (size_t)((w * 4 + g) * NH + h) * 8);
    unsigned lw_ = (unsigned)(65536 + ((w & 1) * 16 + (w >> 1) * 4) * LAS + l * 16), lr_ = (unsigned)(65536 + c * LAS + (w * 64 + g * 8) * 2);
    asm volatile("" : "+v"(po), "+v"(pc), "+v"(lw_), "+v"(lr_));
    const unsigned tw = (h & 1) ? tb : ta;
    const unsigned tg = wait_gate(ws + WS_FS, ta, (unsigned*)(lds + 8192), tent);
    wait_until(tg + TM_PUT + TM_GET);
    {
      u32x4 pr[4];
#pragma unroll 1
      for (unsigned spins = 0; spins < SPIN_CAP; ++spins) {
        asm volatile("" ::: "memory");
        const u32x2 ck = __builtin_amdgcn_raw_buffer_load_b64(rs, pc, 0, SC1);
#pragma unroll
        for (int i = 0; i < 4; i++) pr[i] = __builtin_amdgcn_raw_buffer_load_b128(rs, po, i * NH * DC * 2, SC1);
#pragma unroll
        for (int i = 0; i < 4; i++) *(u32x4*)(lds + lw_ + i * LAS) = pr[i];
        if (__all(row_sums(pr) == ck[0] && ck[1] == tw)) break;
        __builtin_amdgcn_s_sleep(2);
      }
      __syncthreads();
      u32x4 af[2][2];
#pragma unroll
      for (int tl = 0; tl < 2; tl++)
#pragma unroll
        for (int s = 0; s < 2; s++) af[tl][s] = *(const u32x4*)(lds + lr_ + tl * 16 * LAS + s * 64);
      f32x4 dd[8];
#pragma unroll
      for (int j = 0; j < 8; j++)
        dd[j] = __builtin_amdgcn_mfma_f32_16x16x32_bf16(__builtin_bit_cast(bf16x8, af[0][0]), __builtin_bit_cast(bf16x8, bv[j][0]), f32x4{0.f, 0.f, 0.f, 0.f}, 0, 0, 0);
#pragma unroll
      for (int j = 0; j < 8; j++)
        dd[j] = __builtin_amdgcn_mfma_f32_16x16x32_bf16(__builtin_bit_cast(bf16x8, af[0][1]), __builtin_bit_cast(bf16x8, bv[j][1]), dd[j], 0, 0, 0);
#pragma unroll
      for (int j = 0; j < 8; j++)
        dd[j] = __builtin_amdgcn_mfma_f32_16x16x32_bf16(__builtin_bit_cast(bf16x8, af[1][0]), __builtin_bit_cast(bf16x8, bv[j][0]), dd[j], 0, 0, 0);
#pragma unroll
      for (int j = 0; j < 8; j++)
        dd[j] = __builtin_amdgcn_mfma_f32_16x16x32_bf16(__builtin_bit_cast(bf16x8, af[1][1]), __builtin_bit_cast(bf16x8, bv[j][1]), dd[j], 0, 0, 0);
      f32x4 v[2];
#pragma unroll
      for (int j = 0; j < 8; j++) v[j >> 2][j & 3] = (dd[j][0] + dd[j][1]) + (dd[j][2] + dd[j][3]);
      *(f32x4*)(cred + (w * 4 + g) * 128 + c * 8) = v[0];
      *(f32x4*)(cred + (w * 4 + g) * 128 + c * 8 + 4) = v[1];
    }
    __syncthreads();
    {
      float s = 0.f;
#pragma unroll
      for (int i = 0; i < 8; i++) s += cred[i * 512 + t];
      const float sn = dpp<0xB1>(s);
      if ((t & 1) == 0) *(unsigned*)(cs + t) = cvtpk(s, sn);
    }
    __syncthreads();
    {
      const char* ap = (const char*)cs + ((c >> 1) & 3) * 256 + (g * 32 + (c & 1) * 16) * 2;
      const u32x4 a0 = *(const u32x4*)(ap), a1 = *(const u32x4*)(ap + 16);
      f32x4 dd[8];
#pragma unroll
      for (int j = 0; j < 8; j++)
        dd[j] = __builtin_amdgcn_mfma_f32_16x16x32_bf16(__builtin_bit_cast(bf16x8, a0), __builtin_bit_cast(bf16x8, bo[j][0]), f32x4{0.f, 0.f, 0.f, 0.f}, 0, 0, 0);
#pragma unroll
      for (int j = 0; j < 8; j++)
        dd[j] = __builtin_amdgcn_mfma_f32_16x16x32_bf16(__builtin_bit_cast(bf16x8, a1), __builtin_bit_cast(bf16x8, bo[j][1]), dd[j], 0, 0, 0);
      f32x4 v4[2];
#pragma unroll
      for (int j = 0; j < 8; j++) {
        const float u0 = (l & 8) ? dd[j][1] : dd[j][0], u1 = (l & 8) ? dd[j][3] : dd[j][2];
        const float keep = (l & 8) ? u1 : u0, give = (l & 8) ? u0 : u1;
        v4[j >> 2][j & 3] = keep + dpp<0x128>(give);
      }
      if (g < 2) {
        float* op = outs + (g * 2 + ((l >> 3) & 1)) * 512 + w * 64 + (l & 7) * 8;
        *(f32x4*)op = v4[0];
        *(f32x4*)(op + 4) = v4[1];
      }
    }
    __syncthreads();
    {
      const f32x2 va = *(const f32x2*)(outs + t * 2), vb = *(const f32x2*)(outs + 1024 + t * 2);
      wait_until(tg + TM_PA);
      const unsigned oo = (unsigned)(WS_PA + ((size_t)((t >> 8) * NH + h) * DM + (t & 255) * 2) * 8);
      st16u_sc1(rs, oo, u32x4{__float_as_uint(va[0]), ta, __float_as_uint(va[1]), tb});
      st16u_sc1(rs, oo + 2 * NH * DM * 8, u32x4{__float_as_uint(vb[0]), ta, __float_as_uint(vb[1]), tb});
    }
  if (w < 4) {
    const int bb = h >> 3, n0 = (h & 7) * 64 + w * 16;
    const unsigned og = (unsigned)(WS_PA + ((size_t)(bb * NH + (l >> 3)) * DM + n0) * 8 + (l & 7) * 16);
    u32x4 p[4];
    wait_until(tg + TM_PA + TM_T4);
#pragma unroll 1
    for (unsigned spins = 0; spins < SPIN_CAP; ++spins) {
      asm volatile("" ::: "memory");
#pragma unroll
      for (int i = 0; i < 4; i++) p[i] = ld16u_sc1(rs, og + i * 8 * DM * 8);
      bool ok = true;
#pragma unroll
      for (int i = 0; i < 4; i++) ok = ok && p[i][1] == ta && p[i][3] == tb;
      if (__all(ok)) break;
      __builtin_amdgcn_s_sleep(4);
    }
    float v0 = (__uint_as_float(p[0][0]) + __uint_as_float(p[1][0])) + (__uint_as_float(p[2][0]) + __uint_as_float(p[3][0]));
    float v1 = (__uint_as_float(p[0][2]) + __uint_as_float(p[1][2])) + (__uint_as_float(p[2][2]) + __uint_as_float(p[3][2]));
    v0 += dpp<0x128>(v0); v0 = xrow_sum(v0);
    v1 += dpp<0x128>(v1); v1 = xrow_sum(v1);
    if (l < 8) {
      *(unsigned*)(out + bb * 1024 + n0 + l * 2) = cvtpk(v0, v1);
      if (h == 0 && t == 0) st_u64(ws + WS_EPA, tag);
    }
  }
    }
  }
}

__device__ __forceinline__ void role_A2(int a, const __hip_bfloat16* __restrict__ ckv, char* __restrict__ ws,
                                        __amdgpu_buffer_rsrc_t rs, char* lds) {
  const int t = threadIdx.x, l = t & 63;
  const int w = __builtin_amdgcn_readfirstlane(t >> 6);
  const int g = l >> 4, c = l & 15;
  const unsigned tent = clk();
  u64 ep = 0;
  if (t == 0) ep = ld_u64(ws + WS_EPA);
  const int grp = w >> 2, wq = w & 3;
  const int b = a / SPLIT_A, sp = a % SPLIT_A;
  const int r = l & 31, hh = l >> 5;
  constexpr unsigned CB = (unsigned)((size_t)NB * SK * DC * 2);
  constexpr int NTL = KA / 64;
  const unsigned cvo = (unsigned)((g * DC + 128 * wq + c * 8) * 2);
  const unsigned cso = (unsigned)(((size_t)b * SK + sp * KA + grp * 32) * DC * 2);
  auto load_tile = [&](u32x4 (&pr)[8], int tile) {
    const bool ok = tile < NTL;
    const __amdgpu_buffer_rsrc_t rc = mkrs(ckv, ok ? CB : 0u);
    const unsigned so = ok ? cso + (unsigned)((tile + sp) & (NTL - 1)) * 64 * DC * 2 : 0u;
#pragma unroll
    for (int u = 0; u < 8; u++) pr[u] = __builtin_amdgcn_raw_buffer_load_b128(rc, cvo, so + u * 4 * DC * 2, NT);
  };
  char* img = lds + w * 8192;
  float* sred = (float*)(lds + 65536 + grp * 16384);
  float* mxs = (float*)(lds + 98304 + grp * 512);
  unsigned* pbuf = (unsigned*)(lds + 102400 + grp * 2048);
  float* lall = (float*)(lds + 106496);
  const unsigned imga = (unsigned)(uintptr_t)(__attribute__((address_space(3))) char*)img;
  auto put_tile = [&](u32x4 (&pr)[8]) {
#pragma unroll
    for (int u = 0; u < 8; u++) *(u32x4*)(img + lds_off(4 * u + g, c)) = pr[u];
  };
  u32x4 pre[2][8];
  load_tile(pre[0], 0);
  load_tile(pre[1], 1);
  const u64 tag0 = next_tag(uni64(ep));
  put_tile(pre[0]);
  __builtin_amdgcn_sched_barrier(0);
  load_tile(pre[0], 2);
  unsigned pf[4];
#pragma unroll
  for (int i = 0; i < 4; i++)
    pf[i] = __builtin_amdgcn_raw_buffer_load_b32(mkrs(ckv, CB), (unsigned)t * 64, (unsigned)(a * 8 + ((3 + (i >> 1) + a) & 7)) * 65536u + (i & 1) * 32768u, 0);
  if (t < 64) {
    wait_until(tent + TM_F1);
#pragma unroll 1
    for (unsigned spins = 0; spins < SPIN_CAP; ++spins) {
      __builtin_amdgcn_s_sleep(32);
      if (__all(ld_u64(ws + WS_F1 + (l & 31) * LINE) == tag0)) break;
    }
  }
  if (t == 0) *(u64*)(lds + 110592) = tag0;
  asm volatile("" :: "v"(pf[0]), "v"(pf[1]), "v"(pf[2]), "v"(pf[3]));
  __syncthreads();
  const u64 tag = uni64(*(const u64*)(lds + 110592));
  const unsigned ta = (unsigned)tag, tb = (unsigned)(tag >> 32);
  bf16x8 qf[8];
  {
    char* qi = lds + 65536;
    const unsigned qo = (unsigned)(WS_QABS + (size_t)b * NH * DC * 2 + t * 16);
    u32x4 qv[4];
#pragma unroll
    for (int i = 0; i < 4; i++) qv[i] = ld16u_sc1(rs, qo + i * 8192);
#pragma unroll
    for (int i = 0; i < 4; i++) *(u32x4*)(qi + (i * 8 + w) * 1040 + l * 16) = qv[i];
    __syncthreads();
#pragma unroll
    for (int s = 0; s < 8; s++) qf[s] = __builtin_bit_cast(bf16x8, *(const u32x4*)(qi + r * 1040 + (128 * wq + 16 * s + 8 * hh) * 2));
#pragma unroll
    for (int s = 0; s < 8; s++) asm volatile("" : "+v"(qf[s]));
    __syncthreads();
  }
  f32x16 o[4];
#pragma unroll
  for (int i = 0; i < 4; i++)
#pragma unroll
    for (int j = 0; j < 16; j++) o[i][j] = 0.f;
  float m = -INFINITY, lsum = 0.f;
  const int q4 = (l & 15) >> 2, p4 = l & 3, colh = g & 1;
  auto tile_body = [&](int tile, u32x4 (&nx)[8]) {
    f32x16 s;
#pragma unroll
    for (int j = 0; j < 16; j++) s[j] = 0.f;
    {
      u32x4 af[8];
#pragma unroll
      for (int ks = 0; ks < 8; ks++) af[ks] = *(const u32x4*)(img + lds_off(r, 2 * ks + hh));
      __builtin_amdgcn_sched_barrier(0);
#pragma unroll
      for (int ks = 0; ks < 8; ks++) s = __builtin_amdgcn_mfma_f32_32x32x16_bf16(__builtin_bit_cast(bf16x8, af[ks]), qf[ks], s, 0, 0, 0);
    }
    f32x4* sr = (f32x4*)sred;
#pragma unroll
    for (int k4 = 0; k4 < 4; k4++) sr[(k4 * 4 + wq) * 64 + l] = f32x4{s[4 * k4], s[4 * k4 + 1], s[4 * k4 + 2], s[4 * k4 + 3]};
    __syncthreads();
    float x[4];
    {
      const f32x4 v = (sr[(wq * 4) * 64 + l] + sr[(wq * 4 + 1) * 64 + l]) + (sr[(wq * 4 + 2) * 64 + l] + sr[(wq * 4 + 3) * 64 + l]);
#pragma unroll
      for (int e = 0; e < 4; e += 2) {
        const unsigned r1 = cvtpk(v[e], v[e + 1]);
        const unsigned r2 = cvtpk(bf_lo(r1) * SCALE, bf_hi(r1) * SCALE);
        x[e] = bf_lo(r2); x[e + 1] = bf_hi(r2);
      }
      float mq = fmaxf(fmaxf(x[0], x[1]), fmaxf(x[2], x[3]));
      auto sx = __builtin_amdgcn_permlane32_swap(__float_as_uint(mq), __float_as_uint(mq), false, false);
      mq = fmaxf(__uint_as_float(sx[0]), __uint_as_float(sx[1]));
      if (hh == 0) mxs[r * 4 + wq] = mq;
    }
    __syncthreads();
    const f32x4 m4 = *(const f32x4*)(mxs + r * 4);
    const float mn = fmaxf(fmaxf(m, fmaxf(m4[0], m4[1])), fmaxf(m4[2], m4[3]));
    const float alpha = fexp2((m - mn) * LOG2E);
    {
      const unsigned p0 = cvtpk(fexp2((x[0] - mn) * LOG2E), fexp2((x[1] - mn) * LOG2E));
      const unsigned p1 = cvtpk(fexp2((x[2] - mn) * LOG2E), fexp2((x[3] - mn) * LOG2E));
      *(u32x2*)(pbuf + l * 8 + wq * 2) = u32x2{p0, p1};
      lsum = lsum * alpha + ((bf_lo(p0) + bf_hi(p0)) + (bf_lo(p1) + bf_hi(p1)));
    }
    m = mn;
    __syncthreads();
    unsigned pk[8];
    {
      const u32x4 a0 = *(const u32x4*)(pbuf + l * 8), a1 = *(const u32x4*)(pbuf + l * 8 + 4);
      pk[0] = a0[0]; pk[1] = a0[1]; pk[2] = a0[2]; pk[3] = a0[3]; pk[4] = a1[0]; pk[5] = a1[1]; pk[6] = a1[2]; pk[7] = a1[3];
    }
    if (__any(alpha != 1.f)) {
#pragma unroll
      for (int i = 0; i < 4; i++)
#pragma unroll
        for (int j = 0; j < 16; j++) o[i][j] *= alpha;
    }
#pragma unroll
    for (int s2 = 0; s2 < 2; s2++) {
      u32x2 tv[4][2];
#pragma unroll
      for (int ct = 0; ct < 4; ct++) {
        const int ch = 4 * ct + 2 * colh + (p4 >> 1);
        const unsigned a0 = lds_off(4 * hh + q4, ch) + 8 * (p4 & 1);
        const unsigned a1 = lds_off(8 + 4 * hh + q4, ch) + 8 * (p4 & 1);
        if (s2 == 0) {
          asm volatile("ds_read_b64_tr_b16 %0, %1" : "=v"(tv[ct][0]) : "v"(imga + a0) : "memory");
          asm volatile("ds_read_b64_tr_b16 %0, %1" : "=v"(tv[ct][1]) : "v"(imga + a1) : "memory");
        } else {
          asm volatile("ds_read_b64_tr_b16 %0, %1 offset:4096" : "=v"(tv[ct][0]) : "v"(imga + a0) : "memory");
          asm volatile("ds_read_b64_tr_b16 %0, %1 offset:4096" : "=v"(tv[ct][1]) : "v"(imga + a1) : "memory");
        }
      }
      asm volatile("s_waitcnt lgkmcnt(0)" ::: "memory");
      __builtin_amdgcn_sched_barrier(0);
      u32x4 pb = {pk[4 * s2], pk[4 * s2 + 1], pk[4 * s2 + 2], pk[4 * s2 + 3]};
#pragma unroll
      for (int ct = 0; ct < 4; ct++) {
        u32x4 av = {tv[ct][0][0], tv[ct][0][1], tv[ct][1][0], tv[ct][1][1]};
        o[ct] = __builtin_amdgcn_mfma_f32_32x32x16_bf16(__builtin_bit_cast(bf16x8, av), __builtin_bit_cast(bf16x8, pb), o[ct], 0, 0, 0);
      }
    }
    put_tile(nx);
    __builtin_amdgcn_sched_barrier(0);
    load_tile(nx, tile + 3);
  };
#pragma unroll 1
  for (int tile = 0; tile < NTL; tile += 2) {
    tile_body(tile, pre[1]);
    tile_body(tile + 1, pre[0]);
  }
  constexpr int OST = 516;
  float* ob = (float*)lds;
  float* mlb = (float*)(lds + 32 * OST * 4);
  lall[w * 64 + l] = lsum;
  __syncthreads();
  {
    const float* lp = lall + grp * 256 + r;
    lsum = ((lp[0] + lp[32]) + (lp[64] + lp[96])) + ((lp[128] + lp[160]) + (lp[192] + lp[224]));
  }
  if (grp == 1) {
#pragma unroll
    for (int ct = 0; ct < 4; ct++)
#pragma unroll
      for (int j4 = 0; j4 < 4; j4++)
        *(f32x4*)(ob + r * OST + 128 * wq + 32 * ct + 8 * j4 + 4 * hh) =
            f32x4{o[ct][4 * j4], o[ct][4 * j4 + 1], o[ct][4 * j4 + 2], o[ct][4 * j4 + 3]};
    if (wq == 0 && l < 32) { mlb[2 * r] = m; mlb[2 * r + 1] = lsum; }
  }
  __syncthreads();
  if (grp == 0) {
    const float m1 = mlb[2 * r], l1 = mlb[2 * r + 1];
    const float M = fmaxf(m, m1);
    const float f0 = fexp2((m - M) * LOG2E), f1 = fexp2((m1 - M) * LOG2E);
#pragma unroll
    for (int ct = 0; ct < 4; ct++)
#pragma unroll
      for (int j4 = 0; j4 < 4; j4++) {
        float* p = ob + r * OST + 128 * wq + 32 * ct + 8 * j4 + 4 * hh;
        const f32x4 o1 = *(const f32x4*)p;
        *(f32x4*)p = f32x4{o[ct][4 * j4] * f0 + o1[0] * f1, o[ct][4 * j4 + 1] * f0 + o1[1] * f1,
                           o[ct][4 * j4 + 2] * f0 + o1[2] * f1, o[ct][4 * j4 + 3] * f0 + o1[3] * f1};
      }
    if (wq == 0 && l < 32)
      st16u_sc1(rs, (unsigned)(WS_PAML + ((size_t)(b * SPLIT_A + sp) * NH + r) * 16),
                u32x4{__float_as_uint(M), ta, __float_as_uint(lsum * f0 + l1 * f1), tb});
  }
  float* wgt = mlb + 64;
  unsigned* pcs = (unsigned*)(mlb + 128);
  const unsigned tg = wait_gate(ws + WS_FS, ta, (unsigned*)(mlb + 96), tent);
  if (t < 256) {
    wait_until(tg + TM_ML);
    const unsigned mo = (unsigned)(WS_PAML + ((size_t)(b * SPLIT_A + (t & 7)) * NH + (t >> 3)) * 16);
    u32x4 mv;
#pragma unroll 1
    for (unsigned spins = 0; spins < SPIN_CAP; ++spins) {
      asm volatile("" ::: "memory");
      mv = ld16u_sc1(rs, mo);
      if (__all(mv[1] == ta && mv[3] == tb)) break;
      __builtin_amdgcn_s_sleep(16);
    }
    const float mm = __uint_as_float(mv[0]);
    float M = fmaxf(mm, dpp<0xB1>(mm)); M = fmaxf(M, dpp<0x4E>(M)); M = fmaxf(M, dpp<0x141>(M));
    const float f = fexp2((mm - M) * LOG2E);
    float L = f * __uint_as_float(mv[2]);
    L += dpp<0xB1>(L); L += dpp<0x4E>(L); L += dpp<0x141>(L);
    if ((t & 7) == sp) wgt[t >> 3] = f / L;
  }
  __syncthreads();
  {
    const unsigned pao = (unsigned)(WS_PAO + (size_t)(b * SPLIT_A + sp) * NH * DC * 2);
    u32x4 pv[4];
#pragma unroll
    for (int i = 0; i < 4; i++) {
      const int hd = i * 8 + w;
      const float f = wgt[hd];
      const f32x4 v0 = *(const f32x4*)(ob + hd * OST + l * 8) * f, v1 = *(const f32x4*)(ob + hd * OST + l * 8 + 4) * f;
      pv[i] = u32x4{cvtpk(v0[0], v0[1]), cvtpk(v0[2], v0[3]), cvtpk(v1[0], v1[1]), cvtpk(v1[2], v1[3])};
      asm volatile("" : "+v"(pv[i]));
    }
    {
      const unsigned cs = row_sums(pv);
      if (c == 0) pcs[g * 8 + w] = cs;
    }
    __syncthreads();
    const u32x2 c2 = *(const u32x2*)(pcs + (t & 15) * 2);
    const unsigned co = t < 16 ? (unsigned)(WS_PAC + (size_t)(b * SPLIT_A + sp) * NH * 8 + t * 16) : 0x7ffffff0u;
    wait_until(tg + TM_PUT);
#pragma unroll
    for (int i = 0; i < 4; i++) st16u_sc1(rs, pao + (unsigned)(i * 512 + t) * 16, pv[i]);
    if (w == 0) st16u_sc1(rs, co, u32x4{c2[0], ta, c2[1], tb});
  }
}

__global__ __launch_bounds__(512, 2) void k_fused(const __hip_bfloat16* __restrict__ xA, const __hip_bfloat16* __restrict__ ckv,
                                                  const __hip_bfloat16* __restrict__ wqA, const __hip_bfloat16* __restrict__ wuk,
                                                  const __hip_bfloat16* __restrict__ wuv, const __hip_bfloat16* __restrict__ woA,
                                                  const __hip_bfloat16* __restrict__ xB, const __hip_bfloat16* __restrict__ kc,
                                                  const __hip_bfloat16* __restrict__ vc, const __hip_bfloat16* __restrict__ wqB,
                                                  const __hip_bfloat16* __restrict__ woB, char* __restrict__ ws,
                                                  __hip_bfloat16* __restrict__ out) {
  __shared__ __attribute__((aligned(16))) char lds[131072];
  asm volatile("" :: "s"(xA), "s"(ckv), "s"(wqA), "s"(wuk), "s"(wuv), "s"(woA), "s"(xB), "s"(kc), "s"(vc), "s"(wqB), "s"(woB), "s"(ws), "s"(out));
  const int blk = blockIdx.x, xcd = blk & 7, r = blk >> 3;
  unsigned pfv = 0;
  {
    u64 pc; unsigned len;
    asm volatile("s_getpc_b64 %0\n.Lcode_begin:\n\ts_mov_b32 %1, .Lcode_end-.Lcode_begin" : "=s"(pc), "=s"(len));
    const unsigned idx = r < 5 ? (unsigned)((r * 8 + (threadIdx.x >> 6) + xcd * 5) % 40) : 1000u;
    const __amdgpu_buffer_rsrc_t rc = mkrs((const void*)pc, len);
    pfv = __builtin_amdgcn_raw_buffer_load_b32(rc, idx * 1024 + (threadIdx.x & 63) * 16, 0, 0);
  }
  const __amdgpu_buffer_rsrc_t rs = mkrs(ws, (unsigned)WS_END);
  if (r < 8) {
    if (r < 4) role_A1(xcd * 4 + r, xA, wqA, wuk, wuv, woA, ws, rs, out, lds);
    else role_A2(xcd * 4 + r - 4, ckv, ws, rs, lds);
  } else {
    const int rr = r - 8, hq = rr / 6;
    role_B(((0x75643120u >> (4 * xcd)) & 7) + 8 * hq, rr - 6 * hq, xcd, ckv, xB, kc, vc, wqB, woB, ws, rs, out, lds);
  }
  asm volatile(".Lcode_end:" :: "v"(pfv));
}

extern "C" void kernel_launch(void* const* d_in, const int* in_sizes, int n_in,
                              void* d_out, int out_size, void* d_ws, size_t ws_size,
                              hipStream_t stream) {
  k_fused<<<256, 512, 0, stream>>>((const __hip_bfloat16*)d_in[0], (const __hip_bfloat16*)d_in[1], (const __hip_bfloat16*)d_in[2],
                                   (const __hip_bfloat16*)d_in[3], (const __hip_bfloat16*)d_in[4], (const __hip_bfloat16*)d_in[5],
                                   (const __hip_bfloat16*)d_in[6], (const __hip_bfloat16*)d_in[7], (const __hip_bfloat16*)d_in[8],
                                   (const __hip_bfloat16*)d_in[9], (const __hip_bfloat16*)d_in[10], (char*)d_ws,
                                   (__hip_bfloat16*)d_out);
}
```

```cpp
#include <hip/hip_runtime.h>
#include <hip/hip_bf16.h>
#include <stdint.h>

typedef __attribute__((ext_vector_type(4))) unsigned u32x4;
typedef __attribute__((ext_vector_type(2))) unsigned u32x2;
typedef __attribute__((ext_vector_type(4))) float f32x4;
typedef __attribute__((ext_vector_type(2))) float f32x2;
typedef __attribute__((ext_vector_type(3))) float f32x3;
typedef __attribute__((ext_vector_type(16))) float f32x16;
typedef __attribute__((ext_vector_type(8))) __bf16 bf16x8;
typedef __attribute__((ext_vector_type(2))) __bf16 bf16x2;
typedef __attribute__((address_space(1))) unsigned gu32;
typedef __attribute__((address_space(1))) unsigned long long gu64;
typedef unsigned long long u64;

constexpr int NB = 4;
constexpr int NH = 32;
constexpr int SK = 4096;
constexpr int DH = 128;
constexpr int DM = 512;
constexpr int DC = 512;
constexpr int HD = NH * DH;

constexpr int SPLIT_A = 8;
constexpr int KA = SK / SPLIT_A;
constexpr int SS = 171;
constexpr int NI = 86;
constexpr int N0 = 43;
constexpr int RD = 9;
constexpr int NP = NI - 8 * RD + 8;
constexpr int NP4 = (NP + 3) & ~3;
constexpr int RST = 144;
constexpr int RE = RD - 1;
constexpr int PRIOF = 4;

constexpr unsigned TM_F1 = 1600;
constexpr unsigned TM_EP = 1800;
constexpr unsigned TM_FC = 3200;
constexpr unsigned TM_ML = 1040;
constexpr unsigned TM_PUT = 1160;
constexpr unsigned TM_GET = 110;
constexpr unsigned TM_PA = 1625;
constexpr unsigned TM_T4 = 60;
constexpr unsigned TM_SW = 1720;

__device__ __forceinline__ unsigned sched(int x) { return (unsigned)((x * 338 >> 4) + (-355) + (x >= 72 ? 30 : 0)); }
constexpr int X_REP = 48, X_LOOK = 76;
constexpr int R_LOOK = 4;

constexpr float SCALE = 0.08838834764831845f;
constexpr float LOG2E = 1.4426950408889634f;
constexpr int NT = 2;
constexpr int SC1 = 16;

constexpr size_t WS_EP   = 0;
constexpr int LINE = 256;
constexpr size_t WS_FS   = 4096;
constexpr size_t WS_FK   = 6144;
constexpr size_t WS_F1   = 8192;
constexpr size_t WS_CK   = WS_F1 + 32 * LINE;
constexpr size_t WS_QABS = WS_CK + 224 * LINE;
constexpr size_t WS_PAML = WS_QABS + (size_t)NB * NH * DC * 2;
constexpr size_t WS_PAC  = WS_PAML + (size_t)NB * SPLIT_A * NH * 16;
constexpr size_t WS_PAO  = WS_PAC + (size_t)NB * SPLIT_A * NH * 8;
constexpr size_t WS_PA   = WS_PAO + (size_t)NB * SPLIT_A * NH * DC * 2;
constexpr size_t WS_PBG  = WS_PA + (size_t)NB * NH * DM * 8;
constexpr size_t WS_END  = WS_PBG + (size_t)NB * 64 * DM * 8;

__device__ __forceinline__ float bf_lo(unsigned u) { return __uint_as_float(u << 16); }
__device__ __forceinline__ float bf_hi(unsigned u) { return __uint_as_float(u & 0xffff0000u); }
__device__ __forceinline__ unsigned cvtpk(float lo, float hi) {
  f32x2 v = {lo, hi};
  return __builtin_bit_cast(unsigned, __builtin_convertvector(v, bf16x2));
}
__device__ __forceinline__ float rbf(float x) { return __uint_as_float(cvtpk(x, x) << 16); }
__device__ __forceinline__ float dot2(unsigned k, unsigned q, float acc) {
  return __builtin_amdgcn_fdot2_f32_bf16(__builtin_bit_cast(bf16x2, k), __builtin_bit_cast(bf16x2, q), acc, false);
}
template <int CTRL> __device__ __forceinline__ float dpp(float x) {
  return __builtin_bit_cast(float, __builtin_amdgcn_mov_dpp(__builtin_bit_cast(int, x), CTRL, 0xf, 0xf, true));
}
__device__ __forceinline__ float sum16(float x) {
  x += dpp<0x128>(x);
  x += dpp<0x124>(x);
  x += dpp<0x4E>(x);
  x += dpp<0xB1>(x);
  return x;
}
__device__ __forceinline__ float xrow_max(float x) {
  auto s = __builtin_amdgcn_permlane16_swap(__float_as_uint(x), __float_as_uint(x), false, false);
  x = fmaxf(__uint_as_float(s[0]), __uint_as_float(s[1]));
  auto t = __builtin_amdgcn_permlane32_swap(__float_as_uint(x), __float_as_uint(x), false, false);
  return fmaxf(__uint_as_float(t[0]), __uint_as_float(t[1]));
}
__device__ __forceinline__ float xrow_sum(float x) {
  auto s = __builtin_amdgcn_permlane16_swap(__float_as_uint(x), __float_as_uint(x), false, false);
  x = __uint_as_float(s[0]) + __uint_as_float(s[1]);
  auto t = __builtin_amdgcn_permlane32_swap(__float_as_uint(x), __float_as_uint(x), false, false);
  return __uint_as_float(t[0]) + __uint_as_float(t[1]);
}
__device__ __forceinline__ float fexp2(float x) { return __builtin_amdgcn_exp2f(x); }
__device__ __forceinline__ __amdgpu_buffer_rsrc_t mkrs(const void* p, unsigned bytes) {
  return __builtin_amdgcn_make_buffer_rsrc((void*)p, 0, (int)bytes, 0x00020000);
}


constexpr unsigned SPIN_CAP = 1u << 21;
__device__ __forceinline__ u64 ld_u64(const void* p) {
  return __hip_atomic_load((gu64*)p, __ATOMIC_RELAXED, __HIP_MEMORY_SCOPE_AGENT);
}
__device__ __forceinline__ void st_u64(void* p, u64 v) {
  __hip_atomic_store((gu64*)p, v, __ATOMIC_RELAXED, __HIP_MEMORY_SCOPE_AGENT);
}
__device__ __forceinline__ u64 next_tag(u64 e) { return e + 0x0000000100000001ull; }
__device__ __forceinline__ u64 uni64(u64 v) {
  const unsigned lo = __builtin_amdgcn_readfirstlane((unsigned)v), hi = __builtin_amdgcn_readfirstlane((unsigned)(v >> 32));
  return ((u64)hi << 32) | lo;
}
__device__ __forceinline__ void signal_flag(char* flag, u64 tag) {
  asm volatile("s_waitcnt vmcnt(0)" ::: "memory");
  __syncthreads();
  if (threadIdx.x == 0) st_u64(flag, tag);
}
template <int CTRL> __device__ __forceinline__ int dppi(int x) { return __builtin_amdgcn_mov_dpp(x, CTRL, 0xf, 0xf, true); }
__device__ __forceinline__ int sum8i(int x) { x += dppi<0xB1>(x); x += dppi<0x4E>(x); x += dppi<0x141>(x); return x; }
__device__ __forceinline__ unsigned row_sums(const u32x4 (&p)[4]) {
  unsigned c4[4];
#pragma unroll
  for (int i = 0; i < 4; i++) c4[i] = (p[i][0] + p[i][1]) + (p[i][2] + p[i][3]);
  auto s0 = __builtin_amdgcn_permlane32_swap(c4[0], c4[2], false, false);
  auto s1 = __builtin_amdgcn_permlane32_swap(c4[1], c4[3], false, false);
  auto s2 = __builtin_amdgcn_permlane16_swap(s0[0] + s0[1], s1[0] + s1[1], false, false);
  int x = (int)(s2[0] + s2[1]);
  x += dppi<0x128>(x);
  return (unsigned)sum8i(x);
}
__device__ __forceinline__ unsigned clk() { return (unsigned)__builtin_amdgcn_s_memrealtime(); }
__device__ __forceinline__ void wait_until(unsigned t) {
#pragma unroll 1
  for (int spins = 0; spins < 4096 && (int)(t - clk()) > 0; ++spins) __builtin_amdgcn_s_sleep(1);
}
__device__ __forceinline__ unsigned ref_time(unsigned v) {
  const unsigned t0 = __builtin_amdgcn_readfirstlane(v);
  return t0 + (unsigned)__builtin_amdgcn_readfirstlane(sum8i((int)(v - t0)) >> 3);
}
__device__ __forceinline__ unsigned wait_gate(const char* flag, unsigned ta, unsigned* slot, unsigned tent) {
  if (threadIdx.x < 64) {
    u64 v = 0;
    wait_until(tent + TM_FC);
#pragma unroll 1
    for (unsigned spins = 0; spins < SPIN_CAP; ++spins) {
      v = ld_u64(flag + (threadIdx.x & 7) * LINE);
      if (__all((unsigned)(v >> 32) == ta)) break;
      __builtin_amdgcn_s_sleep(12);
    }
    const unsigned tr = ref_time((unsigned)v);
    if (threadIdx.x == 0) *slot = tr;
  }
  __syncthreads();
  return __builtin_amdgcn_readfirstlane(*slot);
}
__device__ __forceinline__ void st16u_sc1(__amdgpu_buffer_rsrc_t rs, unsigned off, u32x4 v) {
  __builtin_amdgcn_raw_buffer_store_b128(v, rs, off, 0, SC1);
}
__device__ __forceinline__ u32x4 ld16u_sc1(__amdgpu_buffer_rsrc_t rs, unsigned off) {
  return __builtin_amdgcn_raw_buffer_load_b128(rs, off, 0, SC1);
}


__device__ __forceinline__ void item_dots(const u32x4 (&ks)[4], const u32x4 q, float (&s_)[4]) {
#pragma unroll
  for (int u = 0; u < 4; u++) {
    float a = dot2(ks[u][0], q[0], 0.f);
    a = dot2(ks[u][1], q[1], a);
    a = dot2(ks[u][2], q[2], a);
    a = dot2(ks[u][3], q[3], a);
    s_[u] = a;
    asm volatile("" : "+v"(s_[u]));
  }
}
__device__ __forceinline__ void item_unpack(const u32x4 (&vs)[4], f32x2 (&vf)[4][4]) {
#pragma unroll
  for (int u = 0; u < 4; u++)
#pragma unroll
    for (int j = 0; j < 4; j++) { vf[u][j] = f32x2{bf_lo(vs[u][j]), bf_hi(vs[u][j])}; asm volatile("" : "+v"(vf[u][j])); }
}
__device__ __forceinline__ f32x2 rows_sum(const float (&o)[8]) {
  float r4[4], r2[2];
#pragma unroll
  for (int j = 0; j < 4; j++) {
    auto sx = __builtin_amdgcn_permlane32_swap(__float_as_uint(o[j]), __float_as_uint(o[j + 4]), false, false);
    r4[j] = __uint_as_float(sx[0]) + __uint_as_float(sx[1]);
  }
#pragma unroll
  for (int j = 0; j < 2; j++) {
    auto sx = __builtin_amdgcn_permlane16_swap(__float_as_uint(r4[j]), __float_as_uint(r4[j + 2]), false, false);
    r2[j] = __uint_as_float(sx[0]) + __uint_as_float(sx[1]);
  }
  return f32x2{r2[0], r2[1]};
}
__device__ __forceinline__ void role_B(int h, int j6, int xcd, const __hip_bfloat16* __restrict__ ckv, const __hip_bfloat16* __restrict__ xB,
                                       const __hip_bfloat16* __restrict__ kc, const __hip_bfloat16* __restrict__ vc,
                                       const __hip_bfloat16* __restrict__ wqB, const __hip_bfloat16* __restrict__ woB,
                                       char* __restrict__ ws, __amdgpu_buffer_rsrc_t rs,
                                       __hip_bfloat16* __restrict__ out, char* lds) {
  const int t = threadIdx.x, l = t & 63;
  const int w = __builtin_amdgcn_readfirstlane(t >> 6);
  const int g = l >> 4, c = l & 15;
  const int pr = j6 >= 3 ? 1 : 0, k = j6 - 3 * pr;
  const unsigned tent = clk();
  const int b0 = 2 * pr;
  const int e0 = k == 1 ? (h & 1) : (k >> 1);
  u32x4 xr[2][2], wr[16];
  {
    const __amdgpu_buffer_rsrc_t rx = mkrs(xB, NB * DM * 2);
    const int rq_ = (0x203120 >> (4 * j6)) & 3;
    const int cw = w ^ ((rq_ & 2) << 1), r0 = rq_ & 1;
    const unsigned xo = (unsigned)((b0 * DM + cw * 64 + g * 16) * 2);
#pragma unroll
    for (int bb = 0; bb < 2; bb++)
#pragma unroll
      for (int i = 0; i < 2; i++) xr[bb][i] = __builtin_amdgcn_raw_buffer_load_b128(rx, xo + bb * DM * 2, (i ^ r0) * 16, 0);
    const __amdgpu_buffer_rsrc_t rq = mkrs(wqB, DM * HD * 2);
    const unsigned vo = (unsigned)(((cw * 64 + g * 16) * HD + h * DH + c * 8) * 2);
    const unsigned vo0 = vo + r0 * 8 * HD * 2, vo1 = vo + (1 - r0) * 8 * HD * 2;
#pragma unroll
    for (int i = 0; i < 16; i++) wr[i] = __builtin_amdgcn_raw_buffer_load_b128(rq, i < 8 ? vo0 : vo1, (i & 7) * HD * 2, 0);
  }
  constexpr unsigned KVB = (unsigned)((size_t)NB * NH * SK * DH * 2);
  const unsigned lo = (unsigned)(g * 256 + c * 16);
  const unsigned hb0 = ((unsigned)((b0 + e0) * NH + h) << 20) + (k == 1 ? (unsigned)SS << 12 : 0u);
  const unsigned hb1 = ((unsigned)((b0 + 1 - e0) * NH + h) << 20) + ((unsigned)SS << 12);
  auto seg1 = [&](int x) { return k == 1 && x >= N0; };
  auto unit = [&](int x) { const int y = seg1(x) ? x - N0 : x; return 4 * (y >> 1) + (y & 1); };
  auto soff = [&](int x) { return (seg1(x) ? hb1 : hb0) + ((unsigned)unit(x) << 12); };
  auto sco = [&](int x) { return (unsigned)((unit(x) + (seg1(x) ? 88 : 0)) * 64); };
  auto whole = [&](int x) { return x != NI - 1 && !(k == 1 && x == N0 - 1); };
  auto ld4 = [&](u32x4 (&d)[4], const void* p, bool ok, unsigned so) {
    const __amdgpu_buffer_rsrc_t r = mkrs(p, ok ? KVB : 0u);
#pragma unroll
    for (int u = 0; u < 4; u++) d[u] = __builtin_amdgcn_raw_buffer_load_b128(r, lo + u * 1024, so, NT);
  };
  unsigned pf;
  {
    const int n = ((h >> 3) * 6 + j6) * 8 + w, u = n >> 4, a = xcd * 4 + u / 3;
    pf = __builtin_amdgcn_raw_buffer_load_b32(mkrs(ckv, h == 31 ? 0 : NB * SK * DC * 2), (unsigned)l * 64, (unsigned)(a * 8 + ((5 + u % 3 + a) & 7)) * 65536u + (n & 15) * 4096u, 0);
  }
  u32x4 ra[4], rb[4], ea[4], eb[4];
  ld4(ra, kc, true, soff(w));
  ld4(rb, kc, true, soff(w) + 8192);
  u64* eps = (u64*)(lds + 33808);
  u64 epv = 0;
  if (t == 0) epv = ld_u64(ws + WS_EP);
  float* red = (float*)lds;
  unsigned* qs = (unsigned*)(lds + 32768);
  unsigned* cnt = (unsigned*)(lds + 33856);
  float* mxd = (float*)(lds + 33872);
  float* lw = (float*)(lds + 33920);
  float* rec0 = (float*)(lds + 36864);
  char* prv = lds + 45056 + w * 1024;
  constexpr unsigned L_SC = 53248;
  float* recd = (float*)(lds + 65536);
  const unsigned lb = (unsigned)(uintptr_t)(__attribute__((address_space(3))) char*)lds;
  if (t == 0) { cnt[0] = 16u; cnt[1] = 8u * (RD + 1); cnt[2] = 0u; cnt[3] = 0u; cnt[6] = tent - 0x1000000u; mxd[0] = -INFINITY; mxd[1] = -INFINITY; }
  if (t < 64 * (NP4 - NP)) *(f32x2*)(recd + (NP + (t >> 6)) * RST + 2 * l) = f32x2{0.f, 0.f};
  if (t >= 384) *(float*)(lds + L_SC + ((t & 64) ? 172 : 84) * 64 + l * 4) = -INFINITY;
  {
    float a0[8], a1[8];
#pragma unroll
    for (int j = 0; j < 8; j++) { a0[j] = 0.f; a1[j] = 0.f; }
#pragma unroll
    for (int i = 0; i < 16; i += 2) {
      const unsigned x0 = xr[0][i >> 3][(i >> 1) & 3], x1 = xr[1][i >> 3][(i >> 1) & 3];
#pragma unroll
      for (int j = 0; j < 4; j++) {
        const unsigned pl = __builtin_amdgcn_perm(wr[i + 1][j], wr[i][j], 0x05040100u);
        const unsigned ph = __builtin_amdgcn_perm(wr[i + 1][j], wr[i][j], 0x07060302u);
        a0[2 * j] = dot2(pl, x0, a0[2 * j]); a0[2 * j + 1] = dot2(ph, x0, a0[2 * j + 1]);
        a1[2 * j] = dot2(pl, x1, a1[2 * j]); a1[2 * j + 1] = dot2(ph, x1, a1[2 * j + 1]);
      }
    }
#pragma unroll
    for (int j = 0; j < 8; j++) asm volatile("" : "+v"(a0[j]), "+v"(a1[j]));
    __builtin_amdgcn_sched_barrier(0);
    ld4(ea, kc, true, soff(w + 8));
    ld4(eb, kc, true, soff(w + 8) + 8192);
    __builtin_amdgcn_sched_barrier(0);
    float* rp = red + (w * 4 + g) * 256 + c * 8;
    *(f32x4*)(rp) = f32x4{a0[0], a0[1], a0[2], a0[3]};
    *(f32x4*)(rp + 4) = f32x4{a0[4], a0[5], a0[6], a0[7]};
    *(f32x4*)(rp + 128) = f32x4{a1[0], a1[1], a1[2], a1[3]};
    *(f32x4*)(rp + 132) = f32x4{a1[4], a1[5], a1[6], a1[7]};
  }
  __syncthreads();
  {
    const int o = t >> 1, hf = t & 1;
    float s = 0.f;
#pragma unroll
    for (int i = 0; i < 16; i++) s += red[(hf * 16 + i) * 256 + o];
    s += dpp<0xB1>(s);
    const float sn = dpp<0x4E>(s);
    if ((t & 3) == 0) qs[o >> 1] = cvtpk(s, sn);
    if (t == 0) { *eps = epv; st_u64(ws + WS_CK + (h * 6 + j6) * LINE, next_tag(epv)); }
  }
  __syncthreads();
  u32x4 q = *(const u32x4*)(qs + e0 * 64 + c * 4);
  const u32x4 q1 = *(const u32x4*)(qs + (1 - e0) * 64 + c * 4);
  auto lds_cnt = [&](int i) { return (unsigned)__builtin_amdgcn_readfirstlane(__hip_atomic_load(cnt + i, __ATOMIC_RELAXED, __HIP_MEMORY_SCOPE_WORKGROUP)); };
  auto lds_inc = [&](int i) {
    asm volatile("s_waitcnt lgkmcnt(0)" ::: "memory");
    if (l == 0) asm volatile("ds_add_u32 %0, %1" :: "v"(lb + 33856u + i * 4), "v"(1u) : "memory");
  };
  u32x4 wo[16];
  __builtin_amdgcn_sched_barrier(0);
#pragma unroll
  for (int i = 0; i < 16; i++) asm volatile("" : "=v"(wo[i]));
  const int nseg = k == 1 ? 2 : 1, sp = k == 1 ? 1 : 0;
  auto store_off = [&](int sg) {
    const int b = b0 + (sg ? 1 - e0 : e0);
    const unsigned og_ = (unsigned)(WS_PBG + ((size_t)(b * 64 + h * 2 + sp) * DM + w * 64 + c * 4 + g) * 8);
    return (g & 1) == 0 ? og_ : 0x7ffffff0u;
  };
  const unsigned apo = (unsigned)((c & 1) * 272 + g * 64);
  auto project = [&](const char* ap, float M, float L) {
    u32x4 a[4];
#pragma unroll
    for (int s = 0; s < 4; s++) a[s] = *(const u32x4*)(ap + s * 16);
    __builtin_amdgcn_sched_barrier(0);
    L += dpp<0xB1>(L); L += dpp<0x4E>(L); L += dpp<0x141>(L);
    __builtin_amdgcn_sched_barrier(0);
    f32x4 d[4];
#pragma unroll
    for (int s = 0; s < 4; s++)
#pragma unroll
      for (int j = 0; j < 4; j++)
        d[j] = __builtin_amdgcn_mfma_f32_16x16x32_bf16(__builtin_bit_cast(bf16x8, a[s]), __builtin_bit_cast(bf16x8, wo[4 * s + j]),
                                                       s ? d[j] : f32x4{0.f, 0.f, 0.f, 0.f}, 0, 0, 0);
    return f32x3{g < 2 ? d[0][0] + d[0][1] : d[3][0] + d[3][1], g < 2 ? d[1][0] + d[1][1] : M, g < 2 ? d[2][0] + d[2][1] : L};
  };
  f32x3 seg0 = {0.f, 0.f, 0.f};
  auto load_wo = [&]() {
    const __amdgpu_buffer_rsrc_t ro = mkrs(woB, HD * DM * 2);
    const unsigned vo = (unsigned)(((h * DH + g * 32) * DM + w * 64 + c * 4) * 2);
#pragma unroll
    for (int i = 0; i < 16; i++) {
      const u32x2 u0 = __builtin_amdgcn_raw_buffer_load_b64(ro, vo + 2 * i * DM * 2, 0, 0);
      const u32x2 u1 = __builtin_amdgcn_raw_buffer_load_b64(ro, vo + (2 * i + 1) * DM * 2, 0, 0);
      wo[i] = u32x4{u0[0], u0[1], u1[0], u1[1]};
    }
  };
  asm volatile("" :: "v"(pf));
  unsigned tp = *(const unsigned*)eps + 1u;
  asm volatile("" : "+v"(tp));
  {
    float m = -INFINITY, s_[4];
    const unsigned swo = lb + L_SC + g * 16 + (c & 3) * 4;
    auto scores = [&](unsigned so, bool ok) {
      const bool b0_ = c & 1, b1_ = c & 2;
      const float r0 = (b0_ ? s_[1] : s_[0]) + dpp<0xB1>(b0_ ? s_[0] : s_[1]);
      const float r1 = (b0_ ? s_[3] : s_[2]) + dpp<0xB1>(b0_ ? s_[2] : s_[3]);
      float r = (b1_ ? r1 : r0) + dpp<0x4E>(b1_ ? r0 : r1);
      r += dpp<0x128>(r);
      r += dpp<0x124>(r);
      r = ok ? rbf(rbf(r) * SCALE) : -INFINITY;
      m = fmaxf(m, r);
      asm volatile("ds_write_b32 %0, %1" :: "v"(swo + so), "v"(r) : "memory");
    };
    auto flush_max = [&](int sg) {
      float mw = fmaxf(m, dpp<0xB1>(m));
      mw = xrow_max(fmaxf(mw, dpp<0x4E>(mw)));
      if (l == 0) asm volatile("ds_max_f32 %0, %1" :: "v"(lb + 33872u + sg * 4), "v"(mw) : "memory");
      m = -INFINITY;
    };
    bool sw = k != 1, first = true, wl = false;
    u32x2 krep = {0u, ~tp};
    const int xw = 16 + 16 * (h >> 3);
    int x = w;
#pragma unroll 1
    while (x < NI) {
      if (!sw && x >= N0) { flush_max(0); q = q1; sw = true; }
      if (!wl && x >= xw) { load_wo(); wl = true; }
      if (x == X_LOOK) krep = __builtin_amdgcn_raw_buffer_load_b64(rs, (unsigned)WS_FK + (l & 7) * LINE, 0, SC1);
      unsigned dr; u64 sv;
      asm volatile("s_mov_b64 %1, exec\n\ts_mov_b64 exec, 1\n\tds_add_rtn_u32 %0, %2, %3\n\ts_mov_b64 exec, %1"
                   : "=&v"(dr), "=&s"(sv) : "v"(lb + 33856u), "v"(1u), "v"(ra[0][0]) : "memory");
      __builtin_amdgcn_sched_barrier(0);
      item_dots(ra, q, s_);
      asm volatile("s_waitcnt lgkmcnt(0)" : "+v"(dr) :: "memory");
      const int xn = __builtin_amdgcn_readfirstlane(dr);
      const bool more = xn < NI;
      const void* pn = more ? (const void*)kc : (const void*)vc;
      const unsigned sn = soff(more ? xn : w);
      if (xn > X_LOOK + 6) wait_until(lds_cnt(6) + sched(more ? xn - NI : w) + 60);
      __builtin_amdgcn_sched_barrier(0);
      ld4(ra, pn, true, sn);
      __builtin_amdgcn_sched_barrier(0);
      scores(sco(x), true);
      item_dots(rb, q, s_);
      __builtin_amdgcn_sched_barrier(0);
      ld4(rb, pn, !more || whole(xn), sn + 8192);
      __builtin_amdgcn_sched_barrier(0);
      scores(sco(x) + 128, whole(x));
      if (xn == X_REP && h >= 8 && h < 16 && j6 == 2) {
        if (l == 0) st_u64(ws + WS_FK + (h - 8) * LINE, ((u64)tp << 32) | (clk() + 1060));
      }
      if (x == X_LOOK && __all(krep[1] == tp)) {
        const unsigned tr = ref_time(krep[0]);
        if (l == 0) cnt[6] = tr;
      }
      if (first) {
        item_dots(ea, q, s_); scores(sco(w + 8), true);
        item_dots(eb, q, s_); scores(sco(w + 8) + 128, true);
        first = false;
      }
      x = xn;
    }
    if (!wl) {
      load_wo();
#pragma unroll
      for (int i = 0; i < 16; i++) asm volatile("" : "+v"(wo[i]));
    }
    flush_max(k == 1 && !sw ? 0 : 1);
    lds_inc(2);
    wait_until(lds_cnt(6) + sched(w + 8));
    ld4(ea, vc, true, soff(w + 8));
    ld4(eb, vc, true, soff(w + 8) + 8192);
  }
#pragma unroll
  for (int s = 0; s < 4; s++) {
    u32x4 o[4];
#pragma unroll
    for (int e = 0; e < 4; e++)
#pragma unroll
      for (int j = 0; j < 4; j++) o[j][e] = __builtin_amdgcn_perm(wo[4 * s + e][2 + (j >> 1)], wo[4 * s + e][j >> 1], (j & 1) ? 0x07060302u : 0x05040100u);
#pragma unroll
    for (int j = 0; j < 4; j++) wo[4 * s + j] = o[j];
    asm volatile("" : "+v"(wo[4 * s]), "+v"(wo[4 * s + 1]), "+v"(wo[4 * s + 2]), "+v"(wo[4 * s + 3]));
  }
  for (unsigned spins = 0; lds_cnt(2) != 8u && spins < SPIN_CAP; ++spins) __builtin_amdgcn_s_sleep(1);
  asm volatile("" ::: "memory");
  if (h >= 8 && h < 16 && j6 == 2 && t == 0) st_u64(ws + WS_FS + (h - 8) * LINE, ((u64)tp << 32) | (clk() + 70));
  u32x2 srep = {0u, 0u};
  const float M0 = __builtin_bit_cast(float, __builtin_amdgcn_readfirstlane(__builtin_bit_cast(int, mxd[k == 1 ? 0 : 1])));
  const float M1 = __builtin_bit_cast(float, __builtin_amdgcn_readfirstlane(__builtin_bit_cast(int, mxd[1])));
  auto weights = [&](unsigned so, float M) {
    const f32x4 sc = *(const f32x4*)(lds + L_SC + so);
    return f32x4{fexp2((sc[0] - M) * LOG2E), fexp2((sc[1] - M) * LOG2E), fexp2((sc[2] - M) * LOG2E), fexp2((sc[3] - M) * LOG2E)};
  };
  float acc[8];
#pragma unroll
  for (int j = 0; j < 8; j++) acc[j] = 0.f;
  f32x2 vf[4][4];
  f32x4 pa, pb;
  unsigned sb = 0;
  auto get_p = [&](int x) {
    const float M = seg1(x) ? M1 : M0;
    pa = weights(sco(x) + g * 16, M); pb = weights(sco(x) + 128 + g * 16, M);
    asm volatile("" : "+v"(pa), "+v"(pb));
    sb = lds_cnt(6);
  };
  auto fma_v = [&](const f32x4 p) {
#pragma unroll
    for (int u = 0; u < 4; u++)
#pragma unroll
      for (int j = 0; j < 4; j++) {
        acc[2 * j]     = fmaf(p[u], vf[u][j][0], acc[2 * j]);
        acc[2 * j + 1] = fmaf(p[u], vf[u][j][1], acc[2 * j + 1]);
      }
  };
  auto step = [&](int xn) {
    const unsigned sn = soff(xn);
    wait_until(sb + sched(xn));
    __builtin_amdgcn_sched_barrier(0);
    ld4(ra, vc, true, sn);
    __builtin_amdgcn_sched_barrier(0);
    fma_v(pa);
#pragma unroll
    for (int j = 0; j < 8; j++) asm volatile("" : "+v"(acc[j]));
    item_unpack(rb, vf);
    __builtin_amdgcn_sched_barrier(0);
    ld4(rb, vc, whole(xn), sn + 8192);
    __builtin_amdgcn_sched_barrier(0);
    fma_v(pb);
  };
  auto put_acc = [&](float* rec_) {
    *(f32x2*)(rec_ + c * 8 + g * 2) = rows_sum(acc);
#pragma unroll
    for (int j = 0; j < 8; j++) acc[j] = 0.f;
  };
  if (w >= 4) __builtin_amdgcn_s_setprio(1);
  get_p(w);
  item_unpack(ra, vf);
  step(w + 16);
  get_p(w + 8);
  item_unpack(ea, vf); fma_v(pa);
  item_unpack(eb, vf); fma_v(pb);
  {
    const bool two = t < 192;
    const f32x4 p0 = weights(t * 16, t < 352 ? M0 : M1), p1 = weights(two ? (t + 512) * 16 : 172 * 64, two ? M1 : 0.f);
    const float s0 = (p0[0] + p0[1]) + (p0[2] + p0[3]), s1 = (p1[0] + p1[1]) + (p1[2] + p1[3]);
    const bool f0 = k == 1 && t < 352;
    const float l0 = xrow_sum(sum16(f0 ? s0 : 0.f)), l1 = xrow_sum(sum16((f0 ? 0.f : s0) + (two ? s1 : 0.f)));
    if (l < 2) lw[l * 8 + w] = l ? l1 : l0;
  }
  bool sw = k != 1;
  int x = w + 16;
#pragma unroll 1
  for (; x < 8 * RD; x += 8) {
    const int rd = x >> 3;
    if (rd == PRIOF) __builtin_amdgcn_s_setprio(0);
    if (!sw && x >= N0) { put_acc(rec0 + w * 128); lds_inc(3); sw = true; }
    if (w == 3 && rd == R_LOOK + 1 && __all(srep[1] == tp)) {
      const unsigned tr = ref_time(srep[0]);
      if (l == 0) cnt[6] = tr;
    }
    if (w == 3 && rd == R_LOOK) srep = __builtin_amdgcn_raw_buffer_load_b64(rs, (unsigned)WS_FS + (l & 7) * LINE, 0, SC1);
    if (rd == RE && k == 1) {
      for (unsigned spins = 0; lds_cnt(3) != 8u && spins < SPIN_CAP; ++spins) __builtin_amdgcn_s_sleep(1);
      asm volatile("" ::: "memory");
      f32x2 a2 = {0.f, 0.f};
#pragma unroll
      for (int i = 0; i < 8; i++) a2 += *(const f32x2*)(rec0 + i * 128 + 2 * l);
      const unsigned hi = cvtpk(a2[0], a2[1]);
      *(unsigned*)(prv + l * 4) = hi;
      *(unsigned*)(prv + 272 + l * 4) = cvtpk(a2[0] - bf_lo(hi), a2[1] - bf_hi(hi));
      seg0 = project(prv + apo, M0, lw[l & 7]);
    }
    get_p(x);
    __builtin_amdgcn_sched_barrier(0);
    item_unpack(ra, vf);
    step(x + 8);
  }
  put_acc(recd + w * RST);
  unsigned o_rv = 65536u + (g * RST + w * 16 + c) * 4, o_lw = 33920u + 32u + (l & 7) * 4;
  unsigned o_oi = (g < 2 && (c & 1) == 0) ? 41216u + g * 272 + (w * 16 + c) * 2 : 45056u + w * 1024 + 256 + l * 4;
  unsigned o_ap = 41216u + apo, o_st = store_off(nseg - 1);
  unsigned o_s0 = k == 1 ? store_off(0) : 0x7ffffff0u;
  u32x4 sg0 = {__float_as_uint(seg0[0]), __float_as_uint(seg0[1]), __float_as_uint(seg0[2]), tp};
  asm volatile("" : "+v"(o_s0), "+v"(sg0));
  asm volatile("" : "+v"(o_rv), "+v"(o_lw), "+v"(o_oi), "+v"(o_ap), "+v"(o_st));
  {
    {
#pragma unroll 1
      for (;;) {
        get_p(x);
        unsigned dr; u64 sv;
        asm volatile("s_mov_b64 %1, exec\n\ts_mov_b64 exec, 1\n\tds_add_rtn_u32 %0, %2, %3\n\ts_mov_b64 exec, %1"
                     : "=&v"(dr), "=&s"(sv) : "v"(lb + 33860u), "v"(1u), "v"(ra[0][0]) : "memory");
        __builtin_amdgcn_sched_barrier(0);
        item_unpack(ra, vf);
        asm volatile("s_waitcnt lgkmcnt(0)" : "+v"(dr) :: "memory");
        const int xn = __builtin_amdgcn_readfirstlane(dr);
        if (xn >= NI) break;
        step(xn);
        put_acc(recd + (x - 8 * RD + 8) * RST);
        x = xn;
      }
      fma_v(pa);
      item_unpack(rb, vf);
      fma_v(pb);
      put_acc(recd + (x - 8 * RD + 8) * RST);
      asm volatile("s_waitcnt lgkmcnt(0)" ::: "memory");
      __syncthreads();
    }
    {
      float rv_[NP4 / 4];
#pragma unroll
      for (int r = 0; r < NP4 / 4; r++) rv_[r] = *(const float*)(lds + o_rv + r * 4 * RST * 4);
      const float li = *(const float*)(lds + o_lw);
      float a = 0.f, a1 = 0.f;
#pragma unroll
      for (int r = 0; r < NP4 / 4; r++) { if (r & 1) a1 += rv_[r]; else a += rv_[r]; }
      a += a1;
      a = xrow_sum(a);
      const float an = dpp<0xB1>(a);
      const unsigned hi = cvtpk(a, an);
      const unsigned lo2 = cvtpk(a - bf_lo(hi), an - bf_hi(hi));
      *(unsigned*)(lds + o_oi) = g ? lo2 : hi;
      __syncthreads();
      const f32x3 pv = project(lds + o_ap, M1, li);
      st16u_sc1(rs, o_st, u32x4{__float_as_uint(pv[0]), __float_as_uint(pv[1]), __float_as_uint(pv[2]), tp});
      st16u_sc1(rs, o_s0, sg0);
    }
    if (w < nseg) {
      const int sg = w;
      const int b = b0 + (sg ? 1 - e0 : e0);
      const int j64 = (h & 7) * 8 + (h >> 3) * 2 + sp;
      const unsigned og = (unsigned)(WS_PBG + ((size_t)(b * 64 + (l >> 2)) * DM + j64 * 8) * 8 + (l & 3) * 16);
      u32x4 p[4];
      {
        __builtin_amdgcn_s_sleep(4);
        wait_until(sb + TM_SW);
      }
#pragma unroll 1
      for (unsigned spins = 0; spins < SPIN_CAP; ++spins) {
        asm volatile("" ::: "memory");
#pragma unroll
        for (int i = 0; i < 4; i++) p[i] = ld16u_sc1(rs, og + i * 16 * DM * 8);
        if (__all(p[0][3] == tp && p[1][3] == tp && p[2][3] == tp && p[3][3] == tp)) break;
      }
      float v0 = 0.f, v1 = 0.f, v2 = 0.f;
#pragma unroll
      for (int i = 0; i < 4; i++) {
        const float mm = dpp<0xF5>(__uint_as_float(p[i][1]));
        const float Mh = fmaxf(mm, dpp<0x141>(mm));
        const float fq = fexp2((mm - Mh) * LOG2E);
        float L = fq * dpp<0xF5>(__uint_as_float(p[i][2]));
        L += dpp<0x141>(L);
        const float wgt = fq * __builtin_amdgcn_rcpf(L);
        v0 = fmaf(__uint_as_float(p[i][0]), wgt, v0);
        v1 = fmaf(__uint_as_float(p[i][1]), wgt, v1);
        v2 = fmaf(__uint_as_float(p[i][2]), wgt, v2);
      }
      const float u2 = dpp<0xA0>(v2);
      v1 = (l & 1) ? v0 : v1;
      v0 = (l & 1) ? u2 : v0;
      v0 += dpp<0x128>(v0); v0 += dpp<0x124>(v0); v0 = xrow_sum(v0);
      v1 += dpp<0x128>(v1); v1 += dpp<0x124>(v1); v1 = xrow_sum(v1);
      if (l < 4) *(unsigned*)(out + b * 1024 + 512 + j64 * 8 + l * 2) = cvtpk(v0, v1);
    }
  }
}

__device__ __forceinline__ unsigned lds_off(int row, int ch) {
  return 256 * row + 16 * (ch ^ (((row & 3) << 2) | ((row >> 2) & 3)));
}

__device__ __forceinline__ void role_A1(int h, const __hip_bfloat16* __restrict__ xA, const __hip_bfloat16* __restrict__ wqA,
                                        const __hip_bfloat16* __restrict__ wuk, const __hip_bfloat16* __restrict__ wuv,
                                        const __hip_bfloat16* __restrict__ woA, char* __restrict__ ws,
                                        __amdgpu_buffer_rsrc_t rs, __hip_bfloat16* __restrict__ out, char* lds) {
  const int t = threadIdx.x, l = t & 63;
  const int w = __builtin_amdgcn_readfirstlane(t >> 6);
  const int g = l >> 4, c = l & 15;
  const unsigned tent = clk();
  float* red = (float*)(lds + 16384);
  const unsigned vo = (unsigned)(((w * 64 + g * 16) * HD + h * DH + c * 8) * 2);
  u64 tag;
  {
    u32x4 xr[NB][2], wr[16], wk[16];
    const __amdgpu_buffer_rsrc_t rx = mkrs(xA, NB * DM * 2);
    const unsigned xo = (unsigned)((w * 64 + g * 16) * 2);
#pragma unroll
    for (int bb = 0; bb < NB; bb++)
#pragma unroll
      for (int i = 0; i < 2; i++) xr[bb][i] = __builtin_amdgcn_raw_buffer_load_b128(rx, xo + bb * DM * 2 + i * 16, 0, 0);
    const __amdgpu_buffer_rsrc_t rq = mkrs(wqA, DM * HD * 2);
#pragma unroll
    for (int i = 0; i < 16; i++) wr[i] = __builtin_amdgcn_raw_buffer_load_b128(rq, vo, i * HD * 2, NT);
    {
      const __amdgpu_buffer_rsrc_t rku = mkrs(wuk, DC * HD * 2);
#pragma unroll
      for (int i = 0; i < 16; i++) wk[i] = __builtin_amdgcn_raw_buffer_load_b128(rku, vo, i * HD * 2, NT);
    }
    __builtin_amdgcn_sched_barrier(0);
    u64 epv = 0;
    if (t == 0) epv = ld_u64(ws + WS_EP);
    unsigned* qs = (unsigned*)lds;
    unsigned short* qo = (unsigned short*)(lds + 4096);
    float acc[NB][8];
#pragma unroll
    for (int bb = 0; bb < NB; bb++)
#pragma unroll
      for (int j = 0; j < 8; j++) acc[bb][j] = 0.f;
#pragma unroll
    for (int i = 0; i < 16; i += 2) {
#pragma unroll
      for (int j = 0; j < 4; j++) {
        const unsigned pl = __builtin_amdgcn_perm(wr[i + 1][j], wr[i][j], 0x05040100u);
        const unsigned ph = __builtin_amdgcn_perm(wr[i + 1][j], wr[i][j], 0x07060302u);
#pragma unroll
        for (int bb = 0; bb < NB; bb++) {
          const unsigned x = xr[bb][i >> 3][(i >> 1) & 3];
          acc[bb][2 * j] = dot2(pl, x, acc[bb][2 * j]);
          acc[bb][2 * j + 1] = dot2(ph, x, acc[bb][2 * j + 1]);
        }
      }
    }
    float* rp = red + (w * 4 + g) * 512 + c * 8;
#pragma unroll
    for (int bb = 0; bb < NB; bb++) {
      *(f32x4*)(rp + bb * 128) = f32x4{acc[bb][0], acc[bb][1], acc[bb][2], acc[bb][3]};
      *(f32x4*)(rp + bb * 128 + 4) = f32x4{acc[bb][4], acc[bb][5], acc[bb][6], acc[bb][7]};
    }
    __syncthreads();
    {
      float s = 0.f;
#pragma unroll
      for (int i = 0; i < 32; i++) s += red[i * 512 + t];
      const float sn = dpp<0xB1>(s);
      if ((t & 1) == 0) qs[t >> 1] = cvtpk(s, sn);
      if (t == 0) *(u64*)(lds + 2048) = epv;
    }
    __syncthreads();
    tag = next_tag(uni64(*(const u64*)(lds + 2048)));
    {
      u32x4 qq[NB];
#pragma unroll
      for (int bb = 0; bb < NB; bb++) qq[bb] = *(const u32x4*)(qs + bb * 64 + c * 4);
#pragma unroll
      for (int i = 0; i < 16; i++) {
        float s[NB];
#pragma unroll
        for (int bb = 0; bb < NB; bb++) {
          float v = dot2(wk[i][0], qq[bb][0], 0.f);
          v = dot2(wk[i][1], qq[bb][1], v);
          v = dot2(wk[i][2], qq[bb][2], v);
          v = dot2(wk[i][3], qq[bb][3], v);
          s[bb] = sum16(v);
        }
        const float v = (c & 2) ? ((c & 1) ? s[3] : s[2]) : ((c & 1) ? s[1] : s[0]);
        if (c < NB) qo[c * 512 + w * 64 + g * 16 + i] = (unsigned short)(cvtpk(v, v) & 0xffff);
      }
    }
    __syncthreads();
    if (t < 256) {
      const int bb = t >> 6, ch = t & 63;
      st16u_sc1(rs, (unsigned)(WS_QABS + ((size_t)(bb * NH + h) * DC + ch * 8) * 2), *(const u32x4*)(qo + bb * 512 + ch * 8));
    }
    signal_flag(ws + WS_F1 + h * LINE, tag);
  }
  const unsigned ta = (unsigned)tag, tb = (unsigned)(tag >> 32);
  {
    u32x4 bv[8][2], bo[8][2];
    u32x4 pre[2][8], wo[16];
    {
      const __amdgpu_buffer_rsrc_t ruv = mkrs(wuv, DC * HD * 2);
      const __amdgpu_buffer_rsrc_t rwo = mkrs(woA, HD * DM * 2);
      const unsigned wvo = (unsigned)(((h * DH + (l >> 3) * 16) * DM + w * 64 + (l & 7) * 8) * 2);
#pragma unroll
      for (int i = 0; i < 16; i++) pre[i >> 3][i & 7] = __builtin_amdgcn_raw_buffer_load_b128(ruv, vo - g * 8 * HD * 2, ((i >> 3) * 32 + (i & 7)) * HD * 2, NT);
#pragma unroll
      for (int i = 0; i < 16; i++) wo[i] = __builtin_amdgcn_raw_buffer_load_b128(rwo, wvo, i * DM * 2, NT);
    }
#pragma unroll
    for (int j = 0; j < 8; j++)
#pragma unroll
      for (int s = 0; s < 2; s++) {
#pragma unroll
        for (int e = 0; e < 4; e++)
          bv[j][s][e] = __builtin_amdgcn_perm(pre[s][2 * e + 1][j >> 1], pre[s][2 * e][j >> 1], (j & 1) ? 0x07060302u : 0x05040100u);
        asm volatile("" : "+v"(bv[j][s]));
      }
#pragma unroll
    for (int j = 0; j < 8; j++)
#pragma unroll
      for (int s = 0; s < 2; s++) {
#pragma unroll
        for (int e = 0; e < 4; e++)
          bo[j][s][e] = __builtin_amdgcn_perm(wo[8 * s + 2 * e + 1][j >> 1], wo[8 * s + 2 * e][j >> 1], (j & 1) ? 0x07060302u : 0x05040100u);
        asm volatile("" : "+v"(bo[j][s]));
      }
    constexpr int LAS = 1040;
    unsigned short* cs = (unsigned short*)(lds + 16384);
    float* outs = (float*)(lds + 20480);
    float* cred = (float*)(lds + 32768);
    unsigned po = (unsigned)(WS_PAO + ((size_t)(w * 4 * NH + h) * DC + l * 8) * 2);
    unsigned pc = (unsigned)(WS_PAC + (size_t)((w * 4 + g) * NH + h) * 8);
    unsigned lw_ = (unsigned)(65536 + ((w & 1) * 16 + (w >> 1) * 4) * LAS + l * 16), lr_ = (unsigned)(65536 + c * LAS + (w * 64 + g * 8) * 2);
    asm volatile("" : "+v"(po), "+v"(pc), "+v"(lw_), "+v"(lr_));
    const unsigned tw = (h & 1) ? tb : ta;
    if (h == 0 && w == 1) {
      const char* fp = ws + WS_F1 + l * LINE;
      wait_until(tent + TM_EP);
#pragma unroll 1
      for (unsigned spins = 0; spins < SPIN_CAP; ++spins) {
        const u64 f0 = ld_u64(fp), f1 = ld_u64(fp + 64 * LINE), f2 = ld_u64(fp + 128 * LINE), f3 = ld_u64(fp + 192 * LINE);
        if (__all(f0 == tag && f1 == tag && f2 == tag && f3 == tag)) break;
        __builtin_amdgcn_s_sleep(32);
      }
      if (l == 0) st_u64(ws + WS_EP, tag);
    }
    const unsigned tg = wait_gate(ws + WS_FS, ta, (unsigned*)(lds + 8192), tent);
    wait_until(tg + TM_PUT + TM_GET);
    {
      u32x4 pr[4];
#pragma unroll 1
      for (unsigned spins = 0; spins < SPIN_CAP; ++spins) {
        asm volatile("" ::: "memory");
        const u32x2 ck = __builtin_amdgcn_raw_buffer_load_b64(rs, pc, 0, SC1);
#pragma unroll
        for (int i = 0; i < 4; i++) pr[i] = __builtin_amdgcn_raw_buffer_load_b128(rs, po, i * NH * DC * 2, SC1);
#pragma unroll
        for (int i = 0; i < 4; i++) *(u32x4*)(lds + lw_ + i * LAS) = pr[i] - ta;
        if (__all(row_sums(pr) == ck[0] && ck[1] == tw)) break;
        __builtin_amdgcn_s_sleep(2);
      }
      __syncthreads();
      u32x4 af[2][2];
#pragma unroll
      for (int tl = 0; tl < 2; tl++)
#pragma unroll
        for (int s = 0; s < 2; s++) af[tl][s] = *(const u32x4*)(lds + lr_ + tl * 16 * LAS + s * 64);
      f32x4 dd[8];
#pragma unroll
      for (int j = 0; j < 8; j++)
        dd[j] = __builtin_amdgcn_mfma_f32_16x16x32_bf16(__builtin_bit_cast(bf16x8, af[0][0]), __builtin_bit_cast(bf16x8, bv[j][0]), f32x4{0.f, 0.f, 0.f, 0.f}, 0, 0, 0);
#pragma unroll
      for (int j = 0; j < 8; j++)
        dd[j] = __builtin_amdgcn_mfma_f32_16x16x32_bf16(__builtin_bit_cast(bf16x8, af[0][1]), __builtin_bit_cast(bf16x8, bv[j][1]), dd[j], 0, 0, 0);
#pragma unroll
      for (int j = 0; j < 8; j++)
        dd[j] = __builtin_amdgcn_mfma_f32_16x16x32_bf16(__builtin_bit_cast(bf16x8, af[1][0]), __builtin_bit_cast(bf16x8, bv[j][0]), dd[j], 0, 0, 0);
#pragma unroll
      for (int j = 0; j < 8; j++)
        dd[j] = __builtin_amdgcn_mfma_f32_16x16x32_bf16(__builtin_bit_cast(bf16x8, af[1][1]), __builtin_bit_cast(bf16x8, bv[j][1]), dd[j], 0, 0, 0);
      f32x4 v[2];
#pragma unroll
      for (int j = 0; j < 8; j++) v[j >> 2][j & 3] = (dd[j][0] + dd[j][1]) + (dd[j][2] + dd[j][3]);
      *(f32x4*)(cred + (w * 4 + g) * 128 + c * 8) = v[0];
      *(f32x4*)(cred + (w * 4 + g) * 128 + c * 8 + 4) = v[1];
    }
    __syncthreads();
    {
      float s = 0.f;
#pragma unroll
      for (int i = 0; i < 8; i++) s += cred[i * 512 + t];
      const float sn = dpp<0xB1>(s);
      if ((t & 1) == 0) *(unsigned*)(cs + t) = cvtpk(s, sn);
    }
    __syncthreads();
    {
      const char* ap = (const char*)cs + ((c >> 1) & 3) * 256 + (g * 32 + (c & 1) * 16) * 2;
      const u32x4 a0 = *(const u32x4*)(ap), a1 = *(const u32x4*)(ap + 16);
      f32x4 dd[8];
#pragma unroll
      for (int j = 0; j < 8; j++)
        dd[j] = __builtin_amdgcn_mfma_f32_16x16x32_bf16(__builtin_bit_cast(bf16x8, a0), __builtin_bit_cast(bf16x8, bo[j][0]), f32x4{0.f, 0.f, 0.f, 0.f}, 0, 0, 0);
#pragma unroll
      for (int j = 0; j < 8; j++)
        dd[j] = __builtin_amdgcn_mfma_f32_16x16x32_bf16(__builtin_bit_cast(bf16x8, a1), __builtin_bit_cast(bf16x8, bo[j][1]), dd[j], 0, 0, 0);
      f32x4 v4[2];
#pragma unroll
      for (int j = 0; j < 8; j++) {
        const float u0 = (l & 8) ? dd[j][1] : dd[j][0], u1 = (l & 8) ? dd[j][3] : dd[j][2];
        const float keep = (l & 8) ? u1 : u0, give = (l & 8) ? u0 : u1;
        v4[j >> 2][j & 3] = keep + dpp<0x128>(give);
      }
      if (g < 2) {
        float* op = outs + (g * 2 + ((l >> 3) & 1)) * 512 + w * 64 + (l & 7) * 8;
        *(f32x4*)op = v4[0];
        *(f32x4*)(op + 4) = v4[1];
      }
    }
    __syncthreads();
    {
      const f32x2 va = *(const f32x2*)(outs + t * 2), vb = *(const f32x2*)(outs + 1024 + t * 2);
      wait_until(tg + TM_PA);
      const unsigned oo = (unsigned)(WS_PA + ((size_t)((t >> 8) * NH + h) * DM + (t & 255) * 2) * 8);
      st16u_sc1(rs, oo, u32x4{__float_as_uint(va[0]), ta, __float_as_uint(va[1]), tb});
      st16u_sc1(rs, oo + 2 * NH * DM * 8, u32x4{__float_as_uint(vb[0]), ta, __float_as_uint(vb[1]), tb});
    }
  if (w < 4) {
    const int bb = h >> 3, n0 = (h & 7) * 64 + w * 16;
    const unsigned og = (unsigned)(WS_PA + ((size_t)(bb * NH + (l >> 3)) * DM + n0) * 8 + (l & 7) * 16);
    u32x4 p[4];
    wait_until(tg + TM_PA + TM_T4);
#pragma unroll 1
    for (unsigned spins = 0; spins < SPIN_CAP; ++spins) {
      asm volatile("" ::: "memory");
#pragma unroll
      for (int i = 0; i < 4; i++) p[i] = ld16u_sc1(rs, og + i * 8 * DM * 8);
      bool ok = true;
#pragma unroll
      for (int i = 0; i < 4; i++) ok = ok && p[i][1] == ta && p[i][3] == tb;
      if (__all(ok)) break;
      __builtin_amdgcn_s_sleep(4);
    }
    float v0 = (__uint_as_float(p[0][0]) + __uint_as_float(p[1][0])) + (__uint_as_float(p[2][0]) + __uint_as_float(p[3][0]));
    float v1 = (__uint_as_float(p[0][2]) + __uint_as_float(p[1][2])) + (__uint_as_float(p[2][2]) + __uint_as_float(p[3][2]));
    v0 += dpp<0x128>(v0); v0 = xrow_sum(v0);
    v1 += dpp<0x128>(v1); v1 = xrow_sum(v1);
    if (l < 8) *(unsigned*)(out + bb * 1024 + n0 + l * 2) = cvtpk(v0, v1);
  }
  }
}

__device__ __forceinline__ void role_A2(int a, const __hip_bfloat16* __restrict__ ckv, char* __restrict__ ws,
                                        __amdgpu_buffer_rsrc_t rs, char* lds) {
  const int t = threadIdx.x, l = t & 63;
  const int w = __builtin_amdgcn_readfirstlane(t >> 6);
  const int g = l >> 4, c = l & 15;
  const unsigned tent = clk();
  u64 ep = 0;
  if (t == 0) ep = ld_u64(ws + WS_EP);
  const int grp = w >> 2, wq = w & 3;
  const int b = a / SPLIT_A, sp = a % SPLIT_A;
  const int r = l & 31, hh = l >> 5;
  constexpr unsigned CB = (unsigned)((size_t)NB * SK * DC * 2);
  constexpr int NTL = KA / 64;
  const unsigned cvo = (unsigned)((g * DC + 128 * wq + c * 8) * 2);
  const unsigned cso = (unsigned)(((size_t)b * SK + sp * KA + grp * 32) * DC * 2);
  auto load_tile = [&](u32x4 (&pr)[8], int tile) {
    const bool ok = tile < NTL;
    const __amdgpu_buffer_rsrc_t rc = mkrs(ckv, ok ? CB : 0u);
    const unsigned so = ok ? cso + (unsigned)((tile + sp) & (NTL - 1)) * 64 * DC * 2 : 0u;
#pragma unroll
    for (int u = 0; u < 8; u++) pr[u] = __builtin_amdgcn_raw_buffer_load_b128(rc, cvo, so + u * 4 * DC * 2, NT);
  };
  char* img = lds + w * 8192;
  float* sred = (float*)(lds + 65536 + grp * 16384);
  float* mxs = (float*)(lds + 98304 + grp * 512);
  unsigned* pbuf = (unsigned*)(lds + 102400 + grp * 2048);
  float* lall = (float*)(lds + 106496);
  const unsigned imga = (unsigned)(uintptr_t)(__attribute__((address_space(3))) char*)img;
  auto put_tile = [&](u32x4 (&pr)[8]) {
#pragma unroll
    for (int u = 0; u < 8; u++) *(u32x4*)(img + lds_off(4 * u + g, c)) = pr[u];
  };
  u32x4 pre[2][8];
  load_tile(pre[0], 0);
  load_tile(pre[1], 1);
  const u64 tag0 = next_tag(uni64(ep));
  if (t == 0) st_u64(ws + WS_CK + (192 + a) * LINE, tag0);
  put_tile(pre[0]);
  __builtin_amdgcn_sched_barrier(0);
  load_tile(pre[0], 2);
  unsigned pf[4];
#pragma unroll
  for (int i = 0; i < 4; i++)
    pf[i] = __builtin_amdgcn_raw_buffer_load_b32(mkrs(ckv, CB), (unsigned)t * 64, (unsigned)(a * 8 + ((3 + (i >> 1) + a) & 7)) * 65536u + (i & 1) * 32768u, 0);
  if (t < 64) {
    wait_until(tent + TM_F1);
#pragma unroll 1
    for (unsigned spins = 0; spins < SPIN_CAP; ++spins) {
      __builtin_amdgcn_s_sleep(32);
      if (__all(ld_u64(ws + WS_F1 + (l & 31) * LINE) == tag0)) break;
    }
  }
  if (t == 0) *(u64*)(lds + 110592) = tag0;
  asm volatile("" :: "v"(pf[0]), "v"(pf[1]), "v"(pf[2]), "v"(pf[3]));
  __syncthreads();
  const u64 tag = uni64(*(const u64*)(lds + 110592));
  const unsigned ta = (unsigned)tag, tb = (unsigned)(tag >> 32);
  bf16x8 qf[8];
  {
    char* qi = lds + 65536;
    const unsigned qo = (unsigned)(WS_QABS + (size_t)b * NH * DC * 2 + t * 16);
    u32x4 qv[4];
#pragma unroll
    for (int i = 0; i < 4; i++) qv[i] = ld16u_sc1(rs, qo + i * 8192);
#pragma unroll
    for (int i = 0; i < 4; i++) *(u32x4*)(qi + (i * 8 + w) * 1040 + l * 16) = qv[i];
    __syncthreads();
#pragma unroll
    for (int s = 0; s < 8; s++) qf[s] = __builtin_bit_cast(bf16x8, *(const u32x4*)(qi + r * 1040 + (128 * wq + 16 * s + 8 * hh) * 2));
#pragma unroll
    for (int s = 0; s < 8; s++) asm volatile("" : "+v"(qf[s]));
    __syncthreads();
  }
  f32x16 o[4];
#pragma unroll
  for (int i = 0; i < 4; i++)
#pragma unroll
    for (int j = 0; j < 16; j++) o[i][j] = 0.f;
  float m = -INFINITY, lsum = 0.f;
  const int q4 = (l & 15) >> 2, p4 = l & 3, colh = g & 1;
  auto tile_body = [&](int tile, u32x4 (&nx)[8]) {
    f32x16 s;
#pragma unroll
    for (int j = 0; j < 16; j++) s[j] = 0.f;
    {
      u32x4 af[8];
#pragma unroll
      for (int ks = 0; ks < 8; ks++) af[ks] = *(const u32x4*)(img + lds_off(r, 2 * ks + hh));
      __builtin_amdgcn_sched_barrier(0);
#pragma unroll
      for (int ks = 0; ks < 8; ks++) s = __builtin_amdgcn_mfma_f32_32x32x16_bf16(__builtin_bit_cast(bf16x8, af[ks]), qf[ks], s, 0, 0, 0);
    }
    f32x4* sr = (f32x4*)sred;
#pragma unroll
    for (int k4 = 0; k4 < 4; k4++) sr[(k4 * 4 + wq) * 64 + l] = f32x4{s[4 * k4], s[4 * k4 + 1], s[4 * k4 + 2], s[4 * k4 + 3]};
    __syncthreads();
    float x[4];
    {
      const f32x4 v = (sr[(wq * 4) * 64 + l] + sr[(wq * 4 + 1) * 64 + l]) + (sr[(wq * 4 + 2) * 64 + l] + sr[(wq * 4 + 3) * 64 + l]);
#pragma unroll
      for (int e = 0; e < 4; e += 2) {
        const unsigned r1 = cvtpk(v[e], v[e + 1]);
        const unsigned r2 = cvtpk(bf_lo(r1) * SCALE, bf_hi(r1) * SCALE);
        x[e] = bf_lo(r2); x[e + 1] = bf_hi(r2);
      }
      float mq = fmaxf(fmaxf(x[0], x[1]), fmaxf(x[2], x[3]));
      auto sx = __builtin_amdgcn_permlane32_swap(__float_as_uint(mq), __float_as_uint(mq), false, false);
      mq = fmaxf(__uint_as_float(sx[0]), __uint_as_float(sx[1]));
      if (hh == 0) mxs[r * 4 + wq] = mq;
    }
    __syncthreads();
    const f32x4 m4 = *(const f32x4*)(mxs + r * 4);
    const float mn = fmaxf(fmaxf(m, fmaxf(m4[0], m4[1])), fmaxf(m4[2], m4[3]));
    const float alpha = fexp2((m - mn) * LOG2E);
    {
      const unsigned p0 = cvtpk(fexp2((x[0] - mn) * LOG2E), fexp2((x[1] - mn) * LOG2E));
      const unsigned p1 = cvtpk(fexp2((x[2] - mn) * LOG2E), fexp2((x[3] - mn) * LOG2E));
      *(u32x2*)(pbuf + l * 8 + wq * 2) = u32x2{p0, p1};
      lsum = lsum * alpha + ((bf_lo(p0) + bf_hi(p0)) + (bf_lo(p1) + bf_hi(p1)));
    }
    m = mn;
    __syncthreads();
    unsigned pk[8];
    {
      const u32x4 a0 = *(const u32x4*)(pbuf + l * 8), a1 = *(const u32x4*)(pbuf + l * 8 + 4);
      pk[0] = a0[0]; pk[1] = a0[1]; pk[2] = a0[2]; pk[3] = a0[3]; pk[4] = a1[0]; pk[5] = a1[1]; pk[6] = a1[2]; pk[7] = a1[3];
    }
    if (__any(alpha != 1.f)) {
#pragma unroll
      for (int i = 0; i < 4; i++)
#pragma unroll
        for (int j = 0; j < 16; j++) o[i][j] *= alpha;
    }
#pragma unroll
    for (int s2 = 0; s2 < 2; s2++) {
      u32x2 tv[4][2];
#pragma unroll
      for (int ct = 0; ct < 4; ct++) {
        const int ch = 4 * ct + 2 * colh + (p4 >> 1);
        const unsigned a0 = lds_off(4 * hh + q4, ch) + 8 * (p4 & 1);
        const unsigned a1 = lds_off(8 + 4 * hh + q4, ch) + 8 * (p4 & 1);
        if (s2 == 0) {
          asm volatile("ds_read_b64_tr_b16 %0, %1" : "=v"(tv[ct][0]) : "v"(imga + a0) : "memory");
          asm volatile("ds_read_b64_tr_b16 %0, %1" : "=v"(tv[ct][1]) : "v"(imga + a1) : "memory");
        } else {
          asm volatile("ds_read_b64_tr_b16 %0, %1 offset:4096" : "=v"(tv[ct][0]) : "v"(imga + a0) : "memory");
          asm volatile("ds_read_b64_tr_b16 %0, %1 offset:4096" : "=v"(tv[ct][1]) : "v"(imga + a1) : "memory");
        }
      }
      asm volatile("s_waitcnt lgkmcnt(0)" ::: "memory");
      __builtin_amdgcn_sched_barrier(0);
      u32x4 pb = {pk[4 * s2], pk[4 * s2 + 1], pk[4 * s2 + 2], pk[4 * s2 + 3]};
#pragma unroll
      for (int ct = 0; ct < 4; ct++) {
        u32x4 av = {tv[ct][0][0], tv[ct][0][1], tv[ct][1][0], tv[ct][1][1]};
        o[ct] = __builtin_amdgcn_mfma_f32_32x32x16_bf16(__builtin_bit_cast(bf16x8, av), __builtin_bit_cast(bf16x8, pb), o[ct], 0, 0, 0);
      }
    }
    put_tile(nx);
    __builtin_amdgcn_sched_barrier(0);
    load_tile(nx, tile + 3);
  };
#pragma unroll 1
  for (int tile = 0; tile < NTL; tile += 2) {
    tile_body(tile, pre[1]);
    tile_body(tile + 1, pre[0]);
  }
  constexpr int OST = 516;
  float* ob = (float*)lds;
  float* mlb = (float*)(lds + 32 * OST * 4);
  lall[w * 64 + l] = lsum;
  __syncthreads();
  {
    const float* lp = lall + grp * 256 + r;
    lsum = ((lp[0] + lp[32]) + (lp[64] + lp[96])) + ((lp[128] + lp[160]) + (lp[192] + lp[224]));
  }
  if (grp == 1) {
#pragma unroll
    for (int ct = 0; ct < 4; ct++)
#pragma unroll
      for (int j4 = 0; j4 < 4; j4++)
        *(f32x4*)(ob + r * OST + 128 * wq + 32 * ct + 8 * j4 + 4 * hh) =
            f32x4{o[ct][4 * j4], o[ct][4 * j4 + 1], o[ct][4 * j4 + 2], o[ct][4 * j4 + 3]};
    if (wq == 0 && l < 32) { mlb[2 * r] = m; mlb[2 * r + 1] = lsum; }
  }
  __syncthreads();
  if (grp == 0) {
    const float m1 = mlb[2 * r], l1 = mlb[2 * r + 1];
    const float M = fmaxf(m, m1);
    const float f0 = fexp2((m - M) * LOG2E), f1 = fexp2((m1 - M) * LOG2E);
#pragma unroll
    for (int ct = 0; ct < 4; ct++)
#pragma unroll
      for (int j4 = 0; j4 < 4; j4++) {
        float* p = ob + r * OST + 128 * wq + 32 * ct + 8 * j4 + 4 * hh;
        const f32x4 o1 = *(const f32x4*)p;
        *(f32x4*)p = f32x4{o[ct][4 * j4] * f0 + o1[0] * f1, o[ct][4 * j4 + 1] * f0 + o1[1] * f1,
                           o[ct][4 * j4 + 2] * f0 + o1[2] * f1, o[ct][4 * j4 + 3] * f0 + o1[3] * f1};
      }
    if (wq == 0 && l < 32)
      st16u_sc1(rs, (unsigned)(WS_PAML + ((size_t)(b * SPLIT_A + sp) * NH + r) * 16),
                u32x4{__float_as_uint(M), ta, __float_as_uint(lsum * f0 + l1 * f1), tb});
  }
  float* wgt = mlb + 64;
  unsigned* pcs = (unsigned*)(mlb + 128);
  const unsigned tg = wait_gate(ws + WS_FS, ta, (unsigned*)(mlb + 96), tent);
  if (t < 256) {
    wait_until(tg + TM_ML);
    const unsigned mo = (unsigned)(WS_PAML + ((size_t)(b * SPLIT_A + (t & 7)) * NH + (t >> 3)) * 16);
    u32x4 mv;
#pragma unroll 1
    for (unsigned spins = 0; spins < SPIN_CAP; ++spins) {
      asm volatile("" ::: "memory");
      mv = ld16u_sc1(rs, mo);
      if (__all(mv[1] == ta && mv[3] == tb)) break;
      __builtin_amdgcn_s_sleep(16);
    }
    const float mm = __uint_as_float(mv[0]);
    float M = fmaxf(mm, dpp<0xB1>(mm)); M = fmaxf(M, dpp<0x4E>(M)); M = fmaxf(M, dpp<0x141>(M));
    const float f = fexp2((mm - M) * LOG2E);
    float L = f * __uint_as_float(mv[2]);
    L += dpp<0xB1>(L); L += dpp<0x4E>(L); L += dpp<0x141>(L);
    if ((t & 7) == sp) wgt[t >> 3] = f / L;
  }
  __syncthreads();
  {
    const unsigned pao = (unsigned)(WS_PAO + (size_t)(b * SPLIT_A + sp) * NH * DC * 2);
    u32x4 pv[4];
#pragma unroll
    for (int i = 0; i < 4; i++) {
      const int hd = i * 8 + w;
      const float f = wgt[hd];
      const f32x4 v0 = *(const f32x4*)(ob + hd * OST + l * 8) * f, v1 = *(const f32x4*)(ob + hd * OST + l * 8 + 4) * f;
      pv[i] = u32x4{cvtpk(v0[0], v0[1]), cvtpk(v0[2], v0[3]), cvtpk(v1[0], v1[1]), cvtpk(v1[2], v1[3])} + ta;
      asm volatile("" : "+v"(pv[i]));
    }
    {
      const unsigned cs = row_sums(pv);
      if (c == 0) pcs[g * 8 + w] = cs;
    }
    __syncthreads();
    const u32x2 c2 = *(const u32x2*)(pcs + (t & 15) * 2);
    const unsigned co = t < 16 ? (unsigned)(WS_PAC + (size_t)(b * SPLIT_A + sp) * NH * 8 + t * 16) : 0x7ffffff0u;
    wait_until(tg + TM_PUT);
#pragma unroll
    for (int i = 0; i < 4; i++) st16u_sc1(rs, pao + (unsigned)(i * 512 + t) * 16, pv[i]);
    if (w == 0) st16u_sc1(rs, co, u32x4{c2[0], ta, c2[1], tb});
  }
}

__global__ __launch_bounds__(512, 2) void k_fused(const __hip_bfloat16* __restrict__ xA, const __hip_bfloat16* __restrict__ ckv,
                                                  const __hip_bfloat16* __restrict__ wqA, const __hip_bfloat16* __restrict__ wuk,
                                                  const __hip_bfloat16* __restrict__ wuv, const __hip_bfloat16* __restrict__ woA,
                                                  const __hip_bfloat16* __restrict__ xB, const __hip_bfloat16* __restrict__ kc,
                                                  const __hip_bfloat16* __restrict__ vc, const __hip_bfloat16* __restrict__ wqB,
                                                  const __hip_bfloat16* __restrict__ woB, char* __restrict__ ws,
                                                  __hip_bfloat16* __restrict__ out) {
  __shared__ __attribute__((aligned(16))) char lds[131072];
  asm volatile("" :: "s"(xA), "s"(ckv), "s"(wqA), "s"(wuk), "s"(wuv), "s"(woA), "s"(xB), "s"(kc), "s"(vc), "s"(wqB), "s"(woB), "s"(ws), "s"(out));
  const int blk = blockIdx.x, xcd = blk & 7, r = blk >> 3;
  unsigned pfv = 0;
  {
    u64 pc; unsigned len;
    asm volatile("s_getpc_b64 %0\n.Lcode_begin:\n\ts_mov_b32 %1, .Lcode_end-.Lcode_begin" : "=s"(pc), "=s"(len));
    const unsigned idx = r < 5 ? (unsigned)((r * 8 + (threadIdx.x >> 6) + xcd * 5) % 40) : 1000u;
    const __amdgpu_buffer_rsrc_t rc = mkrs((const void*)pc, len);
    pfv = __builtin_amdgcn_raw_buffer_load_b32(rc, idx * 1024 + (threadIdx.x & 63) * 16, 0, 0);
  }
  const __amdgpu_buffer_rsrc_t rs = mkrs(ws, (unsigned)WS_END);
  if (r < 8) {
    if (r < 4) role_A1(xcd * 4 + r, xA, wqA, wuk, wuv, woA, ws, rs, out, lds);
    else role_A2(xcd * 4 + r - 4, ckv, ws, rs, lds);
  } else {
    const int rr = r - 8, hq = rr / 6;
    role_B(((0x75643120u >> (4 * xcd)) & 7) + 8 * hq, rr - 6 * hq, xcd, ckv, xB, kc, vc, wqB, woB, ws, rs, out, lds);
  }
  asm volatile(".Lcode_end:" :: "v"(pfv));
}

extern "C" void kernel_launch(void* const* d_in, const int* in_sizes, int n_in,
                              void* d_out, int out_size, void* d_ws, size_t ws_size,
                              hipStream_t stream) {
  k_fused<<<256, 512, 0, stream>>>((const __hip_bfloat16*)d_in[0], (const __hip_bfloat16*)d_in[1], (const __hip_bfloat16*)d_in[2],
                                   (const __hip_bfloat16*)d_in[3], (const __hip_bfloat16*)d_in[4], (const __hip_bfloat16*)d_in[5],
                                   (const __hip_bfloat16*)d_in[6], (const __hip_bfloat16*)d_in[7], (const __hip_bfloat16*)d_in[8],
                                   (const __hip_bfloat16*)d_in[9], (const __hip_bfloat16*)d_in[10], (char*)d_ws,
                                   (__hip_bfloat16*)d_out);
}
```

```cpp
#include <hip/hip_runtime.h>
#include <hip/hip_bf16.h>
#include <stdint.h>

typedef __attribute__((ext_vector_type(4))) unsigned u32x4;
typedef __attribute__((ext_vector_type(2))) unsigned u32x2;
typedef __attribute__((ext_vector_type(4))) float f32x4;
typedef __attribute__((ext_vector_type(2))) float f32x2;
typedef __attribute__((ext_vector_type(3))) float f32x3;
typedef __attribute__((ext_vector_type(16))) float f32x16;
typedef __attribute__((ext_vector_type(8))) __bf16 bf16x8;
typedef __attribute__((ext_vector_type(2))) __bf16 bf16x2;
typedef __attribute__((address_space(1))) unsigned gu32;
typedef __attribute__((address_space(1))) unsigned long long gu64;
typedef unsigned long long u64;

constexpr int NB = 4;
constexpr int NH = 32;
constexpr int SK = 4096;
constexpr int DH = 128;
constexpr int DM = 512;
constexpr int DC = 512;
constexpr int HD = NH * DH;

constexpr int SPLIT_A = 8;
constexpr int KA = SK / SPLIT_A;
constexpr int SS = 171;
constexpr int NI = 86;
constexpr int N0 = 43;
constexpr int RD = 9;
constexpr int NP = NI - 8 * RD + 8;
constexpr int NP4 = (NP + 3) & ~3;
constexpr int RST = 144;
constexpr int RE = RD - 1;
constexpr int PRIOF = 4;

constexpr unsigned TM_F1 = 1625;
constexpr unsigned TM_EP = 1800;
constexpr unsigned TM_FC = 3200;
constexpr unsigned TM_ML = 1040;
constexpr unsigned TM_PUT = 1160;
constexpr unsigned TM_GET = 110;
constexpr unsigned TM_PA = 1625;
constexpr unsigned TM_T4 = 60;
constexpr unsigned TM_SW = 1720;

__device__ __forceinline__ unsigned sched(int x) { return (unsigned)((x * 338 >> 4) + (-355) + (x >= 72 ? 30 : 0)); }
constexpr int X_REP = 48, X_LOOK = 76;
constexpr int R_LOOK = 4;

constexpr float SCALE = 0.08838834764831845f;
constexpr float LOG2E = 1.4426950408889634f;
constexpr int NT = 2;
constexpr int SC1 = 16;

constexpr size_t WS_EP   = 0;
constexpr int LINE = 256;
constexpr size_t WS_FS   = 4096;
constexpr size_t WS_FK   = 6144;
constexpr size_t WS_F1   = 8192;
constexpr size_t WS_CK   = WS_F1 + 32 * LINE;
constexpr size_t WS_QABS = WS_CK + 224 * LINE;
constexpr size_t WS_PAML = WS_QABS + (size_t)NB * NH * DC * 2;
constexpr size_t WS_PAC  = WS_PAML + (size_t)NB * SPLIT_A * NH * 16;
constexpr size_t WS_PAO  = WS_PAC + (size_t)NB * SPLIT_A * NH * 8;
constexpr size_t WS_PA   = WS_PAO + (size_t)NB * SPLIT_A * NH * DC * 2;
constexpr size_t WS_PBG  = WS_PA + (size_t)NB * NH * DM * 8;
constexpr size_t WS_END  = WS_PBG + (size_t)NB * 64 * DM * 8;

__device__ __forceinline__ float bf_lo(unsigned u) { return __uint_as_float(u << 16); }
__device__ __forceinline__ float bf_hi(unsigned u) { return __uint_as_float(u & 0xffff0000u); }
__device__ __forceinline__ unsigned cvtpk(float lo, float hi) {
  f32x2 v = {lo, hi};
  return __builtin_bit_cast(unsigned, __builtin_convertvector(v, bf16x2));
}
__device__ __forceinline__ float rbf(float x) { return __uint_as_float(cvtpk(x, x) << 16); }
__device__ __forceinline__ float dot2(unsigned k, unsigned q, float acc) {
  return __builtin_amdgcn_fdot2_f32_bf16(__builtin_bit_cast(bf16x2, k), __builtin_bit_cast(bf16x2, q), acc, false);
}
template <int CTRL> __device__ __forceinline__ float dpp(float x) {
  return __builtin_bit_cast(float, __builtin_amdgcn_mov_dpp(__builtin_bit_cast(int, x), CTRL, 0xf, 0xf, true));
}
__device__ __forceinline__ float sum16(float x) {
  x += dpp<0x128>(x);
  x += dpp<0x124>(x);
  x += dpp<0x4E>(x);
  x += dpp<0xB1>(x);
  return x;
}
__device__ __forceinline__ float xrow_max(float x) {
  auto s = __builtin_amdgcn_permlane16_swap(__float_as_uint(x), __float_as_uint(x), false, false);
  x = fmaxf(__uint_as_float(s[0]), __uint_as_float(s[1]));
  auto t = __builtin_amdgcn_permlane32_swap(__float_as_uint(x), __float_as_uint(x), false, false);
  return fmaxf(__uint_as_float(t[0]), __uint_as_float(t[1]));
}
__device__ __forceinline__ float xrow_sum(float x) {
  auto s = __builtin_amdgcn_permlane16_swap(__float_as_uint(x), __float_as_uint(x), false, false);
  x = __uint_as_float(s[0]) + __uint_as_float(s[1]);
  auto t = __builtin_amdgcn_permlane32_swap(__float_as_uint(x), __float_as_uint(x), false, false);
  return __uint_as_float(t[0]) + __uint_as_float(t[1]);
}
__device__ __forceinline__ float fexp2(float x) { return __builtin_amdgcn_exp2f(x); }
__device__ __forceinline__ __amdgpu_buffer_rsrc_t mkrs(const void* p, unsigned bytes) {
  return __builtin_amdgcn_make_buffer_rsrc((void*)p, 0, (int)bytes, 0x00020000);
}


constexpr unsigned SPIN_CAP = 1u << 21;
__device__ __forceinline__ u64 ld_u64(const void* p) {
  return __hip_atomic_load((gu64*)p, __ATOMIC_RELAXED, __HIP_MEMORY_SCOPE_AGENT);
}
__device__ __forceinline__ void st_u64(void* p, u64 v) {
  __hip_atomic_store((gu64*)p, v, __ATOMIC_RELAXED, __HIP_MEMORY_SCOPE_AGENT);
}
__device__ __forceinline__ u64 next_tag(u64 e) { return e + 0x0000000100000001ull; }
__device__ __forceinline__ u64 uni64(u64 v) {
  const unsigned lo = __builtin_amdgcn_readfirstlane((unsigned)v), hi = __builtin_amdgcn_readfirstlane((unsigned)(v >> 32));
  return ((u64)hi << 32) | lo;
}
__device__ __forceinline__ void signal_flag(char* flag, u64 tag) {
  asm volatile("s_waitcnt vmcnt(0)" ::: "memory");
  __syncthreads();
  if (threadIdx.x == 0) st_u64(flag, tag);
}
template <int CTRL> __device__ __forceinline__ int dppi(int x) { return __builtin_amdgcn_mov_dpp(x, CTRL, 0xf, 0xf, true); }
__device__ __forceinline__ int sum8i(int x) { x += dppi<0xB1>(x); x += dppi<0x4E>(x); x += dppi<0x141>(x); return x; }
__device__ __forceinline__ unsigned row_sums(const u32x4 (&p)[4]) {
  unsigned c4[4];
#pragma unroll
  for (int i = 0; i < 4; i++) c4[i] = (p[i][0] + p[i][1]) + (p[i][2] + p[i][3]);
  auto s0 = __builtin_amdgcn_permlane32_swap(c4[0], c4[2], false, false);
  auto s1 = __builtin_amdgcn_permlane32_swap(c4[1], c4[3], false, false);
  auto s2 = __builtin_amdgcn_permlane16_swap(s0[0] + s0[1], s1[0] + s1[1], false, false);
  int x = (int)(s2[0] + s2[1]);
  x += dppi<0x128>(x);
  return (unsigned)sum8i(x);
}
__device__ __forceinline__ unsigned clk() { return (unsigned)__builtin_amdgcn_s_memrealtime(); }
__device__ __forceinline__ void wait_until(unsigned t) {
#pragma unroll 1
  for (int spins = 0; spins < 4096 && (int)(t - clk()) > 0; ++spins) __builtin_amdgcn_s_sleep(1);
}
__device__ __forceinline__ unsigned ref_time(unsigned v) {
  const unsigned t0 = __builtin_amdgcn_readfirstlane(v);
  return t0 + (unsigned)__builtin_amdgcn_readfirstlane(sum8i((int)(v - t0)) >> 3);
}
__device__ __forceinline__ unsigned wait_gate(const char* flag, unsigned ta, unsigned* slot, unsigned tent) {
  if (threadIdx.x < 64) {
    u64 v = 0;
    wait_until(tent + TM_FC);
#pragma unroll 1
    for (unsigned spins = 0; spins < SPIN_CAP; ++spins) {
      v = ld_u64(flag + (threadIdx.x & 7) * LINE);
      if (__all((unsigned)(v >> 32) == ta)) break;
      __builtin_amdgcn_s_sleep(12);
    }
    const unsigned tr = ref_time((unsigned)v);
    if (threadIdx.x == 0) *slot = tr;
  }
  __syncthreads();
  return __builtin_amdgcn_readfirstlane(*slot);
}
__device__ __forceinline__ void st16u_sc1(__amdgpu_buffer_rsrc_t rs, unsigned off, u32x4 v) {
  __builtin_amdgcn_raw_buffer_store_b128(v, rs, off, 0, SC1);
}
__device__ __forceinline__ u32x4 ld16u_sc1(__amdgpu_buffer_rsrc_t rs, unsigned off) {
  return __builtin_amdgcn_raw_buffer_load_b128(rs, off, 0, SC1);
}


__device__ __forceinline__ void item_dots(const u32x4 (&ks)[4], const u32x4 q, float (&s_)[4]) {
#pragma unroll
  for (int u = 0; u < 4; u++) {
    float a = dot2(ks[u][0], q[0], 0.f);
    a = dot2(ks[u][1], q[1], a);
    a = dot2(ks[u][2], q[2], a);
    a = dot2(ks[u][3], q[3], a);
    s_[u] = a;
    asm volatile("" : "+v"(s_[u]));
  }
}
__device__ __forceinline__ void item_unpack(const u32x4 (&vs)[4], f32x2 (&vf)[4][4]) {
#pragma unroll
  for (int u = 0; u < 4; u++)
#pragma unroll
    for (int j = 0; j < 4; j++) { vf[u][j] = f32x2{bf_lo(vs[u][j]), bf_hi(vs[u][j])}; asm volatile("" : "+v"(vf[u][j])); }
}
__device__ __forceinline__ f32x2 rows_sum(const float (&o)[8]) {
  float r4[4], r2[2];
#pragma unroll
  for (int j = 0; j < 4; j++) {
    auto sx = __builtin_amdgcn_permlane32_swap(__float_as_uint(o[j]), __float_as_uint(o[j + 4]), false, false);
    r4[j] = __uint_as_float(sx[0]) + __uint_as_float(sx[1]);
  }
#pragma unroll
  for (int j = 0; j < 2; j++) {
    auto sx = __builtin_amdgcn_permlane16_swap(__float_as_uint(r4[j]), __float_as_uint(r4[j + 2]), false, false);
    r2[j] = __uint_as_float(sx[0]) + __uint_as_float(sx[1]);
  }
  return f32x2{r2[0], r2[1]};
}
__device__ __forceinline__ void role_B(int h, int j6, int xcd, const __hip_bfloat16* __restrict__ ckv, const __hip_bfloat16* __restrict__ xB,
                                       const __hip_bfloat16* __restrict__ kc, const __hip_bfloat16* __restrict__ vc,
                                       const __hip_bfloat16* __restrict__ wqB, const __hip_bfloat16* __restrict__ woB,
                                       char* __restrict__ ws, __amdgpu_buffer_rsrc_t rs,
                                       __hip_bfloat16* __restrict__ out, char* lds) {
  const int t = threadIdx.x, l = t & 63;
  const int w = __builtin_amdgcn_readfirstlane(t >> 6);
  const int g = l >> 4, c = l & 15;
  const int pr = j6 >= 3 ? 1 : 0, k = j6 - 3 * pr;
  const unsigned tent = clk();
  const int b0 = 2 * pr;
  const int e0 = k == 1 ? (h & 1) : (k >> 1);
  u32x4 xr[2][2], wr[16];
  {
    const __amdgpu_buffer_rsrc_t rx = mkrs(xB, NB * DM * 2);
    const int rq_ = (0x203120 >> (4 * j6)) & 3;
    const int cw = w ^ ((rq_ & 2) << 1), r0 = rq_ & 1;
    const unsigned xo = (unsigned)((b0 * DM + cw * 64 + g * 16) * 2);
#pragma unroll
    for (int bb = 0; bb < 2; bb++)
#pragma unroll
      for (int i = 0; i < 2; i++) xr[bb][i] = __builtin_amdgcn_raw_buffer_load_b128(rx, xo + bb * DM * 2, (i ^ r0) * 16, 0);
    const __amdgpu_buffer_rsrc_t rq = mkrs(wqB, DM * HD * 2);
    const unsigned vo = (unsigned)(((cw * 64 + g * 16) * HD + h * DH + c * 8) * 2);
    const unsigned vo0 = vo + r0 * 8 * HD * 2, vo1 = vo + (1 - r0) * 8 * HD * 2;
#pragma unroll
    for (int i = 0; i < 16; i++) wr[i] = __builtin_amdgcn_raw_buffer_load_b128(rq, i < 8 ? vo0 : vo1, (i & 7) * HD * 2, 0);
  }
  constexpr unsigned KVB = (unsigned)((size_t)NB * NH * SK * DH * 2);
  const unsigned lo = (unsigned)(g * 256 + c * 16);
  const unsigned hb0 = ((unsigned)((b0 + e0) * NH + h) << 20) + (k == 1 ? (unsigned)SS << 12 : 0u);
  const unsigned hb1 = ((unsigned)((b0 + 1 - e0) * NH + h) << 20) + ((unsigned)SS << 12);
  auto seg1 = [&](int x) { return k == 1 && x >= N0; };
  auto unit = [&](int x) { const int y = seg1(x) ? x - N0 : x; return 4 * (y >> 1) + (y & 1); };
  auto soff = [&](int x) { return (seg1(x) ? hb1 : hb0) + ((unsigned)unit(x) << 12); };
  auto sco = [&](int x) { return (unsigned)((unit(x) + (seg1(x) ? 88 : 0)) * 64); };
  auto whole = [&](int x) { return x != NI - 1 && !(k == 1 && x == N0 - 1); };
  auto ld4 = [&](u32x4 (&d)[4], const void* p, bool ok, unsigned so) {
    const __amdgpu_buffer_rsrc_t r = mkrs(p, ok ? KVB : 0u);
#pragma unroll
    for (int u = 0; u < 4; u++) d[u] = __builtin_amdgcn_raw_buffer_load_b128(r, lo + u * 1024, so, NT);
  };
  unsigned pf;
  {
    const int n = ((h >> 3) * 6 + j6) * 8 + w, a = xcd * 4 + n / 48;
    const unsigned ln = (unsigned)((n % 48) * 54 + l);
    const unsigned po = (l < 54 && ln < 2560) ? ((3 + (ln >> 9) + a) & 7) * 65536u + (ln & 511) * 128u : 0x7ffffff0u;
    pf = __builtin_amdgcn_raw_buffer_load_b32(mkrs(ckv, NB * SK * DC * 2), po, (unsigned)a * 524288u, 0);
  }
  u32x4 ra[4], rb[4], ea[4], eb[4];
  u64* eps = (u64*)(lds + 33808);
  u64 epv = 0;
  if (t == 0) epv = ld_u64(ws + WS_EP);
  float* red = (float*)lds;
  unsigned* qs = (unsigned*)(lds + 32768);
  unsigned* cnt = (unsigned*)(lds + 33856);
  float* mxd = (float*)(lds + 33872);
  float* lw = (float*)(lds + 33920);
  float* rec0 = (float*)(lds + 36864);
  char* prv = lds + 45056 + w * 1024;
  constexpr unsigned L_SC = 53248;
  float* recd = (float*)(lds + 65536);
  const unsigned lb = (unsigned)(uintptr_t)(__attribute__((address_space(3))) char*)lds;
  if (t == 0) { cnt[0] = 16u; cnt[1] = 8u * (RD + 1); cnt[2] = 0u; cnt[3] = 0u; cnt[6] = tent - 0x1000000u; mxd[0] = -INFINITY; mxd[1] = -INFINITY; }
  if (t < 64 * (NP4 - NP)) *(f32x2*)(recd + (NP + (t >> 6)) * RST + 2 * l) = f32x2{0.f, 0.f};
  if (t >= 384) *(float*)(lds + L_SC + ((t & 64) ? 172 : 84) * 64 + l * 4) = -INFINITY;
  {
    float a0[8], a1[8];
#pragma unroll
    for (int j = 0; j < 8; j++) { a0[j] = 0.f; a1[j] = 0.f; }
#pragma unroll
    for (int i = 0; i < 16; i += 2) {
      const unsigned x0 = xr[0][i >> 3][(i >> 1) & 3], x1 = xr[1][i >> 3][(i >> 1) & 3];
#pragma unroll
      for (int j = 0; j < 4; j++) {
        const unsigned pl = __builtin_amdgcn_perm(wr[i + 1][j], wr[i][j], 0x05040100u);
        const unsigned ph = __builtin_amdgcn_perm(wr[i + 1][j], wr[i][j], 0x07060302u);
        a0[2 * j] = dot2(pl, x0, a0[2 * j]); a0[2 * j + 1] = dot2(ph, x0, a0[2 * j + 1]);
        a1[2 * j] = dot2(pl, x1, a1[2 * j]); a1[2 * j + 1] = dot2(ph, x1, a1[2 * j + 1]);
      }
    }
#pragma unroll
    for (int j = 0; j < 8; j++) asm volatile("" : "+v"(a0[j]), "+v"(a1[j]));
    __builtin_amdgcn_sched_barrier(0);
    ld4(ra, kc, true, soff(w));
    ld4(rb, kc, true, soff(w) + 8192);
    ld4(ea, kc, true, soff(w + 8));
    ld4(eb, kc, true, soff(w + 8) + 8192);
    __builtin_amdgcn_sched_barrier(0);
    float* rp = red + (w * 4 + g) * 256 + c * 8;
    *(f32x4*)(rp) = f32x4{a0[0], a0[1], a0[2], a0[3]};
    *(f32x4*)(rp + 4) = f32x4{a0[4], a0[5], a0[6], a0[7]};
    *(f32x4*)(rp + 128) = f32x4{a1[0], a1[1], a1[2], a1[3]};
    *(f32x4*)(rp + 132) = f32x4{a1[4], a1[5], a1[6], a1[7]};
  }
  __syncthreads();
  {
    const int o = t >> 1, hf = t & 1;
    float s = 0.f;
#pragma unroll
    for (int i = 0; i < 16; i++) s += red[(hf * 16 + i) * 256 + o];
    s += dpp<0xB1>(s);
    const float sn = dpp<0x4E>(s);
    if ((t & 3) == 0) qs[o >> 1] = cvtpk(s, sn);
    if (t == 0) { *eps = epv; st_u64(ws + WS_CK + (h * 6 + j6) * LINE, next_tag(epv)); }
  }
  __syncthreads();
  u32x4 q = *(const u32x4*)(qs + e0 * 64 + c * 4);
  const u32x4 q1 = *(const u32x4*)(qs + (1 - e0) * 64 + c * 4);
  auto lds_cnt = [&](int i) { return (unsigned)__builtin_amdgcn_readfirstlane(__hip_atomic_load(cnt + i, __ATOMIC_RELAXED, __HIP_MEMORY_SCOPE_WORKGROUP)); };
  auto lds_inc = [&](int i) {
    asm volatile("s_waitcnt lgkmcnt(0)" ::: "memory");
    if (l == 0) asm volatile("ds_add_u32 %0, %1" :: "v"(lb + 33856u + i * 4), "v"(1u) : "memory");
  };
  u32x4 wo[16];
  __builtin_amdgcn_sched_barrier(0);
#pragma unroll
  for (int i = 0; i < 16; i++) asm volatile("" : "=v"(wo[i]));
  const int nseg = k == 1 ? 2 : 1, sp = k == 1 ? 1 : 0;
  auto store_off = [&](int sg) {
    const int b = b0 + (sg ? 1 - e0 : e0);
    const unsigned og_ = (unsigned)(WS_PBG + ((size_t)(b * 64 + h * 2 + sp) * DM + w * 64 + c * 4 + g) * 8);
    return (g & 1) == 0 ? og_ : 0x7ffffff0u;
  };
  const unsigned apo = (unsigned)((c & 1) * 272 + g * 64);
  auto project = [&](const char* ap, float M, float L) {
    u32x4 a[4];
#pragma unroll
    for (int s = 0; s < 4; s++) a[s] = *(const u32x4*)(ap + s * 16);
    __builtin_amdgcn_sched_barrier(0);
    L += dpp<0xB1>(L); L += dpp<0x4E>(L); L += dpp<0x141>(L);
    __builtin_amdgcn_sched_barrier(0);
    f32x4 d[4];
#pragma unroll
    for (int s = 0; s < 4; s++)
#pragma unroll
      for (int j = 0; j < 4; j++)
        d[j] = __builtin_amdgcn_mfma_f32_16x16x32_bf16(__builtin_bit_cast(bf16x8, a[s]), __builtin_bit_cast(bf16x8, wo[4 * s + j]),
                                                       s ? d[j] : f32x4{0.f, 0.f, 0.f, 0.f}, 0, 0, 0);
    return f32x3{g < 2 ? d[0][0] + d[0][1] : d[3][0] + d[3][1], g < 2 ? d[1][0] + d[1][1] : M, g < 2 ? d[2][0] + d[2][1] : L};
  };
  f32x3 seg0 = {0.f, 0.f, 0.f};
  auto load_wo = [&]() {
    const __amdgpu_buffer_rsrc_t ro = mkrs(woB, HD * DM * 2);
    const unsigned vo = (unsigned)(((h * DH + g * 32) * DM + w * 64 + c * 4) * 2);
#pragma unroll
    for (int i = 0; i < 16; i++) {
      const u32x2 u0 = __builtin_amdgcn_raw_buffer_load_b64(ro, vo + 2 * i * DM * 2, 0, 0);
      const u32x2 u1 = __builtin_amdgcn_raw_buffer_load_b64(ro, vo + (2 * i + 1) * DM * 2, 0, 0);
      wo[i] = u32x4{u0[0], u0[1], u1[0], u1[1]};
    }
  };
  asm volatile("" :: "v"(pf));
  unsigned tp = *(const unsigned*)eps + 1u;
  asm volatile("" : "+v"(tp));
  {
    float m = -INFINITY, s_[4];
    const unsigned swo = lb + L_SC + g * 16 + (c & 3) * 4;
    auto scores = [&](unsigned so, bool ok) {
      const bool b0_ = c & 1, b1_ = c & 2;
      const float r0 = (b0_ ? s_[1] : s_[0]) + dpp<0xB1>(b0_ ? s_[0] : s_[1]);
      const float r1 = (b0_ ? s_[3] : s_[2]) + dpp<0xB1>(b0_ ? s_[2] : s_[3]);
      float r = (b1_ ? r1 : r0) + dpp<0x4E>(b1_ ? r0 : r1);
      r += dpp<0x128>(r);
      r += dpp<0x124>(r);
      r = ok ? rbf(rbf(r) * SCALE) : -INFINITY;
      m = fmaxf(m, r);
      asm volatile("ds_write_b32 %0, %1" :: "v"(swo + so), "v"(r) : "memory");
    };
    auto flush_max = [&](int sg) {
      float mw = fmaxf(m, dpp<0xB1>(m));
      mw = xrow_max(fmaxf(mw, dpp<0x4E>(mw)));
      if (l == 0) asm volatile("ds_max_f32 %0, %1" :: "v"(lb + 33872u + sg * 4), "v"(mw) : "memory");
      m = -INFINITY;
    };
    bool sw = k != 1, first = true, wl = false;
    u32x2 krep = {0u, ~tp};
    const int xw = 16 + 16 * (h >> 3);
    int x = w;
#pragma unroll 1
    while (x < NI) {
      if (!sw && x >= N0) { flush_max(0); q = q1; sw = true; }
      if (!wl && x >= xw) { load_wo(); wl = true; }
      if (x == X_LOOK) krep = __builtin_amdgcn_raw_buffer_load_b64(rs, (unsigned)WS_FK + (l & 7) * LINE, 0, SC1);
      unsigned dr; u64 sv;
      asm volatile("s_mov_b64 %1, exec\n\ts_mov_b64 exec, 1\n\tds_add_rtn_u32 %0, %2, %3\n\ts_mov_b64 exec, %1"
                   : "=&v"(dr), "=&s"(sv) : "v"(lb + 33856u), "v"(1u), "v"(ra[0][0]) : "memory");
      __builtin_amdgcn_sched_barrier(0);
      item_dots(ra, q, s_);
      asm volatile("s_waitcnt lgkmcnt(0)" : "+v"(dr) :: "memory");
      const int xn = __builtin_amdgcn_readfirstlane(dr);
      const bool more = xn < NI;
      const void* pn = more ? (const void*)kc : (const void*)vc;
      const unsigned sn = soff(more ? xn : w);
      if (xn > X_LOOK + 6) wait_until(lds_cnt(6) + sched(more ? xn - NI : w) + 60);
      __builtin_amdgcn_sched_barrier(0);
      ld4(ra, pn, true, sn);
      __builtin_amdgcn_sched_barrier(0);
      scores(sco(x), true);
      item_dots(rb, q, s_);
      __builtin_amdgcn_sched_barrier(0);
      ld4(rb, pn, !more || whole(xn), sn + 8192);
      __builtin_amdgcn_sched_barrier(0);
      scores(sco(x) + 128, whole(x));
      if (xn == X_REP && h >= 8 && h < 16 && j6 == 2) {
        if (l == 0) st_u64(ws + WS_FK + (h - 8) * LINE, ((u64)tp << 32) | (clk() + 1080));
      }
      if (x == X_LOOK && __all(krep[1] == tp)) {
        const unsigned tr = ref_time(krep[0]);
        if (l == 0) cnt[6] = tr;
      }
      if (first) {
        item_dots(ea, q, s_); scores(sco(w + 8), true);
        item_dots(eb, q, s_); scores(sco(w + 8) + 128, true);
        first = false;
      }
      x = xn;
    }
    if (!wl) {
      load_wo();
#pragma unroll
      for (int i = 0; i < 16; i++) asm volatile("" : "+v"(wo[i]));
    }
    flush_max(k == 1 && !sw ? 0 : 1);
    lds_inc(2);
    wait_until(lds_cnt(6) + sched(w + 8));
    ld4(ea, vc, true, soff(w + 8));
    ld4(eb, vc, true, soff(w + 8) + 8192);
  }
#pragma unroll
  for (int s = 0; s < 4; s++) {
    u32x4 o[4];
#pragma unroll
    for (int e = 0; e < 4; e++)
#pragma unroll
      for (int j = 0; j < 4; j++) o[j][e] = __builtin_amdgcn_perm(wo[4 * s + e][2 + (j >> 1)], wo[4 * s + e][j >> 1], (j & 1) ? 0x07060302u : 0x05040100u);
#pragma unroll
    for (int j = 0; j < 4; j++) wo[4 * s + j] = o[j];
    asm volatile("" : "+v"(wo[4 * s]), "+v"(wo[4 * s + 1]), "+v"(wo[4 * s + 2]), "+v"(wo[4 * s + 3]));
  }
  for (unsigned spins = 0; lds_cnt(2) != 8u && spins < SPIN_CAP; ++spins) __builtin_amdgcn_s_sleep(1);
  asm volatile("" ::: "memory");
  if (h >= 8 && h < 16 && j6 == 2 && t == 0) st_u64(ws + WS_FS + (h - 8) * LINE, ((u64)tp << 32) | (clk() + 70));
  u32x2 srep = {0u, 0u};
  const float M0 = __builtin_bit_cast(float, __builtin_amdgcn_readfirstlane(__builtin_bit_cast(int, mxd[k == 1 ? 0 : 1])));
  const float M1 = __builtin_bit_cast(float, __builtin_amdgcn_readfirstlane(__builtin_bit_cast(int, mxd[1])));
  auto weights = [&](unsigned so, float M) {
    const f32x4 sc = *(const f32x4*)(lds + L_SC + so);
    return f32x4{fexp2((sc[0] - M) * LOG2E), fexp2((sc[1] - M) * LOG2E), fexp2((sc[2] - M) * LOG2E), fexp2((sc[3] - M) * LOG2E)};
  };
  float acc[8];
#pragma unroll
  for (int j = 0; j < 8; j++) acc[j] = 0.f;
  f32x2 vf[4][4];
  f32x4 pa, pb;
  unsigned sb = 0;
  auto get_p = [&](int x) {
    const float M = seg1(x) ? M1 : M0;
    pa = weights(sco(x) + g * 16, M); pb = weights(sco(x) + 128 + g * 16, M);
    asm volatile("" : "+v"(pa), "+v"(pb));
    sb = lds_cnt(6);
  };
  auto fma_v = [&](const f32x4 p) {
#pragma unroll
    for (int u = 0; u < 4; u++)
#pragma unroll
      for (int j = 0; j < 4; j++) {
        acc[2 * j]     = fmaf(p[u], vf[u][j][0], acc[2 * j]);
        acc[2 * j + 1] = fmaf(p[u], vf[u][j][1], acc[2 * j + 1]);
      }
  };
  auto step = [&](int xn) {
    const unsigned sn = soff(xn);
    wait_until(sb + sched(xn));
    __builtin_amdgcn_sched_barrier(0);
    ld4(ra, vc, true, sn);
    __builtin_amdgcn_sched_barrier(0);
    fma_v(pa);
#pragma unroll
    for (int j = 0; j < 8; j++) asm volatile("" : "+v"(acc[j]));
    item_unpack(rb, vf);
    __builtin_amdgcn_sched_barrier(0);
    ld4(rb, vc, whole(xn), sn + 8192);
    __builtin_amdgcn_sched_barrier(0);
    fma_v(pb);
  };
  auto put_acc = [&](float* rec_) {
    *(f32x2*)(rec_ + c * 8 + g * 2) = rows_sum(acc);
#pragma unroll
    for (int j = 0; j < 8; j++) acc[j] = 0.f;
  };
  if (w >= 4) __builtin_amdgcn_s_setprio(1);
  get_p(w);
  item_unpack(ra, vf);
  step(w + 16);
  get_p(w + 8);
  item_unpack(ea, vf); fma_v(pa);
  item_unpack(eb, vf); fma_v(pb);
  {
    const bool two = t < 192;
    const f32x4 p0 = weights(t * 16, t < 352 ? M0 : M1), p1 = weights(two ? (t + 512) * 16 : 172 * 64, two ? M1 : 0.f);
    const float s0 = (p0[0] + p0[1]) + (p0[2] + p0[3]), s1 = (p1[0] + p1[1]) + (p1[2] + p1[3]);
    const bool f0 = k == 1 && t < 352;
    const float l0 = xrow_sum(sum16(f0 ? s0 : 0.f)), l1 = xrow_sum(sum16((f0 ? 0.f : s0) + (two ? s1 : 0.f)));
    if (l < 2) lw[l * 8 + w] = l ? l1 : l0;
  }
  bool sw = k != 1;
  int x = w + 16;
#pragma unroll 1
  for (; x < 8 * RD; x += 8) {
    const int rd = x >> 3;
    if (rd == PRIOF) __builtin_amdgcn_s_setprio(0);
    if (!sw && x >= N0) { put_acc(rec0 + w * 128); lds_inc(3); sw = true; }
    if (w == 3 && rd == R_LOOK + 1 && __all(srep[1] == tp)) {
      const unsigned tr = ref_time(srep[0]);
      if (l == 0) cnt[6] = tr;
    }
    if (w == 3 && rd == R_LOOK) srep = __builtin_amdgcn_raw_buffer_load_b64(rs, (unsigned)WS_FS + (l & 7) * LINE, 0, SC1);
    if (rd == RE && k == 1) {
      for (unsigned spins = 0; lds_cnt(3) != 8u && spins < SPIN_CAP; ++spins) __builtin_amdgcn_s_sleep(1);
      asm volatile("" ::: "memory");
      f32x2 a2 = {0.f, 0.f};
#pragma unroll
      for (int i = 0; i < 8; i++) a2 += *(const f32x2*)(rec0 + i * 128 + 2 * l);
      const unsigned hi = cvtpk(a2[0], a2[1]);
      *(unsigned*)(prv + l * 4) = hi;
      *(unsigned*)(prv + 272 + l * 4) = cvtpk(a2[0] - bf_lo(hi), a2[1] - bf_hi(hi));
      seg0 = project(prv + apo, M0, lw[l & 7]);
    }
    get_p(x);
    __builtin_amdgcn_sched_barrier(0);
    item_unpack(ra, vf);
    step(x + 8);
  }
  put_acc(recd + w * RST);
  unsigned o_rv = 65536u + (g * RST + w * 16 + c) * 4, o_lw = 33920u + 32u + (l & 7) * 4;
  unsigned o_oi = (g < 2 && (c & 1) == 0) ? 41216u + g * 272 + (w * 16 + c) * 2 : 45056u + w * 1024 + 256 + l * 4;
  unsigned o_ap = 41216u + apo, o_st = store_off(nseg - 1);
  unsigned o_s0 = k == 1 ? store_off(0) : 0x7ffffff0u;
  u32x4 sg0 = {__float_as_uint(seg0[0]), __float_as_uint(seg0[1]), __float_as_uint(seg0[2]), tp};
  asm volatile("" : "+v"(o_s0), "+v"(sg0));
  asm volatile("" : "+v"(o_rv), "+v"(o_lw), "+v"(o_oi), "+v"(o_ap), "+v"(o_st));
  {
    {
#pragma unroll 1
      for (;;) {
        get_p(x);
        unsigned dr; u64 sv;
        asm volatile("s_mov_b64 %1, exec\n\ts_mov_b64 exec, 1\n\tds_add_rtn_u32 %0, %2, %3\n\ts_mov_b64 exec, %1"
                     : "=&v"(dr), "=&s"(sv) : "v"(lb + 33860u), "v"(1u), "v"(ra[0][0]) : "memory");
        __builtin_amdgcn_sched_barrier(0);
        item_unpack(ra, vf);
        asm volatile("s_waitcnt lgkmcnt(0)" : "+v"(dr) :: "memory");
        const int xn = __builtin_amdgcn_readfirstlane(dr);
        if (xn >= NI) break;
        step(xn);
        put_acc(recd + (x - 8 * RD + 8) * RST);
        x = xn;
      }
      fma_v(pa);
      item_unpack(rb, vf);
      fma_v(pb);
      put_acc(recd + (x - 8 * RD + 8) * RST);
      asm volatile("s_waitcnt lgkmcnt(0)" ::: "memory");
      __syncthreads();
    }
    {
      float rv_[NP4 / 4];
#pragma unroll
      for (int r = 0; r < NP4 / 4; r++) rv_[r] = *(const float*)(lds + o_rv + r * 4 * RST * 4);
      const float li = *(const float*)(lds + o_lw);
      float a = 0.f, a1 = 0.f;
#pragma unroll
      for (int r = 0; r < NP4 / 4; r++) { if (r & 1) a1 += rv_[r]; else a += rv_[r]; }
      a += a1;
      a = xrow_sum(a);
      const float an = dpp<0xB1>(a);
      const unsigned hi = cvtpk(a, an);
      const unsigned lo2 = cvtpk(a - bf_lo(hi), an - bf_hi(hi));
      *(unsigned*)(lds + o_oi) = g ? lo2 : hi;
      __syncthreads();
      const f32x3 pv = project(lds + o_ap, M1, li);
      st16u_sc1(rs, o_st, u32x4{__float_as_uint(pv[0]), __float_as_uint(pv[1]), __float_as_uint(pv[2]), tp});
      st16u_sc1(rs, o_s0, sg0);
    }
    if (w < nseg) {
      const int sg = w;
      const int b = b0 + (sg ? 1 - e0 : e0);
      const int j64 = (h & 7) * 8 + (h >> 3) * 2 + sp;
      const unsigned og = (unsigned)(WS_PBG + ((size_t)(b * 64 + (l >> 2)) * DM + j64 * 8) * 8 + (l & 3) * 16);
      u32x4 p[4];
      {
        __builtin_amdgcn_s_sleep(4);
        wait_until(sb + TM_SW);
      }
#pragma unroll 1
      for (unsigned spins = 0; spins < SPIN_CAP; ++spins) {
        asm volatile("" ::: "memory");
#pragma unroll
        for (int i = 0; i < 4; i++) p[i] = ld16u_sc1(rs, og + i * 16 * DM * 8);
        if (__all(p[0][3] == tp && p[1][3] == tp && p[2][3] == tp && p[3][3] == tp)) break;
      }
      float v0 = 0.f, v1 = 0.f, v2 = 0.f;
#pragma unroll
      for (int i = 0; i < 4; i++) {
        const float mm = dpp<0xF5>(__uint_as_float(p[i][1]));
        const float Mh = fmaxf(mm, dpp<0x141>(mm));
        const float fq = fexp2((mm - Mh) * LOG2E);
        float L = fq * dpp<0xF5>(__uint_as_float(p[i][2]));
        L += dpp<0x141>(L);
        const float wgt = fq * __builtin_amdgcn_rcpf(L);
        v0 = fmaf(__uint_as_float(p[i][0]), wgt, v0);
        v1 = fmaf(__uint_as_float(p[i][1]), wgt, v1);
        v2 = fmaf(__uint_as_float(p[i][2]), wgt, v2);
      }
      const float u2 = dpp<0xA0>(v2);
      v1 = (l & 1) ? v0 : v1;
      v0 = (l & 1) ? u2 : v0;
      v0 += dpp<0x128>(v0); v0 += dpp<0x124>(v0); v0 = xrow_sum(v0);
      v1 += dpp<0x128>(v1); v1 += dpp<0x124>(v1); v1 = xrow_sum(v1);
      if (l < 4) *(unsigned*)(out + b * 1024 + 512 + j64 * 8 + l * 2) = cvtpk(v0, v1);
    }
  }
}

__device__ __forceinline__ unsigned lds_off(int row, int ch) {
  return 256 * row + 16 * (ch ^ (((row & 3) << 2) | ((row >> 2) & 3)));
}

__device__ __forceinline__ void role_A1(int h, const __hip_bfloat16* __restrict__ xA, const __hip_bfloat16* __restrict__ wqA,
                                        const __hip_bfloat16* __restrict__ wuk, const __hip_bfloat16* __restrict__ wuv,
                                        const __hip_bfloat16* __restrict__ woA, char* __restrict__ ws,
                                        __amdgpu_buffer_rsrc_t rs, __hip_bfloat16* __restrict__ out, char* lds) {
  const int t = threadIdx.x, l = t & 63;
  const int w = __builtin_amdgcn_readfirstlane(t >> 6);
  const int g = l >> 4, c = l & 15;
  const unsigned tent = clk();
  float* red = (float*)(lds + 16384);
  const unsigned vo = (unsigned)(((w * 64 + g * 16) * HD + h * DH + c * 8) * 2);
  u64 tag;
  {
    u32x4 xr[NB][2], wr[16], wk[16];
    const __amdgpu_buffer_rsrc_t rx = mkrs(xA, NB * DM * 2);
    const unsigned xo = (unsigned)((w * 64 + g * 16) * 2);
#pragma unroll
    for (int bb = 0; bb < NB; bb++)
#pragma unroll
      for (int i = 0; i < 2; i++) xr[bb][i] = __builtin_amdgcn_raw_buffer_load_b128(rx, xo + bb * DM * 2 + i * 16, 0, 0);
    const __amdgpu_buffer_rsrc_t rq = mkrs(wqA, DM * HD * 2);
#pragma unroll
    for (int i = 0; i < 16; i++) wr[i] = __builtin_amdgcn_raw_buffer_load_b128(rq, vo, i * HD * 2, NT);
    {
      const __amdgpu_buffer_rsrc_t rku = mkrs(wuk, DC * HD * 2);
#pragma unroll
      for (int i = 0; i < 16; i++) wk[i] = __builtin_amdgcn_raw_buffer_load_b128(rku, vo, i * HD * 2, NT);
    }
    __builtin_amdgcn_sched_barrier(0);
    u64 epv = 0;
    if (t == 0) epv = ld_u64(ws + WS_EP);
    unsigned* qs = (unsigned*)lds;
    unsigned short* qo = (unsigned short*)(lds + 4096);
    float acc[NB][8];
#pragma unroll
    for (int bb = 0; bb < NB; bb++)
#pragma unroll
      for (int j = 0; j < 8; j++) acc[bb][j] = 0.f;
#pragma unroll
    for (int i = 0; i < 16; i += 2) {
#pragma unroll
      for (int j = 0; j < 4; j++) {
        const unsigned pl = __builtin_amdgcn_perm(wr[i + 1][j], wr[i][j], 0x05040100u);
        const unsigned ph = __builtin_amdgcn_perm(wr[i + 1][j], wr[i][j], 0x07060302u);
#pragma unroll
        for (int bb = 0; bb < NB; bb++) {
          const unsigned x = xr[bb][i >> 3][(i >> 1) & 3];
          acc[bb][2 * j] = dot2(pl, x, acc[bb][2 * j]);
          acc[bb][2 * j + 1] = dot2(ph, x, acc[bb][2 * j + 1]);
        }
      }
    }
    float* rp = red + (w * 4 + g) * 512 + c * 8;
#pragma unroll
    for (int bb = 0; bb < NB; bb++) {
      *(f32x4*)(rp + bb * 128) = f32x4{acc[bb][0], acc[bb][1], acc[bb][2], acc[bb][3]};
      *(f32x4*)(rp + bb * 128 + 4) = f32x4{acc[bb][4], acc[bb][5], acc[bb][6], acc[bb][7]};
    }
    __syncthreads();
    {
      float s = 0.f;
#pragma unroll
      for (int i = 0; i < 32; i++) s += red[i * 512 + t];
      const float sn = dpp<0xB1>(s);
      if ((t & 1) == 0) qs[t >> 1] = cvtpk(s, sn);
      if (t == 0) *(u64*)(lds + 2048) = epv;
    }
    __syncthreads();
    tag = next_tag(uni64(*(const u64*)(lds + 2048)));
    {
      u32x4 qq[NB];
#pragma unroll
      for (int bb = 0; bb < NB; bb++) qq[bb] = *(const u32x4*)(qs + bb * 64 + c * 4);
#pragma unroll
      for (int i = 0; i < 16; i++) {
        float s[NB];
#pragma unroll
        for (int bb = 0; bb < NB; bb++) {
          float v = dot2(wk[i][0], qq[bb][0], 0.f);
          v = dot2(wk[i][1], qq[bb][1], v);
          v = dot2(wk[i][2], qq[bb][2], v);
          v = dot2(wk[i][3], qq[bb][3], v);
          s[bb] = sum16(v);
        }
        const float v = (c & 2) ? ((c & 1) ? s[3] : s[2]) : ((c & 1) ? s[1] : s[0]);
        if (c < NB) qo[c * 512 + w * 64 + g * 16 + i] = (unsigned short)(cvtpk(v, v) & 0xffff);
      }
    }
    __syncthreads();
    if (t < 256) {
      const int bb = t >> 6, ch = t & 63;
      st16u_sc1(rs, (unsigned)(WS_QABS + ((size_t)(bb * NH + h) * DC + ch * 8) * 2), *(const u32x4*)(qo + bb * 512 + ch * 8));
    }
    signal_flag(ws + WS_F1 + h * LINE, tag);
  }
  const unsigned ta = (unsigned)tag, tb = (unsigned)(tag >> 32);
  {
    u32x4 bv[8][2], bo[8][2];
    u32x4 pre[2][8], wo[16];
    {
      const __amdgpu_buffer_rsrc_t ruv = mkrs(wuv, DC * HD * 2);
      const __amdgpu_buffer_rsrc_t rwo = mkrs(woA, HD * DM * 2);
      const unsigned wvo = (unsigned)(((h * DH + (l >> 3) * 16) * DM + w * 64 + (l & 7) * 8) * 2);
#pragma unroll
      for (int i = 0; i < 16; i++) pre[i >> 3][i & 7] = __builtin_amdgcn_raw_buffer_load_b128(ruv, vo - g * 8 * HD * 2, ((i >> 3) * 32 + (i & 7)) * HD * 2, NT);
#pragma unroll
      for (int i = 0; i < 16; i++) wo[i] = __builtin_amdgcn_raw_buffer_load_b128(rwo, wvo, i * DM * 2, NT);
    }
#pragma unroll
    for (int j = 0; j < 8; j++)
#pragma unroll
      for (int s = 0; s < 2; s++) {
#pragma unroll
        for (int e = 0; e < 4; e++)
          bv[j][s][e] = __builtin_amdgcn_perm(pre[s][2 * e + 1][j >> 1], pre[s][2 * e][j >> 1], (j & 1) ? 0x07060302u : 0x05040100u);
        asm volatile("" : "+v"(bv[j][s]));
      }
#pragma unroll
    for (int j = 0; j < 8; j++)
#pragma unroll
      for (int s = 0; s < 2; s++) {
#pragma unroll
        for (int e = 0; e < 4; e++)
          bo[j][s][e] = __builtin_amdgcn_perm(wo[8 * s + 2 * e + 1][j >> 1], wo[8 * s + 2 * e][j >> 1], (j & 1) ? 0x07060302u : 0x05040100u);
        asm volatile("" : "+v"(bo[j][s]));
      }
    constexpr int LAS = 1040;
    unsigned short* cs = (unsigned short*)(lds + 16384);
    float* outs = (float*)(lds + 20480);
    float* cred = (float*)(lds + 32768);
    unsigned po = (unsigned)(WS_PAO + ((size_t)(w * 4 * NH + h) * DC + l * 8) * 2);
    unsigned pc = (unsigned)(WS_PAC + (size_t)((w * 4 + g) * NH + h) * 8);
    unsigned lw_ = (unsigned)(65536 + ((w & 1) * 16 + (w >> 1) * 4) * LAS + l * 16), lr_ = (unsigned)(65536 + c * LAS + (w * 64 + g * 8) * 2);
    asm volatile("" : "+v"(po), "+v"(pc), "+v"(lw_), "+v"(lr_));
    const unsigned tw = (h & 1) ? tb : ta;
    if (h == 0 && w == 1) {
      const char* fp = ws + WS_F1 + l * LINE;
      wait_until(tent + TM_EP);
#pragma unroll 1
      for (unsigned spins = 0; spins < SPIN_CAP; ++spins) {
        const u64 f0 = ld_u64(fp), f1 = ld_u64(fp + 64 * LINE), f2 = ld_u64(fp + 128 * LINE), f3 = ld_u64(fp + 192 * LINE);
        if (__all(f0 == tag && f1 == tag && f2 == tag && f3 == tag)) break;
        __builtin_amdgcn_s_sleep(32);
      }
      if (l == 0) st_u64(ws + WS_EP, tag);
    }
    const unsigned tg = wait_gate(ws + WS_FS, ta, (unsigned*)(lds + 8192), tent);
    wait_until(tg + TM_PUT + TM_GET);
    {
      u32x4 pr[4];
#pragma unroll 1
      for (unsigned spins = 0; spins < SPIN_CAP; ++spins) {
        asm volatile("" ::: "memory");
        const u32x2 ck = __builtin_amdgcn_raw_buffer_load_b64(rs, pc, 0, SC1);
#pragma unroll
        for (int i = 0; i < 4; i++) pr[i] = __builtin_amdgcn_raw_buffer_load_b128(rs, po, i * NH * DC * 2, SC1);
#pragma unroll
        for (int i = 0; i < 4; i++) *(u32x4*)(lds + lw_ + i * LAS) = pr[i] - ta;
        if (__all(row_sums(pr) == ck[0] && ck[1] == tw)) break;
        __builtin_amdgcn_s_sleep(2);
      }
      __syncthreads();
      u32x4 af[2][2];
#pragma unroll
      for (int tl = 0; tl < 2; tl++)
#pragma unroll
        for (int s = 0; s < 2; s++) af[tl][s] = *(const u32x4*)(lds + lr_ + tl * 16 * LAS + s * 64);
      f32x4 dd[8];
#pragma unroll
      for (int j = 0; j < 8; j++)
        dd[j] = __builtin_amdgcn_mfma_f32_16x16x32_bf16(__builtin_bit_cast(bf16x8, af[0][0]), __builtin_bit_cast(bf16x8, bv[j][0]), f32x4{0.f, 0.f, 0.f, 0.f}, 0, 0, 0);
#pragma unroll
      for (int j = 0; j < 8; j++)
        dd[j] = __builtin_amdgcn_mfma_f32_16x16x32_bf16(__builtin_bit_cast(bf16x8, af[0][1]), __builtin_bit_cast(bf16x8, bv[j][1]), dd[j], 0, 0, 0);
#pragma unroll
      for (int j = 0; j < 8; j++)
        dd[j] = __builtin_amdgcn_mfma_f32_16x16x32_bf16(__builtin_bit_cast(bf16x8, af[1][0]), __builtin_bit_cast(bf16x8, bv[j][0]), dd[j], 0, 0, 0);
#pragma unroll
      for (int j = 0; j < 8; j++)
        dd[j] = __builtin_amdgcn_mfma_f32_16x16x32_bf16(__builtin_bit_cast(bf16x8, af[1][1]), __builtin_bit_cast(bf16x8, bv[j][1]), dd[j], 0, 0, 0);
      f32x4 v[2];
#pragma unroll
      for (int j = 0; j < 8; j++) v[j >> 2][j & 3] = (dd[j][0] + dd[j][1]) + (dd[j][2] + dd[j][3]);
      *(f32x4*)(cred + (w * 4 + g) * 128 + c * 8) = v[0];
      *(f32x4*)(cred + (w * 4 + g) * 128 + c * 8 + 4) = v[1];
    }
    __syncthreads();
    {
      float s = 0.f;
#pragma unroll
      for (int i = 0; i < 8; i++) s += cred[i * 512 + t];
      const float sn = dpp<0xB1>(s);
      if ((t & 1) == 0) *(unsigned*)(cs + t) = cvtpk(s, sn);
    }
    __syncthreads();
    {
      const char* ap = (const char*)cs + ((c >> 1) & 3) * 256 + (g * 32 + (c & 1) * 16) * 2;
      const u32x4 a0 = *(const u32x4*)(ap), a1 = *(const u32x4*)(ap + 16);
      f32x4 dd[8];
#pragma unroll
      for (int j = 0; j < 8; j++)
        dd[j] = __builtin_amdgcn_mfma_f32_16x16x32_bf16(__builtin_bit_cast(bf16x8, a0), __builtin_bit_cast(bf16x8, bo[j][0]), f32x4{0.f, 0.f, 0.f, 0.f}, 0, 0, 0);
#pragma unroll
      for (int j = 0; j < 8; j++)
        dd[j] = __builtin_amdgcn_mfma_f32_16x16x32_bf16(__builtin_bit_cast(bf16x8, a1), __builtin_bit_cast(bf16x8, bo[j][1]), dd[j], 0, 0, 0);
      f32x4 v4[2];
#pragma unroll
      for (int j = 0; j < 8; j++) {
        const float u0 = (l & 8) ? dd[j][1] : dd[j][0], u1 = (l & 8) ? dd[j][3] : dd[j][2];
        const float keep = (l & 8) ? u1 : u0, give = (l & 8) ? u0 : u1;
        v4[j >> 2][j & 3] = keep + dpp<0x128>(give);
      }
      if (g < 2) {
        float* op = outs + (g * 2 + ((l >> 3) & 1)) * 512 + w * 64 + (l & 7) * 8;
        *(f32x4*)op = v4[0];
        *(f32x4*)(op + 4) = v4[1];
      }
    }
    __syncthreads();
    {
      const f32x2 va = *(const f32x2*)(outs + t * 2), vb = *(const f32x2*)(outs + 1024 + t * 2);
      wait_until(tg + TM_PA);
      const unsigned oo = (unsigned)(WS_PA + ((size_t)((t >> 8) * NH + h) * DM + (t & 255) * 2) * 8);
      st16u_sc1(rs, oo, u32x4{__float_as_uint(va[0]), ta, __float_as_uint(va[1]), tb});
      st16u_sc1(rs, oo + 2 * NH * DM * 8, u32x4{__float_as_uint(vb[0]), ta, __float_as_uint(vb[1]), tb});
    }
  if (w < 4) {
    const int bb = h >> 3, n0 = (h & 7) * 64 + w * 16;
    const unsigned og = (unsigned)(WS_PA + ((size_t)(bb * NH + (l >> 3)) * DM + n0) * 8 + (l & 7) * 16);
    u32x4 p[4];
    wait_until(tg + TM_PA + TM_T4);
#pragma unroll 1
    for (unsigned spins = 0; spins < SPIN_CAP; ++spins) {
      asm volatile("" ::: "memory");
#pragma unroll
      for (int i = 0; i < 4; i++) p[i] = ld16u_sc1(rs, og + i * 8 * DM * 8);
      bool ok = true;
#pragma unroll
      for (int i = 0; i < 4; i++) ok = ok && p[i][1] == ta && p[i][3] == tb;
      if (__all(ok)) break;
      __builtin_amdgcn_s_sleep(4);
    }
    float v0 = (__uint_as_float(p[0][0]) + __uint_as_float(p[1][0])) + (__uint_as_float(p[2][0]) + __uint_as_float(p[3][0]));
    float v1 = (__uint_as_float(p[0][2]) + __uint_as_float(p[1][2])) + (__uint_as_float(p[2][2]) + __uint_as_float(p[3][2]));
    v0 += dpp<0x128>(v0); v0 = xrow_sum(v0);
    v1 += dpp<0x128>(v1); v1 = xrow_sum(v1);
    if (l < 8) *(unsigned*)(out + bb * 1024 + n0 + l * 2) = cvtpk(v0, v1);
  }
  }
}

__device__ __forceinline__ void role_A2(int a, const __hip_bfloat16* __restrict__ ckv, char* __restrict__ ws,
                                        __amdgpu_buffer_rsrc_t rs, char* lds) {
  const int t = threadIdx.x, l = t & 63;
  const int w = __builtin_amdgcn_readfirstlane(t >> 6);
  const int g = l >> 4, c = l & 15;
  const unsigned tent = clk();
  u64 ep = 0;
  if (t == 0) ep = ld_u64(ws + WS_EP);
  const int grp = w >> 2, wq = w & 3;
  const int b = a / SPLIT_A, sp = a % SPLIT_A;
  const int r = l & 31, hh = l >> 5;
  constexpr unsigned CB = (unsigned)((size_t)NB * SK * DC * 2);
  constexpr int NTL = KA / 64;
  const unsigned cvo = (unsigned)((g * DC + 128 * wq + c * 8) * 2);
  const unsigned cso = (unsigned)(((size_t)b * SK + sp * KA + grp * 32) * DC * 2);
  auto load_tile = [&](u32x4 (&pr)[8], int tile) {
    const bool ok = tile < NTL;
    const __amdgpu_buffer_rsrc_t rc = mkrs(ckv, ok ? CB : 0u);
    const unsigned so = ok ? cso + (unsigned)((tile + sp) & (NTL - 1)) * 64 * DC * 2 : 0u;
#pragma unroll
    for (int u = 0; u < 8; u++) pr[u] = __builtin_amdgcn_raw_buffer_load_b128(rc, cvo, so + u * 4 * DC * 2, NT);
  };
  char* img = lds + w * 8192;
  float* sred = (float*)(lds + 65536 + grp * 16384);
  float* mxs = (float*)(lds + 98304 + grp * 512);
  unsigned* pbuf = (unsigned*)(lds + 102400 + grp * 2048);
  float* lall = (float*)(lds + 106496);
  const unsigned imga = (unsigned)(uintptr_t)(__attribute__((address_space(3))) char*)img;
  auto put_tile = [&](u32x4 (&pr)[8]) {
#pragma unroll
    for (int u = 0; u < 8; u++) *(u32x4*)(img + lds_off(4 * u + g, c)) = pr[u];
  };
  u32x4 pre[2][8];
  load_tile(pre[0], 0);
  load_tile(pre[1], 1);
  const u64 tag0 = next_tag(uni64(ep));
  if (t == 0) st_u64(ws + WS_CK + (192 + a) * LINE, tag0);
  put_tile(pre[0]);
  __builtin_amdgcn_sched_barrier(0);
  load_tile(pre[0], 2);
  if (t < 64) {
    wait_until(tent + TM_F1);
#pragma unroll 1
    for (unsigned spins = 0; spins < SPIN_CAP; ++spins) {
      __builtin_amdgcn_s_sleep(32);
      if (__all(ld_u64(ws + WS_F1 + (l & 31) * LINE) == tag0)) break;
    }
  }
  if (t == 0) *(u64*)(lds + 110592) = tag0;
  __syncthreads();
  const u64 tag = uni64(*(const u64*)(lds + 110592));
  const unsigned ta = (unsigned)tag, tb = (unsigned)(tag >> 32);
  bf16x8 qf[8];
  {
    char* qi = lds + 65536;
    const unsigned qo = (unsigned)(WS_QABS + (size_t)b * NH * DC * 2 + t * 16);
    u32x4 qv[4];
#pragma unroll
    for (int i = 0; i < 4; i++) qv[i] = ld16u_sc1(rs, qo + i * 8192);
#pragma unroll
    for (int i = 0; i < 4; i++) *(u32x4*)(qi + (i * 8 + w) * 1040 + l * 16) = qv[i];
    __syncthreads();
#pragma unroll
    for (int s = 0; s < 8; s++) qf[s] = __builtin_bit_cast(bf16x8, *(const u32x4*)(qi + r * 1040 + (128 * wq + 16 * s + 8 * hh) * 2));
#pragma unroll
    for (int s = 0; s < 8; s++) asm volatile("" : "+v"(qf[s]));
    __syncthreads();
  }
  f32x16 o[4];
#pragma unroll
  for (int i = 0; i < 4; i++)
#pragma unroll
    for (int j = 0; j < 16; j++) o[i][j] = 0.f;
  float m = -INFINITY, lsum = 0.f;
  const int q4 = (l & 15) >> 2, p4 = l & 3, colh = g & 1;
  auto tile_body = [&](int tile, u32x4 (&nx)[8]) {
    f32x16 s;
#pragma unroll
    for (int j = 0; j < 16; j++) s[j] = 0.f;
    {
      u32x4 af[8];
#pragma unroll
      for (int ks = 0; ks < 8; ks++) af[ks] = *(const u32x4*)(img + lds_off(r, 2 * ks + hh));
      __builtin_amdgcn_sched_barrier(0);
#pragma unroll
      for (int ks = 0; ks < 8; ks++) s = __builtin_amdgcn_mfma_f32_32x32x16_bf16(__builtin_bit_cast(bf16x8, af[ks]), qf[ks], s, 0, 0, 0);
    }
    f32x4* sr = (f32x4*)sred;
#pragma unroll
    for (int k4 = 0; k4 < 4; k4++) sr[(k4 * 4 + wq) * 64 + l] = f32x4{s[4 * k4], s[4 * k4 + 1], s[4 * k4 + 2], s[4 * k4 + 3]};
    __syncthreads();
    float x[4];
    {
      const f32x4 v = (sr[(wq * 4) * 64 + l] + sr[(wq * 4 + 1) * 64 + l]) + (sr[(wq * 4 + 2) * 64 + l] + sr[(wq * 4 + 3) * 64 + l]);
#pragma unroll
      for (int e = 0; e < 4; e += 2) {
        const unsigned r1 = cvtpk(v[e], v[e + 1]);
        const unsigned r2 = cvtpk(bf_lo(r1) * SCALE, bf_hi(r1) * SCALE);
        x[e] = bf_lo(r2); x[e + 1] = bf_hi(r2);
      }
      float mq = fmaxf(fmaxf(x[0], x[1]), fmaxf(x[2], x[3]));
      auto sx = __builtin_amdgcn_permlane32_swap(__float_as_uint(mq), __float_as_uint(mq), false, false);
      mq = fmaxf(__uint_as_float(sx[0]), __uint_as_float(sx[1]));
      if (hh == 0) mxs[r * 4 + wq] = mq;
    }
    __syncthreads();
    const f32x4 m4 = *(const f32x4*)(mxs + r * 4);
    const float mn = fmaxf(fmaxf(m, fmaxf(m4[0], m4[1])), fmaxf(m4[2], m4[3]));
    const float alpha = fexp2((m - mn) * LOG2E);
    {
      const unsigned p0 = cvtpk(fexp2((x[0] - mn) * LOG2E), fexp2((x[1] - mn) * LOG2E));
      const unsigned p1 = cvtpk(fexp2((x[2] - mn) * LOG2E), fexp2((x[3] - mn) * LOG2E));
      *(u32x2*)(pbuf + l * 8 + wq * 2) = u32x2{p0, p1};
      lsum = lsum * alpha + ((bf_lo(p0) + bf_hi(p0)) + (bf_lo(p1) + bf_hi(p1)));
    }
    m = mn;
    __syncthreads();
    unsigned pk[8];
    {
      const u32x4 a0 = *(const u32x4*)(pbuf + l * 8), a1 = *(const u32x4*)(pbuf + l * 8 + 4);
      pk[0] = a0[0]; pk[1] = a0[1]; pk[2] = a0[2]; pk[3] = a0[3]; pk[4] = a1[0]; pk[5] = a1[1]; pk[6] = a1[2]; pk[7] = a1[3];
    }
    if (__any(alpha != 1.f)) {
#pragma unroll
      for (int i = 0; i < 4; i++)
#pragma unroll
        for (int j = 0; j < 16; j++) o[i][j] *= alpha;
    }
#pragma unroll
    for (int s2 = 0; s2 < 2; s2++) {
      u32x2 tv[4][2];
#pragma unroll
      for (int ct = 0; ct < 4; ct++) {
        const int ch = 4 * ct + 2 * colh + (p4 >> 1);
        const unsigned a0 = lds_off(4 * hh + q4, ch) + 8 * (p4 & 1);
        const unsigned a1 = lds_off(8 + 4 * hh + q4, ch) + 8 * (p4 & 1);
        if (s2 == 0) {
          asm volatile("ds_read_b64_tr_b16 %0, %1" : "=v"(tv[ct][0]) : "v"(imga + a0) : "memory");
          asm volatile("ds_read_b64_tr_b16 %0, %1" : "=v"(tv[ct][1]) : "v"(imga + a1) : "memory");
        } else {
          asm volatile("ds_read_b64_tr_b16 %0, %1 offset:4096" : "=v"(tv[ct][0]) : "v"(imga + a0) : "memory");
          asm volatile("ds_read_b64_tr_b16 %0, %1 offset:4096" : "=v"(tv[ct][1]) : "v"(imga + a1) : "memory");
        }
      }
      asm volatile("s_waitcnt lgkmcnt(0)" ::: "memory");
      __builtin_amdgcn_sched_barrier(0);
      u32x4 pb = {pk[4 * s2], pk[4 * s2 + 1], pk[4 * s2 + 2], pk[4 * s2 + 3]};
#pragma unroll
      for (int ct = 0; ct < 4; ct++) {
        u32x4 av = {tv[ct][0][0], tv[ct][0][1], tv[ct][1][0], tv[ct][1][1]};
        o[ct] = __builtin_amdgcn_mfma_f32_32x32x16_bf16(__builtin_bit_cast(bf16x8, av), __builtin_bit_cast(bf16x8, pb), o[ct], 0, 0, 0);
      }
    }
    put_tile(nx);
    __builtin_amdgcn_sched_barrier(0);
    load_tile(nx, tile + 3);
  };
#pragma unroll 1
  for (int tile = 0; tile < NTL; tile += 2) {
    tile_body(tile, pre[1]);
    tile_body(tile + 1, pre[0]);
  }
  constexpr int OST = 516;
  float* ob = (float*)lds;
  float* mlb = (float*)(lds + 32 * OST * 4);
  lall[w * 64 + l] = lsum;
  __syncthreads();
  {
    const float* lp = lall + grp * 256 + r;
    lsum = ((lp[0] + lp[32]) + (lp[64] + lp[96])) + ((lp[128] + lp[160]) + (lp[192] + lp[224]));
  }
  if (grp == 1) {
#pragma unroll
    for (int ct = 0; ct < 4; ct++)
#pragma unroll
      for (int j4 = 0; j4 < 4; j4++)
        *(f32x4*)(ob + r * OST + 128 * wq + 32 * ct + 8 * j4 + 4 * hh) =
            f32x4{o[ct][4 * j4], o[ct][4 * j4 + 1], o[ct][4 * j4 + 2], o[ct][4 * j4 + 3]};
    if (wq == 0 && l < 32) { mlb[2 * r] = m; mlb[2 * r + 1] = lsum; }
  }
  __syncthreads();
  if (grp == 0) {
    const float m1 = mlb[2 * r], l1 = mlb[2 * r + 1];
    const float M = fmaxf(m, m1);
    const float f0 = fexp2((m - M) * LOG2E), f1 = fexp2((m1 - M) * LOG2E);
#pragma unroll
    for (int ct = 0; ct < 4; ct++)
#pragma unroll
      for (int j4 = 0; j4 < 4; j4++) {
        float* p = ob + r * OST + 128 * wq + 32 * ct + 8 * j4 + 4 * hh;
        const f32x4 o1 = *(const f32x4*)p;
        *(f32x4*)p = f32x4{o[ct][4 * j4] * f0 + o1[0] * f1, o[ct][4 * j4 + 1] * f0 + o1[1] * f1,
                           o[ct][4 * j4 + 2] * f0 + o1[2] * f1, o[ct][4 * j4 + 3] * f0 + o1[3] * f1};
      }
    if (wq == 0 && l < 32)
      st16u_sc1(rs, (unsigned)(WS_PAML + ((size_t)(b * SPLIT_A + sp) * NH + r) * 16),
                u32x4{__float_as_uint(M), ta, __float_as_uint(lsum * f0 + l1 * f1), tb});
  }
  float* wgt = mlb + 64;
  unsigned* pcs = (unsigned*)(mlb + 128);
  const unsigned tg = wait_gate(ws + WS_FS, ta, (unsigned*)(mlb + 96), tent);
  if (t < 256) {
    wait_until(tg + TM_ML);
    const unsigned mo = (unsigned)(WS_PAML + ((size_t)(b * SPLIT_A + (t & 7)) * NH + (t >> 3)) * 16);
    u32x4 mv;
#pragma unroll 1
    for (unsigned spins = 0; spins < SPIN_CAP; ++spins) {
      asm volatile("" ::: "memory");
      mv = ld16u_sc1(rs, mo);
      if (__all(mv[1] == ta && mv[3] == tb)) break;
      __builtin_amdgcn_s_sleep(16);
    }
    const float mm = __uint_as_float(mv[0]);
    float M = fmaxf(mm, dpp<0xB1>(mm)); M = fmaxf(M, dpp<0x4E>(M)); M = fmaxf(M, dpp<0x141>(M));
    const float f = fexp2((mm - M) * LOG2E);
    float L = f * __uint_as_float(mv[2]);
    L += dpp<0xB1>(L); L += dpp<0x4E>(L); L += dpp<0x141>(L);
    if ((t & 7) == sp) wgt[t >> 3] = f / L;
  }
  __syncthreads();
  {
    const unsigned pao = (unsigned)(WS_PAO + (size_t)(b * SPLIT_A + sp) * NH * DC * 2);
    u32x4 pv[4];
#pragma unroll
    for (int i = 0; i < 4; i++) {
      const int hd = i * 8 + w;
      const float f = wgt[hd];
      const f32x4 v0 = *(const f32x4*)(ob + hd * OST + l * 8) * f, v1 = *(const f32x4*)(ob + hd * OST + l * 8 + 4) * f;
      pv[i] = u32x4{cvtpk(v0[0], v0[1]), cvtpk(v0[2], v0[3]), cvtpk(v1[0], v1[1]), cvtpk(v1[2], v1[3])} + ta;
      asm volatile("" : "+v"(pv[i]));
    }
    {
      const unsigned cs = row_sums(pv);
      if (c == 0) pcs[g * 8 + w] = cs;
    }
    __syncthreads();
    const u32x2 c2 = *(const u32x2*)(pcs + (t & 15) * 2);
    const unsigned co = t < 16 ? (unsigned)(WS_PAC + (size_t)(b * SPLIT_A + sp) * NH * 8 + t * 16) : 0x7ffffff0u;
    wait_until(tg + TM_PUT);
#pragma unroll
    for (int i = 0; i < 4; i++) st16u_sc1(rs, pao + (unsigned)(i * 512 + t) * 16, pv[i]);
    if (w == 0) st16u_sc1(rs, co, u32x4{c2[0], ta, c2[1], tb});
  }
}

__global__ __launch_bounds__(512, 2) void k_fused(const __hip_bfloat16* __restrict__ xA, const __hip_bfloat16* __restrict__ ckv,
                                                  const __hip_bfloat16* __restrict__ wqA, const __hip_bfloat16* __restrict__ wuk,
                                                  const __hip_bfloat16* __restrict__ wuv, const __hip_bfloat16* __restrict__ woA,
                                                  const __hip_bfloat16* __restrict__ xB, const __hip_bfloat16* __restrict__ kc,
                                                  const __hip_bfloat16* __restrict__ vc, const __hip_bfloat16* __restrict__ wqB,
                                                  const __hip_bfloat16* __restrict__ woB, char* __restrict__ ws,
                                                  __hip_bfloat16* __restrict__ out) {
  __shared__ __attribute__((aligned(16))) char lds[131072];
  asm volatile("" :: "s"(xA), "s"(ckv), "s"(wqA), "s"(wuk), "s"(wuv), "s"(woA), "s"(xB), "s"(kc), "s"(vc), "s"(wqB), "s"(woB), "s"(ws), "s"(out));
  const int blk = blockIdx.x, xcd = blk & 7, r = blk >> 3;
  unsigned pfv = 0;
  {
    u64 pc; unsigned len;
    asm volatile("s_getpc_b64 %0\n.Lcode_begin:\n\ts_mov_b32 %1, .Lcode_end-.Lcode_begin" : "=s"(pc), "=s"(len));
    const unsigned idx = r < 5 ? (unsigned)((r * 8 + (threadIdx.x >> 6) + xcd * 5) % 40) : 1000u;
    const __amdgpu_buffer_rsrc_t rc = mkrs((const void*)pc, len);
    pfv = __builtin_amdgcn_raw_buffer_load_b32(rc, idx * 1024 + (threadIdx.x & 63) * 16, 0, 0);
  }
  const __amdgpu_buffer_rsrc_t rs = mkrs(ws, (unsigned)WS_END);
  if (r < 8) {
    if (r < 4) role_A1(xcd * 4 + r, xA, wqA, wuk, wuv, woA, ws, rs, out, lds);
    else role_A2(xcd * 4 + r - 4, ckv, ws, rs, lds);
  } else {
    const int rr = r - 8, hq = rr / 6;
    role_B(((0x75643120u >> (4 * xcd)) & 7) + 8 * hq, rr - 6 * hq, xcd, ckv, xB, kc, vc, wqB, woB, ws, rs, out, lds);
  }
  asm volatile(".Lcode_end:" :: "v"(pfv));
}

extern "C" void kernel_launch(void* const* d_in, const int* in_sizes, int n_in,
                              void* d_out, int out_size, void* d_ws, size_t ws_size,
                              hipStream_t stream) {
  k_fused<<<256, 512, 0, stream>>>((const __hip_bfloat16*)d_in[0], (const __hip_bfloat16*)d_in[1], (const __hip_bfloat16*)d_in[2],
                                   (const __hip_bfloat16*)d_in[3], (const __hip_bfloat16*)d_in[4], (const __hip_bfloat16*)d_in[5],
                                   (const __hip_bfloat16*)d_in[6], (const __hip_bfloat16*)d_in[7], (const __hip_bfloat16*)d_in[8],
                                   (const __hip_bfloat16*)d_in[9], (const __hip_bfloat16*)d_in[10], (char*)d_ws,
                                   (__hip_bfloat16*)d_out);
}
```
